```python
import torch

def _softmax(x, axis=-1):
    return torch.softmax(x, dim=axis)

def _gelu(x):
    return torch.nn.functional.gelu(x, approximate='tanh')

def _silu(x):
    return torch.nn.functional.silu(x)

def _sigmoid(x):
    return torch.sigmoid(x)

def _layernorm(x, axis=-1, eps=1e-05):
    m = x.mean(dim=axis, keepdim=True)
    v = x.var(dim=axis, keepdim=True, unbiased=False)
    return (x - m) / torch.sqrt(v + eps)

def _scatter_add(tgt, idx, src, axis=0):
    return tgt.clone().index_add_(axis, idx, src)

def _segment_sum(src, seg, n):
    out = torch.zeros((n,) + src.shape[1:], dtype=src.dtype)
    return out.index_add_(0, seg, src)

def _pad(x, pw):
    flat = tuple((v for pair in reversed(pw) for v in pair))
    return torch.nn.functional.pad(x, flat)
kernel_name = 'adaLN_cond__compressed_kv_cla__dense_activation_relu__decode'

def setup_inputs():

    _w = lambda a: (lambda t: t.to(torch.bfloat16) if t.is_floating_point() else t)(torch.as_tensor(a))

    def _A_setup():
        B, H, Sq, Sk, D, M = (8, 32, 1, 4096, 128, 512)
        Dc = 4 * D
        return {'x': _w((lambda *s: torch.randn(*s, dtype=torch.bfloat16) * 0.05)(B, Sq, M)), 'c_kv': _w((lambda *s: torch.randn(*s, dtype=torch.bfloat16) * 1.6)(B, Sk, Dc)), 'w_q': _w((lambda *s: torch.randn(*s, dtype=torch.bfloat16) * 1.6)(M, H, D) * M ** (-0.5)), 'w_uk': _w((lambda *s: torch.randn(*s, dtype=torch.bfloat16) * 1.6)(Dc, H, D) * Dc ** (-0.5)), 'w_uv': _w((lambda *s: torch.randn(*s, dtype=torch.bfloat16) * 1.6)(Dc, H, D) * Dc ** (-0.5)), 'w_o': _w((lambda *s: torch.randn(*s, dtype=torch.bfloat16) * 1.6)(H, D, M) * (H * D) ** (-0.5))}
    _A_in = _A_setup()

    def _B_setup():
        B, S, D, Dff = (8, 1, 512, 1792)
        return {'x': _w((lambda *s: torch.randn(*s, dtype=torch.bfloat16) * 0.05)(B, S, D)), 'w1': _w((lambda *s: torch.randn(*s, dtype=torch.bfloat16) * 0.05)(D, Dff)), 'w2': _w((lambda *s: torch.randn(*s, dtype=torch.bfloat16) * 0.05)(Dff, D))}
    _B_in = _B_setup()
    return {'A_x': _A_in['x'], 'A_c_kv': _A_in['c_kv'], 'A_w_q': _A_in['w_q'], 'A_w_uk': _A_in['w_uk'], 'A_w_uv': _A_in['w_uv'], 'A_w_o': _A_in['w_o'], 'B_x': _B_in['x'], 'B_w1': _B_in['w1'], 'B_w2': _B_in['w2'], 'c': _w((lambda *s: torch.randn(*s, dtype=torch.bfloat16) * 0.2)(8, 512) * 8.0), 'w_mod': _w((lambda *s: torch.randn(*s, dtype=torch.bfloat16) * 0.2)(512, 6 * 512) * 512 ** (-0.5) * 8.0)}

def reference(A_x, A_c_kv, A_w_q, A_w_uk, A_w_uv, A_w_o, B_x, B_w1, B_w2, c, w_mod):

    def _A_ref(x, c_kv, w_q, w_uk, w_uv, w_o):
        B, Sq, M = (x.shape[0], x.shape[1], x.shape[2])
        Sk = c_kv.shape[1]
        H, D = (w_q.shape[1], w_q.shape[2])
        q = torch.einsum('bqm,mhd->bhqd', x, w_q)
        k = torch.einsum('bkc,chd->bhkd', c_kv, w_uk)
        v = torch.einsum('bkc,chd->bhkd', c_kv, w_uv)
        scores = torch.einsum('bhqd,bhkd->bhqk', q, k) * D ** (-0.5)
        off = Sk - Sq
        qi = torch.arange(Sq)[:, None]
        kj = torch.arange(Sk)[None, :]
        cmask = kj <= qi + off
        scores = torch.where(cmask[None, None], scores, -1000000000.0)
        p = _softmax(scores, axis=-1)
        ctx = torch.einsum('bhqk,bhkd->bhqd', p, v)
        return torch.einsum('bhqd,hdm->bqm', ctx, w_o)

    def _B_ref(x, w1, w2):
        h = torch.einsum('bsd,df->bsf', x, w1)
        zero = torch.zeros_like(h)
        h = torch.maximum(h, zero)
        return torch.einsum('bsf,fd->bsd', h, w2)
    x = A_x
    mod = (_silu(c) @ w_mod).reshape(c.shape[0], 6, 1, -1)
    sa, ca, ga, sb, cb, gb = (mod[:, 0].clone(), mod[:, 1].clone(), mod[:, 2].clone(), mod[:, 3].clone(), mod[:, 4].clone(), mod[:, 5].clone())
    h = _layernorm(x, axis=-1) * (1 + ca) + sa
    x = x + ga * _A_ref(h, A_c_kv, A_w_q, A_w_uk, A_w_uv, A_w_o)
    h = _layernorm(x, axis=-1) * (1 + cb) + sb
    x = x + gb * _B_ref(h, B_w1, B_w2)
    return x
```

```cpp
#include <hip/hip_runtime.h>
#include <hip/hip_bf16.h>
typedef unsigned short u16;
__device__ __forceinline__ float bf2f(u16 b){ return __uint_as_float(((unsigned)b)<<16); }
__device__ __forceinline__ u16 f2bf(float f){ unsigned u=__float_as_uint(f); u += 0x7FFFu + ((u>>16)&1u); return (u16)(u>>16);}

constexpr int B=8,H=32,SK=4096,D=128,M=512,DC=512,DFF=1792;

__global__ void k_mod(const u16* c, const u16* w, float* mod){
  __shared__ float a[8][512];
  for(int i=threadIdx.x;i<8*512;i+=blockDim.x){ float v=bf2f(c[i]); a[i/512][i%512]=v/(1.f+__expf(-v)); }
  __syncthreads();
  int n=blockIdx.x*blockDim.x+threadIdx.x;
  float acc[8]={0};
  for(int k=0;k<512;k++){ float wv=bf2f(w[(size_t)k*3072+n]); for(int b=0;b<8;b++) acc[b]+=a[b][k]*wv; }
  for(int b=0;b<8;b++) mod[b*3072+n]=acc[b];
}
__device__ void ln_mod(const float* x  , const float* mod, int soff, int coff, float (*h)[512]){
  __shared__ float mean[8], rstd[8];
  if(threadIdx.x<8){ int b=threadIdx.x; float s=0; for(int i=0;i<512;i++) s+=x[b*512+i]; s/=512; float v=0; for(int i=0;i<512;i++){float d=x[b*512+i]-s; v+=d*d;} v/=512; mean[b]=s; rstd[b]=1.f/sqrtf(v+1e-5f);}
  __syncthreads();
  for(int i=threadIdx.x;i<8*512;i+=blockDim.x){ int b=i/512,m=i%512; h[b][m]=(x[i]-mean[b])*rstd[b]*(1.f+mod[b*3072+coff+m])+mod[b*3072+soff+m]; }
  __syncthreads();
}
__global__ void k_q(const u16* x, const float* mod, const u16* wq, float* q){
  __shared__ float xs[8*512]; __shared__ float h[8][512];
  for(int i=threadIdx.x;i<8*512;i+=blockDim.x) xs[i]=bf2f(x[i]);
  __syncthreads();
  ln_mod(xs,mod,0,512,h);
  int n=blockIdx.x*blockDim.x+threadIdx.x;
  float acc[8]={0};
  for(int k=0;k<512;k++){ float wv=bf2f(wq[(size_t)k*4096+n]); for(int b=0;b<8;b++) acc[b]+=h[b][k]*wv; }
  for(int b=0;b<8;b++) q[b*4096+n]=acc[b];
}
__global__ void k_qabs(const float* q, const u16* wuk, float* qabs){
  int h=blockIdx.x; int c=blockIdx.y*64+threadIdx.x;
  __shared__ float qs[8][128];
  for(int i=threadIdx.x;i<8*128;i+=64) qs[i/128][i%128]=q[(i/128)*4096+h*128+i%128];
  __syncthreads();
  float acc[8]={0};
  for(int d=0;d<128;d++){ float wv=bf2f(wuk[(size_t)c*4096+h*128+d]); for(int b=0;b<8;b++) acc[b]+=qs[b][d]*wv; }
  for(int b=0;b<8;b++) qabs[(b*32+h)*512+c]=acc[b]*0.08838834764831845f;
}
__global__ void k_attn(const float* qabs, const u16* ckv, float* part, float* ml){
  int s=blockIdx.x,b=blockIdx.y; int t=threadIdx.x;
  __shared__ float qs[32][512]; __shared__ float sc[32][128];
  for(int i=t;i<32*512;i+=256) qs[i/512][i%512]=qabs[b*32*512+i];
  __syncthreads();
  const u16* kv=ckv+((size_t)b*4096+s*128)*512;
  { int key=t&127; int hh=(t>>7)*16; float acc[16]={0};
    for(int c=0;c<512;c++){ float v=bf2f(kv[(size_t)key*512+c]); for(int j=0;j<16;j++) acc[j]+=qs[hh+j][c]*v; }
    for(int j=0;j<16;j++) sc[hh+j][key]=acc[j]; }
  __syncthreads();
  if(t<32){ float m=-1e30f; for(int k=0;k<128;k++) m=fmaxf(m,sc[t][k]); float l=0; for(int k=0;k<128;k++){ float p=__expf(sc[t][k]-m); sc[t][k]=p; l+=p;} ml[((b*32+s)*32+t)*2]=m; ml[((b*32+s)*32+t)*2+1]=l; }
  __syncthreads();
  for(int cc=0;cc<2;cc++){ int c=t+cc*256; float acc[32]={0};
    for(int k=0;k<128;k++){ float v=bf2f(kv[(size_t)k*512+c]); for(int j=0;j<32;j++) acc[j]+=sc[j][k]*v; }
    for(int j=0;j<32;j++) part[((size_t)(b*32+s)*32+j)*512+c]=acc[j]; }
}
__global__ void k_comb(const float* part, const float* ml, const u16* wuv, float* ctx){
  int h=blockIdx.x,b=blockIdx.y,t=threadIdx.x;
  __shared__ float o[512]; __shared__ float w[32];
  if(t==0){ float m=-1e30f; for(int s=0;s<32;s++) m=fmaxf(m,ml[((b*32+s)*32+h)*2]); float l=0; for(int s=0;s<32;s++){ w[s]=__expf(ml[((b*32+s)*32+h)*2]-m); l+=w[s]*ml[((b*32+s)*32+h)*2+1]; } for(int s=0;s<32;s++) w[s]/=l; }
  __syncthreads();
  for(int c=t;c<512;c+=128){ float a=0; for(int s=0;s<32;s++) a+=w[s]*part[((size_t)(b*32+s)*32+h)*512+c]; o[c]=a; }
  __syncthreads();
  float a=0; for(int c=0;c<512;c++) a+=o[c]*bf2f(wuv[(size_t)c*4096+h*128+t]);
  ctx[b*4096+h*128+t]=a;
}
__global__ void k_wo(const float* ctx, const u16* wo, const u16* x, const float* mod, float* x1){
  int m=blockIdx.x*64+threadIdx.x,b=blockIdx.y;
  float a=0; for(int k=0;k<4096;k++) a+=ctx[b*4096+k]*bf2f(wo[(size_t)k*512+m]);
  x1[b*512+m]=bf2f(x[b*512+m])+mod[b*3072+1024+m]*a;
}
__global__ void k_f1(const float* x1, const float* mod, const u16* w1, float* f){
  __shared__ float xs[8*512]; __shared__ float h[8][512];
  for(int i=threadIdx.x;i<8*512;i+=blockDim.x) xs[i]=x1[i];
  __syncthreads();
  ln_mod(xs,mod,1536,2048,h);
  int n=blockIdx.x*blockDim.x+threadIdx.x;
  float acc[8]={0};
  for(int k=0;k<512;k++){ float wv=bf2f(w1[(size_t)k*1792+n]); for(int b=0;b<8;b++) acc[b]+=h[b][k]*wv; }
  for(int b=0;b<8;b++) f[b*1792+n]=fmaxf(acc[b],0.f);
}
__global__ void k_f2(const float* f, const u16* w2, const float* x1, const float* mod, u16* out){
  int m=blockIdx.x*64+threadIdx.x,b=blockIdx.y;
  float a=0; for(int k=0;k<1792;k++) a+=f[b*1792+k]*bf2f(w2[(size_t)k*512+m]);
  out[b*512+m]=f2bf(x1[b*512+m]+mod[b*3072+2560+m]*a);
}
extern "C" void kernel_launch(void* const* d_in, const int* in_sizes, int n_in, void* d_out, int out_size, void* d_ws, size_t ws_size, hipStream_t stream){
  const u16* x=(const u16*)d_in[0]; const u16* ckv=(const u16*)d_in[1]; const u16* wq=(const u16*)d_in[2]; const u16* wuk=(const u16*)d_in[3];
  const u16* wuv=(const u16*)d_in[4]; const u16* wo=(const u16*)d_in[5]; const u16* w1=(const u16*)d_in[7]; const u16* w2=(const u16*)d_in[8];
  const u16* c=(const u16*)d_in[9]; const u16* wmod=(const u16*)d_in[10];
  float* ws=(float*)d_ws;
  float* mod=ws; float* q=mod+8*3072; float* qabs=q+8*4096; float* ml=qabs+8*32*512; float* ctx=ml+8*32*32*2; float* x1=ctx+8*4096; float* f=x1+8*512; float* part=f+8*1792;
  k_mod<<<48,64,0,stream>>>(c,wmod,mod);
  k_q<<<64,64,0,stream>>>(x,mod,wq,q);
  k_qabs<<<dim3(32,8),64,0,stream>>>(q,wuk,qabs);
  k_attn<<<dim3(32,8),256,0,stream>>>(qabs,ckv,part,ml);
  k_comb<<<dim3(32,8),128,0,stream>>>(part,ml,wuv,ctx);
  k_wo<<<dim3(8,8),64,0,stream>>>(ctx,wo,x,mod,x1);
  k_f1<<<28,64,0,stream>>>(x1,mod,w1,f);
  k_f2<<<dim3(8,8),64,0,stream>>>(f,w2,x1,mod,(u16*)d_out);
}
```

```cpp
#include <hip/hip_runtime.h>
#include <hip/hip_bf16.h>
#pragma clang diagnostic ignored "-Wint-to-pointer-cast"

typedef unsigned short u16;
typedef unsigned long long u64;
typedef unsigned v4u __attribute__((ext_vector_type(4)));
typedef unsigned v2u __attribute__((ext_vector_type(2)));
typedef float f32x2 __attribute__((ext_vector_type(2)));
typedef float f32x4 __attribute__((ext_vector_type(4)));
typedef float f32x16 __attribute__((ext_vector_type(16)));
typedef short s16x4 __attribute__((ext_vector_type(4)));
typedef short bf16x8 __attribute__((ext_vector_type(8)));
typedef __bf16 bf16x2 __attribute__((ext_vector_type(2)));
#define GAS __attribute__((address_space(1)))
#define LAS __attribute__((address_space(3)))
#define DI __device__ __forceinline__
#define RLX_AGENT __ATOMIC_RELAXED, __HIP_MEMORY_SCOPE_AGENT
typedef __amdgpu_buffer_rsrc_t rsrc_t;

constexpr int WS_FLAGS = 4096, FLAG_STR = 128;
constexpr int WS_ARENA = 65536, ASTR = 58368;
constexpr int A_QP = 0, A_HM = 4096, A_QA = 6144, A_ML = 8192, A_X1 = 8448, A_PART = 9216, A_OUTP = 41984, A_YP = 50176;
constexpr int WS_BYTES = WS_ARENA + 128 * ASTR;
constexpr unsigned SENT = 0xFFFFFFFFu;
constexpr int OOB = 0x7FFFFF00;
constexpr int SC1 = 16;
constexpr int SPIN_G = 1 << 14;
constexpr int SPIN_L = 1 << 20;
constexpr int LDS_BYTES = 163840;
constexpr float EPS = 1e-5f;
constexpr float QSCALE = 0.08838834764831845f * 1.4426950408889634f;
constexpr float BUMP = 40.f;

#define STAMP(k) do {} while (0)

DI float bflo(unsigned u) { return __uint_as_float(u << 16); }
DI float bfhi(unsigned u) { return __uint_as_float(u & 0xFFFF0000u); }
DI unsigned pk(float a, float b) { f32x2 v = {a, b}; return __builtin_bit_cast(unsigned, __builtin_convertvector(v, bf16x2)); }
DI f32x4 mfma16(v4u a, v4u b, f32x4 c) {
  return __builtin_amdgcn_mfma_f32_16x16x32_bf16(__builtin_bit_cast(bf16x8, a), __builtin_bit_cast(bf16x8, b), c, 0, 0, 0);
}
DI f32x16 mfma32(v4u a, v4u b, f32x16 c) {
  return __builtin_amdgcn_mfma_f32_32x32x16_bf16(__builtin_bit_cast(bf16x8, a), __builtin_bit_cast(bf16x8, b), c, 0, 0, 0);
}
DI v4u ldg(rsrc_t rs, int off) { return __builtin_amdgcn_raw_buffer_load_b128(rs, off, 0, SC1); }
DI void stg(rsrc_t rs, int off, v4u v) { __builtin_amdgcn_raw_buffer_store_b128(v, rs, off, 0, SC1); }
DI unsigned umax4(v4u v) { return max(max(v.x, v.y), max(v.z, v.w)); }
DI v4u f2u(f32x4 v) { return __builtin_bit_cast(v4u, v); }
DI f32x4 u2f(v4u v) { return __builtin_bit_cast(f32x4, v); }
DI v4u lds128(unsigned a, int o = 0) { return *(LAS v4u*)((LAS char*)a + o); }
DI v2u ldstr(unsigned a, int o = 0) { return __builtin_bit_cast(v2u, __builtin_amdgcn_ds_read_tr16_b64_v4i16((LAS s16x4*)((LAS char*)a + o))); }
DI void bar() {
  __builtin_amdgcn_sched_barrier(0);
  asm volatile("s_waitcnt lgkmcnt(0)\n\ts_barrier" ::: "memory");
  __builtin_amdgcn_sched_barrier(0);
}
#define VMCNT(n) asm volatile("s_waitcnt vmcnt(" #n ")" ::: "memory")
template <bool NT> DI void dma16(const void* base, unsigned voff, unsigned dst) {
  if (NT) asm volatile("s_mov_b32 m0, %2\n\ts_nop 0\n\tglobal_load_lds_dwordx4 %0, %1 nt" :: "v"(voff), "s"(base), "s"(dst) : "memory");
  else asm volatile("s_mov_b32 m0, %2\n\ts_nop 0\n\tglobal_load_lds_dwordx4 %0, %1" :: "v"(voff), "s"(base), "s"(dst) : "memory");
}
template <bool NT> DI void dma_run(const void* base, unsigned voff, unsigned dst, int n, int gstr, int lstr) {
  const unsigned char* b = (const unsigned char*)base;
#pragma unroll 1
  for (int i = 0; i < n; ++i) { dma16<NT>(b, voff, dst); b += gstr; dst += lstr; }
}
DI void lsig(unsigned a, int lane) {
  asm volatile("s_mov_b64 exec, 1\n\tds_add_u32 %0, %1\n\ts_mov_b64 exec, -1" :: "v"(a), "v"(1u) : "memory");
}
DI void lwait(unsigned a, unsigned want) {
#pragma unroll 1
  for (int i = 0; i < SPIN_L; ++i) {
    unsigned v = __builtin_amdgcn_readfirstlane(*(volatile LAS unsigned*)a);
    if (v >= want) break;
  }
  asm volatile("" ::: "memory");
}
template <int CTRL> DI float dppf(float v) {
  return __builtin_bit_cast(float, __builtin_amdgcn_update_dpp(0, __builtin_bit_cast(int, v), CTRL, 0xF, 0xF, true));
}
DI float row16_max(float v) {
  v = fmaxf(v, dppf<0x128>(v)); v = fmaxf(v, dppf<0x124>(v)); v = fmaxf(v, dppf<0x122>(v)); return fmaxf(v, dppf<0x121>(v));
}
DI float row16_sum(float v) { v += dppf<0x128>(v); v += dppf<0x124>(v); v += dppf<0x122>(v); return v + dppf<0x121>(v); }
DI float xsum16(float v) { auto r = __builtin_amdgcn_permlane16_swap(__float_as_uint(v), __float_as_uint(v), false, false); return __uint_as_float(r[0]) + __uint_as_float(r[1]); }
DI float xsum32(float v) { auto r = __builtin_amdgcn_permlane32_swap(__float_as_uint(v), __float_as_uint(v), false, false); return __uint_as_float(r[0]) + __uint_as_float(r[1]); }
DI float xmax16(float v) { auto r = __builtin_amdgcn_permlane16_swap(__float_as_uint(v), __float_as_uint(v), false, false); return fmaxf(__uint_as_float(r[0]), __uint_as_float(r[1])); }
DI float xmax32(float v) { auto r = __builtin_amdgcn_permlane32_swap(__float_as_uint(v), __float_as_uint(v), false, false); return fmaxf(__uint_as_float(r[0]), __uint_as_float(r[1])); }
DI float wave_sum(float v) { return xsum32(xsum16(row16_sum(v))); }

template <int N> DI void gather(rsrc_t rs, int off0, int str, v4u (&r)[N]) {
#pragma unroll 1
  for (int sp = 0; sp < SPIN_G; ++sp) {
    asm volatile("" ::: "memory");
#pragma unroll
    for (int i = 0; i < N; ++i) r[i] = __builtin_amdgcn_raw_buffer_load_b128(rs, off0, i * str, SC1);
    __builtin_amdgcn_sched_barrier(0);
    unsigned m = 0;
#pragma unroll
    for (int i = 0; i < N; ++i) m = max(m, umax4(r[i]));
    if (!__any(m == SENT)) break;
  }
}
DI void wait_flag(const unsigned char* ws, int wid, u64 tag) {
  const GAS u64* f = (const GAS u64*)(ws + WS_FLAGS + wid * FLAG_STR);
#pragma unroll 1
  for (int sp = 0; sp < SPIN_G; ++sp) {
    if (__all(__hip_atomic_load(f, RLX_AGENT) == tag)) break;
    __builtin_amdgcn_s_sleep(4);
  }
  asm volatile("" ::: "memory");
}
DI int arena(int wid) { return WS_ARENA + wid * ASTR; }

DI unsigned tr_addr(unsigned region, int lane, int col4, int t) {
  const int g = lane >> 4, q = (lane & 15) >> 2;
  return region + (8 * g + 4 * t + q) * 256 + 16 * ((col4 >> 3) ^ ((q << 2) | ((2 * g + t) & 3))) + 8 * ((col4 >> 2) & 1);
}
DI v4u tr_frag(unsigned a0, unsigned a1, int ks) {
  const v2u lo = ldstr(a0, ks * 8192), hi = ldstr(a1, ks * 8192);
  return v4u{lo.x, lo.y, hi.x, hi.y};
}

struct Params {
  const u16 *x, *ckv, *wq, *wuk, *wuv, *wo, *w1, *w2, *c, *wmod;
  u16* out;
  unsigned char* ws;
};

constexpr unsigned W0 = 0, W1 = 32768, W2 = 65536, L_SC = 98304  , L_LNX = L_SC + 8320  ,
                   L_A1 = L_LNX + 2048  , L_A2 = L_A1 + 2176, L_A3 = L_A2 + 2176, L_A4 = L_A3 + 2176,
                   L_A5 = L_A4 + 2176  , L_A6 = L_A5 + 8320  ;
constexpr unsigned T_P = 131072  , T_REF = T_P + 16384  , T_XCH = T_REF + 2048  ,
                   T_LF = T_XCH + 256  , T_CNT = T_LF + 256;
constexpr unsigned C_LAND = T_CNT  , C_PRDY = T_CNT + 32  , C_FREE = T_CNT + 64  , C_QST = T_CNT + 96, C_QRD = T_CNT + 100,
                   C_XCH = T_CNT + 104  ;

DI void gather4_act(rsrc_t rs, int off0, int str, int t, unsigned act) {
  v4u r[4];
  gather<4>(rs, off0, str, r);
  const f32x4 s = (u2f(r[0]) + u2f(r[1])) + (u2f(r[2]) + u2f(r[3]));
  *(LAS v2u*)(act + ((t >> 2) & 7) * 272 + (16 * (t >> 5) + 4 * (t & 3)) * 2) = v2u{pk(s.x, s.y), pk(s.z, s.w)};
}

DI void weight_wg(const Params& p, u64 tag, int g, int wi, int wave, int lane) {
  unsigned char* ws = p.ws;
  const rsrc_t rs = __builtin_amdgcn_make_buffer_rsrc(ws, 0, WS_BYTES, 0x00020000);
  const int h = 4 * g + (wi >> 2), j = wi & 3, wid = 16 * g + wi, my = arena(wid);
  const int l15 = lane & 15, lq = lane >> 4, b8 = lane & 7;
  const bool bvalid = l15 < 8;
  const bool ffn = j >= 2 && h < 28;
  const int fg = 2 * h + (j - 2);

  if (wave < 4) {
    const int w = wave;
    const int ch = l15 ^ ((lq << 2) | w);
    const int row0 = 4 * w + lq;
    const unsigned dst0 = w * 1024;
    if (j == 0) {
      const unsigned v = (128 * (h & 3) + row0) * 6144 + (ch >> 3) * 1024 + g * 128 + (ch & 7) * 16;
      dma_run<true>(p.wmod, v, W2 + dst0, 8, 16 * 6144, 4096);
    }
    {
      const unsigned v = (128 * j + row0) * 8192 + h * 256 + ch * 16;
      dma_run<true>(p.wq, v, W0 + dst0, 8, 16 * 8192, 4096);
      const unsigned u = (128 * j + 32 * w + lq) * 8192 + h * 256 + (l15 ^ (lq << 2)) * 16;
      dma_run<true>(p.wuk, u, W1 + w * 8192, 8, 4 * 8192, 1024);
    }
    const int p4 = 4 * (lane & 3);
    if (j == 0) {
      VMCNT(16);
      bar();
      STAMP(2);
      const unsigned s0 = tr_addr(W2, lane, 16 * w + p4, 0), s1 = tr_addr(W2, lane, 16 * w + p4, 1);
      const unsigned c0 = tr_addr(W2, lane, 64 + 16 * w + p4, 0), c1 = tr_addr(W2, lane, 64 + 16 * w + p4, 1);
      const unsigned ba = L_SC + b8 * 1040 + (128 * (h & 3) + 8 * lq) * 2;
      f32x4 sa = {0.f, 0.f, 0.f, 0.f};
      const float one = (h & 3) == 0 ? 1.f : 0.f;
      f32x4 ca = {one, one, one, one};
#pragma unroll
      for (int ks = 0; ks < 4; ++ks) {
        const v4u bf = lds128(ba, ks * 64);
        sa = mfma16(tr_frag(s0, s1, ks), bf, sa);
        ca = mfma16(tr_frag(c0, c1, ks), bf, ca);
      }
      const f32x4 ln = u2f(lds128(L_LNX + b8 * 256 + (16 * w + 4 * lq) * 4));
      stg(rs, bvalid ? my + A_HM + w * 512 + l15 * 64 + lq * 16 : OOB, f2u(sa + ln * ca));
      STAMP(3);
      VMCNT(9);
    } else {
      VMCNT(8);
    }
    bar();
    STAMP(4);
    f32x4 z4 = {0.f, 0.f, 0.f, 0.f};
    {
      v4u fq[2][4];
#pragma unroll
      for (int tt = 0; tt < 2; ++tt) {
        const unsigned a0 = tr_addr(W0, lane, 32 * w + 16 * tt + p4, 0), a1 = tr_addr(W0, lane, 32 * w + 16 * tt + p4, 1);
#pragma unroll
        for (int ks = 0; ks < 4; ++ks) fq[tt][ks] = tr_frag(a0, a1, ks);
      }
      bar();
      f32x4 q0 = z4, q1 = z4;
#pragma unroll
      for (int ks = 0; ks < 4; ++ks) {
        const v4u bf = lds128(L_A1 + b8 * 272 + lq * 16, ks * 64);
        q0 = mfma16(fq[0][ks], bf, q0);
        q1 = mfma16(fq[1][ks], bf, q1);
      }
      const int o = bvalid ? my + A_QP + w * 1024 + l15 * 64 + lq * 16 : OOB;
      stg(rs, o, f2u(q0));
      stg(rs, o + 512, f2u(q1));
      STAMP(6);
    }
    {
      VMCNT(2);
      v4u fk[2][4];
#pragma unroll
      for (int tt = 0; tt < 2; ++tt) {
        const int row = 32 * w + 8 * (l15 >> 2) + 4 * tt + (l15 & 3);
#pragma unroll
        for (int ks = 0; ks < 4; ++ks) fk[tt][ks] = lds128(W1 + row * 256 + 16 * ((4 * ks + lq) ^ ((l15 & 3) << 2)));
      }
      bar();
      f32x4 a0 = z4, a1 = z4;
#pragma unroll
      for (int ks = 0; ks < 4; ++ks) {
        const v4u bf = lds128(L_A2 + b8 * 272 + lq * 16, ks * 64);
        a0 = mfma16(fk[0][ks], bf, a0);
        a1 = mfma16(fk[1][ks], bf, a1);
      }
      a0 *= QSCALE; a1 *= QSCALE;
      stg(rs, bvalid ? my + A_QA + l15 * 256 + w * 64 + lq * 16 : OOB, v4u{pk(a0.x, a0.y), pk(a0.z, a0.w), pk(a1.x, a1.y), pk(a1.z, a1.w)});
      STAMP(8);
    }
    bar();
    if (j == 1) {
      const unsigned v = (row0 + (ch >> 3) * 256) * 6144 + 2048 + h * 128 + (ch & 7) * 16;
      dma_run<true>(p.wmod, v, W1 + dst0, 16, 16 * 6144, 4096);
    }
    {
      const unsigned v = (128 * j + 32 * lq + w) * 8192 + h * 256 + ch * 16;
      dma_run<true>(p.wuv, v, W0 + dst0, 8, 4 * 8192, 4096);
    }
    if (ffn) {
      const unsigned v = (row0 + (ch >> 2) * 128) * 3584 + fg * 64 + (ch & 3) * 16;
      dma_run<false>(p.w1, v, W1 + dst0, 8, 16 * 3584, 4096);
      const unsigned u = (32 * fg + row0) * 1024 + ch * 16;
      dma_run<true>(p.w2, u, W2 + dst0, 4, 256, 8192);
      dma_run<true>(p.w2, u + 16 * 1024, W2 + dst0 + 4096, 4, 256, 8192);
    }
    VMCNT(0);
    bar();
    STAMP(9);
    if (j == 1) {
      const unsigned s0 = tr_addr(W1, lane, 16 * w + p4, 0), s1 = tr_addr(W1, lane, 16 * w + p4, 1);
      const unsigned c0 = tr_addr(W1, lane, 64 + 16 * w + p4, 0), c1 = tr_addr(W1, lane, 64 + 16 * w + p4, 1);
      const unsigned ba = L_SC + b8 * 1040 + lq * 16;
      f32x4 m0 = z4, m1 = z4;
#pragma unroll 2
      for (int ks = 0; ks < 8; ++ks) {
        m0 = mfma16(tr_frag(s0, s1, ks), lds128(ba, ks * 64), m0);
        m1 = mfma16(tr_frag(c0, c1, ks), lds128(ba, 512 + ks * 64), m1);
      }
      stg(rs, bvalid ? my + A_HM + l15 * 256 + w * 64 + lq * 16 : OOB, f2u(m0 + m1));
    }
    {
      v4u fv[2][4];
#pragma unroll
      for (int tt = 0; tt < 2; ++tt) {
        const unsigned a0 = tr_addr(W0, lane, 32 * w + 2 * p4 + 4 * tt, 0), a1 = tr_addr(W0, lane, 32 * w + 2 * p4 + 4 * tt, 1);
#pragma unroll
        for (int ks = 0; ks < 4; ++ks) fv[tt][ks] = tr_frag(a0, a1, ks);
      }
      bar();
      f32x4 c0 = z4, c1 = z4;
#pragma unroll
      for (int ks = 0; ks < 4; ++ks) {
        const v4u bf = lds128(L_A3 + b8 * 272 + lq * 16, ks * 64);
        c0 = mfma16(fv[0][ks], bf, c0);
        c1 = mfma16(fv[1][ks], bf, c1);
      }
      if (bvalid) *(LAS v4u*)(L_A4 + l15 * 272 + w * 64 + lq * 16) = v4u{pk(c0.x, c0.y), pk(c0.z, c0.w), pk(c1.x, c1.y), pk(c1.z, c1.w)};
      bar();
      STAMP(13);
    }
    if (ffn) {
      v4u f1[4][4], f2[4][2];
      const int nt = w & 1;
#pragma unroll
      for (int kq = 0; kq < 4; ++kq) {
        const unsigned a0 = tr_addr(W1, lane, 32 * kq + 16 * nt + p4, 0), a1 = tr_addr(W1, lane, 32 * kq + 16 * nt + p4, 1);
#pragma unroll
        for (int ks = 0; ks < 4; ++ks) f1[kq][ks] = tr_frag(a0, a1, ks);
      }
#pragma unroll
      for (int u = 0; u < 4; ++u)
#pragma unroll
        for (int tt = 0; tt < 2; ++tt)
          f2[u][tt] = tr_frag(tr_addr(W2 + w * 8192, lane, 32 * u + 2 * p4 + 4 * tt, 0), tr_addr(W2 + w * 8192, lane, 32 * u + 2 * p4 + 4 * tt, 1), 0);
      bar();
      if (w < 2) {
        f32x4 z0 = z4, z1 = z4;
#pragma unroll
        for (int kq = 0; kq < 4; ++kq)
#pragma unroll
          for (int ks = 0; ks < 4; ks += 2) {
            z0 = mfma16(f1[kq][ks], lds128(L_A5 + b8 * 1040 + lq * 16, kq * 256 + ks * 64), z0);
            z1 = mfma16(f1[kq][ks + 1], lds128(L_A5 + b8 * 1040 + lq * 16, kq * 256 + ks * 64 + 64), z1);
          }
        z0 += z1;
        const f32x4 r = {z0.x < 0.f ? 0.f : z0.x, z0.y < 0.f ? 0.f : z0.y, z0.z < 0.f ? 0.f : z0.z, z0.w < 0.f ? 0.f : z0.w};
        if (bvalid) *(LAS v2u*)(L_A6 + l15 * 80 + nt * 32 + lq * 8) = v2u{pk(r.x, r.y), pk(r.z, r.w)};
      }
      bar();
      const v4u bf = lds128(L_A6 + b8 * 80 + lq * 16);
      const int o = bvalid ? my + A_YP + l15 * 1024 + w * 256 + lq * 16 : OOB;
#pragma unroll
      for (int u = 0; u < 4; ++u) {
        const f32x4 y0 = mfma16(f2[u][0], bf, z4), y1 = mfma16(f2[u][1], bf, z4);
        stg(rs, o + u * 64, v4u{pk(y0.x, y0.y), pk(y0.z, y0.w), pk(y1.x, y1.y), pk(y1.z, y1.w)});
      }
      STAMP(16);
    }
  } else {
    if (wave < 8) {
      const int gw = wave - 4, t = gw * 64 + lane;
      v4u cr[2], xr[2];
      if (j <= 1) {
#pragma unroll
        for (int r = 0; r < 2; ++r) {
          cr[r] = *(const GAS v4u*)(p.c + (2 * gw + r) * 512 + lane * 8);
          xr[r] = *(const GAS v4u*)(p.x + (2 * gw + r) * 512 + lane * 8);
        }
        __builtin_amdgcn_sched_barrier(0);
#pragma unroll
        for (int r = 0; r < 2; ++r) {
          const int b = 2 * gw + r;
          const v4u cv = cr[r];
          v4u o;
#pragma unroll
          for (int e = 0; e < 4; ++e) {
            const float a = bflo(cv[e]), c = bfhi(cv[e]);
            o[e] = pk(a * __builtin_amdgcn_rcpf(1.f + __expf(-a)), c * __builtin_amdgcn_rcpf(1.f + __expf(-c)));
          }
          *(LAS v4u*)(L_SC + b * 1040 + lane * 16) = o;
        }
      }
      if (j == 0) {
#pragma unroll
        for (int r = 0; r < 2; ++r) {
          const int b = 2 * gw + r;
          const v4u xv = xr[r];
          float v[8];
#pragma unroll
          for (int e = 0; e < 4; ++e) { v[2 * e] = bflo(xv[e]); v[2 * e + 1] = bfhi(xv[e]); }
          float s = 0.f;
#pragma unroll
          for (int e = 0; e < 8; ++e) s += v[e];
          const float mean = wave_sum(s) * (1.f / 512);
          float q = 0.f;
#pragma unroll
          for (int e = 0; e < 8; ++e) { v[e] -= mean; q += v[e] * v[e]; }
          const float rstd = rsqrtf(wave_sum(q) * (1.f / 512) + EPS);
          if ((lane >> 3) == g) {
            *(LAS f32x4*)(L_LNX + b * 256 + b8 * 32) = f32x4{v[0] * rstd, v[1] * rstd, v[2] * rstd, v[3] * rstd};
            *(LAS f32x4*)(L_LNX + b * 256 + b8 * 32 + 16) = f32x4{v[4] * rstd, v[5] * rstd, v[6] * rstd, v[7] * rstd};
          }
        }
        bar();
      }
      bar();
      wait_flag(ws, bvalid ? 32 * j + 4 * l15 : 4 * h + (lane & 3), tag);
      gather4_act(rs, arena(32 * j + 16 * (t >> 7)) + A_HM + (t & 127) * 16, 4 * ASTR, t, L_A1);
      STAMP(5);
      bar();
      gather4_act(rs, arena(4 * h) + A_QP + t * 16, ASTR, t, L_A2);
      STAMP(7);
      bar();
      bar();
      wait_flag(ws, t & 127, tag);
      bar();
      float wgt;
      {
        const int mo = arena(32 * gw + (lane & 31)) + A_ML + h * 8;
        v2u ml;
#pragma unroll 1
        for (int sp = 0; sp < SPIN_G; ++sp) {
          ml = __builtin_amdgcn_raw_buffer_load_b64(rs, mo, 0, SC1);
          if (!__any(max(ml.x, ml.y) == SENT)) break;
          __builtin_amdgcn_s_sleep(2);
        }
        STAMP(11);
        const float r = __uint_as_float(ml.x), e = __builtin_amdgcn_exp2f(r - row16_max(r));
        wgt = e / row16_sum(e * __uint_as_float(ml.y));
      }
      v4u d[8];
      const int po = my + A_PART + gw * 8192 + lane * 16;
#pragma unroll
      for (int i = 0; i < 8; ++i) d[i] = ldg(rs, po + i * 1024);
      __builtin_amdgcn_sched_barrier(0);
      v4u sel[8];
      {
        const unsigned half = (lane & 1) ? 0xFFFF0000u : 0x0000FFFFu;
        const int dw = (lane & 7) >> 1;
#pragma unroll
        for (int i = 0; i < 8; ++i) {
          const float wv = __shfl(wgt, 4 * i + lq);
          const unsigned hi = pk(wv, wv);
          const float rest = wv - bflo(hi);
          const unsigned v = (bvalid ? hi : pk(rest, rest)) & half;
          sel[i] = v4u{dw == 0 ? v : 0u, dw == 1 ? v : 0u, dw == 2 ? v : 0u, dw == 3 ? v : 0u};
        }
      }
#pragma unroll 1
      for (int sp = 0; sp < SPIN_G; ++sp) {
        unsigned m = 0;
#pragma unroll
        for (int i = 0; i < 8; ++i) m = max(m, umax4(d[i]));
        if (!__any(m == SENT)) break;
        asm volatile("" ::: "memory");
#pragma unroll
        for (int i = 0; i < 8; ++i) d[i] = ldg(rs, po + i * 1024);
      }
      STAMP(12);
#pragma unroll
      for (int bb = 0; bb < 2; ++bb) {
        f32x4 o = {0.f, 0.f, 0.f, 0.f};
#pragma unroll
        for (int i = 0; i < 4; ++i) o = mfma16(sel[4 * bb + i], d[4 * bb + i], o);
        const f32x4 s = {xsum32(o.x), xsum32(o.y), xsum32(o.z), xsum32(o.w)};
        if (lane < 32) *(LAS v2u*)(L_A3 + (2 * gw + bb) * 272 + l15 * 16 + lq * 8) = v2u{pk(s.x, s.y), pk(s.z, s.w)};
      }
      bar();
      bar();
    } else {
      const int pw = wave - 8;
      const v4u sv = {SENT, SENT, SENT, SENT};
      if (pw == 0) {
#pragma unroll
        for (int n = 0; n < 9; ++n) stg(rs, my + n * 1024 + lane * 16, sv);
        VMCNT(0);
        if (lane == 0) __hip_atomic_store((GAS u64*)(ws + WS_FLAGS + wid * FLAG_STR), tag, RLX_AGENT);
        STAMP(1);
      }
      if (j == 0) bar();
      bar();
#pragma unroll 4
      for (int n = 0; n < 12; ++n) stg(rs, my + A_PART + (pw * 12 + n) * 1024 + lane * 16, sv);
      bar();
      VMCNT(0);
      bar();
      bar();
      v4u wo[4][8];
      const GAS unsigned char* wb = (const GAS unsigned char*)p.wo + (h * 128 + 8 * lq) * 1024 + pw * 256 + (4 * (l15 & 3) + (l15 >> 2)) * 16;
#pragma unroll
      for (int ks = 0; ks < 4; ++ks) {
        const GAS unsigned char* wk = wb + ((ks + j) & 3) * 32768;
#pragma unroll
        for (int r = 0; r < 8; ++r) wo[ks][r] = *(const GAS v4u*)(wk + r * 1024);
      }
      __builtin_amdgcn_sched_barrier(0);
      bar();
#pragma unroll
      for (int ks = 0; ks < 4; ++ks) {
        v4u o[8];
#pragma unroll
        for (int dd = 0; dd < 4; ++dd)
#pragma unroll
          for (int rp = 0; rp < 4; ++rp) {
            o[2 * dd][rp] = __builtin_amdgcn_perm(wo[ks][2 * rp + 1][dd], wo[ks][2 * rp][dd], 0x05040100u);
            o[2 * dd + 1][rp] = __builtin_amdgcn_perm(wo[ks][2 * rp + 1][dd], wo[ks][2 * rp][dd], 0x07060302u);
          }
#pragma unroll
        for (int t = 0; t < 8; ++t) { wo[ks][t] = o[t]; asm volatile("" : "+v"(wo[ks][t])); }
      }
      STAMP(10);
      bar();
      bar();
      v4u bf[4];
#pragma unroll
      for (int ks = 0; ks < 4; ++ks) bf[ks] = lds128(L_A4 + b8 * 272 + ((ks + j) & 3) * 64 + lq * 16);
      v4u res[4];
#pragma unroll
      for (int tp = 0; tp < 4; ++tp) {
        f32x4 e = {0.f, 0.f, 0.f, 0.f}, o = {0.f, 0.f, 0.f, 0.f};
#pragma unroll
        for (int ks = 0; ks < 4; ++ks) { e = mfma16(wo[ks][2 * tp], bf[ks], e); o = mfma16(wo[ks][2 * tp + 1], bf[ks], o); }
#pragma unroll
        for (int r = 0; r < 4; ++r) res[r][tp] = pk(e[r], o[r]);
      }
      const int oo = bvalid ? my + A_OUTP + l15 * 1024 + pw * 256 + lq * 16 : OOB;
#pragma unroll
      for (int r = 0; r < 4; ++r) stg(rs, oo + r * 64, res[r]);
      STAMP(14);
    }
    if (ffn) {
      const int b = wave - 4;
      v4u mm[4];
      const int mo = arena(33 + 4 * (lane >> 3)) + A_HM + b * 256 + b8 * 32;
      {
        v4u a[2], c[2];
        gather<2>(rs, mo, 16, a);
        gather<2>(rs, mo + 32 * ASTR, 16, c);
        mm[0] = a[0]; mm[1] = a[1]; mm[2] = c[0]; mm[3] = c[1];
      }
      v4u xv[1];
      gather<1>(rs, arena(8 * b + (lane >> 3)) + A_X1 + b8 * 16, 0, xv);
      STAMP(15);
      float v[8], s = 0.f, q = 0.f;
#pragma unroll
      for (int e = 0; e < 4; ++e) { v[2 * e] = bflo(xv[0][e]); v[2 * e + 1] = bfhi(xv[0][e]); }
#pragma unroll
      for (int e = 0; e < 8; ++e) s += v[e];
      const float mean = wave_sum(s) * (1.f / 512);
#pragma unroll
      for (int e = 0; e < 8; ++e) { v[e] -= mean; q += v[e] * v[e]; }
      const float rstd = rsqrtf(wave_sum(q) * (1.f / 512) + EPS);
      float o[8];
#pragma unroll
      for (int e = 0; e < 8; ++e)
        o[e] = v[e] * rstd * (1.f + __uint_as_float(mm[2 + (e >> 2)][e & 3])) + __uint_as_float(mm[e >> 2][e & 3]);
      *(LAS v4u*)(L_A5 + b * 1040 + lane * 16) = v4u{pk(o[0], o[1]), pk(o[2], o[3]), pk(o[4], o[5]), pk(o[6], o[7])};
      bar();
      bar();
    }
  }
}

template <bool V> struct BoolC { static constexpr bool value = V; };
DI unsigned fsw(int row) { return ((row & 3) << 2) | ((row >> 2) & 3); }

template <int N> DI f32x4 reduce_rows(rsrc_t rs, int off0, int str, int lane) {
  v4u d[N];
  gather<N>(rs, off0, str, d);
  const unsigned one = (lane & 15) < 8 ? ((lane & 1) ? 0x3F800000u : 0x00003F80u) : 0u;
  const int dw = (lane & 7) >> 1;
  const v4u sel = {dw == 0 ? one : 0u, dw == 1 ? one : 0u, dw == 2 ? one : 0u, dw == 3 ? one : 0u};
  f32x4 a = {0.f, 0.f, 0.f, 0.f}, b = {0.f, 0.f, 0.f, 0.f};
#pragma unroll
  for (int i = 0; i < N; ++i) { if (i & 1) b = mfma16(sel, d[i], b); else a = mfma16(sel, d[i], a); }
  a += b;
  return f32x4{a.x + dppf<0x128>(a.x), a.y + dppf<0x128>(a.y), a.z + dppf<0x128>(a.z), a.w + dppf<0x128>(a.w)};
}

DI void attn_wg(const Params& p, u64 tag, int b, int s, int wave, int lane) {
  unsigned char* ws = p.ws;
  const rsrc_t rs = __builtin_amdgcn_make_buffer_rsrc(ws, 0, WS_BYTES, 0x00020000);
  const unsigned char* kv = (const unsigned char*)p.ckv + (size_t)(b * 4096 + s * 256) * 1024;
  const int l15 = lane & 15, lq = lane >> 4;

  if (wave >= 8) {
    const int lw = wave - 8;
    unsigned vo[8];
#pragma unroll
    for (int r = 0; r < 8; ++r) vo[r] = (8 * lw + r) * 1024 + 16 * (lane ^ fsw(8 * lw + r));
#pragma unroll
    for (int r = 0; r < 8; ++r) dma16<true>(kv, vo[r], (8 * lw + r) * 1024);
    bar();
#pragma unroll
    for (int t = 1; t < 3; ++t) {
#pragma unroll
      for (int r = 0; r < 8; ++r) dma16<true>(kv + t * 32768, vo[r], t * 32768 + (8 * lw + r) * 1024);
      VMCNT(8);
      lsig(C_LAND + 4 * (t - 1), lane);
    }
    v4u land[4][8];
#pragma unroll
    for (int t = 0; t < 4; ++t) {
#pragma unroll
      for (int r = 0; r < 8; ++r) land[t][r] = __builtin_nontemporal_load((const GAS v4u*)(kv + (4 + t) * 32768 + vo[r]));
      __builtin_amdgcn_sched_barrier(0);
      if (t == 0) { VMCNT(8); lsig(C_LAND + 8, lane); STAMP(31); }
    }
    STAMP(22);
#pragma unroll
    for (int t = 0; t < 4; ++t) {
      lwait(C_FREE + 4 * t, 4);
#pragma unroll
      for (int r = 0; r < 8; ++r) *(LAS v4u*)((LAS char*)(t * 32768 + lw * 8192 + lane * 16) + r * 1024) = land[t][r];
      lsig(C_LAND + 16 + 4 * t, lane);
    }
    STAMP(23);
    return;
  }
  __builtin_amdgcn_s_setprio(2);
  v4u t3[8];
  if (wave >= 4) {
#pragma unroll
    for (int r = 0; r < 8; ++r) {
      const int row = 8 * (wave - 4) + r;
      t3[r] = __builtin_nontemporal_load((const GAS v4u*)(kv + 3 * 32768 + row * 1024 + 16 * (lane ^ fsw(row))));
    }
  }
  if (wave == 0 && lane < 32) *(LAS unsigned*)(T_CNT + lane * 4) = 0u;
  bar();
  __builtin_amdgcn_s_sleep(48);
  wait_flag(ws, 16 * wave + l15, tag);
  STAMP(17);
  {
    const int qo = arena(16 * wave + lq) + A_QA + b * 256 + l15 * 16;
#pragma unroll 1
    for (int sp = 0; sp < SPIN_G; ++sp) {
      const unsigned v = __builtin_amdgcn_raw_buffer_load_b32(rs, arena(16 * wave) + A_QA + b * 256, 0, SC1);
      if (__builtin_amdgcn_readfirstlane(v) != SENT) break;
      __builtin_amdgcn_s_sleep(2);
    }
    STAMP(18);
    v4u q[4];
    gather<4>(rs, qo, 4 * ASTR, q);
#pragma unroll
    for (int n = 0; n < 4; ++n) *(LAS v4u*)(3 * 32768 + (4 * wave + n) * 1024 + 16 * (lane ^ fsw(4 * wave + n))) = q[n];
    lsig(C_QST, lane);
  }
  if (wave < 4) {
    const int kh = wave & 1, hh = wave >> 1;
    const int sg = (0x2130 >> (4 * lq)) & 3;
    unsigned ao[4];
#pragma unroll
    for (int k = 0; k < 4; ++k) ao[k] = l15 * 1024 + 16 * ((4 * k + sg) ^ fsw(l15));
    lwait(C_QST, 8);
    STAMP(19);
    v4u qf[16];
#pragma unroll
    for (int ks = 0; ks < 16; ++ks) qf[ks] = lds128(3 * 32768 + hh * 16384 + ao[ks & 3], (ks >> 2) * 256);
#pragma unroll
    for (int ks = 0; ks < 16; ++ks) asm volatile("" : "+v"(qf[ks]));
    lsig(C_QRD, lane);
    float ref = 0.f, l = 0.f;
    const int head = 16 * hh + l15;
#pragma unroll 1
    for (int t = 0; t < 8; ++t) {
      lwait(C_LAND + 4 * t, 4);
      const unsigned tb = (t & 3) * 32768 + kh * 16384;
      f32x4 s0 = {0.f, 0.f, 0.f, 0.f}, s1 = {0.f, 0.f, 0.f, 0.f};
#pragma unroll
      for (int ks = 0; ks < 16; ks += 2) {
        s0 = mfma16(lds128(tb + ao[ks & 3], (ks >> 2) * 256), qf[ks], s0);
        s1 = mfma16(lds128(tb + ao[(ks + 1) & 3], (ks >> 2) * 256), qf[ks + 1], s1);
      }
      s0 += s1;
      const float m4 = fmaxf(fmaxf(s0.x, s0.y), fmaxf(s0.z, s0.w));
      if (t == 0) {
        const float m = xmax32(xmax16(m4));
        if (lq == 0) *(LAS float*)(T_XCH + kh * 128 + head * 4) = m;
        lsig(C_XCH + 4 * hh, lane);
        lwait(C_XCH + 4 * hh, 2);
        ref = fmaxf(m, *(volatile LAS float*)(T_XCH + (kh ^ 1) * 128 + head * 4));
      } else if (__any(m4 - ref > BUMP)) {
        const float nr = fmaxf(ref, xmax32(xmax16(m4)));
        l *= __builtin_amdgcn_exp2f(ref - nr);
        ref = nr;
      }
      const unsigned p0 = pk(__builtin_amdgcn_exp2f(s0.x - ref), __builtin_amdgcn_exp2f(s0.y - ref));
      const unsigned p1 = pk(__builtin_amdgcn_exp2f(s0.z - ref), __builtin_amdgcn_exp2f(s0.w - ref));
      l += (bflo(p0) + bfhi(p0)) + (bflo(p1) + bfhi(p1));
      *(LAS v2u*)(T_P + t * 2048 + kh * 1024 + head * 32 + lq * 8) = v2u{p0, p1};
      if (lq == 0) *(LAS float*)(T_REF + t * 256 + kh * 128 + head * 4) = ref;
      if (t == 7) {
        const float lt = xsum32(xsum16(l));
        if (lq == 0) *(LAS float*)(T_LF + kh * 128 + head * 4) = lt;
      }
      lsig(C_PRDY + 4 * t, lane);
    }
    if (wave == 0 && s < 8) {
      const int col = 8 * (lane & 7) + 4 * (lq & 1);
      v4u gg[2];
      gather<2>(rs, arena(4 * s + 1) + A_HM + b * 256 + col * 4, 96 * ASTR, gg);
      const v2u xb = *(const GAS v2u*)(p.x + b * 512 + 64 * s + col);
      STAMP(26);
#pragma unroll 1
      for (int sp = 0; sp < SPIN_G; ++sp) {
        const unsigned v = __builtin_amdgcn_raw_buffer_load_b32(rs, arena(2 * lane) + A_ML, 0, SC1);
        if (!__any(v == SENT)) break;
        __builtin_amdgcn_s_sleep(8);
      }
      __builtin_amdgcn_s_sleep(16);
      const f32x4 o = reduce_rows<16>(rs, arena(lane >> 3) + A_OUTP + b * 1024 + s * 128 + (lane & 7) * 16, 8 * ASTR, lane);
      STAMP(27);
      const f32x4 x1 = f32x4{bflo(xb.x), bfhi(xb.x), bflo(xb.y), bfhi(xb.y)} + u2f(gg[0]) * o;
      const bool act = lane < 32 && l15 < 8;
      __builtin_amdgcn_raw_buffer_store_b64(v2u{pk(x1.x, x1.y), pk(x1.z, x1.w)}, rs, act ? arena(8 * b + s) + A_X1 + col * 2 : OOB, 0, SC1);
      STAMP(28);
      const f32x4 y = reduce_rows<7>(rs, arena(4 * lq + 2 + ((lane >> 3) & 1)) + A_YP + b * 1024 + s * 128 + (lane & 7) * 16, 16 * ASTR, lane);
      STAMP(29);
      const f32x4 r = x1 + u2f(gg[1]) * y;
      if (act) *(GAS v2u*)(p.out + b * 512 + 64 * s + col) = v2u{pk(r.x, r.y), pk(r.z, r.w)};
      if (b == 0 && s == 0 && lane == 0) __hip_atomic_store((GAS u64*)ws, tag, RLX_AGENT);
      STAMP(30);
    }
  } else {
    const int cs = wave - 4, hf = lane >> 5, l31 = lane & 31;
    lwait(C_QRD, 4);
#pragma unroll
    for (int r = 0; r < 8; ++r) *(LAS v4u*)((LAS char*)(3 * 32768 + cs * 8192 + lane * 16) + r * 1024) = t3[r];
    lsig(C_LAND + 12, lane);
    unsigned bo[4][2];
    {
      const int q = l15 >> 2, pp = lane & 3, blk = lq & 1;
#pragma unroll
      for (int ct = 0; ct < 4; ++ct)
#pragma unroll
        for (int t = 0; t < 2; ++t)
          bo[ct][t] = (8 * hf + 4 * t + q) * 1024 + cs * 256 + 16 * ((4 * ct + 2 * blk + (pp >> 1)) ^ ((q << 2) | (2 * hf + t))) + 8 * (pp & 1);
    }
    f32x16 acc[4];
#pragma unroll
    for (int ct = 0; ct < 4; ++ct)
#pragma unroll
      for (int r = 0; r < 16; ++r) acc[ct][r] = 0.f;
    float R = 0.f;
    auto tile = [&](int t, auto slow) -> bool {
      lwait(C_LAND + 4 * t, 4);
      const unsigned tb = (t & 3) * 32768;
      v4u vf[2][4];
#pragma unroll
      for (int ct = 0; ct < 4; ++ct) {
        const unsigned a0 = tb + bo[ct][0], a1 = tb + bo[ct][1];
#pragma unroll
        for (int kh = 0; kh < 2; ++kh) {
          const v2u lo = ldstr(a0, kh * 16384), hi = ldstr(a1, kh * 16384);
          vf[kh][ct] = v4u{lo.x, lo.y, hi.x, hi.y};
        }
      }
      lwait(C_PRDY + 4 * t, 4);
      v4u pf[2];
      float rf[2];
#pragma unroll
      for (int kh = 0; kh < 2; ++kh) {
        pf[kh] = lds128(T_P + t * 2048 + l31 * 32 + hf * 16, kh * 1024);
        rf[kh] = *(LAS float*)((LAS char*)(T_REF + t * 256 + l31 * 4) + kh * 128);
      }
      if (t == 0) R = rf[0];
      if constexpr (!decltype(slow)::value) {
        if (__any(rf[0] != R || rf[1] != R)) return false;
      } else {
        const float nr = fmaxf(R, fmaxf(rf[0], rf[1]));
        const float fa = __builtin_amdgcn_exp2f(R - nr);
#pragma unroll
        for (int r = 0; r < 16; ++r) {
          const float f = __shfl(fa, (r & 3) + 8 * (r >> 2) + 4 * hf);
#pragma unroll
          for (int ct = 0; ct < 4; ++ct) acc[ct][r] *= f;
        }
#pragma unroll
        for (int kh = 0; kh < 2; ++kh) {
          const float f = __builtin_amdgcn_exp2f(rf[kh] - nr);
#pragma unroll
          for (int e = 0; e < 4; ++e) pf[kh][e] = pk(bflo(pf[kh][e]) * f, bfhi(pf[kh][e]) * f);
        }
        R = nr;
      }
      lsig(C_FREE + 4 * t, lane);
#pragma unroll
      for (int kh = 0; kh < 2; ++kh)
#pragma unroll
        for (int ct = 0; ct < 4; ++ct) acc[ct] = mfma32(pf[kh], vf[kh][ct], acc[ct]);
      return true;
    };
    int t = 0;
#pragma unroll 1
    for (; t < 8; ++t) if (!tile(t, BoolC<false>{})) break;
#pragma unroll 1
    for (; t < 8; ++t) tile(t, BoolC<true>{});
    const int po = arena(16 * hf + cs) + A_PART + (b * 16 + s) * 256 + l31 * 8;
#pragma unroll
    for (int r = 0; r < 16; ++r)
      __builtin_amdgcn_raw_buffer_store_b64(v2u{pk(acc[0][r], acc[1][r]), pk(acc[2][r], acc[3][r])}, rs, po,
                                            ((r & 3) + 8 * (r >> 2)) * 4 * ASTR, SC1);
    if (cs == 0) {
      const float r0 = *(LAS float*)(T_REF + 7 * 256 + l31 * 4), r1 = *(LAS float*)(T_REF + 7 * 256 + 128 + l31 * 4);
      const float lt = *(LAS float*)(T_LF + l31 * 4) * __builtin_amdgcn_exp2f(r0 - R) + *(LAS float*)(T_LF + 128 + l31 * 4) * __builtin_amdgcn_exp2f(r1 - R);
      __builtin_amdgcn_raw_buffer_store_b64(v2u{__float_as_uint(R), __float_as_uint(lt)}, rs, hf == 0 ? arena(16 * b + s) + A_ML + l31 * 8 : OOB, 0, SC1);
      STAMP(25);
    }
  }
}

__global__ __launch_bounds__(768) void adaln_mla_ffn_kernel(Params p) {
  const int g = blockIdx.x & 7, idx = blockIdx.x >> 3;
  const int wave = __builtin_amdgcn_readfirstlane(threadIdx.x >> 6), lane = threadIdx.x & 63;
  const u64 tag = *(const GAS u64*)p.ws * 6364136223846793005ull + 1442695040888963407ull;
  if (idx < 16) attn_wg(p, tag, g, idx, wave, lane);
  else weight_wg(p, tag, g, idx - 16, wave, lane);
}

extern "C" void kernel_launch(void* const* d_in, const int* in_sizes, int n_in, void* d_out, int out_size, void* d_ws, size_t ws_size,
                              hipStream_t stream) {
  Params p;
  p.x = (const u16*)d_in[0]; p.ckv = (const u16*)d_in[1]; p.wq = (const u16*)d_in[2]; p.wuk = (const u16*)d_in[3];
  p.wuv = (const u16*)d_in[4]; p.wo = (const u16*)d_in[5]; p.w1 = (const u16*)d_in[7]; p.w2 = (const u16*)d_in[8];
  p.c = (const u16*)d_in[9]; p.wmod = (const u16*)d_in[10];
  p.out = (u16*)d_out; p.ws = (unsigned char*)d_ws;
  (void)hipFuncSetAttribute((const void*)adaln_mla_ffn_kernel, hipFuncAttributeMaxDynamicSharedMemorySize, LDS_BYTES);
  adaln_mla_ffn_kernel<<<256, 768, LDS_BYTES, stream>>>(p);
}
```

```cpp
#include <hip/hip_runtime.h>
#include <hip/hip_bf16.h>
#pragma clang diagnostic ignored "-Wint-to-pointer-cast"
#pragma clang diagnostic ignored "-Wsometimes-uninitialized"

typedef unsigned short u16;
typedef unsigned long long u64;
typedef unsigned v4u __attribute__((ext_vector_type(4)));
typedef unsigned v2u __attribute__((ext_vector_type(2)));
typedef float f32x2 __attribute__((ext_vector_type(2)));
typedef float f32x4 __attribute__((ext_vector_type(4)));
typedef float f32x16 __attribute__((ext_vector_type(16)));
typedef short s16x4 __attribute__((ext_vector_type(4)));
typedef short bf16x8 __attribute__((ext_vector_type(8)));
typedef __bf16 bf16x2 __attribute__((ext_vector_type(2)));
#define GAS __attribute__((address_space(1)))
#define LAS __attribute__((address_space(3)))
#define DI __device__ __forceinline__
#define RLX_AGENT __ATOMIC_RELAXED, __HIP_MEMORY_SCOPE_AGENT
typedef __amdgpu_buffer_rsrc_t rsrc_t;

#define CODE_LINES 384
#define ATT_DELAY 0
#define D_H 0
#define D_QP 0
#define D_QA 0
#define D_PART 6
#define D_B 20
#define D_C 24
#define D_D 48
#define D_F 8
#define D_ML 4
#define FLAG_AT 1
#define D_PREF 0
#define PRIO_S 1
#define PRIO_PV 2
#define QH_EARLY 0
#define QAH_EARLY 0
#define D_L 36
#define D_FILL 0
#define PREF 1
#define WO_NT 0

constexpr int WS_FLAG1 = 4096, WS_FLAG2 = 8192;
constexpr int WS_EFL = 16384, WS_HH = 32768, WS_HQ = 49152, WS_ARR = 65536, WS_HP = 81920;
constexpr int WS_ARENA = 131072, ASTR = 59392;
constexpr int A_QP = 0, A_HM = 4096, A_QA = 6144, A_ML = 8192, A_X1 = 9216, A_PART = 10240, A_OUTP = 43008, A_YP = 51200;
constexpr int WS_BYTES = WS_ARENA + 128 * ASTR;
constexpr unsigned SENT = 0xFFFFFFFFu;
constexpr int OOB = 0x7FFFFF00;
constexpr int SC1 = 16;
constexpr int SPIN_G = 1 << 15;
constexpr int SPIN_L = 1 << 20;
constexpr int LDS_BYTES = 163840;
constexpr float EPS = 1e-5f;
constexpr float QSCALE = 0.08838834764831845f * 1.4426950408889634f;
constexpr float BUMP = 40.f;

#define STAMP(k) do {} while (0)
#define STAMPV(k) do {} while (0)
#define STAMPD(k) do {} while (0)
#define DUMP(k) do {} while (0)
#define DUMPC(k, c) do {} while (0)

template <class T> DI void pin(T& v) { asm volatile("" : "+v"(v)); }
DI float bflo(unsigned u) { return __uint_as_float(u << 16); }
DI float bfhi(unsigned u) { return __uint_as_float(u & 0xFFFF0000u); }
DI unsigned pk(float a, float b) { f32x2 v = {a, b}; return __builtin_bit_cast(unsigned, __builtin_convertvector(v, bf16x2)); }
DI f32x4 mfma16(v4u a, v4u b, f32x4 c) {
  return __builtin_amdgcn_mfma_f32_16x16x32_bf16(__builtin_bit_cast(bf16x8, a), __builtin_bit_cast(bf16x8, b), c, 0, 0, 0);
}
DI f32x16 mfma32(v4u a, v4u b, f32x16 c) {
  return __builtin_amdgcn_mfma_f32_32x32x16_bf16(__builtin_bit_cast(bf16x8, a), __builtin_bit_cast(bf16x8, b), c, 0, 0, 0);
}
DI v4u ldg(rsrc_t rs, int off, int so = 0) { return __builtin_amdgcn_raw_buffer_load_b128(rs, off, so, SC1); }
DI void stg(rsrc_t rs, int off, v4u v) { __builtin_amdgcn_raw_buffer_store_b128(v, rs, off, 0, SC1); }
DI unsigned umax4(v4u v) { return max(max(v.x, v.y), max(v.z, v.w)); }
DI v4u f2u(f32x4 v) { return __builtin_bit_cast(v4u, v); }
DI f32x4 u2f(v4u v) { return __builtin_bit_cast(f32x4, v); }
DI bool nan4(f32x4 v) { return __builtin_isunordered(v.x, v.y) | __builtin_isunordered(v.z, v.w); }
DI v4u lds128(unsigned a, int o = 0) { return *(LAS v4u*)((LAS char*)a + o); }
DI v2u ldstr(unsigned a, int o = 0) { return __builtin_bit_cast(v2u, __builtin_amdgcn_ds_read_tr16_b64_v4i16((LAS s16x4*)((LAS char*)a + o))); }
DI void bar() {
  __builtin_amdgcn_sched_barrier(0);
  asm volatile("s_waitcnt lgkmcnt(0)\n\ts_barrier" ::: "memory");
  __builtin_amdgcn_sched_barrier(0);
}
#define VMCNT_(n) asm volatile("s_waitcnt vmcnt(" #n ")" ::: "memory")
#define VMCNT(n) VMCNT_(n)
#define FENCE() asm volatile("" ::: "memory")
template <bool NT> DI void dma16(const void* base, unsigned voff, unsigned dst) {
  if (NT) asm volatile("s_mov_b32 m0, %2\n\ts_nop 0\n\tglobal_load_lds_dwordx4 %0, %1 nt" :: "v"(voff), "s"(base), "s"(dst) : "memory");
  else asm volatile("s_mov_b32 m0, %2\n\ts_nop 0\n\tglobal_load_lds_dwordx4 %0, %1" :: "v"(voff), "s"(base), "s"(dst) : "memory");
}
template <bool NT> DI void dma_run(const void* base, unsigned voff, unsigned dst, int n, int gstr, int lstr) {
  const unsigned char* b = (const unsigned char*)base;
#pragma unroll 1
  for (int i = 0; i < n; ++i) { VMCNT(7); dma16<NT>(b, voff, dst); b += gstr; dst += lstr; }
}
template <int N> DI void nap() { if (N > 0) __builtin_amdgcn_s_sleep(N); }
DI void lsig(unsigned a, unsigned v = 1u) { asm volatile("s_mov_b64 exec, 1\n\tds_add_u32 %0, %1\n\ts_mov_b64 exec, -1" :: "v"(a), "v"(v) : "memory"); }
DI unsigned lpeek(unsigned a) { return *(volatile LAS unsigned*)a; }
template <bool NAP = false> DI void lwait(unsigned a, unsigned want) {
  unsigned v, n = SPIN_L;
  if (NAP)
    asm volatile("1:\n\tds_read_b32 %0, %2\n\ts_waitcnt lgkmcnt(0)\n\tv_cmp_le_u32 vcc, %3, %0\n\ts_cbranch_vccnz 2f\n\t"
                 "s_sleep 1\n\ts_sub_u32 %1, %1, 1\n\ts_cbranch_scc0 1b\n2:" : "=&v"(v), "+s"(n) : "v"(a), "s"(want) : "vcc", "scc", "memory");
  else
    asm volatile("1:\n\tds_read_b32 %0, %2\n\ts_waitcnt lgkmcnt(0)\n\tv_cmp_le_u32 vcc, %3, %0\n\ts_cbranch_vccnz 2f\n\t"
                 "s_sub_u32 %1, %1, 1\n\ts_cbranch_scc0 1b\n2:" : "=&v"(v), "+s"(n) : "v"(a), "s"(want) : "vcc", "scc", "memory");
}
template <int CTRL> DI float dppf(float v) {
  return __builtin_bit_cast(float, __builtin_amdgcn_update_dpp(0, __builtin_bit_cast(int, v), CTRL, 0xF, 0xF, true));
}
DI float row16_max(float v) {
  v = fmaxf(v, dppf<0x128>(v)); v = fmaxf(v, dppf<0x124>(v)); v = fmaxf(v, dppf<0x122>(v)); return fmaxf(v, dppf<0x121>(v));
}
DI float row16_sum(float v) { v += dppf<0x128>(v); v += dppf<0x124>(v); v += dppf<0x122>(v); return v + dppf<0x121>(v); }
DI float xsum16(float v) { auto r = __builtin_amdgcn_permlane16_swap(__float_as_uint(v), __float_as_uint(v), false, false); return __uint_as_float(r[0]) + __uint_as_float(r[1]); }
DI float xsum32(float v) { auto r = __builtin_amdgcn_permlane32_swap(__float_as_uint(v), __float_as_uint(v), false, false); return __uint_as_float(r[0]) + __uint_as_float(r[1]); }
DI float xmax16(float v) { auto r = __builtin_amdgcn_permlane16_swap(__float_as_uint(v), __float_as_uint(v), false, false); return fmaxf(__uint_as_float(r[0]), __uint_as_float(r[1])); }
DI float xmax32(float v) { auto r = __builtin_amdgcn_permlane32_swap(__float_as_uint(v), __float_as_uint(v), false, false); return fmaxf(__uint_as_float(r[0]), __uint_as_float(r[1])); }
DI float wave_sum(float v) { return xsum32(xsum16(row16_sum(v))); }

template <int N> DI bool any_sent(const v4u (&r)[N]) {
  unsigned m = 0;
#pragma unroll
  for (int i = 0; i < N; ++i) m = max(m, umax4(r[i]));
  return __any(m == SENT);
}
template <int N> DI void load_n(rsrc_t rs, int off0, int str, v4u (&r)[N]) {
  FENCE();
#pragma unroll
  for (int i = 0; i < N; ++i) r[i] = ldg(rs, off0, i * str);
  __builtin_amdgcn_sched_barrier(0);
}
template <int N, int NAP = 0> DI void gather(rsrc_t rs, int off0, int str, v4u (&r)[N]) {
#pragma unroll 1
  for (int sp = 0; sp < SPIN_G; ++sp) {
    load_n<N>(rs, off0, str, r);
    if (!any_sent<N>(r)) break;
    nap<NAP>();
  }
}
DI u64 get_tag(const unsigned char* ws) {
  u64 e;
  asm volatile("s_load_dwordx2 %0, %1, 0x0\n\ts_waitcnt lgkmcnt(0)" : "=s"(e) : "s"(ws) : "memory");
  return e * 6364136223846793005ull + 1442695040888963407ull;
}
DI bool flags_up(v4u f, u64 tag) {
  const unsigned lo = (unsigned)tag, hi = (unsigned)(tag >> 32);
  return __all(f.x == lo && f.y == hi && f.z == lo && f.w == hi);
}
DI void wait_flags(rsrc_t rs, int off, u64 tag) {
#pragma unroll 1
  for (int sp = 0; sp < SPIN_G; ++sp) {
    FENCE();
    if (flags_up(ldg(rs, off), tag)) break;
    __builtin_amdgcn_s_sleep(8);
  }
  FENCE();
}
typedef unsigned s8u __attribute__((ext_vector_type(8)));
DI s8u sload8(const unsigned char* a) {
  s8u v;
  asm volatile("s_load_dwordx8 %0, %1, 0x0 glc\n\ts_waitcnt lgkmcnt(0)" : "=s"(v) : "s"(a) : "memory");
  return v;
}
DI void wait_hints(const unsigned char* a, unsigned want, unsigned span) {
#pragma unroll 1
  for (int sp = 0; sp < SPIN_G; ++sp) {
    const s8u v = sload8(a) - want;
    if (max(max(max(v[0], v[1]), max(v[2], v[3])), max(max(v[4], v[5]), max(v[6], v[7]))) <= span) break;
  }
}
DI int arena(int wid) { return WS_ARENA + wid * ASTR; }

DI unsigned tr_addr(unsigned region, int lane, int col4, int t) {
  const int g = lane >> 4, q = (lane & 15) >> 2;
  return region + (8 * g + 4 * t + q) * 256 + 16 * ((col4 >> 3) ^ ((q << 2) | ((2 * g + t) & 3))) + 8 * ((col4 >> 2) & 1);
}
DI v4u tr_frag(unsigned a0, unsigned a1, int ks) {
  const v2u lo = ldstr(a0, ks * 8192), hi = ldstr(a1, ks * 8192);
  return v4u{lo.x, lo.y, hi.x, hi.y};
}
DI void panel_frags(unsigned region, int lane, int col_a, int col_b, v4u (&f)[2][4]) {
  const unsigned a0 = tr_addr(region, lane, col_a, 0), a1 = tr_addr(region, lane, col_a, 1);
  const unsigned b0 = tr_addr(region, lane, col_b, 0), b1 = tr_addr(region, lane, col_b, 1);
#pragma unroll
  for (int ks = 0; ks < 4; ++ks) { f[0][ks] = tr_frag(a0, a1, ks); f[1][ks] = tr_frag(b0, b1, ks); }
}
DI void gemv2(const v4u (&f)[2][4], unsigned act, f32x4& d0, f32x4& d1) {
  v4u bf[4];
#pragma unroll
  for (int ks = 0; ks < 4; ++ks) bf[ks] = lds128(act, ks * 64);
  __builtin_amdgcn_sched_barrier(0);
#pragma unroll
  for (int ks = 0; ks < 4; ++ks) { d0 = mfma16(f[0][ks], bf[ks], d0); d1 = mfma16(f[1][ks], bf[ks], d1); }
}
DI v4u pk8(f32x4 a, f32x4 b) { return v4u{pk(a.x, a.y), pk(a.z, a.w), pk(b.x, b.y), pk(b.z, b.w)}; }
DI v4u silu8(v4u cv) {
  v4u o;
#pragma unroll
  for (int e = 0; e < 4; ++e) {
    const float a = bflo(cv[e]), c = bfhi(cv[e]);
    o[e] = pk(a * __builtin_amdgcn_rcpf(1.f + __expf(-a)), c * __builtin_amdgcn_rcpf(1.f + __expf(-c)));
  }
  return o;
}

struct Params {
  const u16 *x, *ckv, *wq, *wuk, *wuv, *wo, *w1, *w2, *c, *wmod;
  u16* out;
  unsigned char* ws;
};

constexpr unsigned W0 = 0, W1 = 32768, W2 = 65536, L_SC = 98304  , L_LNX = L_SC + 4224  ,
                   L_A1 = L_LNX + 2048  , L_A2 = L_A1 + 2176, L_A3 = L_A2 + 2176, L_A4 = L_A3 + 2176, L_CNT = L_A4 + 2176, L_RAW = L_CNT + 128  ;
constexpr unsigned T_SP = 131072, T_P = T_SP + 16384, T_FAC = T_P + 8192, T_CNT = T_FAC + 1024,
                   L_A5 = 65536  , L_A6 = L_A5 + 8320  ;
constexpr unsigned C_LAND = T_CNT + 32, C_FREE = C_LAND + 4, C_SRDY = C_LAND + 8, C_PRDY = C_LAND + 12,
                   C_QST = T_CNT + 192, C_QRD = T_CNT + 196;
constexpr unsigned L_JUNK = LDS_BYTES - 256;

DI void gather4_act(rsrc_t rs, int off0, int str, unsigned dst) {
  f32x4 s;
#pragma unroll 1
  for (int sp = 0; sp < SPIN_G; ++sp) {
    v4u r[4];
    load_n<4>(rs, off0, str, r);
    s = (u2f(r[0]) + u2f(r[1])) + (u2f(r[2]) + u2f(r[3]));
    if (!__any(nan4(s)) || !any_sent<4>(r)) break;
  }
  *(LAS v2u*)dst = v2u{pk(s.x, s.y), pk(s.z, s.w)};
}
template <int N> DI f32x4 reduce_rows(rsrc_t rs, int off0, int offl, v4u sel, int& rounds) {
  f32x4 a;
#pragma unroll 1
  for (int sp = 0; sp < SPIN_G; ++sp) {
    v4u d[N];
    FENCE();
#pragma unroll
    for (int i = 0; i < N; ++i) d[i] = ldg(rs, i == N - 1 ? offl : off0, i * 16 * ASTR);
    __builtin_amdgcn_sched_barrier(0);
    f32x4 b = {0.f, 0.f, 0.f, 0.f};
    a = b;
#pragma unroll
    for (int i = 0; i < N; i += 2) { a = mfma16(sel, d[i], a); b = mfma16(sel, d[i + 1], b); }
    a += b;
    rounds = sp + 1;
    if (!__any(a.x != a.x) || !any_sent<N>(d)) break;
  }
#pragma unroll
  for (int r = 0; r < 4; ++r) { a[r] += dppf<0x128>(a[r]); a[r] += dppf<0x124>(a[r]); }
  return a;
}
DI v4u pick(bool lo, v4u a, v4u b) { return v4u{lo ? a.x : b.x, lo ? a.y : b.y, lo ? a.z : b.z, lo ? a.w : b.w}; }
DI void st32(rsrc_t rs, int off, unsigned v) { __builtin_amdgcn_raw_buffer_store_b32(v, rs, off, 0, SC1); }

DI void weight_wg(const Params& p, int g, int wi, int wave, int lane, unsigned vq) {
  unsigned char* ws = p.ws;
  const rsrc_t rs = __builtin_amdgcn_make_buffer_rsrc(ws, 0, WS_BYTES, 0x00020000);
  const int h = 4 * g + (wi >> 2), j = wi & 3, wid = 16 * g + wi, my = arena(wid);
  const int l15 = lane & 15, lq = lane >> 4, b8 = lane & 7;
  const bool bvalid = l15 < 8;
  const int w = wave & 3;
  const int ch = l15 ^ ((lq << 2) | w);
  const unsigned dst0 = w * 1024;
  const unsigned vuv = (128 * j + 16 * ((4 * w + lq) & 7) + (w >> 1)) * 8192 + h * 256 + ch * 16;
  const f32x4 z4 = {0.f, 0.f, 0.f, 0.f};

  if (wave >= 8) {
    VMCNT(8);
    STAMP(32);
    if (j == 0) {
      bar();
      dma_run<true>(p.wuk, vq, W1 + dst0, 8, 16 * 8192, 4096);
    }
    VMCNT(0);
    STAMP(44);
    bar();
    bar();
    bar();
    v4u wo[4][8];
    const GAS unsigned char* wb = (const GAS unsigned char*)p.wo + (h * 128 + 8 * lq) * 1024 + w * 256 + (4 * (l15 & 3) + (l15 >> 2)) * 16;
    bar();
    nap<D_L>();
#pragma unroll
    for (int ks = 0; ks < 4; ++ks) {
      const GAS unsigned char* wk = wb + ((ks + j) & 3) * 32768;
#pragma unroll
      for (int r = 0; r < 8; ++r)
        wo[ks][r] = WO_NT ? __builtin_nontemporal_load((const GAS v4u*)(wk + r * 1024)) : *(const GAS v4u*)(wk + r * 1024);
      if (ks == 0) { VMCNT(0); bar(); }
    }
    __builtin_amdgcn_sched_barrier(0);
#pragma unroll
    for (int ks = 0; ks < 4; ++ks) {
      v4u o[8];
#pragma unroll
      for (int dd = 0; dd < 4; ++dd)
#pragma unroll
        for (int rp = 0; rp < 4; ++rp) {
          o[2 * dd][rp] = __builtin_amdgcn_perm(wo[ks][2 * rp + 1][dd], wo[ks][2 * rp][dd], 0x05040100u);
          o[2 * dd + 1][rp] = __builtin_amdgcn_perm(wo[ks][2 * rp + 1][dd], wo[ks][2 * rp][dd], 0x07060302u);
        }
#pragma unroll
      for (int t = 0; t < 8; ++t) { wo[ks][t] = o[t]; pin(wo[ks][t]); }
    }
    STAMP(10);
    unsigned ba[4];
#pragma unroll
    for (int ks = 0; ks < 4; ++ks) { ba[ks] = L_A4 + b8 * 272 + ((ks + j) & 3) * 64 + lq * 16; pin(ba[ks]); }
    int oo = my + A_OUTP + b8 * 1024 + w * 256 + (l15 >> 3) * 64 + lq * 16;
    pin(oo);
    __builtin_amdgcn_s_setprio(2);
    bar();
    bar();
    v4u bf[4];
#pragma unroll
    for (int ks = 0; ks < 4; ++ks) bf[ks] = lds128(ba[ks]);
    v4u r0, r1;
#pragma unroll
    for (int tp = 0; tp < 4; ++tp) {
      f32x4 e = z4, o = z4;
#pragma unroll
      for (int ks = 0; ks < 4; ++ks) { e = mfma16(wo[ks][2 * tp], bf[ks], e); o = mfma16(wo[ks][2 * tp + 1], bf[ks], o); }
      r0[tp] = bvalid ? pk(e[0], o[0]) : pk(e[1], o[1]);
      r1[tp] = bvalid ? pk(e[2], o[2]) : pk(e[3], o[3]);
    }
    stg(rs, oo, r0);
    stg(rs, oo + 128, r1);
    STAMP(14);
    if (wave == 8) { DUMP(10); DUMP(14); DUMP(32); DUMP(44); }
  } else if (wave >= 4) {
    __builtin_amdgcn_s_setprio(2);
    const int t = w * 64 + lane;
    const u64 tag = get_tag(ws);
    *(LAS unsigned*)L_CNT = 0u;
    VMCNT(0);
    const unsigned raw = L_RAW + w * 3072 + lane * 16;
    const v4u cr = lds128(raw), xr0 = lds128(raw, 1024), xr1 = lds128(raw, 2048);
    if (j <= 2) *(LAS v4u*)(L_SC + (2 * w + (lane >> 5)) * 528 + (lane & 31) * 16) = silu8(cr);
    if (j == 0) {
#pragma unroll
      for (int r = 0; r < 2; ++r) {
        const v4u xv = r ? xr1 : xr0;
        f32x4 v0 = {bflo(xv.x), bfhi(xv.x), bflo(xv.y), bfhi(xv.y)}, v1 = {bflo(xv.z), bfhi(xv.z), bflo(xv.w), bfhi(xv.w)};
        const f32x4 sv4 = v0 + v1;
        const float mean = wave_sum((sv4.x + sv4.y) + (sv4.z + sv4.w)) * (1.f / 512);
        v0 -= mean; v1 -= mean;
        const f32x4 qv4 = v0 * v0 + v1 * v1;
        const float rstd = __builtin_amdgcn_rsqf(wave_sum((qv4.x + qv4.y) + (qv4.z + qv4.w)) * (1.f / 512) + EPS);
        if ((lane >> 3) == g) {
          *(LAS f32x4*)(L_LNX + (2 * w + r) * 256 + b8 * 32) = v0 * rstd;
          *(LAS f32x4*)(L_LNX + (2 * w + r) * 256 + b8 * 32 + 16) = v1 * rstd;
        }
      }
    }
    int ho = arena(32 * j + 16 * (t >> 7)) + A_HM + (t & 127) * 16, qo = arena(4 * h) + A_QP + t * 16;
    unsigned ad = L_A1 + ((t >> 2) & 7) * 272 + (16 * (t >> 5) + 4 * (t & 3)) * 2;
    int hh = lane < 32 && j == 0 ? WS_HH + (4 * lane + (g >> 1)) * 128 + (4 * (g & 1) + (h & 3)) * 4 : OOB;
    int hq = lane < 8 ? WS_HQ + (16 * lane + ((wi - 5 * g) & 15)) * 128 + g * 4 : OOB;
    pin(ho); pin(qo); pin(ad); pin(hh); pin(hq);
    int hp = lane < 8 ? WS_HP + (4 * h + (lane & 3)) * 128 + j * 4 + (lane >> 2) * 16 : OOB;
    pin(hp);
    STAMP(45);
    if (j == 0) {
      bar();
      if (w == 1) st32(rs, hh, (unsigned)tag);
    }
    STAMPD(34); STAMPD(46);
    if (w == 2) {
      const unsigned char* fl = ws + WS_EFL + wid * 128;
#pragma unroll 1
      for (int sp = 0; sp < SPIN_G; ++sp) {
        s8u a, b, c;
        asm volatile("s_load_dwordx8 %0, %3, 0x0 glc\n\ts_load_dwordx8 %1, %3, 0x20 glc\n\ts_load_dwordx8 %2, %3, 0x40 glc\n\ts_waitcnt lgkmcnt(0)"
                     : "=&s"(a), "=&s"(b), "=&s"(c) : "s"(fl) : "memory");
        const unsigned lo = (unsigned)tag;
        if ((((a[0] ^ lo) | (a[2] ^ lo) | (a[4] ^ lo) | (a[6] ^ lo)) | ((b[0] ^ lo) | (b[2] ^ lo) | (b[4] ^ lo) | (b[6] ^ lo)) |
             ((c[0] ^ lo) | (c[2] ^ lo) | (c[4] ^ lo) | (c[6] ^ lo))) == 0) break;
      }
      wait_flags(rs, WS_EFL + wid * 128 + min(lane, 5) * 16, tag);
      STAMPV(46);
    }
    if (w == 3) { wait_hints(ws + WS_HH + wid * 128, (unsigned)tag, 0); STAMPV(34); nap<D_H>(); }
    bar();
    if (QH_EARLY && w == 1) st32(rs, hp, (unsigned)tag);
    gather4_act(rs, ho, 4 * ASTR, ad);
    STAMP(5);
    if (!QH_EARLY && w == 1) st32(rs, hp, (unsigned)tag);
    bar();
    wait_hints(ws + WS_HP + wid * 128, (unsigned)tag, 0);
    nap<D_QP>();
    if (QAH_EARLY && w == 1) st32(rs, hq, (unsigned)tag + 2);
    gather4_act(rs, qo, ASTR, ad + (L_A2 - L_A1));
    STAMP(7);
    bar();
    if (!QAH_EARLY && w == 1) st32(rs, hq, (unsigned)tag + 2);
    bar();
    STAMPD(47);
    {
      nap<D_FILL>();
      const v4u sv = {SENT, SENT, SENT, SENT};
      const int nf = wid < 56 ? 58 : 50;
#pragma unroll 1
      for (int n = 9 + w; n < nf; n += 4) __builtin_amdgcn_raw_buffer_store_b128(sv, rs, my + lane * 16, n * 1024, SC1);
      VMCNT(0);
      lsig(L_CNT);
      if (w == 3) {
        lwait<true>(L_CNT, 4);
        if (lane == 0) __hip_atomic_store((GAS u64*)(ws + WS_FLAG2 + wid * 8), tag, RLX_AGENT);
        STAMPV(47);
      }
    }
    bar();
    int mo = arena(32 * w + (lane & 31)) + A_ML + h * 16;
    int po = my + A_PART + w * 8192 + lane * 16;
    unsigned o3 = L_A3 + (2 * w + (lane >> 5)) * 272 + l15 * 16 + (lq & 1) * 8;
    v4u msk;
    {
      const unsigned half = (lane & 1) ? 0xFFFF0000u : 0x0000FFFFu;
      const int dw = b8 >> 1;
      msk = v4u{dw == 0 ? half : 0u, dw == 1 ? half : 0u, dw == 2 ? half : 0u, dw == 3 ? half : 0u};
    }
    pin(mo); pin(po); pin(o3); pin(msk);
    wait_hints(ws + WS_ARR + (4 * h + w) * 128, (unsigned)(tag & 255) * 0x01010101u, 0);
    STAMP(11);
    nap<D_ML>();
    FENCE();
    v4u ml = ldg(rs, mo);
    nap<D_PART>();
    v4u d[8];
    load_n<8>(rs, po, 1024, d);
    if (__builtin_expect(!__all(ml.y == (unsigned)tag && ml.w == (unsigned)(tag >> 32)), 0)) {
#pragma unroll 1
      for (int sp = 0; sp < SPIN_G; ++sp) {
        FENCE();
        ml = ldg(rs, mo);
        if (__all(ml.y == (unsigned)tag && ml.w == (unsigned)(tag >> 32))) break;
      }
    }
    v4u sel[8];
    {
      const float r = __uint_as_float(ml.x), e = __builtin_amdgcn_exp2f(r - row16_max(r));
      const float wgt = e * __builtin_amdgcn_rcpf(row16_sum(e * __uint_as_float(ml.z)));
#pragma unroll
      for (int i = 0; i < 8; ++i) {
        const float wv = __shfl(wgt, 4 * i + lq);
        const unsigned hi = pk(wv, wv);
        const float rest = wv - bflo(hi);
        const unsigned v = bvalid ? hi : pk(rest, rest);
        sel[i] = msk & v;
        pin(sel[i]);
      }
    }
    f32x4 oa, ob;
    int rp = 0;
#pragma unroll 1
    for (int sp = 0; sp < SPIN_G; ++sp) {
      oa = z4; ob = z4;
#pragma unroll
      for (int i = 0; i < 4; ++i) { oa = mfma16(sel[i], d[i], oa); ob = mfma16(sel[4 + i], d[4 + i], ob); }
      rp = sp + 1;
      if (!__any(__builtin_isunordered(oa.x, ob.x)) || !any_sent<8>(d)) break;
      load_n<8>(rs, po, 1024, d);
    }
    STAMP(12);
    {
      f32x4 s;
#pragma unroll
      for (int r = 0; r < 4; ++r) {
        auto x = __builtin_amdgcn_permlane32_swap(__float_as_uint(oa[r]), __float_as_uint(ob[r]), false, false);
        s[r] = __uint_as_float(x[0]) + __uint_as_float(x[1]);
      }
      *(LAS v2u*)o3 = v2u{pk(s.x, s.y), pk(s.z, s.w)};
    }
    bar();
    bar();
    if (w == 1) st32(rs, hq, (unsigned)tag + 3);
    if (w == 2) DUMP(46);
    if (w == 3) { DUMP(47); DUMP(45); DUMP(5); DUMP(7); DUMP(34); DUMP(11); DUMP(12); DUMPC(41, rp); }
    if (w == 0) {
      const u64 tb7 = __builtin_amdgcn_s_memtime();
      const int col = 8 * (lane & 3) + 4 * (lq & 1);
      const v2u xb = *(const GAS v2u*)(p.x + g * 512 + 32 * wi + col);
      v4u gg[4];
      const int go = arena(4 * (wi >> 1) + 1) + A_HM + g * 256 + ((wi & 1) * 32 + col) * 4;
#pragma unroll 1
      for (int sp = 0; sp < SPIN_G; ++sp) {
        FENCE();
#pragma unroll
        for (int i = 0; i < 4; ++i) gg[i] = ldg(rs, go, ((i >> 1) * 96 + (i & 1)) * ASTR);
        if (!any_sent<4>(gg)) break;
      }
      f32x4 x0 = {bflo(xb.x), bfhi(xb.x), bflo(xb.y), bfhi(xb.y)}, ga = u2f(gg[0]) + u2f(gg[1]), gb = u2f(gg[2]) + u2f(gg[3]);
      pin(x0); pin(ga); pin(gb);
      STAMP(26);
      const bool act = lane < 32 && l15 < 4;
      v4u one = msk & (bvalid ? 0x3F803F80u : 0u);
      int oo = arena(lane >> 2) + A_OUTP + g * 1024 + wi * 64 + (lane & 3) * 16;
      int yo = oo + (A_YP - A_OUTP);
      int yl = lane < 32 ? yo : OOB - 48 * ASTR;
      int xo = act ? arena(g) + A_X1 + (32 * wi + col) * 2 : OOB;
      const rsrc_t ro = __builtin_amdgcn_make_buffer_rsrc(p.out, 0, 8192, 0x00020000);
      int fo = act ? (g * 512 + 32 * wi + col) * 2 : OOB;
      pin(one); pin(oo); pin(yo); pin(yl); pin(xo); pin(fo);
      while (__builtin_amdgcn_s_memtime() - tb7 < 64 * D_B) {}
      int r1 = 0, r2 = 0;
      const f32x4 x1 = x0 + ga * reduce_rows<8>(rs, oo, oo, one, r1);
      __builtin_amdgcn_raw_buffer_store_b64(v2u{pk(x1.x, x1.y), pk(x1.z, x1.w)}, rs, xo, 0, SC1);
      STAMP(27);
      nap<D_D>();
      const f32x4 r = x1 + gb * reduce_rows<4>(rs, yo, yl, one, r2);
      STAMP(29);
      __builtin_amdgcn_raw_buffer_store_b64(v2u{pk(r.x, r.y), pk(r.z, r.w)}, ro, fo, 0, 0);
      if (wid == 0 && lane == 0) __hip_atomic_store((GAS u64*)ws, tag, RLX_AGENT);
      STAMP(30);
      DUMP(26); DUMP(27); DUMP(29); DUMP(30); DUMPC(42, r1); DUMPC(43, r2);
    }
  } else {
    __builtin_amdgcn_s_setprio(2);
    const int p4 = 4 * (lane & 3);
    unsigned act = b8 * 272 + lq * 16;
    unsigned s0 = tr_addr(W2, lane, 16 * w + p4, 0), s1 = tr_addr(W2, lane, 16 * w + p4, 1);
    unsigned ba = L_SC + b8 * 528 + lq * 16;
    v4u f[2][4];
    STAMPD(3); STAMPD(1);
    if (w == 3) {
      const u64 tag = get_tag(ws);
      const int fo = lane < 32 ? (j == 0 ? WS_EFL + (4 * lane + (g >> 1)) * 128 + (4 * (g & 1) + (h & 3)) * 8 : OOB)
                   : lane < 36 ? WS_EFL + (4 * h + lane - 32) * 128 + 64 + j * 8 : lane == 36 ? WS_FLAG1 + wid * 8 : OOB;
      VMCNT(0);
      __builtin_amdgcn_raw_buffer_store_b64(v2u{(unsigned)tag, (unsigned)(tag >> 32)}, rs, fo, 0, SC1);
      STAMPV(1);
    }
    if (j == 0) {
      unsigned la = L_LNX + b8 * 256 + (16 * w + 4 * lq) * 4;
      int o = bvalid ? my + A_HM + w * 512 + l15 * 64 + lq * 16 : OOB;
      const float one = (h & 3) == 0 ? 1.f : 0.f;
      f32x4 sa = z4, ca = {one, one, one, one};
      ba += (h & 1) * 256;
      pin(s0); pin(s1); pin(ba); pin(la); pin(o); pin(ca);
      bar();
#pragma unroll
      for (int ks = 0; ks < 4; ++ks) { f[0][ks] = tr_frag(s0, s1, ks); f[1][ks] = tr_frag(s0 ^ 128, s1 ^ 128, ks); }
      const f32x4 ln = u2f(lds128(la));
      gemv2(f, ba, sa, ca);
      stg(rs, o, f2u(sa + ln * ca));
      STAMPV(3);
    }
    STAMP(2);
    bar();
    STAMP(4);
    panel_frags(W0, lane, 32 * w + p4, 32 * w + 16 + p4, f);
    {
      int o = my + A_QP + w * 1024 + l15 * 64 + lq * 16;
      pin(o); pin(act);
      bar();
      f32x4 q0 = z4, q1 = z4;
      gemv2(f, L_A1 + act, q0, q1);
      stg(rs, o, pick(bvalid, f2u(q0), f2u(q1)));
    }
    STAMP(6);
    {
      unsigned ka[2];
#pragma unroll
      for (int tt = 0; tt < 2; ++tt) {
        ka[tt] = W1 + (32 * w + 8 * (l15 >> 2) + 4 * tt + (l15 & 3)) * 256 + 16 * (lq ^ (((l15 & 3) << 2) | ((2 * (l15 >> 2) + tt) & 3)));
        pin(ka[tt]);
      }
      int o = bvalid ? my + A_QA + l15 * 256 + w * 64 + lq * 16 : OOB;
      pin(o);
#pragma unroll
      for (int tt = 0; tt < 2; ++tt)
#pragma unroll
        for (int ks = 0; ks < 4; ++ks) f[tt][ks] = lds128(ka[tt] ^ (ks * 64));
      bar();
      f32x4 a0 = z4, a1 = z4;
      gemv2(f, L_A2 + act, a0, a1);
      stg(rs, o, pk8(a0 * QSCALE, a1 * QSCALE));
    }
    STAMP(8);
    bar();
    nap<D_L>();
    dma_run<true>(p.wuv, vuv, W0 + dst0, 8, 2 * 8192, 4096);
    const bool m2 = j == 1 || j == 2;
    if (m2) dma_run<true>(p.wmod, (256 * (j - 1) + 4 * w + lq + (ch >> 3) * 128) * 6144 + 2048 + h * 128 + (ch & 7) * 16, W2 + dst0, 8, 16 * 6144, 4096);
    VMCNT(0);
    bar();
    STAMP(9);
    if (m2) {
      f32x4 m0 = z4, m1 = z4;
#pragma unroll 1
      for (int ks = 0; ks < 4; ++ks) {
        m0 = mfma16(tr_frag(s0 + ks * 8192, s1 + ks * 8192, 0), lds128(ba + ks * 64), m0);
        m1 = mfma16(tr_frag((s0 ^ 128) + ks * 8192, (s1 ^ 128) + ks * 8192, 0), lds128(ba + ks * 64, 256), m1);
      }
      stg(rs, bvalid ? my + A_HM + l15 * 256 + w * 64 + lq * 16 : OOB, f2u(m0 + m1));
    }
    panel_frags(W0, lane, 32 * w + 2 * p4, 32 * w + 2 * p4 + 4, f);
    unsigned o = bvalid ? L_A4 + l15 * 272 + w * 64 + lq * 16 : L_JUNK;
    pin(o);
    bar();
    STAMP(35);
    f32x4 c0 = z4, c1 = z4;
    gemv2(f, L_A3 + act, c0, c1);
    *(LAS v4u*)o = pk8(c0, c1);
    bar();
    STAMP(13);
    if (w == 3) DUMP(1);
    if (w == 0) { DUMP(35); DUMP(13); DUMP(2); DUMP(3); DUMP(4); DUMP(6); DUMP(8); DUMP(9); }
  }
}

DI void mfma_pair(f32x4& d0, f32x4& d1, v4u a0, v4u a1, v4u b) {
  asm volatile("v_mfma_f32_16x16x32_bf16 %0, %2, %4, %0\n\tv_mfma_f32_16x16x32_bf16 %1, %3, %4, %1" : "+v"(d0), "+v"(d1) : "v"(a0), "v"(a1), "v"(b));
}
DI void mfma_pair0(f32x4& d0, f32x4& d1, v4u a0, v4u a1, v4u b) {
  asm volatile("v_mfma_f32_16x16x32_bf16 %0, %2, %4, 0\n\tv_mfma_f32_16x16x32_bf16 %1, %3, %4, 0" : "=&v"(d0), "=&v"(d1) : "v"(a0), "v"(a1), "v"(b));
}
template <int OFF> DI void rd_inplace(v4u& a, unsigned ad) { asm volatile("ds_read_b128 %0, %1 offset:%2" : "+v"(a) : "v"(ad), "i"(OFF) : "memory"); }
template <int O0, int O1> DI void peek2(unsigned& a, unsigned& b, unsigned cb) {
  asm volatile("ds_read_b32 %0, %2 offset:%3\n\tds_read_b32 %1, %2 offset:%4" : "=&v"(a), "=&v"(b) : "v"(cb), "i"(O0), "i"(O1) : "memory");
}
template <int W> DI void wait2(unsigned& a, unsigned& b) { asm volatile("s_waitcnt lgkmcnt(%2)" : "+v"(a), "+v"(b) : "i"(W)); }
template <int W, bool FIRST> DI void score_pair(f32x4& s0, f32x4& s1, v4u a, v4u q0, v4u q1) {
  if constexpr (FIRST)
    asm volatile("s_waitcnt lgkmcnt(%5)\n\tv_mfma_f32_16x16x32_bf16 %0, %2, %3, 0\n\tv_mfma_f32_16x16x32_bf16 %1, %2, %4, 0"
                 : "=&v"(s0), "=&v"(s1) : "v"(a), "v"(q0), "v"(q1), "i"(W));
  else
    asm volatile("s_waitcnt lgkmcnt(%5)\n\tv_mfma_f32_16x16x32_bf16 %0, %2, %3, %0\n\tv_mfma_f32_16x16x32_bf16 %1, %2, %4, %1"
                 : "+v"(s0), "+v"(s1) : "v"(a), "v"(q0), "v"(q1), "i"(W));
}
template <int OFF> DI void put_scores(unsigned sa, f32x4 s0, f32x4 s1) {
  asm volatile("s_nop 4\n\tds_write_b128 %0, %1 offset:%3\n\tds_write_b128 %0, %2 offset:%4" :: "v"(sa), "v"(s0), "v"(s1), "i"(OFF), "i"(OFF + 1024) : "memory");
}
template <int V> struct IntC { static constexpr int value = V; };
template <int I, int N, class F> DI void static_for(F&& f) { if constexpr (I < N) { f(IntC<I>{}); static_for<I + 1, N>(f); } }
constexpr unsigned slot_c(int t) { return ((0x02310210u >> ((4 * t) & 31)) & 3) * 32768; }
template <unsigned A> DI void csig(unsigned cb, unsigned v) {
  asm volatile("s_mov_b64 exec, 1\n\tds_add_u32 %0, %1 offset:%2\n\ts_mov_b64 exec, -1" :: "v"(cb), "v"(v), "i"(A - T_CNT) : "memory");
}
template <unsigned A> DI unsigned cpeek(unsigned cb) { return *(volatile LAS unsigned*)((LAS char*)cb + (A - T_CNT)); }
#define SPIN_HEAD "s_mov_b32 %[n], 0x100000\n1:\n\ts_sub_u32 %[n], %[n], 1\n\ts_cbranch_scc1 2f\n\t"
#define SPIN_TAIL "s_waitcnt lgkmcnt(0)\n\tv_cmp_gt_u32 vcc, %[w], %[c]\n\ts_cbranch_vccnz 1b\n2:"
template <unsigned A, unsigned W> DI void cwait(unsigned cb) {
  unsigned c, n;
  asm volatile(SPIN_HEAD "ds_read_b32 %[c], %[cb] offset:%[o]\n\t" SPIN_TAIL
               : [c] "=&v"(c), [n] "=&s"(n) : [cb] "v"(cb), [o] "i"(A - T_CNT), [w] "i"(W) : "vcc", "scc", "memory");
}
DI constexpr unsigned fsw(int row) { return ((row & 3) << 2) | ((row >> 1) & 2) | ((row >> 3) & 1); }
DI unsigned slot_of(int t) { return ((0x02310210u >> ((4 * t) & 31)) & 3) * 32768; }
#define DMA4(AUX, base, dst, v0, v1, v2, v3) \
  asm volatile("s_mov_b32 m0, %5\n\ts_waitcnt vmcnt(31)\n\tglobal_load_lds_dwordx4 %0, %4 " AUX "\n\t" \
               "s_waitcnt vmcnt(31)\n\tglobal_load_lds_dwordx4 %1, %4 offset:1024 " AUX "\n\t" \
               "s_waitcnt vmcnt(31)\n\tglobal_load_lds_dwordx4 %2, %4 offset:2048 " AUX "\n\t" \
               "s_waitcnt vmcnt(31)\n\tglobal_load_lds_dwordx4 %3, %4 offset:3072 " AUX \
               :: "v"(v0), "v"(v1), "v"(v2), "v"(v3), "s"(base), "s"(dst) : "memory")

DI void ffn_part(const Params& p, int b, int s, int wave, u64 tag);
DI void attn_wg(const Params& p, int b, int s, int wave, int lane) {
  unsigned char* ws = p.ws;
  const rsrc_t rs = __builtin_amdgcn_make_buffer_rsrc(ws, 0, WS_BYTES, 0x00020000);
  const int l15 = lane & 15, lq = lane >> 4, w = wave & 3;
  const f32x4 z4 = {0.f, 0.f, 0.f, 0.f};
  const unsigned char* hl = ws + WS_HQ + (16 * b + s) * 128;

  if (wave == 0) *(LAS unsigned*)(T_CNT + lane * 4) = lane < 8 ? 8u : 0u;
  nap<ATT_DELAY>();
  bar();
  if (wave == 8) {
    unsigned vo[16];
#pragma unroll
    for (int r = 0; r < 16; ++r) vo[r] = 16 * (lane ^ fsw(r));
    const unsigned char* kv = (const unsigned char*)p.ckv + (size_t)(b * 4096 + s * 32) * 1024;
    STAMP(33); STAMPD(31);
#pragma unroll 1
    for (int t = 0; t < 8; ++t) {
      bool told = false;
      if (t >= 3) {
        const unsigned c = t == 5 ? C_QRD : C_FREE + 16 * (t < 5 ? t - 3 : t - 4), want = t == 5 ? 4 : 8;
        if (__builtin_amdgcn_readfirstlane(lpeek(c)) < want) { VMCNT(0); lsig(C_LAND + 16 * t - 16); told = true; lwait<true>(c, want); }
      }
      const unsigned dst = slot_of(t);
#pragma unroll
      for (int i = 0; i < 8; ++i)
        DMA4("nt", kv + i * 4096, dst + i * 4096, vo[4 * (i & 3)], vo[4 * (i & 3) + 1], vo[4 * (i & 3) + 2], vo[4 * (i & 3) + 3]);
      if (!told) lsig(C_LAND + 16 * t - 16);
      kv += 16 * 32768;
    }
    STAMP(22);
    FENCE();
    const v4u f2 = ldg(rs, WS_FLAG2 + lane * 16);
    const u64 tag = get_tag(ws);
    if (!flags_up(f2, tag)) wait_flags(rs, WS_FLAG2 + lane * 16, tag);
    VMCNT(0);
    lsig(C_LAND + 16 * 7);
    STAMP(23);
    DUMP(33); DUMP(31); DUMP(22); DUMP(23);
    return;
  }
  const u64 tag = get_tag(ws);
  __builtin_amdgcn_s_setprio(2);
  if (wave == 9) {
    unsigned vo[16];
#pragma unroll
    for (int r = 0; r < 16; ++r) { vo[r] = lq * ASTR + (r & 3) * (4 * ASTR - 1024) + 16 * (l15 ^ fsw(r)); pin(vo[r]); }
    const unsigned char* qa = ws + WS_ARENA + A_QA + b * 256;
    lwait<true>(C_LAND + 16 * FLAG_AT, 1);
    wait_flags(rs, WS_FLAG1 + lane * 16, tag);
    STAMP(17);
    wait_hints(hl, (unsigned)tag + 2, 1);
    STAMP(18);
    nap<D_QA>();
#pragma unroll
    for (int i = 0; i < 8; ++i)
      DMA4("sc1", qa + i * (16 * ASTR), 3 * 32768 + i * 4096, vo[4 * (i & 3)], vo[4 * (i & 3) + 1], vo[4 * (i & 3) + 2], vo[4 * (i & 3) + 3]);
    VMCNT(0);
    lsig(C_QST);
    STAMP(38);
    DUMP(17); DUMP(18); DUMP(38);
    return;
  }
  if (wave >= 10) {
    __builtin_amdgcn_s_setprio(3);
    const int hh = wave - 10;
    unsigned sa = T_SP + hh * 1024 + lane * 16, cb = T_CNT, four = 4u;
    int mlo = lq == 0 ? arena(16 * b + s) + A_ML + hh * 256 + l15 * 16 : OOB;
    int aro = hh == 0 && lane < 32 ? WS_ARR + (4 * lane + (b >> 1)) * 128 + (b & 1) * 16 + s : OOB;
    unsigned tg = (unsigned)tag;
    pin(sa); pin(cb); pin(four); pin(mlo); pin(aro); pin(tg);
    float ref = 0.f, l = 0.f;
    lwait<true>(C_QST, 1);
    static_for<0, 8>([&](auto tc) {
      constexpr int t = decltype(tc)::value;
      if constexpr (t >= 4) cwait<C_FREE + 16 * (t - 2), 8>(cb);
      f32x4 p0, p1, p2, p3;
      unsigned c, n;
      asm volatile(SPIN_HEAD "ds_read_b32 %[c], %[cb] offset:%[o]\n\tds_read_b128 %[p0], %[sa] offset:%[s0]\n\tds_read_b128 %[p2], %[sa] offset:%[s2]\n\t"
                   "ds_read_b128 %[p1], %[sa] offset:%[s1]\n\tds_read_b128 %[p3], %[sa] offset:%[s3]\n\t" SPIN_TAIL
                   : [c] "=&v"(c), [n] "=&s"(n), [p0] "=&v"(p0), [p1] "=&v"(p1), [p2] "=&v"(p2), [p3] "=&v"(p3)
                   : [cb] "v"(cb), [sa] "v"(sa), [o] "i"(C_SRDY + 16 * t - T_CNT), [w] "i"(4), [s0] "i"((t & 1) * 8192),
                     [s1] "i"((t & 1) * 8192 + 2048), [s2] "i"((t & 1) * 8192 + 4096), [s3] "i"((t & 1) * 8192 + 6144) : "vcc", "scc", "memory");
      if constexpr (t == 7) __builtin_amdgcn_raw_buffer_store_b8((unsigned char)tg, rs, aro, 0, SC1);
      f32x4 a = (p0 - ref) + p2, c4 = (p1 - ref) + p3;
      const float m = fmaxf(fmaxf(fmaxf(a.x, a.y), fmaxf(a.z, a.w)), fmaxf(fmaxf(c4.x, c4.y), fmaxf(c4.z, c4.w)));
      unsigned sig = four;
      if constexpr (t == 0) {
        ref = xmax32(xmax16(m));
        a -= ref; c4 -= ref;
      } else if (__builtin_expect(__any(m > BUMP), 0)) {
        const float up = fmaxf(0.f, xmax32(xmax16(m)));
        const float fa = __builtin_amdgcn_exp2f(-up);
        *(LAS float*)(T_FAC + t * 128 + hh * 64 + l15 * 4) = fa;
        l *= fa;
        ref += up;
        a = (p0 - ref) + p2; c4 = (p1 - ref) + p3;
        sig = 4u + (1u << hh);
      }
      const v4u pf = {pk(__builtin_amdgcn_exp2f(a.x), __builtin_amdgcn_exp2f(a.y)), pk(__builtin_amdgcn_exp2f(a.z), __builtin_amdgcn_exp2f(a.w)),
                      pk(__builtin_amdgcn_exp2f(c4.x), __builtin_amdgcn_exp2f(c4.y)), pk(__builtin_amdgcn_exp2f(c4.z), __builtin_amdgcn_exp2f(c4.w))};
      *(LAS v4u*)((LAS char*)sa + (T_P - T_SP) + (t & 3) * 2048) = pf;
      csig<C_PRDY + 16 * t>(cb, sig);
#pragma unroll
      for (int e = 0; e < 4; ++e) {
        const unsigned u = pf[e];
        l = __builtin_amdgcn_fdot2_f32_bf16(__builtin_bit_cast(bf16x2, u), __builtin_bit_cast(bf16x2, 0x3F803F80u), l, false);
      }
    });
    stg(rs, mlo, v4u{__float_as_uint(ref), (unsigned)tag, __float_as_uint(xsum32(xsum16(l))), (unsigned)(tag >> 32)});
    STAMP(24);
    if (hh == 0) DUMP(24);
    return;
  }
  if (wave < 4) {
    __builtin_amdgcn_s_setprio(PRIO_S);
    const int kh = wave & 1, ch = wave >> 1;
    const int sg = (0x2130 >> (4 * lq)) & 3;
    unsigned ao[4];
#pragma unroll
    for (int k = 0; k < 4; ++k) ao[k] = l15 * 1024 + ch * 512 + 16 * ((4 * k + sg) ^ fsw(l15));
    unsigned sa = T_SP + wave * 2048 + lane * 16, cb = T_CNT, one = 1u;
    unsigned al[4], ah[4];
#pragma unroll
    for (int k = 0; k < 4; ++k) { al[k] = ao[k] + kh * 16384; ah[k] = al[k] + 65536; pin(al[k]); pin(ah[k]); }
    pin(sa); pin(cb); pin(one);
    v4u af[2][8], qf[2][8];
    auto rd = [&](unsigned sl, int ks) { return lds128(sl < 65536 ? al[ks & 3] : ah[ks & 3], (sl & 32768) + (ks >> 2) * 256); };
#pragma unroll
    for (int t = 0; t < 2; ++t) {
      lwait<true>(C_LAND + 16 * t, 1);
#pragma unroll
      for (int ks = 0; ks < 8; ++ks) af[t][ks] = rd(t * 32768, ks);
      lsig(C_FREE + 16 * t);
    }
    unsigned qa[4];
#pragma unroll
    for (int k = 0; k < 4; ++k) { qa[k] = 3 * 32768 + ao[k]; pin(qa[k]); }
    lwait(C_QST, 1);
    STAMP(19);
    unsigned nx2 = cpeek<C_LAND + 32>(cb), nxt = cpeek<C_LAND + 48>(cb);
    f32x4 s0, s1;
#pragma unroll 1
    for (int sp = 0; sp < SPIN_G; ++sp) {
#pragma unroll
      for (int ks = 0; ks < 8; ++ks) {
#pragma unroll
        for (int hh = 0; hh < 2; ++hh) qf[hh][ks] = lds128(qa[ks & 3], hh * 16384 + (ks >> 2) * 256);
        __builtin_amdgcn_sched_barrier(0);
      }
      s0 = z4; s1 = z4;
#pragma unroll
      for (int ks = 0; ks < 8; ++ks) { s0 = mfma16(af[0][ks], qf[0][ks], s0); s1 = mfma16(af[0][ks], qf[1][ks], s1); }
      if (__builtin_expect(!__any(__builtin_isunordered(s0.x, s1.x)), 1) || !(any_sent<8>(qf[0]) || any_sent<8>(qf[1]))) break;
#pragma unroll 1
      for (int r = 0; r < 32; ++r)
        asm volatile("s_mov_b32 m0, %2\n\ts_nop 0\n\tglobal_load_lds_dwordx4 %0, %1 sc1"
                     :: "v"(lq * ASTR + 16 * (l15 ^ fsw(r))), "s"(ws + WS_ARENA + A_QA + b * 256 + r * (4 * ASTR)), "s"(3 * 32768 + r * 1024) : "memory");
      VMCNT(0);
    }
    *(LAS f32x4*)sa = s0;
    *(LAS f32x4*)((LAS char*)sa + 1024) = s1;
    csig<C_SRDY>(cb, one);
    csig<C_QRD>(cb, one);
    STAMP(20);
    auto refill = [&](v4u& a, auto slc, auto ksc) {
      constexpr unsigned sl = decltype(slc)::value;
      constexpr int ks = decltype(ksc)::value;
      rd_inplace<(sl & 32768) + (ks >> 2) * 256>(a, sl < 65536 ? al[ks & 3] : ah[ks & 3]);
    };
    static_for<0, 8>([&](auto k) { refill(af[0][decltype(k)::value], IntC<slot_c(2)>{}, k); });
    bool miss = __any(nx2 < 1);
    if (!miss) csig<C_FREE + 32>(cb, one);
    static_for<1, 8>([&](auto tc) {
      constexpr int t = decltype(tc)::value;
      const bool was = miss;
      if constexpr (t < 6) miss = __any(nxt < 1);
      unsigned use;
      peek2<C_PRDY + 16 * (t - 2) - T_CNT, C_LAND + 16 * (t < 5 ? t + 3 : 8) - T_CNT>(use, nxt, cb);
      static_for<0, 8>([&](auto k) {
        constexpr int ks = decltype(k)::value;
        score_pair<(t < 6 ? 9 : 9 - ks), ks == 0>(s0, s1, af[t & 1][ks], qf[0][ks], qf[1][ks]);
        if constexpr (t < 6) refill(af[t & 1][ks], IntC<slot_c(t + 2)>{}, k);
      });
      wait2<(t < 6 ? 8 : 0)>(use, nxt);
      if (__builtin_expect(__any(use < 8), 0)) cwait<C_PRDY + 16 * (t - 2), 8>(cb);
      put_scores<(t & 1) * 8192>(sa, s0, s1);
      csig<C_SRDY + 16 * t>(cb, one);
      if constexpr (t < 4) { if (!miss) csig<C_FREE + 16 * (t + 2)>(cb, one); }
      if constexpr (t < 7) {
        if (was) {
          cwait<C_LAND + 16 * (t + 1), 1>(cb);
          static_for<0, 8>([&](auto k) { refill(af[(t + 1) & 1][decltype(k)::value], IntC<slot_c(t + 1)>{}, k); });
          if constexpr (t < 5) csig<C_FREE + 16 * (t + 1)>(cb, one);
        }
      }
    });
    asm volatile("s_waitcnt lgkmcnt(0)" ::: "memory");
    STAMP(21);
    if (wave == 0) { DUMP(19); DUMP(20); DUMP(21); }
  } else {
    __builtin_amdgcn_s_setprio(PRIO_PV);
    unsigned bo[2];
    {
      const int q = l15 >> 2, pp = lane & 3;
#pragma unroll
      for (int c = 0; c < 2; ++c) {
        bo[c] = (4 * lq + q) * 1024 + w * 256 + 16 * ((2 * c + (pp >> 1)) ^ fsw(4 * lq + q)) + 8 * (pp & 1);
      }
    }
    unsigned pa = T_P + lane * 16, cb = T_CNT, one = 1u;
    int po = arena(16 * lq + w) + A_PART + (b * 16 + s) * 256 + l15 * 16;
    unsigned vl[8];
#pragma unroll
    for (int ct = 0; ct < 8; ++ct) { vl[ct] = bo[ct & 1] ^ ((ct >> 1) * 64); pin(vl[ct]); }
    pin(pa); pin(cb); pin(one); pin(po);
    f32x4 acc[2][8];
    v4u vf[2][8];
    auto rd = [&](unsigned sl, int ct) {
      unsigned a = vl[ct];
      if (sl >= 65536) asm volatile("v_add_u32 %0, 0x10000, %1" : "=v"(a) : "v"(vl[ct]));
      const v2u lo = ldstr(a, sl & 32768), hi = ldstr(a, (sl & 32768) + 16384);
      return v4u{lo.x, lo.y, hi.x, hi.y};
    };
#pragma unroll
    for (int t = 0; t < 2; ++t) {
      lwait<true>(C_LAND + 16 * t, 1);
#pragma unroll
      for (int ct = 0; ct < 8; ++ct) vf[t][ct] = rd(t * 32768, ct);
      lsig(C_FREE + 16 * t);
    }
    bool miss = false;
    lwait<true>(C_SRDY, 1);
    static_for<0, 8>([&](auto tc) {
      constexpr int t = decltype(tc)::value;
      v4u p0, p1;
      unsigned c, n, nxt;
      asm volatile(SPIN_HEAD "ds_read_b32 %[c], %[cb] offset:%[o]\n\tds_read_b128 %[p0], %[pa] offset:%[s0]\n\t"
                   "ds_read_b128 %[p1], %[pa] offset:%[s1]\n\tds_read_b32 %[x], %[cb] offset:%[ox]\n\t" SPIN_TAIL
                   : [c] "=&v"(c), [n] "=&s"(n), [p0] "=&v"(p0), [p1] "=&v"(p1), [x] "=&v"(nxt)
                   : [cb] "v"(cb), [pa] "v"(pa), [o] "i"(C_PRDY + 16 * t - T_CNT), [w] "i"(8), [s0] "i"((t & 3) * 2048),
                     [s1] "i"((t & 3) * 2048 + 1024), [ox] "i"(C_LAND + 16 * (t + 2) - T_CNT) : "vcc", "scc", "memory");
      if constexpr (t > 0) {
        if (__builtin_expect(__any(c > 8u), 0)) {
#pragma unroll 1
          for (int ht = 0; ht < 2; ++ht)
            if (!__any(c & (1u << ht))) *(LAS float*)(T_FAC + t * 128 + ht * 64 + l15 * 4) = 1.f;
          const f32x4 f0 = u2f(lds128(T_FAC + t * 128 + lq * 16)), f1 = u2f(lds128(T_FAC + t * 128 + lq * 16, 64));
#pragma unroll
          for (int ct = 0; ct < 8; ++ct) { acc[0][ct] *= f0; acc[1][ct] *= f1; pin(acc[0][ct]); pin(acc[1][ct]); }
          asm volatile("s_nop 4");
        }
      }
      if constexpr (t < 7) {
#pragma unroll
        for (int ct = 0; ct < 8; ++ct) {
          if constexpr (t) mfma_pair(acc[0][ct], acc[1][ct], p0, p1, vf[t & 1][ct]); else mfma_pair0(acc[0][ct], acc[1][ct], p0, p1, vf[0][ct]);
          if constexpr (t < 6) vf[t & 1][ct] = rd(slot_c(t + 2), ct);
          __builtin_amdgcn_sched_barrier(0);
        }
        const bool was = miss;
        if constexpr (t < 6) {
          miss = __any(nxt < 1);
          if constexpr (t < 4) { if (!miss) csig<C_FREE + 16 * (t + 2)>(cb, one); }
        }
        if (was) {
          cwait<C_LAND + 16 * (t + 1), 1>(cb);
#pragma unroll
          for (int ct = 0; ct < 8; ++ct) vf[(t + 1) & 1][ct] = rd(slot_c(t + 1), ct);
          if constexpr (t < 5) csig<C_FREE + 16 * (t + 1)>(cb, one);
        }
      } else {
#pragma unroll
        for (int ct = 0; ct < 8; ++ct) acc[0][ct] = mfma16(p0, vf[1][ct], acc[0][ct]);
#pragma unroll
        for (int ct = 0; ct < 8; ++ct) acc[1][ct] = mfma16(p1, vf[1][ct], acc[1][ct]);
      }
    });
#pragma unroll
    for (int ht = 0; ht < 2; ++ht)
#pragma unroll
      for (int r = 0; r < 4; ++r)
        __builtin_amdgcn_raw_buffer_store_b128(v4u{pk(acc[ht][0][r], acc[ht][1][r]), pk(acc[ht][2][r], acc[ht][3][r]),
                                                   pk(acc[ht][4][r], acc[ht][5][r]), pk(acc[ht][6][r], acc[ht][7][r])},
                                               rs, po, (16 * ht + r) * 4 * ASTR, SC1);
    STAMP(25);
    if (w == 0) DUMP(25);
  }
  if (b == 7 || s >= 8) return;
  ffn_part(p, b, s, wave, tag);
}

DI void ffn_part(const Params& p, int b, int s, int wave, u64 tag) {
  int lane;
  asm volatile("v_mbcnt_lo_u32_b32 %0, -1, 0\n\tv_mbcnt_hi_u32_b32 %0, -1, %0" : "=v"(lane));
  unsigned char* ws = p.ws;
  const rsrc_t rs = __builtin_amdgcn_make_buffer_rsrc(ws, 0, WS_BYTES, 0x00020000);
  const int l15 = lane & 15, lq = lane >> 4, w = wave & 3;
  const f32x4 z4 = {0.f, 0.f, 0.f, 0.f};
  const unsigned char* hl = ws + WS_HQ + (16 * b + s) * 128;
  __builtin_amdgcn_s_setprio(2);
  const int fg = 8 * b + s, b8 = lane & 7, p4 = 4 * (lane & 3);
  const bool bvalid = l15 < 8;
  const int ch = l15 ^ ((lq << 2) | w), row0 = 4 * w + lq;
  bar();
  nap<D_F>();
  v4u mm[8];
  const int mo = arena(33 + 4 * (lane >> 3)) + A_HM + wave * 256 + b8 * 32;
  auto load_mod = [&]() {
    FENCE();
#pragma unroll
    for (int i = 0; i < 8; ++i) mm[i] = ldg(rs, mo + (i & 1) * 16, ((i >> 2) * 32 + ((i >> 1) & 1)) * ASTR);
    FENCE();
  };
  load_mod();
  if (wave < 4) {
    dma_run<false>(p.w1, (row0 + (ch >> 2) * 128) * 3584 + fg * 64 + (ch & 3) * 16, W0 + w * 1024, 8, 16 * 3584, 4096);
  } else {
    const unsigned u = (32 * fg + row0) * 1024 + ch * 16;
    dma_run<true>(p.w2, u, W1 + w * 1024, 4, 256, 8192);
    dma_run<true>(p.w2, u + 16 * 1024, W1 + w * 1024 + 4096, 4, 256, 8192);
  }
#pragma unroll 1
  for (int sp = 0; sp < SPIN_G; ++sp) {
    if (!any_sent<8>(mm)) break;
    load_mod();
  }
  f32x4 sb[2], cb[2];
#pragma unroll
  for (int i = 0; i < 2; ++i) {
    sb[i] = u2f(mm[i]) + u2f(mm[2 + i]); cb[i] = u2f(mm[4 + i]) + u2f(mm[6 + i]) + 1.f;
    pin(sb[i]); pin(cb[i]);
  }
  VMCNT(0);
  bar();
  STAMP(28);
  v4u fw[16];
  if (wave < 2) {
#pragma unroll
    for (int kq = 0; kq < 4; ++kq) {
      const unsigned a0 = tr_addr(W0, lane, 32 * kq + 16 * w + p4, 0), a1 = tr_addr(W0, lane, 32 * kq + 16 * w + p4, 1);
#pragma unroll
      for (int ks = 0; ks < 4; ++ks) fw[4 * kq + ks] = tr_frag(a0, a1, ks);
    }
  } else {
#pragma unroll
    for (int i = 0; i < 8; ++i) {
      const int c4 = 32 * (i >> 1) + 2 * p4 + 4 * (i & 1);
      fw[i] = tr_frag(tr_addr(W1 + w * 8192, lane, c4, 0), tr_addr(W1 + w * 8192, lane, c4, 1), 0);
      fw[8 + i] = fw[i];
    }
  }
  int xo = arena(wave) + A_X1 + lane * 16;
  unsigned o5 = L_A5 + wave * 1040 + lane * 16;
  unsigned a5 = L_A5 + b8 * 1040 + lq * 16, a6w = bvalid ? L_A6 + l15 * 80 + w * 32 + lq * 8 : L_JUNK, a6r = L_A6 + b8 * 80 + lq * 16;
  int yo = arena(fg) + A_YP + b8 * 1024 + w * 256 + (l15 >> 3) * 64 + lq * 16;
  pin(xo); pin(o5); pin(a5); pin(a6w); pin(a6r); pin(yo);
#pragma unroll
  for (int i = 0; i < 16; ++i) pin(fw[i]);
  STAMPD(40);
  if (wave == 0) { wait_hints(hl, (unsigned)tag + 3, 0); STAMPV(40); nap<D_C>(); }
  bar();
  {
    v4u xv[1];
    gather<1>(rs, xo, 0, xv);
    STAMP(15);
    const f32x4 v0 = {bflo(xv[0].x), bfhi(xv[0].x), bflo(xv[0].y), bfhi(xv[0].y)}, v1 = {bflo(xv[0].z), bfhi(xv[0].z), bflo(xv[0].w), bfhi(xv[0].w)};
    const f32x4 sv4 = v0 + v1, qv4 = v0 * v0 + v1 * v1;
    const float mean = wave_sum((sv4.x + sv4.y) + (sv4.z + sv4.w)) * (1.f / 512);
    const float msq = wave_sum((qv4.x + qv4.y) + (qv4.z + qv4.w)) * (1.f / 512);
    const float rstd = __builtin_amdgcn_rsqf(fmaxf(msq - mean * mean, 0.f) + EPS);
    *(LAS v4u*)o5 = pk8((v0 - mean) * rstd * cb[0] + sb[0], (v1 - mean) * rstd * cb[1] + sb[1]);
    if (wave == 0) { DUMP(15); DUMP(40); DUMP(28); }
  }
  bar();
  STAMP(36);
  if (wave < 2) {
    f32x4 z0 = z4, z1 = z4;
#pragma unroll
    for (int hq = 0; hq < 2; ++hq) {
      v4u bf[8];
#pragma unroll
      for (int i = 0; i < 8; ++i) bf[i] = lds128(a5, hq * 512 + i * 64);
      if (hq == 0) __builtin_amdgcn_sched_barrier(0);
#pragma unroll
      for (int i = 0; i < 8; i += 2) { z0 = mfma16(fw[8 * hq + i], bf[i], z0); z1 = mfma16(fw[8 * hq + i + 1], bf[i + 1], z1); }
    }
    z0 += z1;
    *(LAS v2u*)a6w = v2u{pk(z0.x < 0.f ? 0.f : z0.x, z0.y < 0.f ? 0.f : z0.y), pk(z0.z < 0.f ? 0.f : z0.z, z0.w < 0.f ? 0.f : z0.w)};
  }
  bar();
  if (wave >= 4) {
    STAMP(37);
    const v4u bf = lds128(a6r);
#pragma unroll
    for (int i = 0; i < 2; ++i) {
      const v4u ya = pk8(mfma16(fw[4 * i], bf, z4), mfma16(fw[4 * i + 1], bf, z4)), yb = pk8(mfma16(fw[4 * i + 2], bf, z4), mfma16(fw[4 * i + 3], bf, z4));
      stg(rs, yo + i * 128, pick(bvalid, ya, yb));
    }
    STAMP(16);
    if (wave == 4) { DUMP(36); DUMP(37); DUMP(16); }
  }
}

__global__ __launch_bounds__(768) void adaln_mla_ffn_kernel(Params p) {
  STAMP(0);
  asm volatile("s_load_dword s90, %0, 0x0\n\ts_load_dword s91, %0, 0x40" :: "s"(__builtin_amdgcn_kernarg_segment_ptr()) : "s90", "s91");
  const int g = blockIdx.x & 7, idx = blockIdx.x >> 3;
  const int wave = __builtin_amdgcn_readfirstlane(threadIdx.x >> 6), lane = threadIdx.x & 63;
  if (wave == 0) {
    u64 pc;
    asm volatile("s_getpc_b64 %0" : "=s"(pc));
    const unsigned line = min(idx * ((CODE_LINES + 31) / 32) + min(lane, (CODE_LINES + 31) / 32 - 1), CODE_LINES - 1);
    asm volatile("s_mov_b32 m0, %2\n\ts_nop 0\n\tglobal_load_lds_dword %0, %1" :: "v"(line * 128), "s"(pc & ~255ull), "s"(L_JUNK) : "memory");
  }
  const int wi = idx & 15, h = 4 * g + (wi >> 2), j = wi & 3, w = wave & 3, lq = lane >> 4;
  const int ch = (lane & 15) ^ ((lq << 2) | w);
  unsigned vq = (128 * j + 4 * w + lq) * 8192 + h * 256 + ch * 16;
  unsigned v1 = j ? vq : (128 * (h & 3) + 4 * w + lq) * 6144 + (ch >> 3) * 1024 + g * 128 + (ch & 7) * 16;
  unsigned vc = (2 * w + (lane >> 5)) * 1024 + (j ? j - 1 : (h >> 1) & 1) * 512 + (lane & 31) * 16, vx = w * 2048 + lane * 16;
  pin(vq); pin(v1); pin(vc); pin(vx);
  asm volatile("" :: "s"(p.x), "s"(p.ckv), "s"(p.wq), "s"(p.wuk), "s"(p.wuv), "s"(p.wo), "s"(p.w1), "s"(p.w2), "s"(p.c), "s"(p.wmod),
               "s"(p.out), "s"(p.ws));
  STAMP(39);
  if (wave >= 8) {
    if (idx >= 16) {
      dma_run<true>(j ? p.wq : p.wmod, v1, (j ? W0 : W2) + w * 1024, 8, j ? 16 * 8192 : 16 * 6144, 4096);
      dma_run<true>(j ? p.wuk : p.wq, vq, (j ? W1 : W0) + w * 1024, 8, 16 * 8192, 4096);
    } else if (PREF) {
      nap<D_PREF>();
      dma_run<true>(p.wq, vq - 96 * j * 8192, T_CNT + 1024 + w * 1024, 2, 16 * 8192, 0);
      dma_run<true>(p.wuk, vq, T_CNT + 1024 + w * 1024, 8, 16 * 8192, 0);
    }
  } else if (idx >= 16) {
    if (wave >= 4) {
      if (j <= 2) dma16<false>(p.c, vc, L_RAW + w * 3072);
      if (j == 0) { dma16<false>(p.x, vx, L_RAW + w * 3072 + 1024); dma16<false>(p.x + 512, vx, L_RAW + w * 3072 + 2048); }
    } else if (wave == 3) {
      const rsrc_t rs = __builtin_amdgcn_make_buffer_rsrc(p.ws, 0, WS_BYTES, 0x00020000);
#pragma unroll 1
      for (int n = 0; n < 8; ++n)
        __builtin_amdgcn_raw_buffer_store_b128(v4u{SENT, SENT, SENT, SENT}, rs, lane * 16, arena(16 * g + wi) + n * 1024, SC1);
    }
  }
  FENCE();
  if (idx < 16) attn_wg(p, g, idx, wave, lane);
  else weight_wg(p, g, wi, wave, lane, vq);
}

extern "C" void kernel_launch(void* const* d_in, const int* in_sizes, int n_in, void* d_out, int out_size, void* d_ws, size_t ws_size,
                              hipStream_t stream) {
  Params p;
  p.x = (const u16*)d_in[0]; p.ckv = (const u16*)d_in[1]; p.wq = (const u16*)d_in[2]; p.wuk = (const u16*)d_in[3];
  p.wuv = (const u16*)d_in[4]; p.wo = (const u16*)d_in[5]; p.w1 = (const u16*)d_in[7]; p.w2 = (const u16*)d_in[8];
  p.c = (const u16*)d_in[9]; p.wmod = (const u16*)d_in[10];
  p.out = (u16*)d_out; p.ws = (unsigned char*)d_ws;
  (void)hipFuncSetAttribute((const void*)adaln_mla_ffn_kernel, hipFuncAttributeMaxDynamicSharedMemorySize, LDS_BYTES);
  adaln_mla_ffn_kernel<<<256, 768, LDS_BYTES, stream>>>(p);
}
```

```cpp
#include <hip/hip_runtime.h>
#include <hip/hip_bf16.h>
#pragma clang diagnostic ignored "-Wint-to-pointer-cast"
#pragma clang diagnostic ignored "-Wsometimes-uninitialized"

typedef unsigned short u16;
typedef unsigned long long u64;
typedef unsigned v4u __attribute__((ext_vector_type(4)));
typedef unsigned v2u __attribute__((ext_vector_type(2)));
typedef float f32x2 __attribute__((ext_vector_type(2)));
typedef float f32x4 __attribute__((ext_vector_type(4)));
typedef float f32x16 __attribute__((ext_vector_type(16)));
typedef short s16x4 __attribute__((ext_vector_type(4)));
typedef short bf16x8 __attribute__((ext_vector_type(8)));
typedef __bf16 bf16x2 __attribute__((ext_vector_type(2)));
#define GAS __attribute__((address_space(1)))
#define LAS __attribute__((address_space(3)))
#define DI __device__ __forceinline__
#define RLX_AGENT __ATOMIC_RELAXED, __HIP_MEMORY_SCOPE_AGENT
typedef __amdgpu_buffer_rsrc_t rsrc_t;

#define CODE_LINES 400
#define ATT_DELAY 0
#define T_H 45
#define D_UK 8
#define T_QP 102
#define T_QA 106
#define T_B 100
#define T_C 190
#define T_D 365
#define D_PART 6
#define D_F 0
#define D_ML 4
#define FLAG_AT 1
#define D_PREF 16
#define PRIO_S 2
#define PRIO_PV 2
#define PF_NT false
#define D_W 0
#define T_PREF 130
#define T_W 40
#define PF_EVEN 24
#define T_FILL 0
#define T_FLAG 220
#define WS_BASE 0
#define FILL_AUX 16
#define T_L 190
#define FILL_EARLY 1
#define D_FILL 0
#define PREF 1
#define WO_NT 1

constexpr int WS_FLAG1 = 4096, WS_FLAG2 = 8192;
constexpr int WS_EFL = 16384  , WS_ARR = 65536, WS_HP = 81920, WS_HB = 98304  ,
              WS_HH = 131072  , WS_HQ = 196608  ;
constexpr int WS_ARENA = 262144, ASTR = 61696;
constexpr int A_H = 0, A_ML = 1024, A_QP = 2048, A_QA = 6144, A_HM = 8192, A_X1 = 9216, A_OUTP = 10240, A_YP = 18432, A_PART = 26624, A_CK = 59392;
constexpr int WS_BYTES = WS_ARENA + 128 * ASTR;
constexpr unsigned SENT = 0xFFFFFFFFu;
constexpr int OOB = 0x7FFFFF00;
constexpr int SC1 = 16;
constexpr int SPIN_H = 1 << 11;
constexpr int SPIN_G = 1 << 19;
constexpr int SPIN_L = 1 << 23;
constexpr int LDS_BYTES = 163840;
constexpr float EPS = 1e-5f;
constexpr float QSCALE = 0.08838834764831845f * 1.4426950408889634f;
constexpr float BUMP = 40.f;

#define STAMP(k) do {} while (0)
#define STAMPV(k) do {} while (0)
#define STAMPD(k) do {} while (0)
#define DUMP(k) do {} while (0)
#define DUMPC(k, c) do {} while (0)

template <class T> DI void pin(T& v) { asm volatile("" : "+v"(v)); }
DI float bflo(unsigned u) { return __uint_as_float(u << 16); }
DI float bfhi(unsigned u) { return __uint_as_float(u & 0xFFFF0000u); }
DI unsigned pk(float a, float b) { f32x2 v = {a, b}; return __builtin_bit_cast(unsigned, __builtin_convertvector(v, bf16x2)); }
DI f32x4 mfma16(v4u a, v4u b, f32x4 c) {
  return __builtin_amdgcn_mfma_f32_16x16x32_bf16(__builtin_bit_cast(bf16x8, a), __builtin_bit_cast(bf16x8, b), c, 0, 0, 0);
}
DI f32x16 mfma32(v4u a, v4u b, f32x16 c) {
  return __builtin_amdgcn_mfma_f32_32x32x16_bf16(__builtin_bit_cast(bf16x8, a), __builtin_bit_cast(bf16x8, b), c, 0, 0, 0);
}
DI v4u ldg(rsrc_t rs, int off, int so = 0) { return __builtin_amdgcn_raw_buffer_load_b128(rs, off, so, SC1); }
DI void stg(rsrc_t rs, int off, v4u v) { __builtin_amdgcn_raw_buffer_store_b128(v, rs, off, 0, SC1); }
DI unsigned umax4(v4u v) { return max(max(v.x, v.y), max(v.z, v.w)); }
DI v4u f2u(f32x4 v) { return __builtin_bit_cast(v4u, v); }
DI f32x4 u2f(v4u v) { return __builtin_bit_cast(f32x4, v); }
DI bool nan4(f32x4 v) { return __builtin_isunordered(v.x, v.y) | __builtin_isunordered(v.z, v.w); }
DI v4u lds128(unsigned a, int o = 0) { return *(LAS v4u*)((LAS char*)a + o); }
DI v2u ldstr(unsigned a, int o = 0) { return __builtin_bit_cast(v2u, __builtin_amdgcn_ds_read_tr16_b64_v4i16((LAS s16x4*)((LAS char*)a + o))); }
DI void bar() {
  __builtin_amdgcn_sched_barrier(0);
  asm volatile("s_waitcnt lgkmcnt(0)\n\ts_barrier" ::: "memory");
  __builtin_amdgcn_sched_barrier(0);
}
#define VMCNT_(n) asm volatile("s_waitcnt vmcnt(" #n ")" ::: "memory")
#define VMCNT(n) VMCNT_(n)
#define FENCE() asm volatile("" ::: "memory")
template <bool NT> DI void dma16(const void* base, unsigned voff, unsigned dst) {
  if (NT) asm volatile("s_mov_b32 m0, %2\n\ts_nop 0\n\tglobal_load_lds_dwordx4 %0, %1 nt" :: "v"(voff), "s"(base), "s"(dst) : "memory");
  else asm volatile("s_mov_b32 m0, %2\n\ts_nop 0\n\tglobal_load_lds_dwordx4 %0, %1" :: "v"(voff), "s"(base), "s"(dst) : "memory");
}
template <bool NT> DI void dma_run(const void* base, unsigned voff, unsigned dst, int n, int gstr, int lstr) {
  const unsigned char* b = (const unsigned char*)base;
#pragma unroll 1
  for (int i = 0; i < n; ++i) { VMCNT(7); dma16<NT>(b, voff, dst); b += gstr; dst += lstr; }
}
template <int N> DI void nap() { if (N > 0) __builtin_amdgcn_s_sleep(N); }
DI void lsig(unsigned a, unsigned v = 1u) { asm volatile("s_mov_b64 exec, 1\n\tds_add_u32 %0, %1\n\ts_mov_b64 exec, -1" :: "v"(a), "v"(v) : "memory"); }
DI unsigned lpeek(unsigned a) { return *(volatile LAS unsigned*)a; }
template <bool NAP = false> DI void lwait(unsigned a, unsigned want) {
  unsigned v, n = SPIN_L;
  if (NAP)
    asm volatile("1:\n\tds_read_b32 %0, %2\n\ts_waitcnt lgkmcnt(0)\n\tv_cmp_le_u32 vcc, %3, %0\n\ts_cbranch_vccnz 2f\n\t"
                 "s_sleep 1\n\ts_sub_u32 %1, %1, 1\n\ts_cbranch_scc0 1b\n2:" : "=&v"(v), "+s"(n) : "v"(a), "s"(want) : "vcc", "scc", "memory");
  else
    asm volatile("1:\n\tds_read_b32 %0, %2\n\ts_waitcnt lgkmcnt(0)\n\tv_cmp_le_u32 vcc, %3, %0\n\ts_cbranch_vccnz 2f\n\t"
                 "s_sub_u32 %1, %1, 1\n\ts_cbranch_scc0 1b\n2:" : "=&v"(v), "+s"(n) : "v"(a), "s"(want) : "vcc", "scc", "memory");
}
template <int CTRL> DI float dppf(float v) {
  return __builtin_bit_cast(float, __builtin_amdgcn_update_dpp(0, __builtin_bit_cast(int, v), CTRL, 0xF, 0xF, true));
}
DI float row16_max(float v) {
  v = fmaxf(v, dppf<0x128>(v)); v = fmaxf(v, dppf<0x124>(v)); v = fmaxf(v, dppf<0x122>(v)); return fmaxf(v, dppf<0x121>(v));
}
DI float row16_sum(float v) { v += dppf<0x128>(v); v += dppf<0x124>(v); v += dppf<0x122>(v); return v + dppf<0x121>(v); }
DI float xsum16(float v) { auto r = __builtin_amdgcn_permlane16_swap(__float_as_uint(v), __float_as_uint(v), false, false); return __uint_as_float(r[0]) + __uint_as_float(r[1]); }
DI float xsum32(float v) { auto r = __builtin_amdgcn_permlane32_swap(__float_as_uint(v), __float_as_uint(v), false, false); return __uint_as_float(r[0]) + __uint_as_float(r[1]); }
DI float xmax16(float v) { auto r = __builtin_amdgcn_permlane16_swap(__float_as_uint(v), __float_as_uint(v), false, false); return fmaxf(__uint_as_float(r[0]), __uint_as_float(r[1])); }
DI float xmax32(float v) { auto r = __builtin_amdgcn_permlane32_swap(__float_as_uint(v), __float_as_uint(v), false, false); return fmaxf(__uint_as_float(r[0]), __uint_as_float(r[1])); }
DI float wave_sum(float v) { return xsum32(xsum16(row16_sum(v))); }

#define XS8(d, a, b) "v_xor_b32_dpp " d ", " a ", " a " row_ror:8 row_mask:0xf bank_mask:0x3\n\tv_xor_b32_dpp " d ", " b ", " b " row_ror:8 row_mask:0xf bank_mask:0xc\n\t"
#define XS4(d, a, b) "v_xor_b32_dpp " d ", " a ", " a " row_half_mirror row_mask:0xf bank_mask:0x5\n\tv_xor_b32_dpp " d ", " b ", " b " row_half_mirror row_mask:0xf bank_mask:0xa\n\t"
#define XQ(d, p) "v_xor_b32_dpp " d ", " d ", " d " quad_perm:" p " row_mask:0xf bank_mask:0xf\n\t"
DI void xor_rows8(const unsigned (&v)[8], unsigned& r0, unsigned& r1) {
  unsigned t0, t1, t2, t3;
  asm volatile("s_nop 1\n\t" XS8("%2", "%6", "%10") XS8("%4", "%8", "%12") XS8("%3", "%7", "%11") XS8("%5", "%9", "%13")
               XS4("%0", "%2", "%4") XS4("%1", "%3", "%5") XQ("%0", "[2,3,0,1]") "s_nop 0\n\t" XQ("%1", "[2,3,0,1]") XQ("%0", "[1,0,3,2]")
               "s_nop 0\n\t" XQ("%1", "[1,0,3,2]")
               : "=&v"(r0), "=&v"(r1), "=&v"(t0), "=&v"(t1), "=&v"(t2), "=&v"(t3)
               : "v"(v[0]), "v"(v[1]), "v"(v[2]), "v"(v[3]), "v"(v[4]), "v"(v[5]), "v"(v[6]), "v"(v[7]));
}
DI unsigned xor_rows2(unsigned a, unsigned b) {
  unsigned r;
  asm volatile("s_nop 1\n\t" XS8("%0", "%1", "%2") "s_nop 1\n\tv_xor_b32_dpp %0, %0, %0 row_half_mirror row_mask:0xf bank_mask:0xf\n\ts_nop 1\n\t"
               XQ("%0", "[2,3,0,1]") "s_nop 1\n\t" XQ("%0", "[1,0,3,2]") "s_nop 1" : "=&v"(r) : "v"(a), "v"(b));
  return r;
}
template <int N> DI bool any_sent(const v4u (&r)[N]) {
  unsigned m = 0;
#pragma unroll
  for (int i = 0; i < N; ++i) m = max(m, umax4(r[i]));
  return __any(m == SENT);
}
template <int N> DI void load_n(rsrc_t rs, int off0, int str, v4u (&r)[N]) {
  FENCE();
#pragma unroll
  for (int i = 0; i < N; ++i) r[i] = ldg(rs, off0, i * str);
  __builtin_amdgcn_sched_barrier(0);
}
template <int N, int NAP = 0> DI void gather(rsrc_t rs, int off0, int str, v4u (&r)[N]) {
#pragma unroll 1
  for (int sp = 0; sp < SPIN_G; ++sp) {
    load_n<N>(rs, off0, str, r);
    if (!any_sent<N>(r)) break;
    nap<NAP>();
  }
}
DI u64 get_tag(const unsigned char* ws) {
  u64 e;
  asm volatile("s_load_dwordx2 %0, %1, 0x0\n\ts_waitcnt lgkmcnt(0)" : "=s"(e) : "s"(ws) : "memory");
  return e * 6364136223846793005ull + 1442695040888963407ull;
}
DI bool flags_up(v4u f, u64 tag) {
  const unsigned lo = (unsigned)tag, hi = (unsigned)(tag >> 32);
  return __all(f.x == lo && f.y == hi && f.z == lo && f.w == hi);
}
DI void wait_flags(rsrc_t rs, int off, u64 tag) {
#pragma unroll 1
  for (int sp = 0; sp < SPIN_G; ++sp) {
    FENCE();
    if (flags_up(ldg(rs, off), tag)) break;
    __builtin_amdgcn_s_sleep(8);
  }
  FENCE();
}
typedef unsigned s8u __attribute__((ext_vector_type(8)));
DI s8u sload8(const unsigned char* a) {
  s8u v;
  asm volatile("s_load_dwordx8 %0, %1, 0x0 glc\n\ts_waitcnt lgkmcnt(0)" : "=s"(v) : "s"(a) : "memory");
  return v;
}
DI void wait_hints(const unsigned char* a, unsigned want, unsigned span) {
#pragma unroll 1
  for (int sp = 0; sp < SPIN_H; ++sp) {
    const s8u v = sload8(a) - want;
    if (max(max(max(v[0], v[1]), max(v[2], v[3])), max(max(v[4], v[5]), max(v[6], v[7]))) <= span) break;
  }
}
DI unsigned max8(s8u v) { return max(max(max(v[0], v[1]), max(v[2], v[3])), max(max(v[4], v[5]), max(v[6], v[7]))); }
DI unsigned now32() { return (unsigned)__builtin_amdgcn_s_memrealtime(); }
constexpr unsigned HWIN = 1u << 20;
DI unsigned latest_time(const unsigned char* a, unsigned t0, unsigned key) {
  unsigned m = 0;
#pragma unroll 1
  for (int sp = 0; sp < SPIN_H; ++sp) {
    m = max8(sload8(a) - (key + t0 - HWIN));
    if (m < 2 * HWIN) return m + t0 - HWIN;
  }
  return t0;
}
DI void wait_until(unsigned t) {
#pragma unroll 1
  for (int sp = 0; sp < SPIN_H; ++sp) if ((int)(now32() - t) >= 0) break;
}
DI int arena(int wid) { return WS_ARENA + wid * ASTR; }

DI unsigned tr_addr(unsigned region, int lane, int col4, int t) {
  const int g = lane >> 4, q = (lane & 15) >> 2;
  return region + (8 * g + 4 * t + q) * 256 + 16 * ((col4 >> 3) ^ ((q << 2) | ((2 * g + t) & 3))) + 8 * ((col4 >> 2) & 1);
}
DI v4u tr_frag(unsigned a0, unsigned a1, int ks) {
  const v2u lo = ldstr(a0, ks * 8192), hi = ldstr(a1, ks * 8192);
  return v4u{lo.x, lo.y, hi.x, hi.y};
}
DI void panel_frags(unsigned region, int lane, int col_a, int col_b, v4u (&f)[2][4]) {
  const unsigned a0 = tr_addr(region, lane, col_a, 0), a1 = tr_addr(region, lane, col_a, 1);
  const unsigned b0 = tr_addr(region, lane, col_b, 0), b1 = tr_addr(region, lane, col_b, 1);
#pragma unroll
  for (int ks = 0; ks < 4; ++ks) { f[0][ks] = tr_frag(a0, a1, ks); f[1][ks] = tr_frag(b0, b1, ks); }
}
DI void gemv2(const v4u (&f)[2][4], unsigned act, f32x4& d0, f32x4& d1) {
  v4u bf[4];
#pragma unroll
  for (int ks = 0; ks < 4; ++ks) bf[ks] = lds128(act, ks * 64);
  __builtin_amdgcn_sched_barrier(0);
#pragma unroll
  for (int ks = 0; ks < 4; ++ks) { d0 = mfma16(f[0][ks], bf[ks], d0); d1 = mfma16(f[1][ks], bf[ks], d1); }
}
DI f32x4 up4(v2u v) { return f32x4{bflo(v.x), bfhi(v.x), bflo(v.y), bfhi(v.y)}; }
DI v4u pk8(f32x4 a, f32x4 b) { return v4u{pk(a.x, a.y), pk(a.z, a.w), pk(b.x, b.y), pk(b.z, b.w)}; }
DI v4u silu8(v4u cv) {
  v4u o;
#pragma unroll
  for (int e = 0; e < 4; ++e) {
    const float a = bflo(cv[e]), c = bfhi(cv[e]);
    o[e] = pk(a * __builtin_amdgcn_rcpf(1.f + __expf(-a)), c * __builtin_amdgcn_rcpf(1.f + __expf(-c)));
  }
  return o;
}

struct Params {
  const u16 *x, *ckv, *wq, *wuk, *wuv, *wo, *w1, *w2, *c, *wmod;
  u16* out;
  unsigned char* ws;
};

constexpr unsigned W0 = 0, W1 = 32768, W2 = 65536, L_SC = 98304  ,
                   L_A1 = L_SC + 4224  , L_A2 = L_A1 + 2176, L_A3 = L_A2 + 2176, L_A4 = L_A3 + 2176, L_CNT = L_A4 + 2176;
constexpr unsigned T_SP = 131072, T_P = T_SP + 16384, T_FAC = T_P + 8192, T_CNT = T_FAC + 1024,
                   L_A5 = 98304  , L_A6 = L_A5 + 8320  ;
constexpr unsigned C_LAND = T_CNT + 32, C_FREE = C_LAND + 4, C_SRDY = C_LAND + 8, C_PRDY = C_LAND + 12,
                   C_QST = T_CNT + 192, C_QRD = T_CNT + 196;
constexpr unsigned M_RAW = T_SP  , M_SC = T_P, M_LNX = M_SC + 1152;
constexpr unsigned L_JUNK = LDS_BYTES - 256;

DI void gather4_act(rsrc_t rs, int off0, int str, unsigned dst, int& rounds) {
  f32x4 s;
#pragma unroll 1
  for (int sp = 0; sp < SPIN_G; ++sp) {
    v4u r[4];
    load_n<4>(rs, off0, str, r);
    s = (u2f(r[0]) + u2f(r[1])) + (u2f(r[2]) + u2f(r[3]));
    rounds = sp + 1;
    if (!__any(nan4(s)) || !any_sent<4>(r)) break;
  }
  *(LAS v2u*)dst = v2u{pk(s.x, s.y), pk(s.z, s.w)};
}
template <int N> DI f32x4 reduce_rows(rsrc_t rs, int off0, int offl, v4u sel, int& rounds) {
  f32x4 a;
#pragma unroll 1
  for (int sp = 0; sp < SPIN_G; ++sp) {
    v4u d[N];
    FENCE();
#pragma unroll
    for (int i = 0; i < N; ++i) d[i] = ldg(rs, i == N - 1 ? offl : off0, i * 16 * ASTR);
    __builtin_amdgcn_sched_barrier(0);
    f32x4 b = {0.f, 0.f, 0.f, 0.f};
    a = b;
#pragma unroll
    for (int i = 0; i < N; i += 2) { a = mfma16(sel, d[i], a); b = mfma16(sel, d[i + 1], b); }
    a += b;
    rounds = sp + 1;
    if (!__any(a.x != a.x) || !any_sent<N>(d)) break;
  }
#pragma unroll
  for (int r = 0; r < 4; ++r) { a[r] += dppf<0x128>(a[r]); a[r] += dppf<0x124>(a[r]); }
  return a;
}
DI v4u pick(bool lo, v4u a, v4u b) { return v4u{lo ? a.x : b.x, lo ? a.y : b.y, lo ? a.z : b.z, lo ? a.w : b.w}; }
DI void st32(rsrc_t rs, int off, unsigned v) { __builtin_amdgcn_raw_buffer_store_b32(v, rs, off, 0, SC1); }

DI void weight_wg(const Params& p, int g, int wi, int wave, int lane, unsigned t0) {
  unsigned char* ws = p.ws;
  const rsrc_t rs = __builtin_amdgcn_make_buffer_rsrc(ws, 0, WS_BYTES, 0x00020000);
  const int h = 4 * g + (wi >> 2), j = wi & 3, wid = 16 * g + wi, my = arena(wid);
  const int l15 = lane & 15, lq = lane >> 4, b8 = lane & 7;
  const bool bvalid = l15 < 8;
  const int w = wave & 3;
  const int ch = l15 ^ ((lq << 2) | w);
  const unsigned dst0 = w * 1024;
  const unsigned vuv = (128 * j + 16 * ((4 * w + lq) & 7) + (w >> 1)) * 8192 + h * 256 + ch * 16;
  const f32x4 z4 = {0.f, 0.f, 0.f, 0.f};

  if (wave >= 8) {
    const unsigned vq = (128 * j + 4 * w + lq) * 8192 + h * 256 + ch * 16;
    wait_until(t0 + T_W);
    dma_run<true>(p.wq, vq, W0 + dst0, 8, 16 * 8192, 4096);
    dma_run<true>(p.wuk, vq, W1 + dst0, 8, 16 * 8192, 4096);
    const unsigned key = (unsigned)get_tag(ws);
    VMCNT(0);
    STAMP(44);
    int hp = lane < 8 ? WS_HP + (4 * h + (lane & 3)) * 128 + j * 4 + (lane >> 2) * 16 : OOB;
    int hq = lane < 32 ? WS_HQ + (4 * lane + j) * 512 + (h & 3) * 128 + g * 4 : OOB;
    pin(hp); pin(hq);
    bar();
    if (w == 0) { const unsigned t = now32(); nap<2>(); st32(rs, hp, t + key); }
    bar();
    if (w == 0) { const unsigned tq = latest_time(ws + WS_HP + wid * 128, t0, key) + T_QP; st32(rs, hq, tq + key); wait_until(tq); }
    bar();
    const unsigned tl = now32() + T_L;
    bar();
    v4u wo[4][8];
    const GAS unsigned char* wb = (const GAS unsigned char*)p.wo + (h * 128 + 8 * lq) * 1024 + w * 256 + (4 * (l15 & 3) + (l15 >> 2)) * 16;
    bar();
    wait_until(tl);
#pragma unroll
    for (int ks = 0; ks < 4; ++ks) {
      const GAS unsigned char* wk = wb + ((ks + j) & 3) * 32768;
#pragma unroll
      for (int r = 0; r < 8; ++r)
        wo[ks][r] = (WO_NT && ks == 0) ? __builtin_nontemporal_load((const GAS v4u*)(wk + r * 1024)) : *(const GAS v4u*)(wk + r * 1024);
      if (ks == 0) { VMCNT(0); bar(); }
    }
    __builtin_amdgcn_sched_barrier(0);
#pragma unroll
    for (int ks = 0; ks < 4; ++ks) {
      v4u o[8];
#pragma unroll
      for (int dd = 0; dd < 4; ++dd)
#pragma unroll
        for (int rp = 0; rp < 4; ++rp) {
          o[2 * dd][rp] = __builtin_amdgcn_perm(wo[ks][2 * rp + 1][dd], wo[ks][2 * rp][dd], 0x05040100u);
          o[2 * dd + 1][rp] = __builtin_amdgcn_perm(wo[ks][2 * rp + 1][dd], wo[ks][2 * rp][dd], 0x07060302u);
        }
#pragma unroll
      for (int t = 0; t < 8; ++t) { wo[ks][t] = o[t]; pin(wo[ks][t]); }
    }
    STAMP(10);
    unsigned ba[4];
#pragma unroll
    for (int ks = 0; ks < 4; ++ks) { ba[ks] = L_A4 + b8 * 272 + ((ks + j) & 3) * 64 + lq * 16; pin(ba[ks]); }
    int oo = my + A_OUTP + b8 * 1024 + w * 256 + (l15 >> 3) * 64 + lq * 16;
    pin(oo);
    __builtin_amdgcn_s_setprio(2);
    bar();
    bar();
    v4u bf[4];
#pragma unroll
    for (int ks = 0; ks < 4; ++ks) bf[ks] = lds128(ba[ks]);
    v4u r0, r1;
#pragma unroll
    for (int tp = 0; tp < 4; ++tp) {
      f32x4 e = z4, o = z4;
#pragma unroll
      for (int ks = 0; ks < 4; ++ks) { e = mfma16(wo[ks][2 * tp], bf[ks], e); o = mfma16(wo[ks][2 * tp + 1], bf[ks], o); }
      r0[tp] = bvalid ? pk(e[0], o[0]) : pk(e[1], o[1]);
      r1[tp] = bvalid ? pk(e[2], o[2]) : pk(e[3], o[3]);
    }
    stg(rs, oo, r0);
    stg(rs, oo + 128, r1);
    STAMP(14);
    if (wave == 8) { DUMP(10); DUMP(14); DUMP(44); }
  } else if (wave >= 4) {
    __builtin_amdgcn_s_setprio(2);
    const int t = w * 64 + lane;
    const u64 tag = get_tag(ws);
    v4u msk;
    {
      const unsigned half = (lane & 1) ? 0xFFFF0000u : 0x0000FFFFu;
      const int dw = b8 >> 1;
      msk = v4u{dw == 0 ? half : 0u, dw == 1 ? half : 0u, dw == 2 ? half : 0u, dw == 3 ? half : 0u};
    }
    v4u sela = msk & (bvalid ? 0x3F803F80u : 0u), selb = msk & (bvalid ? 0u : 0x3F803F80u);
    int ho = arena(32 * j + 16 * (w >> 1) + 2 * lq) + A_H + (w & 1) * 512 + l15 * 16, qo = arena(4 * h) + A_QP + t * 16;
    unsigned hd = L_A1 + (l15 >> 1) * 272 + (32 * w + 16 * (lq >> 1) + 8 * (lane & 1) + 4 * (lq & 1)) * 2;
    unsigned ad = L_A2 + ((t >> 2) & 7) * 272 + (16 * (t >> 5) + 4 * (t & 3)) * 2;
    pin(msk); pin(sela); pin(selb); pin(ho); pin(qo); pin(hd); pin(ad);
    STAMPD(34); STAMPD(46);
    if (w == 2) {
      const unsigned char* fl = ws + WS_EFL + wid * 256;
#pragma unroll 1
      for (int sp = 0; sp < SPIN_H; ++sp) {
        s8u a, b, c, d, e;
        asm volatile("s_load_dwordx8 %0, %5, 0x0 glc\n\ts_load_dwordx8 %1, %5, 0x20 glc\n\ts_load_dwordx8 %2, %5, 0x80 glc\n\t"
                     "s_load_dwordx8 %3, %5, 0xa0 glc\n\ts_load_dwordx8 %4, %5, 0x40 glc\n\ts_waitcnt lgkmcnt(0)"
                     : "=&s"(a), "=&s"(b), "=&s"(c), "=&s"(d), "=&s"(e) : "s"(fl) : "memory");
        const s8u x = (a ^ (unsigned)tag) | (b ^ (unsigned)tag) | (c ^ (unsigned)tag) | (d ^ (unsigned)tag) | (e ^ (unsigned)tag);
        if ((x[0] | x[2] | x[4] | x[6]) == 0) break;
        nap<2>();
      }
      wait_flags(rs, WS_EFL + wid * 256 + (lane < 6 ? lane * 16 : 128 + (lane & 3) * 16), tag);
      STAMPV(46);
    }
    if (w == 3) {
      const unsigned char* hl = ws + WS_HH + wid * 512;
      const unsigned kb = (unsigned)tag + t0 - HWIN;
      unsigned m = 0;
#pragma unroll 1
      for (int sp = 0; sp < SPIN_H; ++sp) {
        v4u a, b, c, d;
        asm volatile("s_load_dwordx4 %0, %4, 0x0 glc\n\ts_load_dwordx4 %1, %4, 0x80 glc\n\ts_load_dwordx4 %2, %4, 0x100 glc\n\t"
                     "s_load_dwordx4 %3, %4, 0x180 glc\n\ts_waitcnt lgkmcnt(0)" : "=&s"(a), "=&s"(b), "=&s"(c), "=&s"(d) : "s"(hl) : "memory");
        m = max(max(umax4(a - kb), umax4(b - kb)), max(umax4(c - kb), umax4(d - kb)));
        if (m < 2 * HWIN) break;
      }
      STAMPV(34);
      wait_until(m + t0 - HWIN + T_H);
    }
    bar();
    int rh = 0, rq = 0;
    {
      f32x4 hv;
#pragma unroll 1
      for (int sp = 0; sp < SPIN_G; ++sp) {
        v4u r[4];
        FENCE();
#pragma unroll
        for (int i = 0; i < 4; ++i) r[i] = ldg(rs, ho + (i & 1) * 256, (i >> 1) * 8 * ASTR);
        __builtin_amdgcn_sched_barrier(0);
        hv = mfma16(selb, r[3], mfma16(sela, r[2], mfma16(selb, r[1], mfma16(sela, r[0], z4))));
        rh = sp + 1;
        if (!__any(hv.x != hv.x) || !any_sent<4>(r)) break;
      }
      *(LAS v2u*)hd = v2u{pk(hv.x, hv.y), pk(hv.z, hv.w)};
    }
    STAMP(5);
    bar();
    bar();
    gather4_act(rs, qo, ASTR, ad, rq);
    STAMP(7);
    bar();
    bar();
    if (j == 1 || j == 2)
      *(LAS v4u*)(L_SC + (2 * w + (lane >> 5)) * 528 + (lane & 31) * 16) =
          silu8(*(const GAS v4u*)(p.c + (2 * w + (lane >> 5)) * 512 + (j - 1) * 256 + (lane & 31) * 8));
    if (w == 3) {
      nap<D_FILL>();
#pragma unroll 1
      for (int n = wid < 8 ? 9 : 10; n < (wid < 56 ? 26 : 18); ++n)
        stg(rs, my + lane * 16 + n * 1024, v4u{SENT, SENT, SENT, SENT});
      VMCNT(0);
      if (lane == 0) __hip_atomic_store((GAS u64*)(ws + WS_FLAG2 + wid * 8), tag, RLX_AGENT);
    }
    bar();
    if (w == 0) wait_flags(rs, WS_FLAG2 + lane * 16, tag);
    int mo = arena(32 * w + (lane & 31)) + A_ML + h * 16;
    int po = my + A_PART + w * 8192 + lane * 16;
    unsigned o3 = L_A3 + (2 * w + (lane >> 5)) * 272 + l15 * 16 + (lq & 1) * 8;
    int hb = lane < 16 ? WS_HB + (16 * lane + ((wi - 5 * g) & 15)) * 128 + g * 4 : OOB;
    int co = lane < 32 ? mo + (A_CK - A_ML) + j * 512 : OOB;
    pin(mo); pin(po); pin(o3); pin(hb); pin(co);
    wait_hints(ws + WS_ARR + (4 * h + w) * 128, (unsigned)(tag & 255) * 0x01010101u, 0);
    STAMP(11);
    nap<D_ML>();
    FENCE();
    v4u ml = ldg(rs, mo);
    nap<D_PART>();
    v4u d[8], ck;
    load_n<8>(rs, po, 1024, d);
    ck = ldg(rs, co);
    __builtin_amdgcn_sched_barrier(0);
    if (__builtin_expect(!__all(ml.y == (unsigned)tag && ml.w == (unsigned)(tag >> 32)), 0)) {
#pragma unroll 1
      for (int sp = 0; sp < SPIN_G; ++sp) {
        FENCE();
        ml = ldg(rs, mo);
        if (__all(ml.y == (unsigned)tag && ml.w == (unsigned)(tag >> 32))) break;
      }
    }
    v4u sel[8];
    {
      const float r = __uint_as_float(ml.x), e = __builtin_amdgcn_exp2f(r - row16_max(r));
      const float wgt = e * __builtin_amdgcn_rcpf(row16_sum(e * __uint_as_float(ml.z)));
#pragma unroll
      for (int i = 0; i < 8; ++i) {
        const float wv = __shfl(wgt, 4 * i + lq);
        const unsigned hi = pk(wv, wv);
        const float rest = wv - bflo(hi);
        const unsigned v = bvalid ? hi : pk(rest, rest);
        sel[i] = msk & v;
        pin(sel[i]);
      }
    }
    f32x4 oa, ob;
    int rp = 0;
#pragma unroll 1
    for (int sp = 0; sp < SPIN_G; ++sp) {
      oa = z4; ob = z4;
#pragma unroll
      for (int i = 0; i < 4; ++i) { oa = mfma16(sel[i], d[i], oa); ob = mfma16(sel[4 + i], d[4 + i], ob); }
      rp = sp + 1;
      unsigned xe = 0, xo = 0;
#pragma unroll
      for (int i = 0; i < 8; ++i) { xe ^= d[i].x ^ d[i].z; xo ^= d[i].y ^ d[i].w; }
      unsigned x = xor_rows2(xe ^ ck.x, xo ^ ck.z);
      auto y = __builtin_amdgcn_permlane16_swap(x, x, false, false);
      x = y[0] ^ y[1];
      y = __builtin_amdgcn_permlane32_swap(x, x, false, false);
      if (__all(y[0] == y[1] && (lane >= 32 || (ck.y == (unsigned)tag && ck.w == (unsigned)(tag >> 32))))) break;
      load_n<8>(rs, po, 1024, d);
      ck = ldg(rs, co);
    }
    STAMP(12);
    {
      f32x4 s;
#pragma unroll
      for (int r = 0; r < 4; ++r) {
        auto x = __builtin_amdgcn_permlane32_swap(__float_as_uint(oa[r]), __float_as_uint(ob[r]), false, false);
        s[r] = __uint_as_float(x[0]) + __uint_as_float(x[1]);
      }
      *(LAS v2u*)o3 = v2u{pk(s.x, s.y), pk(s.z, s.w)};
    }
    bar();
    if (w == 1) st32(rs, hb, now32() + (unsigned)tag);
    bar();
    if (w == 2) DUMP(46);
    if (w == 3) { DUMP(5); DUMP(7); DUMP(34); DUMP(11); DUMP(12); DUMPC(41, rp); DUMPC(48, rh); DUMPC(49, rq); }
    if (w == 0) {
      const int col = 8 * (lane & 3) + 4 * (lq & 1);
      const v2u xb = *(const GAS v2u*)(p.x + g * 512 + 32 * wi + col);
      v2u gg[4];
      const int go = arena(4 * (wi >> 1) + 1) + A_HM + g * 128 + ((wi & 1) * 32 + col) * 2;
#pragma unroll 1
      for (int sp = 0; sp < SPIN_G; ++sp) {
        FENCE();
        unsigned m = 0;
#pragma unroll
        for (int i = 0; i < 4; ++i) gg[i] = __builtin_amdgcn_raw_buffer_load_b64(rs, go, ((i >> 1) * 96 + (i & 1)) * ASTR, SC1);
#pragma unroll
        for (int i = 0; i < 4; ++i) m = max(m, max(gg[i].x, gg[i].y));
        if (!__any(m == SENT)) break;
      }
      f32x4 x0 = up4(xb), ga = up4(gg[0]) + up4(gg[1]), gb = up4(gg[2]) + up4(gg[3]);
      pin(x0); pin(ga); pin(gb);
      STAMP(26);
      const bool act = lane < 32 && l15 < 4;
      v4u one = sela;
      int oo = arena(lane >> 2) + A_OUTP + g * 1024 + wi * 64 + (lane & 3) * 16;
      int yo = oo + (A_YP - A_OUTP);
      int yl = lane < 32 ? yo : OOB - 48 * ASTR;
      int xo = act ? arena(g) + A_X1 + (32 * wi + col) * 2 : OOB;
      const rsrc_t ro = __builtin_amdgcn_make_buffer_rsrc(p.out, 0, 8192, 0x00020000);
      int fo = act ? (g * 512 + 32 * wi + col) * 2 : OOB;
      pin(one); pin(oo); pin(yo); pin(yl); pin(xo); pin(fo);
      const unsigned tb = latest_time(ws + WS_HB + (128 + wid) * 128, t0, (unsigned)tag);
      wait_until(tb + T_B);
      int r1 = 0, r2 = 0;
      const f32x4 x1 = x0 + ga * reduce_rows<8>(rs, oo, oo, one, r1);
      __builtin_amdgcn_raw_buffer_store_b64(v2u{pk(x1.x, x1.y), pk(x1.z, x1.w)}, rs, xo, 0, SC1);
      STAMP(27);
      wait_until(tb + T_D);
      const f32x4 r = x1 + gb * reduce_rows<4>(rs, yo, yl, one, r2);
      STAMP(29);
      __builtin_amdgcn_raw_buffer_store_b64(v2u{pk(r.x, r.y), pk(r.z, r.w)}, ro, fo, 0, 0);
      if (wid == 0 && lane == 0) __hip_atomic_store((GAS u64*)ws, tag, RLX_AGENT);
      STAMP(30);
      DUMP(26); DUMP(27); DUMP(29); DUMP(30); DUMPC(42, r1); DUMPC(43, r2);
    }
  } else {
    __builtin_amdgcn_s_setprio(2);
    const int p4 = 4 * (lane & 3);
    unsigned act = b8 * 272 + lq * 16;
    unsigned s0 = tr_addr(W2, lane, 16 * w + p4, 0), s1 = tr_addr(W2, lane, 16 * w + p4, 1);
    unsigned ba = L_SC + b8 * 528 + lq * 16;
    v4u f[2][4];
    STAMPD(1);
    if (w == 3) {
      const v4u sv = {SENT, SENT, SENT, SENT};
#pragma unroll 1
      for (int n = 2; n < 9; ++n) stg(rs, my + lane * 16 + n * 1024, sv);
      const u64 tag = get_tag(ws);
      const int fo = lane < 4 ? WS_EFL + (4 * h + lane) * 256 + 64 + j * 8 : lane == 4 ? WS_FLAG1 + wid * 8 : OOB;
      VMCNT(0);
      __builtin_amdgcn_raw_buffer_store_b64(v2u{(unsigned)tag, (unsigned)(tag >> 32)}, rs, fo, 0, SC1);
      STAMPV(1);
    }
    bar();
    STAMP(4);
    panel_frags(W0, lane, 32 * w + p4, 32 * w + 16 + p4, f);
    {
      int o = my + A_QP + w * 1024 + l15 * 64 + lq * 16;
      pin(o); pin(act);
      bar();
      f32x4 q0 = z4, q1 = z4;
      gemv2(f, L_A1 + act, q0, q1);
      stg(rs, o, pick(bvalid, f2u(q0), f2u(q1)));
    }
    STAMP(6);
    unsigned tl;
    {
      unsigned ka[2];
#pragma unroll
      for (int tt = 0; tt < 2; ++tt) {
        ka[tt] = W1 + (32 * w + 8 * (l15 >> 2) + 4 * tt + (l15 & 3)) * 256 + 16 * (lq ^ (((l15 & 3) << 2) | ((2 * (l15 >> 2) + tt) & 3)));
        pin(ka[tt]);
      }
      int o = bvalid ? my + A_QA + l15 * 256 + w * 64 + lq * 16 : OOB;
      pin(o);
#pragma unroll
      for (int tt = 0; tt < 2; ++tt)
#pragma unroll
        for (int ks = 0; ks < 4; ++ks) f[tt][ks] = lds128(ka[tt] ^ (ks * 64));
      bar();
      tl = now32() + T_L;
      bar();
      f32x4 a0 = z4, a1 = z4;
      gemv2(f, L_A2 + act, a0, a1);
      stg(rs, o, pk8(a0 * QSCALE, a1 * QSCALE));
    }
    STAMP(8);
    bar();
    wait_until(tl);
    dma_run<true>(p.wuv, vuv, W0 + dst0, 8, 2 * 8192, 4096);
    const bool m2 = j == 1 || j == 2;
    if (m2) dma_run<true>(p.wmod, (256 * (j - 1) + 4 * w + lq + (ch >> 3) * 128) * 6144 + 2048 + h * 128 + (ch & 7) * 16, W2 + dst0, 8, 16 * 6144, 4096);
    VMCNT(0);
    bar();
    STAMP(9);
    if (m2) {
      f32x4 m0 = z4, m1 = z4;
#pragma unroll 1
      for (int ks = 0; ks < 4; ++ks) {
        m0 = mfma16(tr_frag(s0 + ks * 8192, s1 + ks * 8192, 0), lds128(ba + ks * 64), m0);
        m1 = mfma16(tr_frag((s0 ^ 128) + ks * 8192, (s1 ^ 128) + ks * 8192, 0), lds128(ba + ks * 64, 256), m1);
      }
      m0 += m1;
      __builtin_amdgcn_raw_buffer_store_b64(v2u{pk(m0.x, m0.y), pk(m0.z, m0.w)}, rs, bvalid ? my + A_HM + l15 * 128 + w * 32 + lq * 8 : OOB, 0, SC1);
    }
    panel_frags(W0, lane, 32 * w + 2 * p4, 32 * w + 2 * p4 + 4, f);
    unsigned o = bvalid ? L_A4 + l15 * 272 + w * 64 + lq * 16 : L_JUNK;
    pin(o);
    bar();
    STAMP(35);
    f32x4 c0 = z4, c1 = z4;
    gemv2(f, L_A3 + act, c0, c1);
    *(LAS v4u*)o = pk8(c0, c1);
    bar();
    STAMP(13);
    if (w == 3) DUMP(1);
    if (w == 0) { DUMP(35); DUMP(13); DUMP(4); DUMP(6); DUMP(8); DUMP(9); }
  }
}

DI void mfma_pair(f32x4& d0, f32x4& d1, v4u a0, v4u a1, v4u b) {
  asm volatile("v_mfma_f32_16x16x32_bf16 %0, %2, %4, %0\n\tv_mfma_f32_16x16x32_bf16 %1, %3, %4, %1" : "+v"(d0), "+v"(d1) : "v"(a0), "v"(a1), "v"(b));
}
DI void mfma_pair0(f32x4& d0, f32x4& d1, v4u a0, v4u a1, v4u b) {
  asm volatile("v_mfma_f32_16x16x32_bf16 %0, %2, %4, 0\n\tv_mfma_f32_16x16x32_bf16 %1, %3, %4, 0" : "=&v"(d0), "=&v"(d1) : "v"(a0), "v"(a1), "v"(b));
}
template <int OFF> DI void rd_inplace(v4u& a, unsigned ad) { asm volatile("ds_read_b128 %0, %1 offset:%2" : "+v"(a) : "v"(ad), "i"(OFF) : "memory"); }
template <int O0, int O1> DI void peek2(unsigned& a, unsigned& b, unsigned cb) {
  asm volatile("ds_read_b32 %0, %2 offset:%3\n\tds_read_b32 %1, %2 offset:%4" : "=&v"(a), "=&v"(b) : "v"(cb), "i"(O0), "i"(O1) : "memory");
}
template <int W> DI void wait2(unsigned& a, unsigned& b) { asm volatile("s_waitcnt lgkmcnt(%2)" : "+v"(a), "+v"(b) : "i"(W)); }
template <int W, bool FIRST> DI void score_pair(f32x4& s0, f32x4& s1, v4u a, v4u q0, v4u q1) {
  if constexpr (FIRST)
    asm volatile("s_waitcnt lgkmcnt(%5)\n\tv_mfma_f32_16x16x32_bf16 %0, %2, %3, 0\n\tv_mfma_f32_16x16x32_bf16 %1, %2, %4, 0"
                 : "=&v"(s0), "=&v"(s1) : "v"(a), "v"(q0), "v"(q1), "i"(W));
  else
    asm volatile("s_waitcnt lgkmcnt(%5)\n\tv_mfma_f32_16x16x32_bf16 %0, %2, %3, %0\n\tv_mfma_f32_16x16x32_bf16 %1, %2, %4, %1"
                 : "+v"(s0), "+v"(s1) : "v"(a), "v"(q0), "v"(q1), "i"(W));
}
template <int OFF> DI void put_scores(unsigned sa, f32x4 s0, f32x4 s1) {
  asm volatile("s_nop 4\n\tds_write_b128 %0, %1 offset:%3\n\tds_write_b128 %0, %2 offset:%4" :: "v"(sa), "v"(s0), "v"(s1), "i"(OFF), "i"(OFF + 1024) : "memory");
}
template <int V> struct IntC { static constexpr int value = V; };
template <int I, int N, class F> DI void static_for(F&& f) { if constexpr (I < N) { f(IntC<I>{}); static_for<I + 1, N>(f); } }
constexpr unsigned slot_c(int t) { return ((0x02310210u >> ((4 * t) & 31)) & 3) * 32768; }
template <unsigned A> DI void csig(unsigned cb, unsigned v) {
  asm volatile("s_mov_b64 exec, 1\n\tds_add_u32 %0, %1 offset:%2\n\ts_mov_b64 exec, -1" :: "v"(cb), "v"(v), "i"(A - T_CNT) : "memory");
}
template <unsigned A> DI unsigned cpeek(unsigned cb) { return *(volatile LAS unsigned*)((LAS char*)cb + (A - T_CNT)); }
#define SPIN_HEAD "s_mov_b32 %[n], 0x800000\n1:\n\ts_sub_u32 %[n], %[n], 1\n\ts_cbranch_scc1 2f\n\t"
#define SPIN_TAIL "s_waitcnt lgkmcnt(0)\n\tv_cmp_gt_u32 vcc, %[w], %[c]\n\ts_cbranch_vccnz 1b\n2:"
template <unsigned A, unsigned W> DI void cwait(unsigned cb) {
  unsigned c, n;
  asm volatile(SPIN_HEAD "ds_read_b32 %[c], %[cb] offset:%[o]\n\t" SPIN_TAIL
               : [c] "=&v"(c), [n] "=&s"(n) : [cb] "v"(cb), [o] "i"(A - T_CNT), [w] "i"(W) : "vcc", "scc", "memory");
}
DI constexpr unsigned fsw(int row) { return ((row & 3) << 2) | ((row >> 1) & 2) | ((row >> 3) & 1); }
DI unsigned slot_of(int t) { return ((0x02310210u >> ((4 * t) & 31)) & 3) * 32768; }
#define DMA4(AUX, base, dst, v0, v1, v2, v3) \
  asm volatile("s_mov_b32 m0, %5\n\ts_waitcnt vmcnt(31)\n\tglobal_load_lds_dwordx4 %0, %4 " AUX "\n\t" \
               "s_waitcnt vmcnt(31)\n\tglobal_load_lds_dwordx4 %1, %4 offset:1024 " AUX "\n\t" \
               "s_waitcnt vmcnt(31)\n\tglobal_load_lds_dwordx4 %2, %4 offset:2048 " AUX "\n\t" \
               "s_waitcnt vmcnt(31)\n\tglobal_load_lds_dwordx4 %3, %4 offset:3072 " AUX \
               :: "v"(v0), "v"(v1), "v"(v2), "v"(v3), "s"(base), "s"(dst) : "memory")

DI void ffn_part(const Params& p, int b, int s, int wave, unsigned t0, u64 tag);
DI void attn_wg(const Params& p, int b, int s, int wave, int lane, unsigned t0) {
  unsigned char* ws = p.ws;
  const rsrc_t rs = __builtin_amdgcn_make_buffer_rsrc(ws, 0, WS_BYTES, 0x00020000);
  const int l15 = lane & 15, lq = lane >> 4, w = wave & 3;
  const f32x4 z4 = {0.f, 0.f, 0.f, 0.f};

  if (wave == 0) *(LAS unsigned*)(T_CNT + lane * 4) = lane < 8 ? 8u : 0u;
  u64 tag = 0;
  if (wave >= 4 && wave != 8) tag = get_tag(ws);
  bar();
  STAMP(59);
  if (wave == 8) {
    unsigned vo[16];
#pragma unroll
    for (int r = 0; r < 16; ++r) vo[r] = 16 * (lane ^ fsw(r));
    const unsigned char* kv = (const unsigned char*)p.ckv + (size_t)(b * 4096 + s * 32) * 1024;
    nap<1>();
    wait_until(t0 + T_PREF);
    STAMP(33); STAMPD(31);
#pragma unroll 1
    for (int t = 0; t < 8; ++t) {
      bool told = false;
      if (t >= 3) {
        const unsigned c = t == 5 ? C_QRD : C_FREE + 16 * (t < 5 ? t - 3 : t - 4), want = t == 5 ? 4 : 8;
        if (__builtin_amdgcn_readfirstlane(lpeek(c)) < want) { VMCNT(0); lsig(C_LAND + 16 * t - 16); told = true; lwait<true>(c, want); }
      }
      const unsigned dst = slot_of(t);
#pragma unroll
      for (int i = 0; i < 8; ++i)
        DMA4("nt", kv + i * 4096, dst + i * 4096, vo[4 * (i & 3)], vo[4 * (i & 3) + 1], vo[4 * (i & 3) + 2], vo[4 * (i & 3) + 3]);
      if (!told) lsig(C_LAND + 16 * t - 16);
      kv += 16 * 32768;
    }
    STAMP(22);

    VMCNT(0);
    lsig(C_LAND + 16 * 7);
    STAMP(23);
    if (s >= 8 || b == 7) { DUMP(33); DUMP(31); DUMP(22); DUMP(23); }
    return;
  }
  __builtin_amdgcn_s_setprio(2);
  if (wave == 9) {
    unsigned vo[16];
#pragma unroll
    for (int r = 0; r < 16; ++r) { vo[r] = lq * ASTR + (r & 3) * (4 * ASTR - 1024) + 16 * (l15 ^ fsw(r)); pin(vo[r]); }
    const unsigned char* qa = ws + WS_ARENA + A_QA + b * 256;
    wait_until(t0 + T_FLAG);
    wait_flags(rs, WS_FLAG1 + lane * 16, tag);
    STAMP(17); STAMPD(47);
    {
      const unsigned char* hl = ws + WS_HQ + (16 * b + s) * 512;
      const unsigned kb = (unsigned)tag + t0 - HWIN;
      unsigned m = 0;
#pragma unroll 1
      for (int sp = 0; sp < SPIN_H; ++sp) {
        s8u a, c, d, e;
        asm volatile("s_load_dwordx8 %0, %4, 0x0 glc\n\ts_load_dwordx8 %1, %4, 0x80 glc\n\ts_load_dwordx8 %2, %4, 0x100 glc\n\t"
                     "s_load_dwordx8 %3, %4, 0x180 glc\n\ts_waitcnt lgkmcnt(0)" : "=&s"(a), "=&s"(c), "=&s"(d), "=&s"(e) : "s"(hl) : "memory");
        m = max(max(max8(a - kb), max8(c - kb)), max(max8(d - kb), max8(e - kb)));
        if (m < 2 * HWIN) break;
      }
      STAMPV(47);
      wait_until(m + t0 - HWIN + T_QA);
    }
    STAMP(18);
#pragma unroll
    for (int i = 0; i < 8; ++i)
      DMA4("sc1", qa + i * (16 * ASTR), 3 * 32768 + i * 4096, vo[4 * (i & 3)], vo[4 * (i & 3) + 1], vo[4 * (i & 3) + 2], vo[4 * (i & 3) + 3]);
    VMCNT(0);
    lsig(C_QST);
    STAMP(38);

    if (s >= 8 || b == 7) { lwait<true>(C_PRDY + 16 * 7, 8); nap<30>(); DUMP(17); DUMP(18); DUMP(38); DUMP(47); DUMP(59); }
    return;
  }
  if (wave >= 10) {
    __builtin_amdgcn_s_setprio(3);
    const int hh = wave - 10;
    unsigned sa = T_SP + hh * 1024 + lane * 16, cb = T_CNT, four = 4u;
    int mlo = lq == 0 ? arena(16 * b + s) + A_ML + hh * 256 + l15 * 16 : OOB;
    int aro = hh == 0 && lane < 32 ? WS_ARR + (4 * lane + (b >> 1)) * 128 + (b & 1) * 16 + s : OOB;
    unsigned tg = (unsigned)tag;
    pin(sa); pin(cb); pin(four); pin(mlo); pin(aro); pin(tg);
    float ref = 0.f, l = 0.f;
    lwait<true>(C_QST, 1);
    static_for<0, 8>([&](auto tc) {
      constexpr int t = decltype(tc)::value;
      if constexpr (t >= 4) cwait<C_FREE + 16 * (t - 2), 8>(cb);
      f32x4 p0, p1, p2, p3;
      unsigned c, n;
      asm volatile(SPIN_HEAD "ds_read_b32 %[c], %[cb] offset:%[o]\n\tds_read_b128 %[p0], %[sa] offset:%[s0]\n\tds_read_b128 %[p2], %[sa] offset:%[s2]\n\t"
                   "ds_read_b128 %[p1], %[sa] offset:%[s1]\n\tds_read_b128 %[p3], %[sa] offset:%[s3]\n\t" SPIN_TAIL
                   : [c] "=&v"(c), [n] "=&s"(n), [p0] "=&v"(p0), [p1] "=&v"(p1), [p2] "=&v"(p2), [p3] "=&v"(p3)
                   : [cb] "v"(cb), [sa] "v"(sa), [o] "i"(C_SRDY + 16 * t - T_CNT), [w] "i"(4), [s0] "i"((t & 1) * 8192),
                     [s1] "i"((t & 1) * 8192 + 2048), [s2] "i"((t & 1) * 8192 + 4096), [s3] "i"((t & 1) * 8192 + 6144) : "vcc", "scc", "memory");
      if constexpr (t == 7) __builtin_amdgcn_raw_buffer_store_b8((unsigned char)tg, rs, aro, 0, SC1);
      f32x4 a = (p0 - ref) + p2, c4 = (p1 - ref) + p3;
      const float m = fmaxf(fmaxf(fmaxf(a.x, a.y), fmaxf(a.z, a.w)), fmaxf(fmaxf(c4.x, c4.y), fmaxf(c4.z, c4.w)));
      unsigned sig = four;
      if constexpr (t == 0) {
        ref = xmax32(xmax16(m));
        a -= ref; c4 -= ref;
      } else if (__builtin_expect(__any(m > BUMP), 0)) {
        const float up = fmaxf(0.f, xmax32(xmax16(m)));
        const float fa = __builtin_amdgcn_exp2f(-up);
        *(LAS float*)(T_FAC + t * 128 + hh * 64 + l15 * 4) = fa;
        l *= fa;
        ref += up;
        a = (p0 - ref) + p2; c4 = (p1 - ref) + p3;
        sig = 4u + (1u << hh);
      }
      const v4u pf = {pk(__builtin_amdgcn_exp2f(a.x), __builtin_amdgcn_exp2f(a.y)), pk(__builtin_amdgcn_exp2f(a.z), __builtin_amdgcn_exp2f(a.w)),
                      pk(__builtin_amdgcn_exp2f(c4.x), __builtin_amdgcn_exp2f(c4.y)), pk(__builtin_amdgcn_exp2f(c4.z), __builtin_amdgcn_exp2f(c4.w))};
      *(LAS v4u*)((LAS char*)sa + (T_P - T_SP) + (t & 3) * 2048) = pf;
      csig<C_PRDY + 16 * t>(cb, sig);
#pragma unroll
      for (int e = 0; e < 4; ++e) {
        const unsigned u = pf[e];
        l = __builtin_amdgcn_fdot2_f32_bf16(__builtin_bit_cast(bf16x2, u), __builtin_bit_cast(bf16x2, 0x3F803F80u), l, false);
      }
    });
    stg(rs, mlo, v4u{__float_as_uint(ref), (unsigned)tag, __float_as_uint(xsum32(xsum16(l))), (unsigned)(tag >> 32)});
    STAMP(24);
    if (hh == 0 && (s >= 8 || b == 7)) DUMP(24);
    return;
  }
  if (wave < 4) {
    __builtin_amdgcn_s_setprio(PRIO_S);
    const int kh = wave & 1, ch = wave >> 1;
    const int sg = (0x2130 >> (4 * lq)) & 3;
    unsigned ao[4];
#pragma unroll
    for (int k = 0; k < 4; ++k) ao[k] = l15 * 1024 + ch * 512 + 16 * ((4 * k + sg) ^ fsw(l15));
    unsigned sa = T_SP + wave * 2048 + lane * 16, cb = T_CNT, one = 1u;
    unsigned al[4], ah[4];
#pragma unroll
    for (int k = 0; k < 4; ++k) { al[k] = ao[k] + kh * 16384; ah[k] = al[k] + 65536; pin(al[k]); pin(ah[k]); }
    pin(sa); pin(cb); pin(one);
    v4u af[2][8], qf[2][8];
    auto rd = [&](unsigned sl, int ks) { return lds128(sl < 65536 ? al[ks & 3] : ah[ks & 3], (sl & 32768) + (ks >> 2) * 256); };
#pragma unroll
    for (int t = 0; t < 2; ++t) {
      lwait<true>(C_LAND + 16 * t, 1);
#pragma unroll
      for (int ks = 0; ks < 8; ++ks) af[t][ks] = rd(t * 32768, ks);
      lsig(C_FREE + 16 * t);
    }
    unsigned qa[4];
#pragma unroll
    for (int k = 0; k < 4; ++k) { qa[k] = 3 * 32768 + ao[k]; pin(qa[k]); }
    lwait(C_QST, 1);
    STAMP(19);
    unsigned nx2 = cpeek<C_LAND + 32>(cb), nxt = cpeek<C_LAND + 48>(cb);
    f32x4 s0, s1;
    int rqa = 0;
#pragma unroll 1
    for (int sp = 0; sp < SPIN_G; ++sp) {
      rqa = sp + 1;
#pragma unroll
      for (int ks = 0; ks < 8; ++ks) {
#pragma unroll
        for (int hh = 0; hh < 2; ++hh) qf[hh][ks] = lds128(qa[ks & 3], hh * 16384 + (ks >> 2) * 256);
        __builtin_amdgcn_sched_barrier(0);
      }
      s0 = z4; s1 = z4;
#pragma unroll
      for (int ks = 0; ks < 8; ++ks) { s0 = mfma16(af[0][ks], qf[0][ks], s0); s1 = mfma16(af[0][ks], qf[1][ks], s1); }
      if (__builtin_expect(!__any(__builtin_isunordered(s0.x, s1.x)), 1) || !(any_sent<8>(qf[0]) || any_sent<8>(qf[1]))) break;
#pragma unroll 1
      for (int r = 0; r < 32; ++r)
        asm volatile("s_mov_b32 m0, %2\n\ts_nop 0\n\tglobal_load_lds_dwordx4 %0, %1 sc1"
                     :: "v"(lq * ASTR + 16 * (l15 ^ fsw(r))), "s"(ws + WS_ARENA + A_QA + b * 256 + r * (4 * ASTR)), "s"(3 * 32768 + r * 1024) : "memory");
      VMCNT(0);
    }
    *(LAS f32x4*)sa = s0;
    *(LAS f32x4*)((LAS char*)sa + 1024) = s1;
    csig<C_SRDY>(cb, one);
    csig<C_QRD>(cb, one);
    STAMP(20);
    auto refill = [&](v4u& a, auto slc, auto ksc) {
      constexpr unsigned sl = decltype(slc)::value;
      constexpr int ks = decltype(ksc)::value;
      rd_inplace<(sl & 32768) + (ks >> 2) * 256>(a, sl < 65536 ? al[ks & 3] : ah[ks & 3]);
    };
    static_for<0, 8>([&](auto k) { refill(af[0][decltype(k)::value], IntC<slot_c(2)>{}, k); });
    bool miss = __any(nx2 < 1);
    if (!miss) csig<C_FREE + 32>(cb, one);
    static_for<1, 8>([&](auto tc) {
      constexpr int t = decltype(tc)::value;
      const bool was = miss;
      if constexpr (t < 6) miss = __any(nxt < 1);
      unsigned use;
      peek2<C_PRDY + 16 * (t - 2) - T_CNT, C_LAND + 16 * (t < 5 ? t + 3 : 8) - T_CNT>(use, nxt, cb);
      static_for<0, 8>([&](auto k) {
        constexpr int ks = decltype(k)::value;
        score_pair<(t < 6 ? 9 : 9 - ks), ks == 0>(s0, s1, af[t & 1][ks], qf[0][ks], qf[1][ks]);
        if constexpr (t < 6) refill(af[t & 1][ks], IntC<slot_c(t + 2)>{}, k);
      });
      wait2<(t < 6 ? 8 : 0)>(use, nxt);
      if (__builtin_expect(__any(use < 8), 0)) cwait<C_PRDY + 16 * (t - 2), 8>(cb);
      put_scores<(t & 1) * 8192>(sa, s0, s1);
      csig<C_SRDY + 16 * t>(cb, one);
      if constexpr (t < 4) { if (!miss) csig<C_FREE + 16 * (t + 2)>(cb, one); }
      if constexpr (t < 7) {
        if (was) {
          cwait<C_LAND + 16 * (t + 1), 1>(cb);
          static_for<0, 8>([&](auto k) { refill(af[(t + 1) & 1][decltype(k)::value], IntC<slot_c(t + 1)>{}, k); });
          if constexpr (t < 5) csig<C_FREE + 16 * (t + 1)>(cb, one);
        }
      }
    });
    asm volatile("s_waitcnt lgkmcnt(0)" ::: "memory");
    STAMP(21);
    if (wave == 0 && (s >= 8 || b == 7)) { DUMP(19); DUMP(20); DUMP(21); DUMPC(50, rqa); }
  } else {
    __builtin_amdgcn_s_setprio(PRIO_PV);
    unsigned bo[2];
    {
      const int q = l15 >> 2, pp = lane & 3;
#pragma unroll
      for (int c = 0; c < 2; ++c) {
        bo[c] = (4 * lq + q) * 1024 + w * 256 + 16 * ((2 * c + (pp >> 1)) ^ fsw(4 * lq + q)) + 8 * (pp & 1);
      }
    }
    unsigned pa = T_P + lane * 16, cb = T_CNT, one = 1u;
    int po = arena(16 * lq + w) + A_PART + (b * 16 + s) * 256 + l15 * 16;
    unsigned vl[8];
#pragma unroll
    for (int ct = 0; ct < 8; ++ct) { vl[ct] = bo[ct & 1] ^ ((ct >> 1) * 64); pin(vl[ct]); }
    int cko = (lane & 2) ? OOB : arena(16 * b + s) + A_CK + w * 512 + (16 * (l15 >> 3) + 4 * lq + 2 * ((l15 >> 2) & 1) + (lane & 1)) * 16;
    pin(pa); pin(cb); pin(one); pin(po); pin(cko);
    f32x4 acc[2][8];
    v4u vf[2][8];
    auto rd = [&](unsigned sl, int ct) {
      unsigned a = vl[ct];
      if (sl >= 65536) asm volatile("v_add_u32 %0, 0x10000, %1" : "=v"(a) : "v"(vl[ct]));
      const v2u lo = ldstr(a, sl & 32768), hi = ldstr(a, (sl & 32768) + 16384);
      return v4u{lo.x, lo.y, hi.x, hi.y};
    };
#pragma unroll
    for (int t = 0; t < 2; ++t) {
      lwait<true>(C_LAND + 16 * t, 1);
#pragma unroll
      for (int ct = 0; ct < 8; ++ct) vf[t][ct] = rd(t * 32768, ct);
      lsig(C_FREE + 16 * t);
    }
    bool miss = false;
    lwait<true>(C_SRDY, 1);
    static_for<0, 8>([&](auto tc) {
      constexpr int t = decltype(tc)::value;
      v4u p0, p1;
      unsigned c, n, nxt;
      asm volatile(SPIN_HEAD "ds_read_b32 %[c], %[cb] offset:%[o]\n\tds_read_b128 %[p0], %[pa] offset:%[s0]\n\t"
                   "ds_read_b128 %[p1], %[pa] offset:%[s1]\n\tds_read_b32 %[x], %[cb] offset:%[ox]\n\t" SPIN_TAIL
                   : [c] "=&v"(c), [n] "=&s"(n), [p0] "=&v"(p0), [p1] "=&v"(p1), [x] "=&v"(nxt)
                   : [cb] "v"(cb), [pa] "v"(pa), [o] "i"(C_PRDY + 16 * t - T_CNT), [w] "i"(8), [s0] "i"((t & 3) * 2048),
                     [s1] "i"((t & 3) * 2048 + 1024), [ox] "i"(C_LAND + 16 * (t + 2) - T_CNT) : "vcc", "scc", "memory");
      if constexpr (t > 0) {
        if (__builtin_expect(__any(c > 8u), 0)) {
#pragma unroll 1
          for (int ht = 0; ht < 2; ++ht)
            if (!__any(c & (1u << ht))) *(LAS float*)(T_FAC + t * 128 + ht * 64 + l15 * 4) = 1.f;
          const f32x4 f0 = u2f(lds128(T_FAC + t * 128 + lq * 16)), f1 = u2f(lds128(T_FAC + t * 128 + lq * 16, 64));
#pragma unroll
          for (int ct = 0; ct < 8; ++ct) { acc[0][ct] *= f0; acc[1][ct] *= f1; pin(acc[0][ct]); pin(acc[1][ct]); }
          asm volatile("s_nop 4");
        }
      }
      if constexpr (t < 7) {
#pragma unroll
        for (int ct = 0; ct < 8; ++ct) {
          if constexpr (t) mfma_pair(acc[0][ct], acc[1][ct], p0, p1, vf[t & 1][ct]); else mfma_pair0(acc[0][ct], acc[1][ct], p0, p1, vf[0][ct]);
          if constexpr (t < 6) vf[t & 1][ct] = rd(slot_c(t + 2), ct);
          __builtin_amdgcn_sched_barrier(0);
        }
        const bool was = miss;
        if constexpr (t < 6) {
          miss = __any(nxt < 1);
          if constexpr (t < 4) { if (!miss) csig<C_FREE + 16 * (t + 2)>(cb, one); }
        }
        if (was) {
          cwait<C_LAND + 16 * (t + 1), 1>(cb);
#pragma unroll
          for (int ct = 0; ct < 8; ++ct) vf[(t + 1) & 1][ct] = rd(slot_c(t + 1), ct);
          if constexpr (t < 5) csig<C_FREE + 16 * (t + 1)>(cb, one);
        }
      } else {
#pragma unroll
        for (int ct = 0; ct < 8; ++ct) acc[0][ct] = mfma16(p0, vf[1][ct], acc[0][ct]);
#pragma unroll
        for (int ct = 0; ct < 8; ++ct) acc[1][ct] = mfma16(p1, vf[1][ct], acc[1][ct]);
      }
    });
    unsigned ce[8], co[8];
#pragma unroll
    for (int ht = 0; ht < 2; ++ht)
#pragma unroll
      for (int r = 0; r < 4; ++r) {
        const v4u v = {pk(acc[ht][0][r], acc[ht][1][r]), pk(acc[ht][2][r], acc[ht][3][r]), pk(acc[ht][4][r], acc[ht][5][r]),
                       pk(acc[ht][6][r], acc[ht][7][r])};
        stg(rs, po + (16 * ht + r) * 4 * ASTR, v);
        ce[4 * ht + r] = v.x ^ v.z; co[4 * ht + r] = v.y ^ v.w;
      }
    {
      unsigned e0, e1, o0, o1;
      xor_rows8(ce, e0, e1);
      xor_rows8(co, o0, o1);
      const bool odd = cko & 16;
      stg(rs, cko, v4u{odd ? e1 : e0, (unsigned)tag, odd ? o1 : o0, (unsigned)(tag >> 32)});
    }
    STAMP(25);
  }
  if (b == 7) return;
  ffn_part(p, b, s, wave, t0, tag);
}

DI void ffn_part(const Params& p, int b, int s, int wave, unsigned t0, u64 tag) {
  int lane;
  asm volatile("v_mbcnt_lo_u32_b32 %0, -1, 0\n\tv_mbcnt_hi_u32_b32 %0, -1, %0" : "=v"(lane));
  unsigned char* ws = p.ws;
  const rsrc_t rs = __builtin_amdgcn_make_buffer_rsrc(ws, 0, WS_BYTES, 0x00020000);
  const int l15 = lane & 15, lq = lane >> 4, w = wave & 3;
  const f32x4 z4 = {0.f, 0.f, 0.f, 0.f};
  __builtin_amdgcn_s_setprio(2);
  const int fg = 8 * b + s, b8 = lane & 7, p4 = 4 * (lane & 3);
  const bool bvalid = l15 < 8;
  const int ch = l15 ^ ((lq << 2) | w), row0 = 4 * w + lq;
  bar();
  STAMP(52);
  nap<D_F>();
  if (s < 8) dma_run<PF_NT>(p.w1, (64 * s + 8 * wave + (lane >> 5)) * 3584 + b * 512 + (lane & 31) * 16, W2 + wave * 1024, 4, 2 * 3584, 0);
  else dma_run<PF_NT>(p.w2, (256 * b + 32 * (s - 8) + 4 * wave) * 1024 + lane * 16, W2 + wave * 1024, 4, 1024, 0);
  if (s >= 8) return;
  v4u mm[4];
  const int mo = arena(33 + 4 * (lane >> 3)) + A_HM + wave * 128 + b8 * 16;
  auto load_mod = [&]() {
    FENCE();
#pragma unroll
    for (int i = 0; i < 4; ++i) mm[i] = ldg(rs, mo, ((i >> 1) * 32 + (i & 1)) * ASTR);
    FENCE();
  };
  load_mod();
  if (wave < 4) {
    dma_run<false>(p.w1, (row0 + (ch >> 2) * 128) * 3584 + fg * 64 + (ch & 3) * 16, W0 + w * 1024, 8, 16 * 3584, 4096);
  } else {
    const unsigned u = (32 * fg + row0) * 1024 + ch * 16;
    dma_run<true>(p.w2, u, W1 + w * 1024, 4, 256, 8192);
    dma_run<true>(p.w2, u + 16 * 1024, W1 + w * 1024 + 4096, 4, 256, 8192);
  }
  STAMP(54);
#pragma unroll 1
  for (int sp = 0; sp < SPIN_G; ++sp) {
    if (!any_sent<4>(mm)) break;
    load_mod();
  }
  f32x4 sb[2], cb[2];
  sb[0] = up4(mm[0].xy) + up4(mm[1].xy); sb[1] = up4(mm[0].zw) + up4(mm[1].zw);
  cb[0] = up4(mm[2].xy) + up4(mm[3].xy) + 1.f; cb[1] = up4(mm[2].zw) + up4(mm[3].zw) + 1.f;
  pin(sb[0]); pin(sb[1]); pin(cb[0]); pin(cb[1]);
  if (wave == 7) wait_flags(rs, WS_FLAG2 + lane * 16, tag);
  VMCNT(0);
  STAMP(56);
  bar();
  STAMP(28);
  v4u fw[16];
  if (wave < 2) {
#pragma unroll
    for (int kq = 0; kq < 4; ++kq) {
      const unsigned a0 = tr_addr(W0, lane, 32 * kq + 16 * w + p4, 0), a1 = tr_addr(W0, lane, 32 * kq + 16 * w + p4, 1);
#pragma unroll
      for (int ks = 0; ks < 4; ++ks) fw[4 * kq + ks] = tr_frag(a0, a1, ks);
    }
  } else {
#pragma unroll
    for (int i = 0; i < 8; ++i) {
      const int c4 = 32 * (i >> 1) + 2 * p4 + 4 * (i & 1);
      fw[i] = tr_frag(tr_addr(W1 + w * 8192, lane, c4, 0), tr_addr(W1 + w * 8192, lane, c4, 1), 0);
      fw[8 + i] = fw[i];
    }
  }
  int xo = arena(wave) + A_X1 + lane * 16;
  unsigned o5 = L_A5 + wave * 1040 + lane * 16;
  unsigned a5 = L_A5 + b8 * 1040 + lq * 16, a6w = bvalid ? L_A6 + l15 * 80 + w * 32 + lq * 8 : L_JUNK, a6r = L_A6 + b8 * 80 + lq * 16;
  int yo = arena(fg) + A_YP + b8 * 1024 + w * 256 + (l15 >> 3) * 64 + lq * 16;
  pin(xo); pin(o5); pin(a5); pin(a6w); pin(a6r); pin(yo);
#pragma unroll
  for (int i = 0; i < 16; ++i) pin(fw[i]);
  STAMP(55);
  STAMPD(40);
  u64 st15 = 0; int rx_ = 0;
  if (wave == 4) { wait_until(latest_time(ws + WS_HB + (16 * b + s) * 128, t0, (unsigned)tag) + T_C); STAMPV(40); }
  bar();
  {
    v4u xv[1];
    int rx = 0;
#pragma unroll 1
    for (int sp = 0; sp < SPIN_G; ++sp) { load_n<1>(rs, xo, 0, xv); rx = sp + 1; if (!any_sent<1>(xv)) break; }
    STAMP(15);
    const f32x4 v0 = {bflo(xv[0].x), bfhi(xv[0].x), bflo(xv[0].y), bfhi(xv[0].y)}, v1 = {bflo(xv[0].z), bfhi(xv[0].z), bflo(xv[0].w), bfhi(xv[0].w)};
    const f32x4 sv4 = v0 + v1, qv4 = v0 * v0 + v1 * v1;
    const float mean = wave_sum((sv4.x + sv4.y) + (sv4.z + sv4.w)) * (1.f / 512);
    const float msq = wave_sum((qv4.x + qv4.y) + (qv4.z + qv4.w)) * (1.f / 512);
    const float rstd = __builtin_amdgcn_rsqf(fmaxf(msq - mean * mean, 0.f) + EPS);
    *(LAS v4u*)o5 = pk8((v0 - mean) * rstd * cb[0] + sb[0], (v1 - mean) * rstd * cb[1] + sb[1]);
  }
  bar();
  STAMP(36);
  if (wave < 2) {
    f32x4 z0 = z4, z1 = z4;
#pragma unroll
    for (int hq = 0; hq < 2; ++hq) {
      v4u bf[8];
#pragma unroll
      for (int i = 0; i < 8; ++i) bf[i] = lds128(a5, hq * 512 + i * 64);
      if (hq == 0) __builtin_amdgcn_sched_barrier(0);
#pragma unroll
      for (int i = 0; i < 8; i += 2) { z0 = mfma16(fw[8 * hq + i], bf[i], z0); z1 = mfma16(fw[8 * hq + i + 1], bf[i + 1], z1); }
    }
    z0 += z1;
    *(LAS v2u*)a6w = v2u{pk(z0.x < 0.f ? 0.f : z0.x, z0.y < 0.f ? 0.f : z0.y), pk(z0.z < 0.f ? 0.f : z0.z, z0.w < 0.f ? 0.f : z0.w)};
  }
  bar();
  if (wave >= 4) {
    STAMP(37);
    const v4u bf = lds128(a6r);
#pragma unroll
    for (int i = 0; i < 2; ++i) {
      const v4u ya = pk8(mfma16(fw[4 * i], bf, z4), mfma16(fw[4 * i + 1], bf, z4)), yb = pk8(mfma16(fw[4 * i + 2], bf, z4), mfma16(fw[4 * i + 3], bf, z4));
      stg(rs, yo + i * 128, pick(bvalid, ya, yb));
    }
    STAMP(16);
    if (wave == 4) { DUMP(36); DUMP(37); DUMP(16); DUMP(40); }
  }
}

__global__ __launch_bounds__(768) void adaln_mla_ffn_kernel(Params p) {
  STAMP(0);
  unsigned k0, k1;
  asm volatile("s_load_dword %0, %2, 0x0\n\ts_load_dword %1, %2, 0x40" : "=&s"(k0), "=&s"(k1) : "s"(__builtin_amdgcn_kernarg_segment_ptr()));
  const unsigned t0 = now32();
  const int g = blockIdx.x & 7, idx = blockIdx.x >> 3;
  const int wave = __builtin_amdgcn_readfirstlane(threadIdx.x >> 6), lane = threadIdx.x & 63;
  if (wave == 0 && (idx & 1)) {
    u64 pc;
    asm volatile("s_getpc_b64 %0" : "=s"(pc));
    const unsigned line = min((idx >> 1) + 16 * lane, CODE_LINES - 1);
    asm volatile("s_mov_b32 m0, %2\n\ts_nop 0\n\tglobal_load_lds_dword %0, %1" :: "v"(line * 128), "s"(pc & ~255ull), "s"(L_JUNK) : "memory");
  }
  const int wi = idx & 15, l15 = lane & 15, lq = lane >> 4, b8 = lane & 7;
  const int ch = l15 ^ ((lq << 2) | wave);
  unsigned v1 = wave < 4 ? (32 * wi + 4 * wave + lq) * 6144 + (ch >> 3) * 1024 + g * 128 + (ch & 7) * 16 : wi * 64 + b8 * 16;
  unsigned vx = lane * 16;
  pin(v1); pin(vx);
  asm volatile("s_waitcnt lgkmcnt(0)" :: "s"(p.x), "s"(p.ckv), "s"(p.wq), "s"(p.wuk), "s"(p.wuv), "s"(p.wo), "s"(p.w1), "s"(p.w2), "s"(p.c),
               "s"(p.wmod), "s"(p.out), "s"(p.ws), "s"(k0), "s"(k1));
  STAMP(39);
  STAMPD(2); STAMPD(3); STAMPD(32);
  if ((idx & 17) == 0) {
    const rsrc_t rs = __builtin_amdgcn_make_buffer_rsrc(p.ws, 0, WS_BYTES, 0x00020000);
    const int my = arena(16 * g + wi) + A_H, r = wave - 4;
    {
      const bool row = wave >= 4;
      if (wave == 3) stg(rs, vx + my, v4u{SENT, SENT, SENT, SENT});
      const unsigned char* a = row ? (const unsigned char*)(p.c + r * 512) : (const unsigned char*)p.wmod;
      const unsigned d = row ? M_RAW + r * 2048 + 1024 : 3 * 32768 + wave * 1024;
      if (row) {
        dma16<false>(p.x + r * 512, vx, d - 1024);
        dma16<false>(a, v1, d);
      } else {
#pragma unroll
        for (int i = 0; i < 4; ++i) dma16<true>(a + i * 16 * 6144, v1, d + i * 4096);
      }
      FENCE();
      __builtin_amdgcn_s_setprio(1);
    }
    if (wave >= 4) {
      unsigned raw = M_RAW + r * 2048 + vx, d = (lane >> 3) == g ? M_LNX + r * 256 + b8 * 32 : L_JUNK, sc = M_SC + r * 144 + lane * 2;
      unsigned rc = M_RAW + r * 2048 + 1024 + lane * 2;
      pin(raw); pin(d); pin(sc); pin(rc);
      VMCNT(0);
      const v4u xr = lds128(raw);
      const float cv = bflo(*(LAS u16*)rc);
      const f32x4 v0 = up4(xr.xy), v2 = up4(xr.zw), sv4 = v0 + v2, qv4 = v0 * v0 + v2 * v2;
      const float mean = wave_sum((sv4.x + sv4.y) + (sv4.z + sv4.w)) * (1.f / 512);
      const float msq = wave_sum((qv4.x + qv4.y) + (qv4.z + qv4.w)) * (1.f / 512);
      const float rstd = __builtin_amdgcn_rsqf(fmaxf(msq - mean * mean, 0.f) + EPS);
      *(LAS f32x4*)d = (v0 - mean) * rstd;
      *(LAS f32x4*)((LAS char*)d + 16) = (v2 - mean) * rstd;
      *(LAS u16*)sc = (u16)pk(cv * __builtin_amdgcn_rcpf(1.f + __expf(-cv)), 0.f);
      STAMPV(32);
      bar();
    } else {
      const int sl = 8 * (g & 1) + (wi >> 1);
      const int p4 = 4 * (lane & 3);
      unsigned s0 = tr_addr(3 * 32768, lane, 16 * wave + p4, 0), s1 = tr_addr(3 * 32768, lane, 16 * wave + p4, 1), c0 = s0 ^ 128, c1 = s1 ^ 128;
      unsigned ba = M_SC + b8 * 144 + lq * 16, la = M_LNX + b8 * 256 + (16 * wave + 4 * lq) * 4;
      int o = l15 < 8 ? my + wave * 256 + l15 * 32 + lq * 8 : OOB;
      int hh = wave == 3 && lane < 32 ? WS_HH + (4 * lane + (g >> 1)) * 512 + (sl >> 2) * 128 + (sl & 3) * 4 : OOB;
      const float one = wi == 0 ? 1.f : 0.f;
      f32x4 sa = {0.f, 0.f, 0.f, 0.f}, ca = {one, one, one, one};
      pin(s0); pin(s1); pin(c0); pin(c1); pin(ba); pin(la); pin(o); pin(hh); pin(ca);
      unsigned key = 0;
      if (wave == 3) {
        const u64 tag = get_tag(p.ws);
        key = (unsigned)tag;
        VMCNT(4);
        __builtin_amdgcn_raw_buffer_store_b64(v2u{(unsigned)tag, (unsigned)(tag >> 32)}, rs,
                                              lane < 32 ? WS_EFL + (4 * lane + (g >> 1)) * 256 + (sl >> 3) * 128 + (sl & 7) * 8 : OOB, 0, SC1);
        VMCNT(1);
      } else {
        VMCNT(0);
      }
      STAMPV(32);
      bar();
      const unsigned tn = now32();
      const v4u b0 = lds128(ba), f0 = tr_frag(s0, s1, 0), g0 = tr_frag(c0, c1, 0), b1 = lds128(ba, 64), f1 = tr_frag(s0, s1, 1), g1 = tr_frag(c0, c1, 1);
      const f32x4 ln = u2f(lds128(la));
      __builtin_amdgcn_sched_barrier(0);
      st32(rs, hh, tn + key);
      STAMPV(2);
      sa = mfma16(f1, b1, mfma16(f0, b0, sa));
      ca = mfma16(g1, b1, mfma16(g0, b0, ca));
      sa += ln * ca;
      __builtin_amdgcn_raw_buffer_store_b64(v2u{pk(sa.x, sa.y), pk(sa.z, sa.w)}, rs, o, 0, SC1);
      STAMPV(3);
    }
  }
  if (PREF && (idx & 17) == 1 && wave >= 8) {
    const unsigned char* a = (const unsigned char*)p.wuk + (128 * (wi & 2) + 128 * (wave & 1) + 64 + 16 * (wave & 2)) * 8192 + (4 * g + (wi >> 2)) * 256;
#pragma unroll 1
    for (int i = 0; i < 8; ++i) dma16<true>(a + i * 32768, lq * 8192 + l15 * 16, T_CNT + 1024 + (wave & 3) * 1024);
  }
  FENCE();
  if (idx < 16) attn_wg(p, g, idx, wave, lane, t0);
  else weight_wg(p, g, wi, wave, lane, t0);
}

extern "C" void kernel_launch(void* const* d_in, const int* in_sizes, int n_in, void* d_out, int out_size, void* d_ws, size_t ws_size,
                              hipStream_t stream) {
  Params p;
  p.x = (const u16*)d_in[0]; p.ckv = (const u16*)d_in[1]; p.wq = (const u16*)d_in[2]; p.wuk = (const u16*)d_in[3];
  p.wuv = (const u16*)d_in[4]; p.wo = (const u16*)d_in[5]; p.w1 = (const u16*)d_in[7]; p.w2 = (const u16*)d_in[8];
  p.c = (const u16*)d_in[9]; p.wmod = (const u16*)d_in[10];
  p.out = (u16*)d_out; p.ws = (unsigned char*)d_ws + WS_BASE;
  (void)hipFuncSetAttribute((const void*)adaln_mla_ffn_kernel, hipFuncAttributeMaxDynamicSharedMemorySize, LDS_BYTES);
  adaln_mla_ffn_kernel<<<256, 768, LDS_BYTES, stream>>>(p);
}
```

```cpp
#include <hip/hip_runtime.h>
#include <hip/hip_bf16.h>
#pragma clang diagnostic ignored "-Wint-to-pointer-cast"
#pragma clang diagnostic ignored "-Wsometimes-uninitialized"

typedef unsigned short u16;
typedef unsigned long long u64;
typedef unsigned v4u __attribute__((ext_vector_type(4)));
typedef unsigned v2u __attribute__((ext_vector_type(2)));
typedef float f32x2 __attribute__((ext_vector_type(2)));
typedef float f32x4 __attribute__((ext_vector_type(4)));
typedef float f32x16 __attribute__((ext_vector_type(16)));
typedef short s16x4 __attribute__((ext_vector_type(4)));
typedef short bf16x8 __attribute__((ext_vector_type(8)));
typedef __bf16 bf16x2 __attribute__((ext_vector_type(2)));
#define GAS __attribute__((address_space(1)))
#define LAS __attribute__((address_space(3)))
#define DI __device__ __forceinline__
#define RLX_AGENT __ATOMIC_RELAXED, __HIP_MEMORY_SCOPE_AGENT
typedef __amdgpu_buffer_rsrc_t rsrc_t;

#define CODE_LINES 400
#define ATT_DELAY 0
#define T_H 62
#define D_UK 8
#define T_QP 102
#define T_QA 106
#define T_B 100
#define T_C 190
#define T_D 365
#define D_PART 6
#define D_F 0
#define D_ML 4
#define FLAG_AT 1
#define D_PREF 16
#define PRIO_S 2
#define PRIO_PV 2
#define PF_NT false
#define D_W 0
#define T_PREF 130
#define T_W 40
#define PF_EVEN 24
#define T_FILL 0
#define T_FLAG 220
#define WS_BASE 0
#define FILL_AUX 16
#define WWIN 7
#define T_L 190
#define FILL_EARLY 1
#define D_FILL 0
#define PREF 1
#define WO_NT 1

constexpr int WS_FLAG1 = 4096, WS_FLAG2 = 8192;
constexpr int WS_EFL = 16384  , WS_ARR = 65536, WS_HP = 81920, WS_HB = 98304  ,
              WS_HH = 131072  , WS_HQ = 196608  ;
constexpr int WS_ARENA = 262144, ASTR = 61696;
constexpr int A_H = 0, A_ML = 1024, A_QP = 2048, A_QA = 6144, A_HM = 8192, A_X1 = 9216, A_OUTP = 10240, A_YP = 18432, A_PART = 26624, A_CK = 59392;
constexpr int WS_BYTES = WS_ARENA + 128 * ASTR;
constexpr unsigned SENT = 0xFFFFFFFFu;
constexpr int OOB = 0x7FFFFF00;
constexpr int SC1 = 16;
constexpr int SPIN_H = 1 << 11;
constexpr int SPIN_G = 1 << 19;
constexpr int SPIN_L = 1 << 23;
constexpr int LDS_BYTES = 163840;
constexpr float EPS = 1e-5f;
constexpr float QSCALE = 0.08838834764831845f * 1.4426950408889634f;
constexpr float BUMP = 40.f;

#define STAMP(k) do {} while (0)
#define STAMPV(k) do {} while (0)
#define STAMPD(k) do {} while (0)
#define DUMP(k) do {} while (0)
#define DUMPC(k, c) do {} while (0)

template <class T> DI void pin(T& v) { asm volatile("" : "+v"(v)); }
DI float bflo(unsigned u) { return __uint_as_float(u << 16); }
DI float bfhi(unsigned u) { return __uint_as_float(u & 0xFFFF0000u); }
DI unsigned pk(float a, float b) { f32x2 v = {a, b}; return __builtin_bit_cast(unsigned, __builtin_convertvector(v, bf16x2)); }
DI f32x4 mfma16(v4u a, v4u b, f32x4 c) {
  return __builtin_amdgcn_mfma_f32_16x16x32_bf16(__builtin_bit_cast(bf16x8, a), __builtin_bit_cast(bf16x8, b), c, 0, 0, 0);
}
DI f32x16 mfma32(v4u a, v4u b, f32x16 c) {
  return __builtin_amdgcn_mfma_f32_32x32x16_bf16(__builtin_bit_cast(bf16x8, a), __builtin_bit_cast(bf16x8, b), c, 0, 0, 0);
}
DI v4u ldg(rsrc_t rs, int off, int so = 0) { return __builtin_amdgcn_raw_buffer_load_b128(rs, off, so, SC1); }
DI void stg(rsrc_t rs, int off, v4u v) { __builtin_amdgcn_raw_buffer_store_b128(v, rs, off, 0, SC1); }
DI unsigned umax4(v4u v) { return max(max(v.x, v.y), max(v.z, v.w)); }
DI v4u f2u(f32x4 v) { return __builtin_bit_cast(v4u, v); }
DI f32x4 u2f(v4u v) { return __builtin_bit_cast(f32x4, v); }
DI bool nan4(f32x4 v) { return __builtin_isunordered(v.x, v.y) | __builtin_isunordered(v.z, v.w); }
DI v4u lds128(unsigned a, int o = 0) { return *(LAS v4u*)((LAS char*)a + o); }
DI v2u ldstr(unsigned a, int o = 0) { return __builtin_bit_cast(v2u, __builtin_amdgcn_ds_read_tr16_b64_v4i16((LAS s16x4*)((LAS char*)a + o))); }
DI void bar() {
  __builtin_amdgcn_sched_barrier(0);
  asm volatile("s_waitcnt lgkmcnt(0)\n\ts_barrier" ::: "memory");
  __builtin_amdgcn_sched_barrier(0);
}
#define VMCNT_(n) asm volatile("s_waitcnt vmcnt(" #n ")" ::: "memory")
#define VMCNT(n) VMCNT_(n)
#define FENCE() asm volatile("" ::: "memory")
template <bool NT> DI void dma16(const void* base, unsigned voff, unsigned dst) {
  if (NT) asm volatile("s_mov_b32 m0, %2\n\ts_nop 0\n\tglobal_load_lds_dwordx4 %0, %1 nt" :: "v"(voff), "s"(base), "s"(dst) : "memory");
  else asm volatile("s_mov_b32 m0, %2\n\ts_nop 0\n\tglobal_load_lds_dwordx4 %0, %1" :: "v"(voff), "s"(base), "s"(dst) : "memory");
}
template <bool NT> DI void dma_run(const void* base, unsigned voff, unsigned dst, int n, int gstr, int lstr) {
  const unsigned char* b = (const unsigned char*)base;
#pragma unroll 1
  for (int i = 0; i < n; ++i) { VMCNT(WWIN); dma16<NT>(b, voff, dst); b += gstr; dst += lstr; }
}
template <int N> DI void nap() { if (N > 0) __builtin_amdgcn_s_sleep(N); }
DI void lsig(unsigned a, unsigned v = 1u) { asm volatile("s_mov_b64 exec, 1\n\tds_add_u32 %0, %1\n\ts_mov_b64 exec, -1" :: "v"(a), "v"(v) : "memory"); }
DI unsigned lpeek(unsigned a) { return *(volatile LAS unsigned*)a; }
template <bool NAP = false> DI void lwait(unsigned a, unsigned want) {
  unsigned v, n = SPIN_L;
  if (NAP)
    asm volatile("1:\n\tds_read_b32 %0, %2\n\ts_waitcnt lgkmcnt(0)\n\tv_cmp_le_u32 vcc, %3, %0\n\ts_cbranch_vccnz 2f\n\t"
                 "s_sleep 1\n\ts_sub_u32 %1, %1, 1\n\ts_cbranch_scc0 1b\n2:" : "=&v"(v), "+s"(n) : "v"(a), "s"(want) : "vcc", "scc", "memory");
  else
    asm volatile("1:\n\tds_read_b32 %0, %2\n\ts_waitcnt lgkmcnt(0)\n\tv_cmp_le_u32 vcc, %3, %0\n\ts_cbranch_vccnz 2f\n\t"
                 "s_sub_u32 %1, %1, 1\n\ts_cbranch_scc0 1b\n2:" : "=&v"(v), "+s"(n) : "v"(a), "s"(want) : "vcc", "scc", "memory");
}
template <int CTRL> DI float dppf(float v) {
  return __builtin_bit_cast(float, __builtin_amdgcn_update_dpp(0, __builtin_bit_cast(int, v), CTRL, 0xF, 0xF, true));
}
DI float row16_max(float v) {
  v = fmaxf(v, dppf<0x128>(v)); v = fmaxf(v, dppf<0x124>(v)); v = fmaxf(v, dppf<0x122>(v)); return fmaxf(v, dppf<0x121>(v));
}
DI float row16_sum(float v) { v += dppf<0x128>(v); v += dppf<0x124>(v); v += dppf<0x122>(v); return v + dppf<0x121>(v); }
DI float xsum16(float v) { auto r = __builtin_amdgcn_permlane16_swap(__float_as_uint(v), __float_as_uint(v), false, false); return __uint_as_float(r[0]) + __uint_as_float(r[1]); }
DI float xsum32(float v) { auto r = __builtin_amdgcn_permlane32_swap(__float_as_uint(v), __float_as_uint(v), false, false); return __uint_as_float(r[0]) + __uint_as_float(r[1]); }
DI float xmax16(float v) { auto r = __builtin_amdgcn_permlane16_swap(__float_as_uint(v), __float_as_uint(v), false, false); return fmaxf(__uint_as_float(r[0]), __uint_as_float(r[1])); }
DI float xmax32(float v) { auto r = __builtin_amdgcn_permlane32_swap(__float_as_uint(v), __float_as_uint(v), false, false); return fmaxf(__uint_as_float(r[0]), __uint_as_float(r[1])); }
DI float wave_sum(float v) { return xsum32(xsum16(row16_sum(v))); }

#define XS8(d, a, b) "v_xor_b32_dpp " d ", " a ", " a " row_ror:8 row_mask:0xf bank_mask:0x3\n\tv_xor_b32_dpp " d ", " b ", " b " row_ror:8 row_mask:0xf bank_mask:0xc\n\t"
#define XS4(d, a, b) "v_xor_b32_dpp " d ", " a ", " a " row_half_mirror row_mask:0xf bank_mask:0x5\n\tv_xor_b32_dpp " d ", " b ", " b " row_half_mirror row_mask:0xf bank_mask:0xa\n\t"
#define XQ(d, p) "v_xor_b32_dpp " d ", " d ", " d " quad_perm:" p " row_mask:0xf bank_mask:0xf\n\t"
DI void xor_rows8(const unsigned (&v)[8], unsigned& r0, unsigned& r1) {
  unsigned t0, t1, t2, t3;
  asm volatile("s_nop 1\n\t" XS8("%2", "%6", "%10") XS8("%4", "%8", "%12") XS8("%3", "%7", "%11") XS8("%5", "%9", "%13")
               XS4("%0", "%2", "%4") XS4("%1", "%3", "%5") XQ("%0", "[2,3,0,1]") "s_nop 0\n\t" XQ("%1", "[2,3,0,1]") XQ("%0", "[1,0,3,2]")
               "s_nop 0\n\t" XQ("%1", "[1,0,3,2]")
               : "=&v"(r0), "=&v"(r1), "=&v"(t0), "=&v"(t1), "=&v"(t2), "=&v"(t3)
               : "v"(v[0]), "v"(v[1]), "v"(v[2]), "v"(v[3]), "v"(v[4]), "v"(v[5]), "v"(v[6]), "v"(v[7]));
}
DI unsigned xor_rows2(unsigned a, unsigned b) {
  unsigned r;
  asm volatile("s_nop 1\n\t" XS8("%0", "%1", "%2") "s_nop 1\n\tv_xor_b32_dpp %0, %0, %0 row_half_mirror row_mask:0xf bank_mask:0xf\n\ts_nop 1\n\t"
               XQ("%0", "[2,3,0,1]") "s_nop 1\n\t" XQ("%0", "[1,0,3,2]") "s_nop 1" : "=&v"(r) : "v"(a), "v"(b));
  return r;
}
template <int N> DI bool any_sent(const v4u (&r)[N]) {
  unsigned m = 0;
#pragma unroll
  for (int i = 0; i < N; ++i) m = max(m, umax4(r[i]));
  return __any(m == SENT);
}
template <int N> DI void load_n(rsrc_t rs, int off0, int str, v4u (&r)[N]) {
  FENCE();
#pragma unroll
  for (int i = 0; i < N; ++i) r[i] = ldg(rs, off0, i * str);
  __builtin_amdgcn_sched_barrier(0);
}
template <int N, int NAP = 0> DI void gather(rsrc_t rs, int off0, int str, v4u (&r)[N]) {
#pragma unroll 1
  for (int sp = 0; sp < SPIN_G; ++sp) {
    load_n<N>(rs, off0, str, r);
    if (!any_sent<N>(r)) break;
    nap<NAP>();
  }
}
DI u64 get_tag(const unsigned char* ws) {
  u64 e;
  asm volatile("s_load_dwordx2 %0, %1, 0x0\n\ts_waitcnt lgkmcnt(0)" : "=s"(e) : "s"(ws) : "memory");
  return e * 6364136223846793005ull + 1442695040888963407ull;
}
DI bool flags_up(v4u f, u64 tag) {
  const unsigned lo = (unsigned)tag, hi = (unsigned)(tag >> 32);
  return __all(f.x == lo && f.y == hi && f.z == lo && f.w == hi);
}
DI void wait_flags(rsrc_t rs, int off, u64 tag) {
#pragma unroll 1
  for (int sp = 0; sp < SPIN_G; ++sp) {
    FENCE();
    if (flags_up(ldg(rs, off), tag)) break;
    __builtin_amdgcn_s_sleep(8);
  }
  FENCE();
}
typedef unsigned s8u __attribute__((ext_vector_type(8)));
DI s8u sload8(const unsigned char* a) {
  s8u v;
  asm volatile("s_load_dwordx8 %0, %1, 0x0 glc\n\ts_waitcnt lgkmcnt(0)" : "=s"(v) : "s"(a) : "memory");
  return v;
}
DI void wait_hints(const unsigned char* a, unsigned want, unsigned span) {
#pragma unroll 1
  for (int sp = 0; sp < SPIN_H; ++sp) {
    const s8u v = sload8(a) - want;
    if (max(max(max(v[0], v[1]), max(v[2], v[3])), max(max(v[4], v[5]), max(v[6], v[7]))) <= span) break;
  }
}
DI unsigned max8(s8u v) { return max(max(max(v[0], v[1]), max(v[2], v[3])), max(max(v[4], v[5]), max(v[6], v[7]))); }
DI unsigned now32() { return (unsigned)__builtin_amdgcn_s_memrealtime(); }
constexpr unsigned HWIN = 1u << 20;
DI unsigned latest_time(const unsigned char* a, unsigned t0, unsigned key) {
  unsigned m = 0;
#pragma unroll 1
  for (int sp = 0; sp < SPIN_H; ++sp) {
    m = max8(sload8(a) - (key + t0 - HWIN));
    if (m < 2 * HWIN) return m + t0 - HWIN;
  }
  return t0;
}
DI void wait_until(unsigned t) {
#pragma unroll 1
  for (int sp = 0; sp < SPIN_H; ++sp) if ((int)(now32() - t) >= 0) break;
}
DI int arena(int wid) { return WS_ARENA + wid * ASTR; }

DI unsigned tr_addr(unsigned region, int lane, int col4, int t) {
  const int g = lane >> 4, q = (lane & 15) >> 2;
  return region + (8 * g + 4 * t + q) * 256 + 16 * ((col4 >> 3) ^ ((q << 2) | ((2 * g + t) & 3))) + 8 * ((col4 >> 2) & 1);
}
DI v4u tr_frag(unsigned a0, unsigned a1, int ks) {
  const v2u lo = ldstr(a0, ks * 8192), hi = ldstr(a1, ks * 8192);
  return v4u{lo.x, lo.y, hi.x, hi.y};
}
DI void panel_frags(unsigned region, int lane, int col_a, int col_b, v4u (&f)[2][4]) {
  const unsigned a0 = tr_addr(region, lane, col_a, 0), a1 = tr_addr(region, lane, col_a, 1);
  const unsigned b0 = tr_addr(region, lane, col_b, 0), b1 = tr_addr(region, lane, col_b, 1);
#pragma unroll
  for (int ks = 0; ks < 4; ++ks) { f[0][ks] = tr_frag(a0, a1, ks); f[1][ks] = tr_frag(b0, b1, ks); }
}
DI void gemv2(const v4u (&f)[2][4], unsigned act, f32x4& d0, f32x4& d1) {
  v4u bf[4];
#pragma unroll
  for (int ks = 0; ks < 4; ++ks) bf[ks] = lds128(act, ks * 64);
  __builtin_amdgcn_sched_barrier(0);
#pragma unroll
  for (int ks = 0; ks < 4; ++ks) { d0 = mfma16(f[0][ks], bf[ks], d0); d1 = mfma16(f[1][ks], bf[ks], d1); }
}
DI f32x4 up4(v2u v) { return f32x4{bflo(v.x), bfhi(v.x), bflo(v.y), bfhi(v.y)}; }
DI v4u pk8(f32x4 a, f32x4 b) { return v4u{pk(a.x, a.y), pk(a.z, a.w), pk(b.x, b.y), pk(b.z, b.w)}; }
DI v4u silu8(v4u cv) {
  v4u o;
#pragma unroll
  for (int e = 0; e < 4; ++e) {
    const float a = bflo(cv[e]), c = bfhi(cv[e]);
    o[e] = pk(a * __builtin_amdgcn_rcpf(1.f + __expf(-a)), c * __builtin_amdgcn_rcpf(1.f + __expf(-c)));
  }
  return o;
}

struct Params {
  const u16 *x, *ckv, *wq, *wuk, *wuv, *wo, *w1, *w2, *c, *wmod;
  u16* out;
  unsigned char* ws;
};

constexpr unsigned W0 = 0, W1 = 32768, W2 = 65536, L_SC = 98304  ,
                   L_A1 = L_SC + 4224  , L_A2 = L_A1 + 2176, L_A3 = L_A2 + 2176, L_A4 = L_A3 + 2176, L_CNT = L_A4 + 2176;
constexpr unsigned T_SP = 131072, T_P = T_SP + 16384, T_FAC = T_P + 8192, T_CNT = T_FAC + 1024,
                   L_A5 = 98304  , L_A6 = L_A5 + 8320  ;
constexpr unsigned C_LAND = T_CNT + 32, C_FREE = C_LAND + 4, C_SRDY = C_LAND + 8, C_PRDY = C_LAND + 12,
                   C_QST = T_CNT + 192, C_QRD = T_CNT + 196;
constexpr unsigned M_RAW = T_SP  , M_SC = T_P, M_LNX = M_SC + 1152;
constexpr unsigned L_JUNK = LDS_BYTES - 256;

DI void gather4_act(rsrc_t rs, int off0, int str, unsigned dst, int& rounds) {
  f32x4 s;
#pragma unroll 1
  for (int sp = 0; sp < SPIN_G; ++sp) {
    v4u r[4];
    load_n<4>(rs, off0, str, r);
    s = (u2f(r[0]) + u2f(r[1])) + (u2f(r[2]) + u2f(r[3]));
    rounds = sp + 1;
    if (!__any(nan4(s)) || !any_sent<4>(r)) break;
  }
  *(LAS v2u*)dst = v2u{pk(s.x, s.y), pk(s.z, s.w)};
}
template <int N> DI f32x4 reduce_rows(rsrc_t rs, int off0, int offl, v4u sel, int& rounds) {
  f32x4 a;
#pragma unroll 1
  for (int sp = 0; sp < SPIN_G; ++sp) {
    v4u d[N];
    FENCE();
#pragma unroll
    for (int i = 0; i < N; ++i) d[i] = ldg(rs, i == N - 1 ? offl : off0, i * 16 * ASTR);
    __builtin_amdgcn_sched_barrier(0);
    f32x4 b = {0.f, 0.f, 0.f, 0.f};
    a = b;
#pragma unroll
    for (int i = 0; i < N; i += 2) { a = mfma16(sel, d[i], a); b = mfma16(sel, d[i + 1], b); }
    a += b;
    rounds = sp + 1;
    if (!__any(a.x != a.x) || !any_sent<N>(d)) break;
  }
#pragma unroll
  for (int r = 0; r < 4; ++r) { a[r] += dppf<0x128>(a[r]); a[r] += dppf<0x124>(a[r]); }
  return a;
}
DI v4u pick(bool lo, v4u a, v4u b) { return v4u{lo ? a.x : b.x, lo ? a.y : b.y, lo ? a.z : b.z, lo ? a.w : b.w}; }
DI void st32(rsrc_t rs, int off, unsigned v) { __builtin_amdgcn_raw_buffer_store_b32(v, rs, off, 0, SC1); }

DI void weight_wg(const Params& p, int g, int wi, int wave, int lane, unsigned t0) {
  unsigned char* ws = p.ws;
  const rsrc_t rs = __builtin_amdgcn_make_buffer_rsrc(ws, 0, WS_BYTES, 0x00020000);
  const int h = 4 * g + (wi >> 2), j = wi & 3, wid = 16 * g + wi, my = arena(wid);
  const int l15 = lane & 15, lq = lane >> 4, b8 = lane & 7;
  const bool bvalid = l15 < 8;
  const int w = wave & 3;
  const int ch = l15 ^ ((lq << 2) | w);
  const unsigned dst0 = w * 1024;
  const unsigned vuv = (128 * j + 16 * ((4 * w + lq) & 7) + (w >> 1)) * 8192 + h * 256 + ch * 16;
  const f32x4 z4 = {0.f, 0.f, 0.f, 0.f};

  if (wave >= 8) {
    const unsigned vq = (128 * j + 4 * w + lq) * 8192 + h * 256 + ch * 16;
    wait_until(t0 + T_W);
    dma_run<true>(p.wq, vq, W0 + dst0, 8, 16 * 8192, 4096);
    dma_run<true>(p.wuk, vq, W1 + dst0, 8, 16 * 8192, 4096);
    const unsigned key = (unsigned)get_tag(ws);
    VMCNT(0);
    STAMP(44);
    int hp = lane < 8 ? WS_HP + (4 * h + (lane & 3)) * 128 + j * 4 + (lane >> 2) * 16 : OOB;
    int hq = lane < 32 ? WS_HQ + (4 * lane + j) * 512 + (h & 3) * 128 + g * 4 : OOB;
    pin(hp); pin(hq);
    bar();
    if (w == 0) { const unsigned t = now32(); nap<2>(); st32(rs, hp, t + key); }
    bar();
    if (w == 0) { const unsigned tq = latest_time(ws + WS_HP + wid * 128, t0, key) + T_QP; st32(rs, hq, tq + key); wait_until(tq); }
    bar();
    const unsigned tl = now32() + T_L;
    bar();
    v4u wo[4][8];
    const GAS unsigned char* wb = (const GAS unsigned char*)p.wo + (h * 128 + 8 * lq) * 1024 + w * 256 + (4 * (l15 & 3) + (l15 >> 2)) * 16;
    bar();
    wait_until(tl);
#pragma unroll
    for (int ks = 0; ks < 4; ++ks) {
      const GAS unsigned char* wk = wb + ((ks + j) & 3) * 32768;
#pragma unroll
      for (int r = 0; r < 8; ++r)
        wo[ks][r] = (WO_NT && ks == 0) ? __builtin_nontemporal_load((const GAS v4u*)(wk + r * 1024)) : *(const GAS v4u*)(wk + r * 1024);
      if (ks == 0) { VMCNT(0); bar(); }
    }
    __builtin_amdgcn_sched_barrier(0);
#pragma unroll
    for (int ks = 0; ks < 4; ++ks) {
      v4u o[8];
#pragma unroll
      for (int dd = 0; dd < 4; ++dd)
#pragma unroll
        for (int rp = 0; rp < 4; ++rp) {
          o[2 * dd][rp] = __builtin_amdgcn_perm(wo[ks][2 * rp + 1][dd], wo[ks][2 * rp][dd], 0x05040100u);
          o[2 * dd + 1][rp] = __builtin_amdgcn_perm(wo[ks][2 * rp + 1][dd], wo[ks][2 * rp][dd], 0x07060302u);
        }
#pragma unroll
      for (int t = 0; t < 8; ++t) { wo[ks][t] = o[t]; pin(wo[ks][t]); }
    }
    STAMP(10);
    unsigned ba[4];
#pragma unroll
    for (int ks = 0; ks < 4; ++ks) { ba[ks] = L_A4 + b8 * 272 + ((ks + j) & 3) * 64 + lq * 16; pin(ba[ks]); }
    int oo = my + A_OUTP + b8 * 1024 + w * 256 + (l15 >> 3) * 64 + lq * 16;
    pin(oo);
    __builtin_amdgcn_s_setprio(2);
    bar();
    bar();
    v4u bf[4];
#pragma unroll
    for (int ks = 0; ks < 4; ++ks) bf[ks] = lds128(ba[ks]);
    v4u r0, r1;
#pragma unroll
    for (int tp = 0; tp < 4; ++tp) {
      f32x4 e = z4, o = z4;
#pragma unroll
      for (int ks = 0; ks < 4; ++ks) { e = mfma16(wo[ks][2 * tp], bf[ks], e); o = mfma16(wo[ks][2 * tp + 1], bf[ks], o); }
      r0[tp] = bvalid ? pk(e[0], o[0]) : pk(e[1], o[1]);
      r1[tp] = bvalid ? pk(e[2], o[2]) : pk(e[3], o[3]);
    }
    stg(rs, oo, r0);
    stg(rs, oo + 128, r1);
    STAMP(14);
    if (wave == 8) { DUMP(10); DUMP(14); DUMP(44); }
  } else if (wave >= 4) {
    __builtin_amdgcn_s_setprio(2);
    const int t = w * 64 + lane;
    const u64 tag = get_tag(ws);
    v4u msk;
    {
      const unsigned half = (lane & 1) ? 0xFFFF0000u : 0x0000FFFFu;
      const int dw = b8 >> 1;
      msk = v4u{dw == 0 ? half : 0u, dw == 1 ? half : 0u, dw == 2 ? half : 0u, dw == 3 ? half : 0u};
    }
    v4u sela = msk & (bvalid ? 0x3F803F80u : 0u), selb = msk & (bvalid ? 0u : 0x3F803F80u);
    int ho = arena(32 * j + 16 * (w >> 1) + 2 * lq) + A_H + (w & 1) * 512 + l15 * 16, qo = arena(4 * h) + A_QP + t * 16;
    unsigned hd = L_A1 + (l15 >> 1) * 272 + (32 * w + 16 * (lq >> 1) + 8 * (lane & 1) + 4 * (lq & 1)) * 2;
    unsigned ad = L_A2 + ((t >> 2) & 7) * 272 + (16 * (t >> 5) + 4 * (t & 3)) * 2;
    pin(msk); pin(sela); pin(selb); pin(ho); pin(qo); pin(hd); pin(ad);
    STAMPD(34); STAMPD(46);
    if (w == 2) {
      const unsigned char* fl = ws + WS_EFL + wid * 256;
#pragma unroll 1
      for (int sp = 0; sp < SPIN_H; ++sp) {
        s8u a, b, c, d, e;
        asm volatile("s_load_dwordx8 %0, %5, 0x0 glc\n\ts_load_dwordx8 %1, %5, 0x20 glc\n\ts_load_dwordx8 %2, %5, 0x80 glc\n\t"
                     "s_load_dwordx8 %3, %5, 0xa0 glc\n\ts_load_dwordx8 %4, %5, 0x40 glc\n\ts_waitcnt lgkmcnt(0)"
                     : "=&s"(a), "=&s"(b), "=&s"(c), "=&s"(d), "=&s"(e) : "s"(fl) : "memory");
        const s8u x = (a ^ (unsigned)tag) | (b ^ (unsigned)tag) | (c ^ (unsigned)tag) | (d ^ (unsigned)tag) | (e ^ (unsigned)tag);
        if ((x[0] | x[2] | x[4] | x[6]) == 0) break;
        nap<2>();
      }
      wait_flags(rs, WS_EFL + wid * 256 + (lane < 6 ? lane * 16 : 128 + (lane & 3) * 16), tag);
      STAMPV(46);
    }
    if (w == 3) {
      const unsigned char* hl = ws + WS_HH + wid * 512;
      const unsigned kb = (unsigned)tag + t0 - HWIN;
      unsigned m = 0;
#pragma unroll 1
      for (int sp = 0; sp < SPIN_H; ++sp) {
        v4u a, b, c, d;
        asm volatile("s_load_dwordx4 %0, %4, 0x0 glc\n\ts_load_dwordx4 %1, %4, 0x80 glc\n\ts_load_dwordx4 %2, %4, 0x100 glc\n\t"
                     "s_load_dwordx4 %3, %4, 0x180 glc\n\ts_waitcnt lgkmcnt(0)" : "=&s"(a), "=&s"(b), "=&s"(c), "=&s"(d) : "s"(hl) : "memory");
        m = max(max(umax4(a - kb), umax4(b - kb)), max(umax4(c - kb), umax4(d - kb)));
        if (m < 2 * HWIN) break;
      }
      STAMPV(34);
      wait_until(m + t0 - HWIN + T_H);
    }
    bar();
    int rh = 0, rq = 0;
    {
      f32x4 hv;
#pragma unroll 1
      for (int sp = 0; sp < SPIN_G; ++sp) {
        v4u r[4];
        FENCE();
#pragma unroll
        for (int i = 0; i < 4; ++i) r[i] = ldg(rs, ho + (i & 1) * 256, (i >> 1) * 8 * ASTR);
        __builtin_amdgcn_sched_barrier(0);
        hv = mfma16(selb, r[3], mfma16(sela, r[2], mfma16(selb, r[1], mfma16(sela, r[0], z4))));
        rh = sp + 1;
        if (!__any(hv.x != hv.x) || !any_sent<4>(r)) break;
      }
      *(LAS v2u*)hd = v2u{pk(hv.x, hv.y), pk(hv.z, hv.w)};
    }
    STAMP(5);
    bar();
    bar();
    gather4_act(rs, qo, ASTR, ad, rq);
    STAMP(7);
    bar();
    bar();
    if (j == 1 || j == 2)
      *(LAS v4u*)(L_SC + (2 * w + (lane >> 5)) * 528 + (lane & 31) * 16) =
          silu8(*(const GAS v4u*)(p.c + (2 * w + (lane >> 5)) * 512 + (j - 1) * 256 + (lane & 31) * 8));
    if (w == 3) {
      nap<D_FILL>();
#pragma unroll 1
      for (int n = wid < 8 ? 9 : 10; n < (wid < 56 ? 26 : 18); ++n)
        stg(rs, my + lane * 16 + n * 1024, v4u{SENT, SENT, SENT, SENT});
      VMCNT(0);
      if (lane == 0) __hip_atomic_store((GAS u64*)(ws + WS_FLAG2 + wid * 8), tag, RLX_AGENT);
    }
    bar();
    if (w == 0) wait_flags(rs, WS_FLAG2 + lane * 16, tag);
    int mo = arena(32 * w + (lane & 31)) + A_ML + h * 16;
    int po = my + A_PART + w * 8192 + lane * 16;
    unsigned o3 = L_A3 + (2 * w + (lane >> 5)) * 272 + l15 * 16 + (lq & 1) * 8;
    int hb = lane < 16 ? WS_HB + (16 * lane + ((wi - 5 * g) & 15)) * 128 + g * 4 : OOB;
    int co = lane < 32 ? mo + (A_CK - A_ML) + j * 512 : OOB;
    pin(mo); pin(po); pin(o3); pin(hb); pin(co);
    wait_hints(ws + WS_ARR + (4 * h + w) * 128, (unsigned)(tag & 255) * 0x01010101u, 0);
    STAMP(11);
    nap<D_ML>();
    FENCE();
    v4u ml = ldg(rs, mo);
    nap<D_PART>();
    v4u d[8], ck;
    load_n<8>(rs, po, 1024, d);
    ck = ldg(rs, co);
    __builtin_amdgcn_sched_barrier(0);
    if (__builtin_expect(!__all(ml.y == (unsigned)tag && ml.w == (unsigned)(tag >> 32)), 0)) {
#pragma unroll 1
      for (int sp = 0; sp < SPIN_G; ++sp) {
        FENCE();
        ml = ldg(rs, mo);
        if (__all(ml.y == (unsigned)tag && ml.w == (unsigned)(tag >> 32))) break;
      }
    }
    v4u sel[8];
    {
      const float r = __uint_as_float(ml.x), e = __builtin_amdgcn_exp2f(r - row16_max(r));
      const float wgt = e * __builtin_amdgcn_rcpf(row16_sum(e * __uint_as_float(ml.z)));
#pragma unroll
      for (int i = 0; i < 8; ++i) {
        const float wv = __shfl(wgt, 4 * i + lq);
        const unsigned hi = pk(wv, wv);
        const float rest = wv - bflo(hi);
        const unsigned v = bvalid ? hi : pk(rest, rest);
        sel[i] = msk & v;
        pin(sel[i]);
      }
    }
    f32x4 oa, ob;
    int rp = 0;
#pragma unroll 1
    for (int sp = 0; sp < SPIN_G; ++sp) {
      oa = z4; ob = z4;
#pragma unroll
      for (int i = 0; i < 4; ++i) { oa = mfma16(sel[i], d[i], oa); ob = mfma16(sel[4 + i], d[4 + i], ob); }
      rp = sp + 1;
      unsigned xe = 0, xo = 0;
#pragma unroll
      for (int i = 0; i < 8; ++i) { xe ^= d[i].x ^ d[i].z; xo ^= d[i].y ^ d[i].w; }
      unsigned x = xor_rows2(xe ^ ck.x, xo ^ ck.z);
      auto y = __builtin_amdgcn_permlane16_swap(x, x, false, false);
      x = y[0] ^ y[1];
      y = __builtin_amdgcn_permlane32_swap(x, x, false, false);
      if (__all(y[0] == y[1] && (lane >= 32 || (ck.y == (unsigned)tag && ck.w == (unsigned)(tag >> 32))))) break;
      load_n<8>(rs, po, 1024, d);
      ck = ldg(rs, co);
    }
    STAMP(12);
    {
      f32x4 s;
#pragma unroll
      for (int r = 0; r < 4; ++r) {
        auto x = __builtin_amdgcn_permlane32_swap(__float_as_uint(oa[r]), __float_as_uint(ob[r]), false, false);
        s[r] = __uint_as_float(x[0]) + __uint_as_float(x[1]);
      }
      *(LAS v2u*)o3 = v2u{pk(s.x, s.y), pk(s.z, s.w)};
    }
    bar();
    if (w == 1) st32(rs, hb, now32() + (unsigned)tag);
    bar();
    if (w == 2) DUMP(46);
    if (w == 3) { DUMP(5); DUMP(7); DUMP(34); DUMP(11); DUMP(12); DUMPC(41, rp); DUMPC(48, rh); DUMPC(49, rq); }
    if (w == 0) {
      const int col = 8 * (lane & 3) + 4 * (lq & 1);
      const v2u xb = *(const GAS v2u*)(p.x + g * 512 + 32 * wi + col);
      v2u gg[4];
      const int go = arena(4 * (wi >> 1) + 1) + A_HM + g * 128 + ((wi & 1) * 32 + col) * 2;
#pragma unroll 1
      for (int sp = 0; sp < SPIN_G; ++sp) {
        FENCE();
        unsigned m = 0;
#pragma unroll
        for (int i = 0; i < 4; ++i) gg[i] = __builtin_amdgcn_raw_buffer_load_b64(rs, go, ((i >> 1) * 96 + (i & 1)) * ASTR, SC1);
#pragma unroll
        for (int i = 0; i < 4; ++i) m = max(m, max(gg[i].x, gg[i].y));
        if (!__any(m == SENT)) break;
      }
      f32x4 x0 = up4(xb), ga = up4(gg[0]) + up4(gg[1]), gb = up4(gg[2]) + up4(gg[3]);
      pin(x0); pin(ga); pin(gb);
      STAMP(26);
      const bool act = lane < 32 && l15 < 4;
      v4u one = sela;
      int oo = arena(lane >> 2) + A_OUTP + g * 1024 + wi * 64 + (lane & 3) * 16;
      int yo = oo + (A_YP - A_OUTP);
      int yl = lane < 32 ? yo : OOB - 48 * ASTR;
      int xo = act ? arena(g) + A_X1 + (32 * wi + col) * 2 : OOB;
      const rsrc_t ro = __builtin_amdgcn_make_buffer_rsrc(p.out, 0, 8192, 0x00020000);
      int fo = act ? (g * 512 + 32 * wi + col) * 2 : OOB;
      pin(one); pin(oo); pin(yo); pin(yl); pin(xo); pin(fo);
      const unsigned tb = latest_time(ws + WS_HB + (128 + wid) * 128, t0, (unsigned)tag);
      wait_until(tb + T_B);
      int r1 = 0, r2 = 0;
      const f32x4 x1 = x0 + ga * reduce_rows<8>(rs, oo, oo, one, r1);
      __builtin_amdgcn_raw_buffer_store_b64(v2u{pk(x1.x, x1.y), pk(x1.z, x1.w)}, rs, xo, 0, SC1);
      STAMP(27);
      wait_until(tb + T_D);
      const f32x4 r = x1 + gb * reduce_rows<4>(rs, yo, yl, one, r2);
      STAMP(29);
      __builtin_amdgcn_raw_buffer_store_b64(v2u{pk(r.x, r.y), pk(r.z, r.w)}, ro, fo, 0, 0);
      if (wid == 0 && lane == 0) __hip_atomic_store((GAS u64*)ws, tag, RLX_AGENT);
      STAMP(30);
      DUMP(26); DUMP(27); DUMP(29); DUMP(30); DUMPC(42, r1); DUMPC(43, r2);
    }
  } else {
    __builtin_amdgcn_s_setprio(2);
    const int p4 = 4 * (lane & 3);
    unsigned act = b8 * 272 + lq * 16;
    unsigned s0 = tr_addr(W2, lane, 16 * w + p4, 0), s1 = tr_addr(W2, lane, 16 * w + p4, 1);
    unsigned ba = L_SC + b8 * 528 + lq * 16;
    v4u f[2][4];
    STAMPD(1);
    if (w == 3) {
      const v4u sv = {SENT, SENT, SENT, SENT};
#pragma unroll 1
      for (int n = 2; n < 9; ++n) stg(rs, my + lane * 16 + n * 1024, sv);
      const u64 tag = get_tag(ws);
      const int fo = lane < 4 ? WS_EFL + (4 * h + lane) * 256 + 64 + j * 8 : lane == 4 ? WS_FLAG1 + wid * 8 : OOB;
      VMCNT(0);
      __builtin_amdgcn_raw_buffer_store_b64(v2u{(unsigned)tag, (unsigned)(tag >> 32)}, rs, fo, 0, SC1);
      STAMPV(1);
    }
    bar();
    STAMP(4);
    panel_frags(W0, lane, 32 * w + p4, 32 * w + 16 + p4, f);
    {
      int o = my + A_QP + w * 1024 + l15 * 64 + lq * 16;
      pin(o); pin(act);
      bar();
      f32x4 q0 = z4, q1 = z4;
      gemv2(f, L_A1 + act, q0, q1);
      stg(rs, o, pick(bvalid, f2u(q0), f2u(q1)));
    }
    STAMP(6);
    unsigned tl;
    {
      unsigned ka[2];
#pragma unroll
      for (int tt = 0; tt < 2; ++tt) {
        ka[tt] = W1 + (32 * w + 8 * (l15 >> 2) + 4 * tt + (l15 & 3)) * 256 + 16 * (lq ^ (((l15 & 3) << 2) | ((2 * (l15 >> 2) + tt) & 3)));
        pin(ka[tt]);
      }
      int o = bvalid ? my + A_QA + l15 * 256 + w * 64 + lq * 16 : OOB;
      pin(o);
#pragma unroll
      for (int tt = 0; tt < 2; ++tt)
#pragma unroll
        for (int ks = 0; ks < 4; ++ks) f[tt][ks] = lds128(ka[tt] ^ (ks * 64));
      bar();
      tl = now32() + T_L;
      bar();
      f32x4 a0 = z4, a1 = z4;
      gemv2(f, L_A2 + act, a0, a1);
      stg(rs, o, pk8(a0 * QSCALE, a1 * QSCALE));
    }
    STAMP(8);
    bar();
    wait_until(tl);
    dma_run<true>(p.wuv, vuv, W0 + dst0, 8, 2 * 8192, 4096);
    const bool m2 = j == 1 || j == 2;
    if (m2) dma_run<true>(p.wmod, (256 * (j - 1) + 4 * w + lq + (ch >> 3) * 128) * 6144 + 2048 + h * 128 + (ch & 7) * 16, W2 + dst0, 8, 16 * 6144, 4096);
    VMCNT(0);
    bar();
    STAMP(9);
    if (m2) {
      f32x4 m0 = z4, m1 = z4;
#pragma unroll 1
      for (int ks = 0; ks < 4; ++ks) {
        m0 = mfma16(tr_frag(s0 + ks * 8192, s1 + ks * 8192, 0), lds128(ba + ks * 64), m0);
        m1 = mfma16(tr_frag((s0 ^ 128) + ks * 8192, (s1 ^ 128) + ks * 8192, 0), lds128(ba + ks * 64, 256), m1);
      }
      m0 += m1;
      __builtin_amdgcn_raw_buffer_store_b64(v2u{pk(m0.x, m0.y), pk(m0.z, m0.w)}, rs, bvalid ? my + A_HM + l15 * 128 + w * 32 + lq * 8 : OOB, 0, SC1);
    }
    panel_frags(W0, lane, 32 * w + 2 * p4, 32 * w + 2 * p4 + 4, f);
    unsigned o = bvalid ? L_A4 + l15 * 272 + w * 64 + lq * 16 : L_JUNK;
    pin(o);
    bar();
    STAMP(35);
    f32x4 c0 = z4, c1 = z4;
    gemv2(f, L_A3 + act, c0, c1);
    *(LAS v4u*)o = pk8(c0, c1);
    bar();
    STAMP(13);
    if (w == 3) DUMP(1);
    if (w == 0) { DUMP(35); DUMP(13); DUMP(4); DUMP(6); DUMP(8); DUMP(9); }
  }
}

DI void mfma_pair(f32x4& d0, f32x4& d1, v4u a0, v4u a1, v4u b) {
  asm volatile("v_mfma_f32_16x16x32_bf16 %0, %2, %4, %0\n\tv_mfma_f32_16x16x32_bf16 %1, %3, %4, %1" : "+v"(d0), "+v"(d1) : "v"(a0), "v"(a1), "v"(b));
}
DI void mfma_pair0(f32x4& d0, f32x4& d1, v4u a0, v4u a1, v4u b) {
  asm volatile("v_mfma_f32_16x16x32_bf16 %0, %2, %4, 0\n\tv_mfma_f32_16x16x32_bf16 %1, %3, %4, 0" : "=&v"(d0), "=&v"(d1) : "v"(a0), "v"(a1), "v"(b));
}
template <int OFF> DI void rd_inplace(v4u& a, unsigned ad) { asm volatile("ds_read_b128 %0, %1 offset:%2" : "+v"(a) : "v"(ad), "i"(OFF) : "memory"); }
template <int O0, int O1> DI void peek2(unsigned& a, unsigned& b, unsigned cb) {
  asm volatile("ds_read_b32 %0, %2 offset:%3\n\tds_read_b32 %1, %2 offset:%4" : "=&v"(a), "=&v"(b) : "v"(cb), "i"(O0), "i"(O1) : "memory");
}
template <int W> DI void wait2(unsigned& a, unsigned& b) { asm volatile("s_waitcnt lgkmcnt(%2)" : "+v"(a), "+v"(b) : "i"(W)); }
template <int W, bool FIRST> DI void score_pair(f32x4& s0, f32x4& s1, v4u a, v4u q0, v4u q1) {
  if constexpr (FIRST)
    asm volatile("s_waitcnt lgkmcnt(%5)\n\tv_mfma_f32_16x16x32_bf16 %0, %2, %3, 0\n\tv_mfma_f32_16x16x32_bf16 %1, %2, %4, 0"
                 : "=&v"(s0), "=&v"(s1) : "v"(a), "v"(q0), "v"(q1), "i"(W));
  else
    asm volatile("s_waitcnt lgkmcnt(%5)\n\tv_mfma_f32_16x16x32_bf16 %0, %2, %3, %0\n\tv_mfma_f32_16x16x32_bf16 %1, %2, %4, %1"
                 : "+v"(s0), "+v"(s1) : "v"(a), "v"(q0), "v"(q1), "i"(W));
}
template <int OFF> DI void put_scores(unsigned sa, f32x4 s0, f32x4 s1) {
  asm volatile("s_nop 4\n\tds_write_b128 %0, %1 offset:%3\n\tds_write_b128 %0, %2 offset:%4" :: "v"(sa), "v"(s0), "v"(s1), "i"(OFF), "i"(OFF + 1024) : "memory");
}
template <int V> struct IntC { static constexpr int value = V; };
template <int I, int N, class F> DI void static_for(F&& f) { if constexpr (I < N) { f(IntC<I>{}); static_for<I + 1, N>(f); } }
constexpr unsigned slot_c(int t) { return ((0x02310210u >> ((4 * t) & 31)) & 3) * 32768; }
template <unsigned A> DI void csig(unsigned cb, unsigned v) {
  asm volatile("s_mov_b64 exec, 1\n\tds_add_u32 %0, %1 offset:%2\n\ts_mov_b64 exec, -1" :: "v"(cb), "v"(v), "i"(A - T_CNT) : "memory");
}
template <unsigned A> DI unsigned cpeek(unsigned cb) { return *(volatile LAS unsigned*)((LAS char*)cb + (A - T_CNT)); }
#define SPIN_HEAD "s_mov_b32 %[n], 0x800000\n1:\n\ts_sub_u32 %[n], %[n], 1\n\ts_cbranch_scc1 2f\n\t"
#define SPIN_TAIL "s_waitcnt lgkmcnt(0)\n\tv_cmp_gt_u32 vcc, %[w], %[c]\n\ts_cbranch_vccnz 1b\n2:"
template <unsigned A, unsigned W> DI void cwait(unsigned cb) {
  unsigned c, n;
  asm volatile(SPIN_HEAD "ds_read_b32 %[c], %[cb] offset:%[o]\n\t" SPIN_TAIL
               : [c] "=&v"(c), [n] "=&s"(n) : [cb] "v"(cb), [o] "i"(A - T_CNT), [w] "i"(W) : "vcc", "scc", "memory");
}
DI constexpr unsigned fsw(int row) { return ((row & 3) << 2) | ((row >> 1) & 2) | ((row >> 3) & 1); }
DI unsigned slot_of(int t) { return ((0x02310210u >> ((4 * t) & 31)) & 3) * 32768; }
#define DMA4(AUX, base, dst, v0, v1, v2, v3) \
  asm volatile("s_mov_b32 m0, %5\n\ts_waitcnt vmcnt(31)\n\tglobal_load_lds_dwordx4 %0, %4 " AUX "\n\t" \
               "s_waitcnt vmcnt(31)\n\tglobal_load_lds_dwordx4 %1, %4 offset:1024 " AUX "\n\t" \
               "s_waitcnt vmcnt(31)\n\tglobal_load_lds_dwordx4 %2, %4 offset:2048 " AUX "\n\t" \
               "s_waitcnt vmcnt(31)\n\tglobal_load_lds_dwordx4 %3, %4 offset:3072 " AUX \
               :: "v"(v0), "v"(v1), "v"(v2), "v"(v3), "s"(base), "s"(dst) : "memory")

DI void ffn_part(const Params& p, int b, int s, int wave, unsigned t0, u64 tag);
DI void attn_wg(const Params& p, int b, int s, int wave, int lane, unsigned t0) {
  unsigned char* ws = p.ws;
  const rsrc_t rs = __builtin_amdgcn_make_buffer_rsrc(ws, 0, WS_BYTES, 0x00020000);
  const int l15 = lane & 15, lq = lane >> 4, w = wave & 3;
  const f32x4 z4 = {0.f, 0.f, 0.f, 0.f};

  if (wave == 0) *(LAS unsigned*)(T_CNT + lane * 4) = lane < 8 ? 8u : 0u;
  u64 tag = 0;
  if (wave >= 4 && wave != 8) tag = get_tag(ws);
  bar();
  STAMP(59);
  if (wave == 8) {
    unsigned vo[16];
#pragma unroll
    for (int r = 0; r < 16; ++r) vo[r] = 16 * (lane ^ fsw(r));
    const unsigned char* kv = (const unsigned char*)p.ckv + (size_t)(b * 4096 + s * 32) * 1024;
    nap<1>();
    wait_until(t0 + T_PREF);
    STAMP(33); STAMPD(31);
#pragma unroll 1
    for (int t = 0; t < 8; ++t) {
      bool told = false;
      if (t >= 3) {
        const unsigned c = t == 5 ? C_QRD : C_FREE + 16 * (t < 5 ? t - 3 : t - 4), want = t == 5 ? 4 : 8;
        if (__builtin_amdgcn_readfirstlane(lpeek(c)) < want) { VMCNT(0); lsig(C_LAND + 16 * t - 16); told = true; lwait<true>(c, want); }
      }
      const unsigned dst = slot_of(t);
#pragma unroll
      for (int i = 0; i < 8; ++i)
        DMA4("nt", kv + i * 4096, dst + i * 4096, vo[4 * (i & 3)], vo[4 * (i & 3) + 1], vo[4 * (i & 3) + 2], vo[4 * (i & 3) + 3]);
      if (!told) lsig(C_LAND + 16 * t - 16);
      kv += 16 * 32768;
    }
    STAMP(22);

    VMCNT(0);
    lsig(C_LAND + 16 * 7);
    STAMP(23);
    if (s >= 8 || b == 7) { DUMP(33); DUMP(31); DUMP(22); DUMP(23); }
    return;
  }
  __builtin_amdgcn_s_setprio(2);
  if (wave == 9) {
    unsigned vo[16];
#pragma unroll
    for (int r = 0; r < 16; ++r) { vo[r] = lq * ASTR + (r & 3) * (4 * ASTR - 1024) + 16 * (l15 ^ fsw(r)); pin(vo[r]); }
    const unsigned char* qa = ws + WS_ARENA + A_QA + b * 256;
    wait_until(t0 + T_FLAG);
    wait_flags(rs, WS_FLAG1 + lane * 16, tag);
    STAMP(17); STAMPD(47);
    {
      const unsigned char* hl = ws + WS_HQ + (16 * b + s) * 512;
      const unsigned kb = (unsigned)tag + t0 - HWIN;
      unsigned m = 0;
#pragma unroll 1
      for (int sp = 0; sp < SPIN_H; ++sp) {
        s8u a, c, d, e;
        asm volatile("s_load_dwordx8 %0, %4, 0x0 glc\n\ts_load_dwordx8 %1, %4, 0x80 glc\n\ts_load_dwordx8 %2, %4, 0x100 glc\n\t"
                     "s_load_dwordx8 %3, %4, 0x180 glc\n\ts_waitcnt lgkmcnt(0)" : "=&s"(a), "=&s"(c), "=&s"(d), "=&s"(e) : "s"(hl) : "memory");
        m = max(max(max8(a - kb), max8(c - kb)), max(max8(d - kb), max8(e - kb)));
        if (m < 2 * HWIN) break;
      }
      STAMPV(47);
      wait_until(m + t0 - HWIN + T_QA);
    }
    STAMP(18);
#pragma unroll
    for (int i = 0; i < 8; ++i)
      DMA4("sc1", qa + i * (16 * ASTR), 3 * 32768 + i * 4096, vo[4 * (i & 3)], vo[4 * (i & 3) + 1], vo[4 * (i & 3) + 2], vo[4 * (i & 3) + 3]);
    VMCNT(0);
    lsig(C_QST);
    STAMP(38);

    if (s >= 8 || b == 7) { lwait<true>(C_PRDY + 16 * 7, 8); nap<30>(); DUMP(17); DUMP(18); DUMP(38); DUMP(47); DUMP(59); }
    return;
  }
  if (wave >= 10) {
    __builtin_amdgcn_s_setprio(3);
    const int hh = wave - 10;
    unsigned sa = T_SP + hh * 1024 + lane * 16, cb = T_CNT, four = 4u;
    int mlo = lq == 0 ? arena(16 * b + s) + A_ML + hh * 256 + l15 * 16 : OOB;
    int aro = hh == 0 && lane < 32 ? WS_ARR + (4 * lane + (b >> 1)) * 128 + (b & 1) * 16 + s : OOB;
    unsigned tg = (unsigned)tag;
    pin(sa); pin(cb); pin(four); pin(mlo); pin(aro); pin(tg);
    float ref = 0.f, l = 0.f;
    lwait<true>(C_QST, 1);
    static_for<0, 8>([&](auto tc) {
      constexpr int t = decltype(tc)::value;
      if constexpr (t >= 4) cwait<C_FREE + 16 * (t - 2), 8>(cb);
      f32x4 p0, p1, p2, p3;
      unsigned c, n;
      asm volatile(SPIN_HEAD "ds_read_b32 %[c], %[cb] offset:%[o]\n\tds_read_b128 %[p0], %[sa] offset:%[s0]\n\tds_read_b128 %[p2], %[sa] offset:%[s2]\n\t"
                   "ds_read_b128 %[p1], %[sa] offset:%[s1]\n\tds_read_b128 %[p3], %[sa] offset:%[s3]\n\t" SPIN_TAIL
                   : [c] "=&v"(c), [n] "=&s"(n), [p0] "=&v"(p0), [p1] "=&v"(p1), [p2] "=&v"(p2), [p3] "=&v"(p3)
                   : [cb] "v"(cb), [sa] "v"(sa), [o] "i"(C_SRDY + 16 * t - T_CNT), [w] "i"(4), [s0] "i"((t & 1) * 8192),
                     [s1] "i"((t & 1) * 8192 + 2048), [s2] "i"((t & 1) * 8192 + 4096), [s3] "i"((t & 1) * 8192 + 6144) : "vcc", "scc", "memory");
      if constexpr (t == 7) __builtin_amdgcn_raw_buffer_store_b8((unsigned char)tg, rs, aro, 0, SC1);
      f32x4 a = (p0 - ref) + p2, c4 = (p1 - ref) + p3;
      const float m = fmaxf(fmaxf(fmaxf(a.x, a.y), fmaxf(a.z, a.w)), fmaxf(fmaxf(c4.x, c4.y), fmaxf(c4.z, c4.w)));
      unsigned sig = four;
      if constexpr (t == 0) {
        ref = xmax32(xmax16(m));
        a -= ref; c4 -= ref;
      } else if (__builtin_expect(__any(m > BUMP), 0)) {
        const float up = fmaxf(0.f, xmax32(xmax16(m)));
        const float fa = __builtin_amdgcn_exp2f(-up);
        *(LAS float*)(T_FAC + t * 128 + hh * 64 + l15 * 4) = fa;
        l *= fa;
        ref += up;
        a = (p0 - ref) + p2; c4 = (p1 - ref) + p3;
        sig = 4u + (1u << hh);
      }
      const v4u pf = {pk(__builtin_amdgcn_exp2f(a.x), __builtin_amdgcn_exp2f(a.y)), pk(__builtin_amdgcn_exp2f(a.z), __builtin_amdgcn_exp2f(a.w)),
                      pk(__builtin_amdgcn_exp2f(c4.x), __builtin_amdgcn_exp2f(c4.y)), pk(__builtin_amdgcn_exp2f(c4.z), __builtin_amdgcn_exp2f(c4.w))};
      *(LAS v4u*)((LAS char*)sa + (T_P - T_SP) + (t & 3) * 2048) = pf;
      csig<C_PRDY + 16 * t>(cb, sig);
#pragma unroll
      for (int e = 0; e < 4; ++e) {
        const unsigned u = pf[e];
        l = __builtin_amdgcn_fdot2_f32_bf16(__builtin_bit_cast(bf16x2, u), __builtin_bit_cast(bf16x2, 0x3F803F80u), l, false);
      }
    });
    stg(rs, mlo, v4u{__float_as_uint(ref), (unsigned)tag, __float_as_uint(xsum32(xsum16(l))), (unsigned)(tag >> 32)});
    STAMP(24);
    if (hh == 0 && (s >= 8 || b == 7)) DUMP(24);
    return;
  }
  if (wave < 4) {
    __builtin_amdgcn_s_setprio(PRIO_S);
    const int kh = wave & 1, ch = wave >> 1;
    const int sg = (0x2130 >> (4 * lq)) & 3;
    unsigned ao[4];
#pragma unroll
    for (int k = 0; k < 4; ++k) ao[k] = l15 * 1024 + ch * 512 + 16 * ((4 * k + sg) ^ fsw(l15));
    unsigned sa = T_SP + wave * 2048 + lane * 16, cb = T_CNT, one = 1u;
    unsigned al[4], ah[4];
#pragma unroll
    for (int k = 0; k < 4; ++k) { al[k] = ao[k] + kh * 16384; ah[k] = al[k] + 65536; pin(al[k]); pin(ah[k]); }
    pin(sa); pin(cb); pin(one);
    v4u af[2][8], qf[2][8];
    auto rd = [&](unsigned sl, int ks) { return lds128(sl < 65536 ? al[ks & 3] : ah[ks & 3], (sl & 32768) + (ks >> 2) * 256); };
#pragma unroll
    for (int t = 0; t < 2; ++t) {
      lwait<true>(C_LAND + 16 * t, 1);
#pragma unroll
      for (int ks = 0; ks < 8; ++ks) af[t][ks] = rd(t * 32768, ks);
      lsig(C_FREE + 16 * t);
    }
    unsigned qa[4];
#pragma unroll
    for (int k = 0; k < 4; ++k) { qa[k] = 3 * 32768 + ao[k]; pin(qa[k]); }
    lwait(C_QST, 1);
    STAMP(19);
    unsigned nx2 = cpeek<C_LAND + 32>(cb), nxt = cpeek<C_LAND + 48>(cb);
    f32x4 s0, s1;
    int rqa = 0;
#pragma unroll 1
    for (int sp = 0; sp < SPIN_G; ++sp) {
      rqa = sp + 1;
#pragma unroll
      for (int ks = 0; ks < 8; ++ks) {
#pragma unroll
        for (int hh = 0; hh < 2; ++hh) qf[hh][ks] = lds128(qa[ks & 3], hh * 16384 + (ks >> 2) * 256);
        __builtin_amdgcn_sched_barrier(0);
      }
      s0 = z4; s1 = z4;
#pragma unroll
      for (int ks = 0; ks < 8; ++ks) { s0 = mfma16(af[0][ks], qf[0][ks], s0); s1 = mfma16(af[0][ks], qf[1][ks], s1); }
      if (__builtin_expect(!__any(__builtin_isunordered(s0.x, s1.x)), 1) || !(any_sent<8>(qf[0]) || any_sent<8>(qf[1]))) break;
#pragma unroll 1
      for (int r = 0; r < 32; ++r)
        asm volatile("s_mov_b32 m0, %2\n\ts_nop 0\n\tglobal_load_lds_dwordx4 %0, %1 sc1"
                     :: "v"(lq * ASTR + 16 * (l15 ^ fsw(r))), "s"(ws + WS_ARENA + A_QA + b * 256 + r * (4 * ASTR)), "s"(3 * 32768 + r * 1024) : "memory");
      VMCNT(0);
    }
    *(LAS f32x4*)sa = s0;
    *(LAS f32x4*)((LAS char*)sa + 1024) = s1;
    csig<C_SRDY>(cb, one);
    csig<C_QRD>(cb, one);
    STAMP(20);
    auto refill = [&](v4u& a, auto slc, auto ksc) {
      constexpr unsigned sl = decltype(slc)::value;
      constexpr int ks = decltype(ksc)::value;
      rd_inplace<(sl & 32768) + (ks >> 2) * 256>(a, sl < 65536 ? al[ks & 3] : ah[ks & 3]);
    };
    static_for<0, 8>([&](auto k) { refill(af[0][decltype(k)::value], IntC<slot_c(2)>{}, k); });
    bool miss = __any(nx2 < 1);
    if (!miss) csig<C_FREE + 32>(cb, one);
    static_for<1, 8>([&](auto tc) {
      constexpr int t = decltype(tc)::value;
      const bool was = miss;
      if constexpr (t < 6) miss = __any(nxt < 1);
      unsigned use;
      peek2<C_PRDY + 16 * (t - 2) - T_CNT, C_LAND + 16 * (t < 5 ? t + 3 : 8) - T_CNT>(use, nxt, cb);
      static_for<0, 8>([&](auto k) {
        constexpr int ks = decltype(k)::value;
        score_pair<(t < 6 ? 9 : 9 - ks), ks == 0>(s0, s1, af[t & 1][ks], qf[0][ks], qf[1][ks]);
        if constexpr (t < 6) refill(af[t & 1][ks], IntC<slot_c(t + 2)>{}, k);
      });
      wait2<(t < 6 ? 8 : 0)>(use, nxt);
      if (__builtin_expect(__any(use < 8), 0)) cwait<C_PRDY + 16 * (t - 2), 8>(cb);
      put_scores<(t & 1) * 8192>(sa, s0, s1);
      csig<C_SRDY + 16 * t>(cb, one);
      if constexpr (t < 4) { if (!miss) csig<C_FREE + 16 * (t + 2)>(cb, one); }
      if constexpr (t < 7) {
        if (was) {
          cwait<C_LAND + 16 * (t + 1), 1>(cb);
          static_for<0, 8>([&](auto k) { refill(af[(t + 1) & 1][decltype(k)::value], IntC<slot_c(t + 1)>{}, k); });
          if constexpr (t < 5) csig<C_FREE + 16 * (t + 1)>(cb, one);
        }
      }
    });
    asm volatile("s_waitcnt lgkmcnt(0)" ::: "memory");
    STAMP(21);
    if (wave == 0 && (s >= 8 || b == 7)) { DUMP(19); DUMP(20); DUMP(21); DUMPC(50, rqa); }
  } else {
    __builtin_amdgcn_s_setprio(PRIO_PV);
    unsigned bo[2];
    {
      const int q = l15 >> 2, pp = lane & 3;
#pragma unroll
      for (int c = 0; c < 2; ++c) {
        bo[c] = (4 * lq + q) * 1024 + w * 256 + 16 * ((2 * c + (pp >> 1)) ^ fsw(4 * lq + q)) + 8 * (pp & 1);
      }
    }
    unsigned pa = T_P + lane * 16, cb = T_CNT, one = 1u;
    int po = arena(16 * lq + w) + A_PART + (b * 16 + s) * 256 + l15 * 16;
    unsigned vl[8];
#pragma unroll
    for (int ct = 0; ct < 8; ++ct) { vl[ct] = bo[ct & 1] ^ ((ct >> 1) * 64); pin(vl[ct]); }
    int cko = (lane & 2) ? OOB : arena(16 * b + s) + A_CK + w * 512 + (16 * (l15 >> 3) + 4 * lq + 2 * ((l15 >> 2) & 1) + (lane & 1)) * 16;
    pin(pa); pin(cb); pin(one); pin(po); pin(cko);
    f32x4 acc[2][8];
    v4u vf[2][8];
    auto rd = [&](unsigned sl, int ct) {
      unsigned a = vl[ct];
      if (sl >= 65536) asm volatile("v_add_u32 %0, 0x10000, %1" : "=v"(a) : "v"(vl[ct]));
      const v2u lo = ldstr(a, sl & 32768), hi = ldstr(a, (sl & 32768) + 16384);
      return v4u{lo.x, lo.y, hi.x, hi.y};
    };
#pragma unroll
    for (int t = 0; t < 2; ++t) {
      lwait<true>(C_LAND + 16 * t, 1);
#pragma unroll
      for (int ct = 0; ct < 8; ++ct) vf[t][ct] = rd(t * 32768, ct);
      lsig(C_FREE + 16 * t);
    }
    bool miss = false;
    lwait<true>(C_SRDY, 1);
    static_for<0, 8>([&](auto tc) {
      constexpr int t = decltype(tc)::value;
      v4u p0, p1;
      unsigned c, n, nxt;
      asm volatile(SPIN_HEAD "ds_read_b32 %[c], %[cb] offset:%[o]\n\tds_read_b128 %[p0], %[pa] offset:%[s0]\n\t"
                   "ds_read_b128 %[p1], %[pa] offset:%[s1]\n\tds_read_b32 %[x], %[cb] offset:%[ox]\n\t" SPIN_TAIL
                   : [c] "=&v"(c), [n] "=&s"(n), [p0] "=&v"(p0), [p1] "=&v"(p1), [x] "=&v"(nxt)
                   : [cb] "v"(cb), [pa] "v"(pa), [o] "i"(C_PRDY + 16 * t - T_CNT), [w] "i"(8), [s0] "i"((t & 3) * 2048),
                     [s1] "i"((t & 3) * 2048 + 1024), [ox] "i"(C_LAND + 16 * (t + 2) - T_CNT) : "vcc", "scc", "memory");
      if constexpr (t > 0) {
        if (__builtin_expect(__any(c > 8u), 0)) {
#pragma unroll 1
          for (int ht = 0; ht < 2; ++ht)
            if (!__any(c & (1u << ht))) *(LAS float*)(T_FAC + t * 128 + ht * 64 + l15 * 4) = 1.f;
          const f32x4 f0 = u2f(lds128(T_FAC + t * 128 + lq * 16)), f1 = u2f(lds128(T_FAC + t * 128 + lq * 16, 64));
#pragma unroll
          for (int ct = 0; ct < 8; ++ct) { acc[0][ct] *= f0; acc[1][ct] *= f1; pin(acc[0][ct]); pin(acc[1][ct]); }
          asm volatile("s_nop 4");
        }
      }
      if constexpr (t < 7) {
#pragma unroll
        for (int ct = 0; ct < 8; ++ct) {
          if constexpr (t) mfma_pair(acc[0][ct], acc[1][ct], p0, p1, vf[t & 1][ct]); else mfma_pair0(acc[0][ct], acc[1][ct], p0, p1, vf[0][ct]);
          if constexpr (t < 6) vf[t & 1][ct] = rd(slot_c(t + 2), ct);
          __builtin_amdgcn_sched_barrier(0);
        }
        const bool was = miss;
        if constexpr (t < 6) {
          miss = __any(nxt < 1);
          if constexpr (t < 4) { if (!miss) csig<C_FREE + 16 * (t + 2)>(cb, one); }
        }
        if (was) {
          cwait<C_LAND + 16 * (t + 1), 1>(cb);
#pragma unroll
          for (int ct = 0; ct < 8; ++ct) vf[(t + 1) & 1][ct] = rd(slot_c(t + 1), ct);
          if constexpr (t < 5) csig<C_FREE + 16 * (t + 1)>(cb, one);
        }
      } else {
#pragma unroll
        for (int ct = 0; ct < 8; ++ct) acc[0][ct] = mfma16(p0, vf[1][ct], acc[0][ct]);
#pragma unroll
        for (int ct = 0; ct < 8; ++ct) acc[1][ct] = mfma16(p1, vf[1][ct], acc[1][ct]);
      }
    });
    unsigned ce[8], co[8];
#pragma unroll
    for (int ht = 0; ht < 2; ++ht)
#pragma unroll
      for (int r = 0; r < 4; ++r) {
        const v4u v = {pk(acc[ht][0][r], acc[ht][1][r]), pk(acc[ht][2][r], acc[ht][3][r]), pk(acc[ht][4][r], acc[ht][5][r]),
                       pk(acc[ht][6][r], acc[ht][7][r])};
        stg(rs, po + (16 * ht + r) * 4 * ASTR, v);
        ce[4 * ht + r] = v.x ^ v.z; co[4 * ht + r] = v.y ^ v.w;
      }
    {
      unsigned e0, e1, o0, o1;
      xor_rows8(ce, e0, e1);
      xor_rows8(co, o0, o1);
      const bool odd = cko & 16;
      stg(rs, cko, v4u{odd ? e1 : e0, (unsigned)tag, odd ? o1 : o0, (unsigned)(tag >> 32)});
    }
    STAMP(25);
  }
  if (b == 7) return;
  ffn_part(p, b, s, wave, t0, tag);
}

DI void ffn_part(const Params& p, int b, int s, int wave, unsigned t0, u64 tag) {
  int lane;
  asm volatile("v_mbcnt_lo_u32_b32 %0, -1, 0\n\tv_mbcnt_hi_u32_b32 %0, -1, %0" : "=v"(lane));
  unsigned char* ws = p.ws;
  const rsrc_t rs = __builtin_amdgcn_make_buffer_rsrc(ws, 0, WS_BYTES, 0x00020000);
  const int l15 = lane & 15, lq = lane >> 4, w = wave & 3;
  const f32x4 z4 = {0.f, 0.f, 0.f, 0.f};
  __builtin_amdgcn_s_setprio(2);
  const int fg = 8 * b + s, b8 = lane & 7, p4 = 4 * (lane & 3);
  const bool bvalid = l15 < 8;
  const int ch = l15 ^ ((lq << 2) | w), row0 = 4 * w + lq;
  bar();
  STAMP(52);
  nap<D_F>();
  if (s < 8) dma_run<PF_NT>(p.w1, (64 * s + 8 * wave + (lane >> 5)) * 3584 + b * 512 + (lane & 31) * 16, W2 + wave * 1024, 4, 2 * 3584, 0);
  else dma_run<PF_NT>(p.w2, (256 * b + 32 * (s - 8) + 4 * wave) * 1024 + lane * 16, W2 + wave * 1024, 4, 1024, 0);
  if (s >= 8) return;
  v4u mm[4];
  const int mo = arena(33 + 4 * (lane >> 3)) + A_HM + wave * 128 + b8 * 16;
  auto load_mod = [&]() {
    FENCE();
#pragma unroll
    for (int i = 0; i < 4; ++i) mm[i] = ldg(rs, mo, ((i >> 1) * 32 + (i & 1)) * ASTR);
    FENCE();
  };
  load_mod();
  if (wave < 4) {
    dma_run<false>(p.w1, (row0 + (ch >> 2) * 128) * 3584 + fg * 64 + (ch & 3) * 16, W0 + w * 1024, 8, 16 * 3584, 4096);
  } else {
    const unsigned u = (32 * fg + row0) * 1024 + ch * 16;
    dma_run<true>(p.w2, u, W1 + w * 1024, 4, 256, 8192);
    dma_run<true>(p.w2, u + 16 * 1024, W1 + w * 1024 + 4096, 4, 256, 8192);
  }
  STAMP(54);
#pragma unroll 1
  for (int sp = 0; sp < SPIN_G; ++sp) {
    if (!any_sent<4>(mm)) break;
    load_mod();
  }
  f32x4 sb[2], cb[2];
  sb[0] = up4(mm[0].xy) + up4(mm[1].xy); sb[1] = up4(mm[0].zw) + up4(mm[1].zw);
  cb[0] = up4(mm[2].xy) + up4(mm[3].xy) + 1.f; cb[1] = up4(mm[2].zw) + up4(mm[3].zw) + 1.f;
  pin(sb[0]); pin(sb[1]); pin(cb[0]); pin(cb[1]);
  if (wave == 7) wait_flags(rs, WS_FLAG2 + lane * 16, tag);
  VMCNT(0);
  STAMP(56);
  bar();
  STAMP(28);
  v4u fw[16];
  if (wave < 2) {
#pragma unroll
    for (int kq = 0; kq < 4; ++kq) {
      const unsigned a0 = tr_addr(W0, lane, 32 * kq + 16 * w + p4, 0), a1 = tr_addr(W0, lane, 32 * kq + 16 * w + p4, 1);
#pragma unroll
      for (int ks = 0; ks < 4; ++ks) fw[4 * kq + ks] = tr_frag(a0, a1, ks);
    }
  } else {
#pragma unroll
    for (int i = 0; i < 8; ++i) {
      const int c4 = 32 * (i >> 1) + 2 * p4 + 4 * (i & 1);
      fw[i] = tr_frag(tr_addr(W1 + w * 8192, lane, c4, 0), tr_addr(W1 + w * 8192, lane, c4, 1), 0);
      fw[8 + i] = fw[i];
    }
  }
  int xo = arena(wave) + A_X1 + lane * 16;
  unsigned o5 = L_A5 + wave * 1040 + lane * 16;
  unsigned a5 = L_A5 + b8 * 1040 + lq * 16, a6w = bvalid ? L_A6 + l15 * 80 + w * 32 + lq * 8 : L_JUNK, a6r = L_A6 + b8 * 80 + lq * 16;
  int yo = arena(fg) + A_YP + b8 * 1024 + w * 256 + (l15 >> 3) * 64 + lq * 16;
  pin(xo); pin(o5); pin(a5); pin(a6w); pin(a6r); pin(yo);
#pragma unroll
  for (int i = 0; i < 16; ++i) pin(fw[i]);
  STAMP(55);
  STAMPD(40);
  u64 st15 = 0; int rx_ = 0;
  if (wave == 4) { wait_until(latest_time(ws + WS_HB + (16 * b + s) * 128, t0, (unsigned)tag) + T_C); STAMPV(40); }
  bar();
  {
    v4u xv[1];
    int rx = 0;
#pragma unroll 1
    for (int sp = 0; sp < SPIN_G; ++sp) { load_n<1>(rs, xo, 0, xv); rx = sp + 1; if (!any_sent<1>(xv)) break; }
    STAMP(15);
    const f32x4 v0 = {bflo(xv[0].x), bfhi(xv[0].x), bflo(xv[0].y), bfhi(xv[0].y)}, v1 = {bflo(xv[0].z), bfhi(xv[0].z), bflo(xv[0].w), bfhi(xv[0].w)};
    const f32x4 sv4 = v0 + v1, qv4 = v0 * v0 + v1 * v1;
    const float mean = wave_sum((sv4.x + sv4.y) + (sv4.z + sv4.w)) * (1.f / 512);
    const float msq = wave_sum((qv4.x + qv4.y) + (qv4.z + qv4.w)) * (1.f / 512);
    const float rstd = __builtin_amdgcn_rsqf(fmaxf(msq - mean * mean, 0.f) + EPS);
    *(LAS v4u*)o5 = pk8((v0 - mean) * rstd * cb[0] + sb[0], (v1 - mean) * rstd * cb[1] + sb[1]);
  }
  bar();
  STAMP(36);
  if (wave < 2) {
    f32x4 z0 = z4, z1 = z4;
#pragma unroll
    for (int hq = 0; hq < 2; ++hq) {
      v4u bf[8];
#pragma unroll
      for (int i = 0; i < 8; ++i) bf[i] = lds128(a5, hq * 512 + i * 64);
      if (hq == 0) __builtin_amdgcn_sched_barrier(0);
#pragma unroll
      for (int i = 0; i < 8; i += 2) { z0 = mfma16(fw[8 * hq + i], bf[i], z0); z1 = mfma16(fw[8 * hq + i + 1], bf[i + 1], z1); }
    }
    z0 += z1;
    *(LAS v2u*)a6w = v2u{pk(z0.x < 0.f ? 0.f : z0.x, z0.y < 0.f ? 0.f : z0.y), pk(z0.z < 0.f ? 0.f : z0.z, z0.w < 0.f ? 0.f : z0.w)};
  }
  bar();
  if (wave >= 4) {
    STAMP(37);
    const v4u bf = lds128(a6r);
#pragma unroll
    for (int i = 0; i < 2; ++i) {
      const v4u ya = pk8(mfma16(fw[4 * i], bf, z4), mfma16(fw[4 * i + 1], bf, z4)), yb = pk8(mfma16(fw[4 * i + 2], bf, z4), mfma16(fw[4 * i + 3], bf, z4));
      stg(rs, yo + i * 128, pick(bvalid, ya, yb));
    }
    STAMP(16);
    if (wave == 4) { DUMP(36); DUMP(37); DUMP(16); DUMP(40); }
  }
}

__global__ __launch_bounds__(768) void adaln_mla_ffn_kernel(Params p) {
  STAMP(0);
  unsigned k0, k1;
  asm volatile("s_load_dword %0, %2, 0x0\n\ts_load_dword %1, %2, 0x40" : "=&s"(k0), "=&s"(k1) : "s"(__builtin_amdgcn_kernarg_segment_ptr()));
  const unsigned t0 = now32();
  const int g = blockIdx.x & 7, idx = blockIdx.x >> 3;
  const int wave = __builtin_amdgcn_readfirstlane(threadIdx.x >> 6), lane = threadIdx.x & 63;
  if (wave == 0 && (idx & 1)) {
    u64 pc;
    unsigned left;
    asm volatile("s_getpc_b64 %0\n99:\n\ts_mov_b32 %1, 98f-99b-4\n\t.subsection 1\n98:\n\t.subsection 0" : "=s"(pc), "=s"(left));
    asm volatile("s_mov_b32 m0, %2\n\ts_nop 0\n\tglobal_load_lds_dword %0, %1" :: "v"(min(((idx >> 1) + 16 * min(lane, 21)) * 128u, left)), "s"(pc), "s"(L_JUNK) : "memory");
  }
  const int wi = idx & 15, l15 = lane & 15, lq = lane >> 4, b8 = lane & 7;
  const int ch = l15 ^ ((lq << 2) | wave);
  unsigned v1 = wave < 4 ? (32 * wi + 4 * wave + lq) * 6144 + (ch >> 3) * 1024 + g * 128 + (ch & 7) * 16 : wi * 64 + b8 * 16;
  unsigned vx = lane * 16;
  pin(v1); pin(vx);
  asm volatile("s_waitcnt lgkmcnt(0)" :: "s"(p.x), "s"(p.ckv), "s"(p.wq), "s"(p.wuk), "s"(p.wuv), "s"(p.wo), "s"(p.w1), "s"(p.w2), "s"(p.c),
               "s"(p.wmod), "s"(p.out), "s"(p.ws), "s"(k0), "s"(k1));
  STAMP(39);
  STAMPD(2); STAMPD(3); STAMPD(32);
  if ((idx & 17) == 0) {
    const rsrc_t rs = __builtin_amdgcn_make_buffer_rsrc(p.ws, 0, WS_BYTES, 0x00020000);
    const int my = arena(16 * g + wi) + A_H, r = wave - 4;
    {
      const bool row = wave >= 4;
      if (wave == 3) stg(rs, vx + my, v4u{SENT, SENT, SENT, SENT});
      const unsigned char* a = row ? (const unsigned char*)(p.c + r * 512) : (const unsigned char*)p.wmod;
      const unsigned d = row ? M_RAW + r * 2048 + 1024 : 3 * 32768 + wave * 1024;
      if (row) {
        dma16<false>(p.x + r * 512, vx, d - 1024);
        dma16<false>(a, v1, d);
      } else {
#pragma unroll
        for (int i = 0; i < 4; ++i) dma16<true>(a + i * 16 * 6144, v1, d + i * 4096);
      }
      FENCE();
      __builtin_amdgcn_s_setprio(1);
    }
    if (wave >= 4) {
      unsigned raw = M_RAW + r * 2048 + vx, d = (lane >> 3) == g ? M_LNX + r * 256 + b8 * 32 : L_JUNK, sc = M_SC + r * 144 + lane * 2;
      unsigned rc = M_RAW + r * 2048 + 1024 + lane * 2;
      pin(raw); pin(d); pin(sc); pin(rc);
      VMCNT(0);
      const v4u xr = lds128(raw);
      const float cv = bflo(*(LAS u16*)rc);
      *(LAS u16*)sc = (u16)pk(cv * __builtin_amdgcn_rcpf(1.f + __expf(-cv)), 0.f);
      bar();
      const f32x4 v0 = up4(xr.xy), v2 = up4(xr.zw), sv4 = v0 + v2, qv4 = v0 * v0 + v2 * v2;
      const float mean = wave_sum((sv4.x + sv4.y) + (sv4.z + sv4.w)) * (1.f / 512);
      const float msq = wave_sum((qv4.x + qv4.y) + (qv4.z + qv4.w)) * (1.f / 512);
      const float rstd = __builtin_amdgcn_rsqf(fmaxf(msq - mean * mean, 0.f) + EPS);
      *(LAS f32x4*)d = (v0 - mean) * rstd;
      *(LAS f32x4*)((LAS char*)d + 16) = (v2 - mean) * rstd;
      STAMPV(32);
      bar();
    } else {
      const int sl = 8 * (g & 1) + (wi >> 1);
      const int p4 = 4 * (lane & 3);
      unsigned s0 = tr_addr(3 * 32768, lane, 16 * wave + p4, 0), s1 = tr_addr(3 * 32768, lane, 16 * wave + p4, 1), c0 = s0 ^ 128, c1 = s1 ^ 128;
      unsigned ba = M_SC + b8 * 144 + lq * 16, la = M_LNX + b8 * 256 + (16 * wave + 4 * lq) * 4;
      int o = l15 < 8 ? my + wave * 256 + l15 * 32 + lq * 8 : OOB;
      int hh = wave == 3 && lane < 32 ? WS_HH + (4 * lane + (g >> 1)) * 512 + (sl >> 2) * 128 + (sl & 3) * 4 : OOB;
      const float one = wi == 0 ? 1.f : 0.f;
      f32x4 sa = {0.f, 0.f, 0.f, 0.f}, ca = {one, one, one, one};
      pin(s0); pin(s1); pin(c0); pin(c1); pin(ba); pin(la); pin(o); pin(hh); pin(ca);
      unsigned key = 0;
      if (wave == 3) {
        const u64 tag = get_tag(p.ws);
        key = (unsigned)tag;
        VMCNT(4);
        __builtin_amdgcn_raw_buffer_store_b64(v2u{(unsigned)tag, (unsigned)(tag >> 32)}, rs,
                                              lane < 32 ? WS_EFL + (4 * lane + (g >> 1)) * 256 + (sl >> 3) * 128 + (sl & 7) * 8 : OOB, 0, SC1);
        VMCNT(1);
      } else {
        VMCNT(0);
      }
      STAMPV(32);
      bar();
      const unsigned tn = now32();
      const v4u b0 = lds128(ba), f0 = tr_frag(s0, s1, 0), g0 = tr_frag(c0, c1, 0), b1 = lds128(ba, 64), f1 = tr_frag(s0, s1, 1), g1 = tr_frag(c0, c1, 1);
      __builtin_amdgcn_sched_barrier(0);
      st32(rs, hh, tn + key);
      STAMPV(2);
      sa = mfma16(f1, b1, mfma16(f0, b0, sa));
      ca = mfma16(g1, b1, mfma16(g0, b0, ca));
      bar();
      sa += u2f(lds128(la)) * ca;
      __builtin_amdgcn_raw_buffer_store_b64(v2u{pk(sa.x, sa.y), pk(sa.z, sa.w)}, rs, o, 0, SC1);
      STAMPV(3);
    }
  }
  if (PREF && (idx & 17) == 1 && wave >= 8) {
    const unsigned char* a = (const unsigned char*)p.wuk + (128 * (wi & 2) + 128 * (wave & 1) + 64 + 16 * (wave & 2)) * 8192 + (4 * g + (wi >> 2)) * 256;
#pragma unroll 1
    for (int i = 0; i < 8; ++i) dma16<true>(a + i * 32768, lq * 8192 + l15 * 16, T_CNT + 1024 + (wave & 3) * 1024);
  }
  FENCE();
  if (idx < 16) attn_wg(p, g, idx, wave, lane, t0);
  else weight_wg(p, g, wi, wave, lane, t0);
}

extern "C" void kernel_launch(void* const* d_in, const int* in_sizes, int n_in, void* d_out, int out_size, void* d_ws, size_t ws_size,
                              hipStream_t stream) {
  Params p;
  p.x = (const u16*)d_in[0]; p.ckv = (const u16*)d_in[1]; p.wq = (const u16*)d_in[2]; p.wuk = (const u16*)d_in[3];
  p.wuv = (const u16*)d_in[4]; p.wo = (const u16*)d_in[5]; p.w1 = (const u16*)d_in[7]; p.w2 = (const u16*)d_in[8];
  p.c = (const u16*)d_in[9]; p.wmod = (const u16*)d_in[10];
  p.out = (u16*)d_out; p.ws = (unsigned char*)d_ws + WS_BASE;
  (void)hipFuncSetAttribute((const void*)adaln_mla_ffn_kernel, hipFuncAttributeMaxDynamicSharedMemorySize, LDS_BYTES);
  adaln_mla_ffn_kernel<<<256, 768, LDS_BYTES, stream>>>(p);
}
```

```cpp
#include <hip/hip_runtime.h>
#include <hip/hip_bf16.h>
#pragma clang diagnostic ignored "-Wint-to-pointer-cast"
#pragma clang diagnostic ignored "-Wsometimes-uninitialized"

typedef unsigned short u16;
typedef unsigned long long u64;
typedef unsigned v4u __attribute__((ext_vector_type(4)));
typedef unsigned v2u __attribute__((ext_vector_type(2)));
typedef float f32x2 __attribute__((ext_vector_type(2)));
typedef float f32x4 __attribute__((ext_vector_type(4)));
typedef float f32x16 __attribute__((ext_vector_type(16)));
typedef short s16x4 __attribute__((ext_vector_type(4)));
typedef short bf16x8 __attribute__((ext_vector_type(8)));
typedef __bf16 bf16x2 __attribute__((ext_vector_type(2)));
#define GAS __attribute__((address_space(1)))
#define LAS __attribute__((address_space(3)))
#define DI __device__ __forceinline__
#define RLX_AGENT __ATOMIC_RELAXED, __HIP_MEMORY_SCOPE_AGENT
typedef __amdgpu_buffer_rsrc_t rsrc_t;

#define ATT_DELAY 0
#define T_H 62
#define D_UK 8
#define T_QP 125
#define T_QA 112
#define T_B 80
#define T_C 158
#define T_D 295
#define ARR_EARLY 0
#define T_ML 62
#define T_PART 118
#define T_B6 64
#define D_F 0
#define D_ML 2
#define FLAG_AT 1
#define D_PREF 16
#define PRIO_S 2
#define PRIO_PV 1
#define PF_NT false
#define D_W 0
#define T_PREF 0
#define T_W 40
#define PF_EVEN 24
#define T_FILL 0
#define T_FLAG 260
#define WS_BASE 0
#define FILL_AUX 16
#define WWIN 7
#define DRY 0
#define T_EF 220
#define T_LQ 190
#define T_LQ2 100
#define PF_UK 8
#define T_UK 190
#define T_FP 540
#define T_RP 560
#define T_HOLD 45
#define HELP 0
#define PRIO_L 3
#define WO_MODE 1
#define T_WO 170
#define FP_ALL 0
#define FP_W2 1
#define T_L 240
#define FILL_EARLY 1
#define D_FILL 0
#define PREF 1
#define WO_NT 1

constexpr int WS_FLAG1 = 4096, WS_FLAG2 = 8192;
constexpr int WS_EFL = 16384  , WS_HP = 81920, WS_HB = 98304  ,
              WS_HH = 131072  , WS_HQ = 196608  , WS_ARR = 262144  ;
constexpr int WS_ARENA = 327680, ASTR = 58624;
constexpr int A_S = 0, A_E = 1024, A_ML = 5120, A_X1 = 6144, A_OUTP = 7168, A_YP = 15360, A_PART = 23552, A_CK = 56320;
constexpr int WS_BYTES = WS_ARENA + 128 * ASTR;
constexpr unsigned SENT = 0xFFFFFFFFu;
constexpr int OOB = 0x7FFFFF00;
constexpr int SC1 = 16;
constexpr int SPIN_H = 1 << 11;
constexpr int SPIN_G = 1 << 19;
constexpr int SPIN_L = 1 << 23;
constexpr int LDS_BYTES = 163840;
constexpr float EPS = 1e-5f;
constexpr float QSCALE = 0.08838834764831845f * 1.4426950408889634f;
constexpr float BUMP = 40.f;

#define STAMP(k) do {} while (0)
#define STAMPV(k) do {} while (0)
#define STAMPD(k) do {} while (0)
#define STV(k) 0
#define DUMP(k) do {} while (0)
#define DUMPC(k, c) do {} while (0)

DI int fresh_lane() {
  int l;
  asm volatile("v_mbcnt_lo_u32_b32 %0, -1, 0\n\tv_mbcnt_hi_u32_b32 %0, -1, %0" : "=v"(l));
  return l;
}
template <class T> DI void pin(T& v) { asm volatile("" : "+v"(v)); }
DI float bflo(unsigned u) { return __uint_as_float(u << 16); }
DI float bfhi(unsigned u) { return __uint_as_float(u & 0xFFFF0000u); }
DI unsigned pk(float a, float b) { f32x2 v = {a, b}; return __builtin_bit_cast(unsigned, __builtin_convertvector(v, bf16x2)); }
DI f32x4 mfma16(v4u a, v4u b, f32x4 c) {
  return __builtin_amdgcn_mfma_f32_16x16x32_bf16(__builtin_bit_cast(bf16x8, a), __builtin_bit_cast(bf16x8, b), c, 0, 0, 0);
}
DI f32x16 mfma32(v4u a, v4u b, f32x16 c) {
  return __builtin_amdgcn_mfma_f32_32x32x16_bf16(__builtin_bit_cast(bf16x8, a), __builtin_bit_cast(bf16x8, b), c, 0, 0, 0);
}
DI v4u ldg(rsrc_t rs, int off, int so = 0) { return __builtin_amdgcn_raw_buffer_load_b128(rs, off, so, SC1); }
DI void stg(rsrc_t rs, int off, v4u v) { __builtin_amdgcn_raw_buffer_store_b128(v, rs, off, 0, SC1); }
DI unsigned umax4(v4u v) { return max(max(v.x, v.y), max(v.z, v.w)); }
DI v4u f2u(f32x4 v) { return __builtin_bit_cast(v4u, v); }
DI f32x4 u2f(v4u v) { return __builtin_bit_cast(f32x4, v); }
DI bool nan4(f32x4 v) { return __builtin_isunordered(v.x, v.y) | __builtin_isunordered(v.z, v.w); }
DI v4u lds128(unsigned a, int o = 0) { return *(LAS v4u*)((LAS char*)a + o); }
DI v2u ldstr(unsigned a, int o = 0) { return __builtin_bit_cast(v2u, __builtin_amdgcn_ds_read_tr16_b64_v4i16((LAS s16x4*)((LAS char*)a + o))); }
DI void bar() {
  __builtin_amdgcn_sched_barrier(0);
  asm volatile("s_waitcnt lgkmcnt(0)\n\ts_barrier" ::: "memory");
  __builtin_amdgcn_sched_barrier(0);
}
#define VMCNT_(n) asm volatile("s_waitcnt vmcnt(" #n ")" ::: "memory")
#define VMCNT(n) VMCNT_(n)
#define FENCE() asm volatile("" ::: "memory")
#define ROLE_END() __builtin_amdgcn_s_waitcnt(0x0F70)
template <bool NT> DI void dma16(const void* base, unsigned voff, unsigned dst) {
  if (NT) asm volatile("s_mov_b32 m0, %2\n\ts_nop 0\n\tglobal_load_lds_dwordx4 %0, %1 nt" :: "v"(voff), "s"(base), "s"(dst) : "memory");
  else asm volatile("s_mov_b32 m0, %2\n\ts_nop 0\n\tglobal_load_lds_dwordx4 %0, %1" :: "v"(voff), "s"(base), "s"(dst) : "memory");
}
template <bool NT> DI void dma_run(const void* base, unsigned voff, unsigned dst, int n, int gstr, int lstr) {
  const unsigned char* b = (const unsigned char*)base;
#pragma unroll 1
  for (int i = 0; i < n; ++i) { VMCNT(WWIN); dma16<NT>(b, voff, dst); b += gstr; dst += lstr; }
}
template <int N> DI void nap() { if (N > 0) __builtin_amdgcn_s_sleep(N); }
DI void lsig(unsigned a, unsigned v = 1u) { asm volatile("s_mov_b64 exec, 1\n\tds_add_u32 %0, %1\n\ts_mov_b64 exec, -1" :: "v"(a), "v"(v) : "memory"); }
DI unsigned lpeek(unsigned a) { return *(volatile LAS unsigned*)a; }
template <bool NAP = false> DI void lwait(unsigned a, unsigned want) {
  unsigned v, n = SPIN_L;
  if (NAP)
    asm volatile("1:\n\tds_read_b32 %0, %2\n\ts_waitcnt lgkmcnt(0)\n\tv_cmp_le_u32 vcc, %3, %0\n\ts_cbranch_vccnz 2f\n\t"
                 "s_sleep 1\n\ts_sub_u32 %1, %1, 1\n\ts_cbranch_scc0 1b\n2:" : "=&v"(v), "+s"(n) : "v"(a), "s"(want) : "vcc", "scc", "memory");
  else
    asm volatile("1:\n\tds_read_b32 %0, %2\n\ts_waitcnt lgkmcnt(0)\n\tv_cmp_le_u32 vcc, %3, %0\n\ts_cbranch_vccnz 2f\n\t"
                 "s_sub_u32 %1, %1, 1\n\ts_cbranch_scc0 1b\n2:" : "=&v"(v), "+s"(n) : "v"(a), "s"(want) : "vcc", "scc", "memory");
}
template <int CTRL> DI float dppf(float v) {
  return __builtin_bit_cast(float, __builtin_amdgcn_update_dpp(0, __builtin_bit_cast(int, v), CTRL, 0xF, 0xF, true));
}
DI float row16_max(float v) {
  asm volatile("s_nop 1\n\tv_max_f32_dpp %0, %0, %0 row_ror:8 row_mask:0xf bank_mask:0xf\n\ts_nop 1\n\tv_max_f32_dpp %0, %0, %0 row_ror:4 row_mask:0xf bank_mask:0xf\n\t"
               "s_nop 1\n\tv_max_f32_dpp %0, %0, %0 row_ror:2 row_mask:0xf bank_mask:0xf\n\ts_nop 1\n\tv_max_f32_dpp %0, %0, %0 row_ror:1 row_mask:0xf bank_mask:0xf" : "+v"(v));
  return v;
}
DI float row16_sum(float v) { v += dppf<0x128>(v); v += dppf<0x124>(v); v += dppf<0x122>(v); return v + dppf<0x121>(v); }
DI float xsum16(float v) { auto r = __builtin_amdgcn_permlane16_swap(__float_as_uint(v), __float_as_uint(v), false, false); return __uint_as_float(r[0]) + __uint_as_float(r[1]); }
DI float xsum32(float v) { auto r = __builtin_amdgcn_permlane32_swap(__float_as_uint(v), __float_as_uint(v), false, false); return __uint_as_float(r[0]) + __uint_as_float(r[1]); }
DI float xmax16(float v) { auto r = __builtin_amdgcn_permlane16_swap(__float_as_uint(v), __float_as_uint(v), false, false); return fmaxf(__uint_as_float(r[0]), __uint_as_float(r[1])); }
DI float xmax32(float v) { auto r = __builtin_amdgcn_permlane32_swap(__float_as_uint(v), __float_as_uint(v), false, false); return fmaxf(__uint_as_float(r[0]), __uint_as_float(r[1])); }
DI float wave_sum(float v) { return xsum32(xsum16(row16_sum(v))); }

#define XS8(d, a, b) "v_xor_b32_dpp " d ", " a ", " a " row_ror:8 row_mask:0xf bank_mask:0x3\n\tv_xor_b32_dpp " d ", " b ", " b " row_ror:8 row_mask:0xf bank_mask:0xc\n\t"
#define XS4(d, a, b) "v_xor_b32_dpp " d ", " a ", " a " row_half_mirror row_mask:0xf bank_mask:0x5\n\tv_xor_b32_dpp " d ", " b ", " b " row_half_mirror row_mask:0xf bank_mask:0xa\n\t"
#define XQ(d, p) "v_xor_b32_dpp " d ", " d ", " d " quad_perm:" p " row_mask:0xf bank_mask:0xf\n\t"
DI void xor_rows8(const unsigned (&v)[8], unsigned& r0, unsigned& r1) {
  unsigned t0, t1, t2, t3;
  asm volatile("s_nop 1\n\t" XS8("%2", "%6", "%10") XS8("%4", "%8", "%12") XS8("%3", "%7", "%11") XS8("%5", "%9", "%13")
               XS4("%0", "%2", "%4") XS4("%1", "%3", "%5") XQ("%0", "[2,3,0,1]") "s_nop 0\n\t" XQ("%1", "[2,3,0,1]") XQ("%0", "[1,0,3,2]")
               "s_nop 0\n\t" XQ("%1", "[1,0,3,2]")
               : "=&v"(r0), "=&v"(r1), "=&v"(t0), "=&v"(t1), "=&v"(t2), "=&v"(t3)
               : "v"(v[0]), "v"(v[1]), "v"(v[2]), "v"(v[3]), "v"(v[4]), "v"(v[5]), "v"(v[6]), "v"(v[7]));
}
DI unsigned xor_rows2(unsigned a, unsigned b) {
  unsigned r;
  asm volatile("s_nop 1\n\t" XS8("%0", "%1", "%2") "s_nop 1\n\tv_xor_b32_dpp %0, %0, %0 row_half_mirror row_mask:0xf bank_mask:0xf\n\ts_nop 1\n\t"
               XQ("%0", "[2,3,0,1]") "s_nop 1\n\t" XQ("%0", "[1,0,3,2]") "s_nop 1" : "=&v"(r) : "v"(a), "v"(b));
  return r;
}
template <int N> DI bool any_sent(const v4u (&r)[N]) {
  unsigned m = 0;
#pragma unroll
  for (int i = 0; i < N; ++i) m = max(m, umax4(r[i]));
  return __any(m == SENT);
}
template <int N> DI void load_n(rsrc_t rs, int off0, int str, v4u (&r)[N]) {
  FENCE();
#pragma unroll
  for (int i = 0; i < N; ++i) r[i] = ldg(rs, off0, i * str);
  __builtin_amdgcn_sched_barrier(0);
}
template <int N, int NAP = 0> DI void gather(rsrc_t rs, int off0, int str, v4u (&r)[N]) {
#pragma unroll 1
  for (int sp = 0; sp < SPIN_G; ++sp) {
    load_n<N>(rs, off0, str, r);
    if (!any_sent<N>(r)) break;
    nap<NAP>();
  }
}
DI u64 get_tag(const unsigned char* ws) {
  u64 e;
  asm volatile("s_load_dwordx2 %0, %1, 0x0\n\ts_waitcnt lgkmcnt(0)" : "=s"(e) : "s"(ws) : "memory");
  return e * 6364136223846793005ull + 1442695040888963407ull;
}
DI bool flags_up(v4u f, u64 tag) {
  const unsigned lo = (unsigned)tag, hi = (unsigned)(tag >> 32);
  return __all(f.x == lo && f.y == hi && f.z == lo && f.w == hi);
}
DI void wait_flags(rsrc_t rs, int off, u64 tag) {
#pragma unroll 1
  for (int sp = 0; sp < SPIN_G; ++sp) {
    FENCE();
    if (flags_up(ldg(rs, off), tag)) break;
    __builtin_amdgcn_s_sleep(8);
  }
  FENCE();
}
typedef unsigned s8u __attribute__((ext_vector_type(8)));
DI s8u sload8(const unsigned char* a) {
  s8u v;
  asm volatile("s_load_dwordx8 %0, %1, 0x0 glc\n\ts_waitcnt lgkmcnt(0)" : "=s"(v) : "s"(a) : "memory");
  return v;
}
DI unsigned max8(s8u v) { return max(max(max(v[0], v[1]), max(v[2], v[3])), max(max(v[4], v[5]), max(v[6], v[7]))); }
DI unsigned now32() { return (unsigned)__builtin_amdgcn_s_memrealtime(); }
constexpr unsigned HWIN = 1u << 20;
DI unsigned latest_time(const unsigned char* a, unsigned t0, unsigned key) {
  unsigned m = 0;
#pragma unroll 1
  for (int sp = 0; sp < SPIN_H; ++sp) {
    m = max8(sload8(a) - (key + t0 - HWIN));
    if (m < 2 * HWIN) return m + t0 - HWIN;
  }
  return t0;
}
template <int STR> DI unsigned latest_time32(const unsigned char* a, unsigned t0, unsigned key) {
  const unsigned kb = key + t0 - HWIN;
#pragma unroll 1
  for (int sp = 0; sp < SPIN_H; ++sp) {
    s8u u, v, x, y;
    asm volatile("s_load_dwordx8 %0, %4, 0x0 glc\n\ts_load_dwordx8 %1, %4, %5 glc\n\ts_load_dwordx8 %2, %4, %6 glc\n\t"
                 "s_load_dwordx8 %3, %4, %7 glc\n\ts_waitcnt lgkmcnt(0)"
                 : "=&s"(u), "=&s"(v), "=&s"(x), "=&s"(y) : "s"(a), "i"(STR), "i"(2 * STR), "i"(3 * STR) : "memory");
    const unsigned m = max(max(max8(u - kb), max8(v - kb)), max(max8(x - kb), max8(y - kb)));
    if (m < 2 * HWIN) return m + t0 - HWIN;
  }
  return t0;
}
DI void wait_until(unsigned t) {
#pragma unroll 1
  for (int sp = 0; sp < SPIN_H; ++sp) if ((int)(now32() - t) >= 0) break;
}
DI int arena(int wid) { return WS_ARENA + wid * ASTR; }

DI unsigned tr_addr(unsigned region, int lane, int col4, int t) {
  const int g = lane >> 4, q = (lane & 15) >> 2;
  return region + (8 * g + 4 * t + q) * 256 + 16 * ((col4 >> 3) ^ ((q << 2) | ((2 * g + t) & 3))) + 8 * ((col4 >> 2) & 1);
}
DI v4u tr_frag(unsigned a0, unsigned a1, int ks) {
  const v2u lo = ldstr(a0, ks * 8192), hi = ldstr(a1, ks * 8192);
  return v4u{lo.x, lo.y, hi.x, hi.y};
}
DI void panel_frags(unsigned region, int lane, int col_a, int col_b, v4u (&f)[2][4]) {
  const unsigned a0 = tr_addr(region, lane, col_a, 0), a1 = tr_addr(region, lane, col_a, 1);
  const unsigned b0 = tr_addr(region, lane, col_b, 0), b1 = tr_addr(region, lane, col_b, 1);
#pragma unroll
  for (int ks = 0; ks < 4; ++ks) { f[0][ks] = tr_frag(a0, a1, ks); f[1][ks] = tr_frag(b0, b1, ks); }
}
DI void gemv2(const v4u (&f)[2][4], unsigned act, f32x4& d0, f32x4& d1) {
  v4u bf[4];
#pragma unroll
  for (int ks = 0; ks < 4; ++ks) bf[ks] = lds128(act, ks * 64);
  __builtin_amdgcn_sched_barrier(0);
#pragma unroll
  for (int ks = 0; ks < 4; ++ks) { d0 = mfma16(f[0][ks], bf[ks], d0); d1 = mfma16(f[1][ks], bf[ks], d1); }
}
DI f32x4 up4(v2u v) { return f32x4{bflo(v.x), bfhi(v.x), bflo(v.y), bfhi(v.y)}; }
DI v4u pk8(f32x4 a, f32x4 b) { return v4u{pk(a.x, a.y), pk(a.z, a.w), pk(b.x, b.y), pk(b.z, b.w)}; }
DI v4u silu8(v4u cv) {
  v4u o;
#pragma unroll
  for (int e = 0; e < 4; ++e) {
    const float a = bflo(cv[e]), c = bfhi(cv[e]);
    o[e] = pk(a * __builtin_amdgcn_rcpf(1.f + __expf(-a)), c * __builtin_amdgcn_rcpf(1.f + __expf(-c)));
  }
  return o;
}

struct Params {
  const u16 *x, *ckv, *wq, *wuk, *wuv, *wo, *w1, *w2, *c, *wmod;
  u16* out;
  unsigned char* ws;
};

constexpr unsigned W0 = 0, W1 = 32768, W2 = 65536, L_SC = 98304  , L_A1 = L_SC + 4224  ,
                   L_A3 = L_A1 + 4224  , L_A4 = L_A3 + 2176, L_CNT = L_A4 + 2176,
                   M_RAW = 114688  , M_SC = M_RAW + 16384  , M_LNX = M_SC + 1152  ;
constexpr unsigned T_SP = 131072, T_P = T_SP + 16384, T_FAC = T_P + 8192, T_CNT = T_FAC + 1024,
                   L_A5 = 98304  , L_A6 = L_A5 + 8320  ;
constexpr unsigned C_LAND = T_CNT + 32, C_FREE = C_LAND + 4, C_SRDY = C_LAND + 8, C_PRDY = C_LAND + 12,
                   C_QST = T_CNT + 192, C_QRD = T_CNT + 196, C_HOLD = T_CNT + 200;
constexpr unsigned L_JUNK = LDS_BYTES - 256;

template <int N> DI f32x4 reduce_rows(rsrc_t rs, int off0, int offl, v4u sel, int& rounds) {
  f32x4 a;
#pragma unroll 1
  for (int sp = 0; sp < SPIN_G; ++sp) {
    v4u d[N];
    FENCE();
#pragma unroll
    for (int i = 0; i < N; ++i) d[i] = ldg(rs, i == N - 1 ? offl : off0, i * 16 * ASTR);
    __builtin_amdgcn_sched_barrier(0);
    f32x4 b = {0.f, 0.f, 0.f, 0.f};
    a = b;
#pragma unroll
    for (int i = 0; i < N; i += 2) { a = mfma16(sel, d[i], a); b = mfma16(sel, d[i + 1], b); }
    a += b;
    rounds = sp + 1;
    if (!__any(a.x != a.x) || !any_sent<N>(d)) break;
  }
#pragma unroll
  for (int r = 0; r < 4; ++r) { a[r] += dppf<0x128>(a[r]); a[r] += dppf<0x124>(a[r]); }
  return a;
}
DI v4u pick(bool lo, v4u a, v4u b) { return v4u{lo ? a.x : b.x, lo ? a.y : b.y, lo ? a.z : b.z, lo ? a.w : b.w}; }
DI void st32(rsrc_t rs, int off, unsigned v) { __builtin_amdgcn_raw_buffer_store_b32(v, rs, off, 0, SC1); }

DI void weight_wg(const Params& p, int g, int wi, int wave, int lane, unsigned t0) {
  unsigned char* ws = p.ws;
  const rsrc_t rs = __builtin_amdgcn_make_buffer_rsrc(ws, 0, WS_BYTES, 0x00020000);
  const int h = 4 * g + (wi >> 2), j = wi & 3, wid = 16 * g + wi, my = arena(wid);
  const int l15 = lane & 15, lq = lane >> 4, b8 = lane & 7;
  const bool bvalid = l15 < 8, isq = j < 2;
  const int w = wave & 3;
  const int ch = l15 ^ ((lq << 2) | w);
  const unsigned dst0 = w * 1024;
  const unsigned vuv = (128 * j + 16 * ((4 * w + lq) & 7) + (w >> 1)) * 8192 + h * 256 + ch * 16;
  const f32x4 z4 = {0.f, 0.f, 0.f, 0.f};

  if (wave >= 8) {
    if (!isq) wait_until(t0 + T_UK);
    if (!isq) dma_run<true>(p.wuk, (256 * (j - 2) + 4 * w + lq) * 8192 + h * 256 + ch * 16, W0 + dst0, 16, 16 * 8192, 4096);
    const unsigned key = (unsigned)get_tag(ws);
    int hp = isq && lane < 8 ? WS_HP + (4 * h + 2 + (lane & 1)) * 128 + j * 4 + (lane >> 1) * 8 : OOB;
    pin(hp);
    VMCNT(0);
    STAMP(44);
    if (!isq) lsig(L_CNT);
    bar();
    const unsigned tb1 = now32();
    nap<3>();
    if (w == 0) st32(rs, hp, tb1 + key);
    bar();
    v4u wo[4][8];
    const GAS unsigned char* wb = (const GAS unsigned char*)p.wo + (h * 128 + 8 * lq) * 1024 + w * 256 + (4 * (l15 & 3) + (l15 >> 2)) * 16;
    bar();
    wait_until(tb1 + (isq ? T_WO + T_QP : T_WO));
#pragma unroll
    for (int ks = 0; ks < 4; ++ks) {
      const GAS unsigned char* wk = wb + ((ks + j) & 3) * 32768;
#pragma unroll
      for (int r = 0; r < 8; ++r) wo[ks][r] = ks == 0 ? __builtin_nontemporal_load((const GAS v4u*)(wk + r * 1024)) : *(const GAS v4u*)(wk + r * 1024);
      if (ks == 0) VMCNT(0);
    }
    bar();
    __builtin_amdgcn_sched_barrier(0);
#pragma unroll
    for (int ks = 0; ks < 4; ++ks) {
      v4u o[8];
#pragma unroll
      for (int dd = 0; dd < 4; ++dd)
#pragma unroll
        for (int rp = 0; rp < 4; ++rp) {
          o[2 * dd][rp] = __builtin_amdgcn_perm(wo[ks][2 * rp + 1][dd], wo[ks][2 * rp][dd], 0x05040100u);
          o[2 * dd + 1][rp] = __builtin_amdgcn_perm(wo[ks][2 * rp + 1][dd], wo[ks][2 * rp][dd], 0x07060302u);
        }
#pragma unroll
      for (int t = 0; t < 8; ++t) { wo[ks][t] = o[t]; pin(wo[ks][t]); }
    }
    STAMP(10);
    unsigned ba[4];
#pragma unroll
    for (int ks = 0; ks < 4; ++ks) { ba[ks] = L_A4 + b8 * 272 + ((ks + (WO_MODE ? j : 2 * j)) & 3) * 64 + lq * 16; pin(ba[ks]); }
    int oo = my + A_OUTP + (16 * w + 4 * (l15 >> 3) + lq) * 128 + b8 * 16;
    pin(oo);
    __builtin_amdgcn_s_setprio(2);
    bar();
    bar();
    v4u bf[4];
#pragma unroll
    for (int ks = 0; ks < 4; ++ks) bf[ks] = lds128(ba[ks]);
    v4u r0, r1;
#pragma unroll
    for (int tp = 0; tp < 4; ++tp) {
      f32x4 e = z4, o = z4;
#pragma unroll
      for (int ks = 0; ks < 4; ++ks) { e = mfma16(wo[ks][2 * tp], bf[ks], e); o = mfma16(wo[ks][2 * tp + 1], bf[ks], o); }
      r0[tp] = bvalid ? pk(e[0], o[0]) : pk(e[1], o[1]);
      r1[tp] = bvalid ? pk(e[2], o[2]) : pk(e[3], o[3]);
    }
    stg(rs, oo, r0);
    stg(rs, oo + 1024, r1);
    STAMP(14);
    if (wave == 8) { DUMP(10); DUMP(14); if (isq) DUMP(44); else { const unsigned stv60 = STV(44); DUMP(60); } }
    ROLE_END();
  } else if (wave >= 4) {
    __builtin_amdgcn_s_setprio(2);
    const int t = w * 64 + lane;
    const u64 tag = get_tag(ws);
    v4u msk;
    {
      const unsigned half = (lane & 1) ? 0xFFFF0000u : 0x0000FFFFu;
      const int dw = b8 >> 1;
      msk = v4u{dw == 0 ? half : 0u, dw == 1 ? half : 0u, dw == 2 ? half : 0u, dw == 3 ? half : 0u};
    }
    v4u sela = msk & (bvalid ? 0x3F803F80u : 0u), selb = msk & (bvalid ? 0u : 0x3F803F80u);
    int go = isq ? arena(64 * j + 16 * w + 4 * (lq >> 1) + 2 + (lq & 1)) + A_S + l15 * 16 : arena(4 * h) + A_E + t * 16;
    unsigned gd = isq ? L_A1 + b8 * 528 + (64 * w + 16 * (lq >> 1) + 8 * (l15 >> 3) + 4 * (lq & 1)) * 2
                      : L_A1 + b8 * 528 + (t >> 3) * 8;
    pin(msk); pin(sela); pin(selb); pin(go); pin(gd);
    STAMPD(34); STAMPD(46);
    if (w == 2) {
      wait_until(t0 + T_EF);
      wait_flags(rs, WS_EFL + wid * 256 + (isq ? l15 * 16 : 0), tag);
      STAMPV(46);
    }
    if (w == 3) {
      unsigned tr;
      if (isq) {
        tr = latest_time32<128>(ws + WS_HH + wid * 512, t0, (unsigned)tag) + T_H;
      } else {
        tr = latest_time(ws + WS_HP + wid * 128, t0, (unsigned)tag) + T_QP;
        lwait(L_CNT, 4);
        const unsigned tn = now32() + 8;
        if ((int)(tn - tr) > 0) tr = tn;
        st32(rs, WS_HQ + (2 * lane + j - 2) * 512 + (h & 3) * 128 + g * 4, tr + (unsigned)tag);
      }
      STAMPV(34);
      wait_until(tr);
    }
    bar();
    int rh = 0;
    if (isq) {
      f32x4 h0, h1;
#pragma unroll 1
      for (int sp = 0; sp < SPIN_G; ++sp) {
        v4u r[8];
        FENCE();
#pragma unroll
        for (int i = 0; i < 8; ++i) r[i] = ldg(rs, go + (i & 3) * 256, (i >> 2) * 8 * ASTR);
        __builtin_amdgcn_sched_barrier(0);
        h0 = mfma16(selb, r[5], mfma16(sela, r[4], mfma16(selb, r[1], mfma16(sela, r[0], z4))));
        h1 = mfma16(selb, r[7], mfma16(sela, r[6], mfma16(selb, r[3], mfma16(sela, r[2], z4))));
        rh = sp + 1;
        if (!__any(__builtin_isunordered(h0.x, h1.x)) || !any_sent<8>(r)) break;
      }
      *(LAS v2u*)gd = v2u{pk(h0.x, h0.y), pk(h0.z, h0.w)};
      *(LAS v2u*)((LAS char*)gd + 64) = v2u{pk(h1.x, h1.y), pk(h1.z, h1.w)};
    } else {
      f32x4 q;
#pragma unroll 1
      for (int sp = 0; sp < SPIN_G; ++sp) {
        v4u r[2];
        load_n<2>(rs, go, ASTR, r);
        q = u2f(r[0]) + u2f(r[1]);
        rh = sp + 1;
        if (!__any(nan4(q)) || !any_sent<2>(r)) break;
      }
      *(LAS v2u*)gd = v2u{pk(q.x, q.y), pk(q.z, q.w)};
    }
    STAMP(5);
    bar();
    bar();
    if (isq)
      *(LAS v4u*)(L_SC + (2 * w + (lane >> 5)) * 528 + (lane & 31) * 16) =
          silu8(*(const GAS v4u*)(p.c + (2 * w + (lane >> 5)) * 512 + j * 256 + (lane & 31) * 8));
    if (w == 3) {
      nap<D_FILL>();
#pragma unroll 1
      for (int n = wid < 8 ? 6 : 7; n < (wid < 56 ? 23 : 15); ++n)
        stg(rs, my + lane * 16 + n * 1024, v4u{SENT, SENT, SENT, SENT});
      VMCNT(0);
      if (lane == 0) __hip_atomic_store((GAS u64*)(ws + WS_FLAG2 + wid * 8), tag, RLX_AGENT);
    }
    bar();
    const int el = (4 * (wi & 1) + (lane & 3)) * 512 + 8 * (wid >> 1) + 4 * (lq & 1), pc = wi * 64 + (lane & 3) * 16;
    int gmo = w == 0 ? arena(4 * g) + A_S + pc + (lq & 1) * 8 : OOB - 100 * ASTR;
    pin(gmo);
    int mo = arena(32 * w + (lane & 31)) + A_ML + h * 16;
    int po = my + A_PART + w * 8192 + lane * 16;
    unsigned o3 = L_A3 + (2 * w + (lane >> 5)) * 272 + l15 * 16 + (lq & 1) * 8;
    int hb = lane < 16 ? WS_HB + (16 * lane + ((wi - 5 * g) & 15)) * 128 + g * 4 : OOB;
    int co = lane < 32 ? mo + (A_CK - A_ML) + j * 512 : OOB;
    int bp = 4 * lq + (bvalid ? 0 : 128);
    unsigned tlo = lane < 32 ? (unsigned)tag : 0u, thi = lane < 32 ? (unsigned)(tag >> 32) : 0u;
    pin(mo); pin(po); pin(o3); pin(hb); pin(co); pin(bp); pin(tlo); pin(thi);
    const unsigned ta = latest_time32<128>(ws + WS_ARR + (4 * h + w) * 512, t0, (unsigned)tag);
    STAMP(11);
    *(LAS unsigned*)(L_CNT + 16 + w * 4) = ta;
    wait_until(ta + T_ML);
    FENCE();
    v4u ml = ldg(rs, mo);
    wait_until(ta + T_PART);
    v4u d[8], ck;
    load_n<8>(rs, po, 1024, d);
    ck = ldg(rs, co);
    __builtin_amdgcn_sched_barrier(0);
    if (__builtin_expect(__any(((ml.y ^ (unsigned)tag) | (ml.w ^ (unsigned)(tag >> 32))) != 0), 0)) {
#pragma unroll 1
      for (int sp = 0; sp < SPIN_G; ++sp) {
        FENCE();
        ml = ldg(rs, mo);
        if (__all(ml.y == (unsigned)tag && ml.w == (unsigned)(tag >> 32))) break;
      }
    }
    v4u sel[8];
    {
      const float r = __uint_as_float(ml.x), e = __builtin_amdgcn_exp2f(r - row16_max(r));
      const float wgt = e * __builtin_amdgcn_rcpf(row16_sum(e * __uint_as_float(ml.z)));
      const unsigned hi = pk(wgt, wgt);
      const float rest = wgt - bflo(hi);
      const int v = lane < 32 ? hi : pk(rest, rest);
      unsigned wv[8];
#pragma unroll
      for (int i = 0; i < 8; ++i) wv[i] = __builtin_amdgcn_ds_bpermute(bp + 16 * i, v);
      __builtin_amdgcn_sched_barrier(0);
#pragma unroll
      for (int i = 0; i < 8; ++i) { sel[i] = msk & wv[i]; pin(sel[i]); }
    }
    f32x4 oa, ob;
    int rp = 0;
#pragma unroll 1
    for (int sp = 0; sp < SPIN_G; ++sp) {
      oa = z4; ob = z4;
      unsigned xe = 0, xo = 0;
#pragma unroll
      for (int i = 0; i < 8; ++i) {
        if (i < 4) oa = mfma16(sel[i], d[i], oa); else ob = mfma16(sel[i], d[i], ob);
        xe = __builtin_amdgcn_bitop3_b32(xe, d[i].x, d[i].z, 0x96);
        xo = __builtin_amdgcn_bitop3_b32(xo, d[i].y, d[i].w, 0x96);
        __builtin_amdgcn_sched_barrier(0);
      }
      rp = sp + 1;
      unsigned x = xor_rows2(xe ^ ck.x, xo ^ ck.z);
      auto y = __builtin_amdgcn_permlane16_swap(x, x, false, false);
      x = y[0] ^ y[1];
      y = __builtin_amdgcn_permlane32_swap(x, x, false, false);
      if (!__any(((y[0] ^ y[1]) | (ck.y ^ tlo) | (ck.w ^ thi)) != 0)) break;
      load_n<8>(rs, po, 1024, d);
      ck = ldg(rs, co);
    }
    ROLE_END();
    STAMP(12);
    {
      f32x4 s;
#pragma unroll
      for (int r = 0; r < 4; ++r) {
        auto x = __builtin_amdgcn_permlane32_swap(__float_as_uint(oa[r]), __float_as_uint(ob[r]), false, false);
        s[r] = __uint_as_float(x[0]) + __uint_as_float(x[1]);
      }
      *(LAS v2u*)o3 = v2u{pk(s.x, s.y), pk(s.z, s.w)};
    }
    bar();
    v2u gg[4], xb;
    v4u f2;
    if (w == 0) {
#pragma unroll
      for (int i = 0; i < 4; ++i) gg[i] = __builtin_amdgcn_raw_buffer_load_b64(rs, gmo, ((i >> 1) * 96 + (i & 1)) * ASTR, SC1);
      f2 = ldg(rs, WS_FLAG2 + lane * 16);
      xb = *(const GAS v2u*)(p.x + el);
    }
    unsigned tb = 0;
    if (w < 2) {
      const v4u tv = lds128(L_CNT + 16) - t0;
      tb = __builtin_amdgcn_readfirstlane(umax4(tv)) + t0 + T_PART + T_B6;
      const unsigned tn = now32() - 6;
      if ((int)(tn - tb) > 0) tb = tn;
    }
    if (w == 1) st32(rs, hb, tb + (unsigned)tag);
    bar();
    if (w == 0) {
#pragma unroll 1
      for (int sp = 0; sp < SPIN_G; ++sp) {
        unsigned m = 0;
#pragma unroll
        for (int i = 0; i < 4; ++i) m = max(m, max(gg[i].x, gg[i].y));
        if (!__any(m == SENT)) break;
        FENCE();
#pragma unroll
        for (int i = 0; i < 4; ++i) gg[i] = __builtin_amdgcn_raw_buffer_load_b64(rs, gmo, ((i >> 1) * 96 + (i & 1)) * ASTR, SC1);
      }
      if (!flags_up(f2, tag)) wait_flags(rs, WS_FLAG2 + lane * 16, tag);
      f32x4 x0 = up4(xb), ga = up4(gg[0]) + up4(gg[1]), gb = up4(gg[2]) + up4(gg[3]);
      pin(x0); pin(ga); pin(gb);
      STAMP(26);
      const bool act = lane < 32 && l15 < 4;
      v4u one = sela;
      int oo = arena(lane >> 2) + A_OUTP + g * 1024 + pc;
      int yo = oo + (A_YP - A_OUTP);
      int yl = lane < 32 ? yo : OOB - 48 * ASTR;
      int xo = act ? arena(g) + A_X1 + pc + lq * 8 : OOB;
      const rsrc_t ro = __builtin_amdgcn_make_buffer_rsrc(p.out, 0, 8192, 0x00020000);
      int fo = act ? el * 2 : OOB;
      pin(one); pin(oo); pin(yo); pin(yl); pin(xo); pin(fo);
      wait_until(tb + T_B);
      int r1 = 0, r2 = 0;
      const f32x4 x1 = x0 + ga * reduce_rows<8>(rs, oo, oo, one, r1);
      __builtin_amdgcn_raw_buffer_store_b64(v2u{pk(x1.x, x1.y), pk(x1.z, x1.w)}, rs, xo, 0, SC1);
      STAMP(27);
      wait_until(latest_time(ws + WS_HB + (128 + wid) * 128, t0, (unsigned)tag) + T_D);
      const f32x4 r = x1 + gb * reduce_rows<4>(rs, yo, yl, one, r2);
      STAMP(29);
      __builtin_amdgcn_raw_buffer_store_b64(v2u{pk(r.x, r.y), pk(r.z, r.w)}, ro, fo, 0, 0);
      if (wid == 0 && lane == 0) __hip_atomic_store((GAS u64*)ws, tag, RLX_AGENT);
      STAMP(30);
      DUMP(26); DUMP(27); DUMP(29); DUMP(30); DUMPC(42, r1); DUMPC(43, r2);
    }
    ROLE_END();
  } else {
    __builtin_amdgcn_s_setprio(2);
    const int p4 = 4 * (lane & 3);
    unsigned act = b8 * 272 + lq * 16;
    unsigned s0 = tr_addr(W2, lane, 16 * w + p4, 0), s1 = tr_addr(W2, lane, 16 * w + p4, 1);
    unsigned ba = L_SC + b8 * 528 + lq * 16;
    v4u f[2][4], f2[2][4];
    unsigned a1 = L_A1 + b8 * 528 + lq * 16;
    int o = my + A_E + (isq ? w * 1024 + (l15 >> 3) * 512 + lq * 128 + b8 * 16 : b8 * 512 + w * 128 + (l15 >> 3) * 64 + lq * 16);
    pin(o); pin(a1); pin(act);
    bar();
    const unsigned tb1 = now32();
    STAMP(4);
    if (isq) {
      panel_frags(W0, lane, 32 * w + p4, 32 * w + 16 + p4, f);
      panel_frags(W1, lane, 32 * w + p4, 32 * w + 16 + p4, f2);
      bar();
      f32x4 q0 = z4, q1 = z4;
      v4u bf[8];
#pragma unroll
      for (int ks = 0; ks < 8; ++ks) bf[ks] = lds128(a1, ks * 64);
      __builtin_amdgcn_sched_barrier(0);
#pragma unroll
      for (int ks = 0; ks < 4; ++ks) { q0 = mfma16(f[0][ks], bf[ks], q0); q1 = mfma16(f[1][ks], bf[ks], q1); }
#pragma unroll
      for (int ks = 0; ks < 4; ++ks) { q0 = mfma16(f2[0][ks], bf[4 + ks], q0); q1 = mfma16(f2[1][ks], bf[4 + ks], q1); }
      stg(rs, o, pick(bvalid, f2u(q0), f2u(q1)));
    } else {
#pragma unroll
      for (int tt = 0; tt < 2; ++tt) {
        const unsigned ka = W0 + (64 * w + 8 * (l15 >> 2) + 4 * tt + (l15 & 3)) * 256 + 16 * (lq ^ (((l15 & 3) << 2) | ((2 * (l15 >> 2) + tt) & 3)));
#pragma unroll
        for (int ks = 0; ks < 4; ++ks) { f[tt][ks] = lds128(ka ^ (ks * 64)); f2[tt][ks] = lds128(ka ^ (ks * 64), 8192); }
      }
      bar();
      f32x4 a0 = z4, a2 = z4, c0 = z4, c2 = z4;
      v4u bf[4];
#pragma unroll
      for (int ks = 0; ks < 4; ++ks) bf[ks] = lds128(a1, ks * 64);
      __builtin_amdgcn_sched_barrier(0);
#pragma unroll
      for (int ks = 0; ks < 4; ++ks) {
        a0 = mfma16(f[0][ks], bf[ks], a0); c0 = mfma16(f2[0][ks], bf[ks], c0);
        a2 = mfma16(f[1][ks], bf[ks], a2); c2 = mfma16(f2[1][ks], bf[ks], c2);
      }
      stg(rs, o, pk8(u2f(pick(bvalid, f2u(a0), f2u(c0))) * QSCALE, u2f(pick(bvalid, f2u(a2), f2u(c2))) * QSCALE));
    }
    STAMP(6);
    bar();
    wait_until(tb1 + (isq ? T_L + T_QP : T_L));
    if (HELP && !isq) {
      const unsigned char* kv = (const unsigned char*)p.ckv + (size_t)(g * 4096 + (7 * 16 + 4 * (wi >> 2) + 2 * (j - 2)) * 32) * 1024;
      dma_run<false>(kv, w * 1024 + lane * 16, W1 + dst0, 16, 4096, 0);
    }
    dma_run<true>(p.wuv, vuv, W0 + dst0, 8, 2 * 8192, 4096);
    const bool m2 = isq;
    if (m2) dma_run<true>(p.wmod, (256 * j + 4 * w + lq + (ch >> 3) * 128) * 6144 + 2048 + h * 128 + (ch & 7) * 16, W2 + dst0, 8, 16 * 6144, 4096);
    VMCNT(0);
    bar();
    STAMP(9);
    if (m2) {
      f32x4 m0 = z4, m1 = z4;
#pragma unroll 1
      for (int ks = 0; ks < 4; ++ks) {
        m0 = mfma16(tr_frag(s0 + ks * 8192, s1 + ks * 8192, 0), lds128(ba + ks * 64), m0);
        m1 = mfma16(tr_frag((s0 ^ 128) + ks * 8192, (s1 ^ 128) + ks * 8192, 0), lds128(ba + ks * 64, 256), m1);
      }
      m0 += m1;
      __builtin_amdgcn_raw_buffer_store_b64(v2u{pk(m0.x, m0.y), pk(m0.z, m0.w)}, rs, bvalid ? my + A_S + (2 * w + (lq >> 1)) * 128 + l15 * 16 + (lq & 1) * 8 : OOB, 0, SC1);
    }
    panel_frags(W0, lane, 32 * w + 2 * p4, 32 * w + 2 * p4 + 4, f);
    unsigned o4 = bvalid ? L_A4 + l15 * 272 + w * 64 + lq * 16 : L_JUNK;
    pin(o4);
    if (g < 7 && (FP_ALL || !isq)) {
      const int e = 2 * (wi >> 2) + (j & 1);
      wait_until(tb1 + (isq ? T_FP + T_QP : T_FP));
      if (!isq) dma_run<false>(p.w1, (64 * e + 16 * w + (lane >> 5)) * 3584 + g * 512 + (lane & 31) * 16, W1 + dst0, 8, 2 * 3584, 0);
      if (FP_ALL == isq && FP_W2) dma_run<false>(p.w2, (256 * g + 32 * e + 8 * w) * 1024 + lane * 16, W1 + dst0, 8, 1024, 0);
    }
    bar();
    STAMP(35);
    f32x4 c0 = z4, c1 = z4;
    gemv2(f, L_A3 + act, c0, c1);
    *(LAS v4u*)o4 = pk8(c0, c1);
    bar();
    STAMP(13);
  }
}

template <int OFF> DI void rd_inplace(v4u& a, unsigned ad) { asm volatile("ds_read_b128 %0, %1 offset:%2" : "+v"(a) : "v"(ad), "i"(OFF) : "memory"); }
template <int O0, int O1> DI void peek2(unsigned& a, unsigned& b, unsigned cb) {
  asm volatile("ds_read_b32 %0, %2 offset:%3\n\tds_read_b32 %1, %2 offset:%4" : "=&v"(a), "=&v"(b) : "v"(cb), "i"(O0), "i"(O1) : "memory");
}
template <int W> DI void wait2(unsigned& a, unsigned& b) { asm volatile("s_waitcnt lgkmcnt(%2)" : "+v"(a), "+v"(b) : "i"(W)); }
template <int W, bool FIRST> DI void score_pair(f32x4& s0, f32x4& s1, v4u a, v4u q0, v4u q1) {
  if constexpr (FIRST)
    asm volatile("s_waitcnt lgkmcnt(%5)\n\tv_mfma_f32_16x16x32_bf16 %0, %2, %3, 0\n\tv_mfma_f32_16x16x32_bf16 %1, %2, %4, 0"
                 : "=&v"(s0), "=&v"(s1) : "v"(a), "v"(q0), "v"(q1), "i"(W));
  else
    asm volatile("s_waitcnt lgkmcnt(%5)\n\tv_mfma_f32_16x16x32_bf16 %0, %2, %3, %0\n\tv_mfma_f32_16x16x32_bf16 %1, %2, %4, %1"
                 : "+v"(s0), "+v"(s1) : "v"(a), "v"(q0), "v"(q1), "i"(W));
}
template <int OFF> DI void put_scores(unsigned sa, f32x4 s0, f32x4 s1) {
  asm volatile("s_nop 4\n\tds_write_b128 %0, %1 offset:%3\n\tds_write_b128 %0, %2 offset:%4" :: "v"(sa), "v"(s0), "v"(s1), "i"(OFF), "i"(OFF + 1024) : "memory");
}
template <int V> struct IntC { static constexpr int value = V; };
template <int I, int N, class F> DI void static_for(F&& f) { if constexpr (I < N) { f(IntC<I>{}); static_for<I + 1, N>(f); } }
constexpr unsigned slot_c(int t) { return ((0x02310210u >> ((4 * t) & 31)) & 3) * 32768; }
template <unsigned A> DI void csig(unsigned cb, unsigned v) {
  asm volatile("s_mov_b64 exec, 1\n\tds_add_u32 %0, %1 offset:%2\n\ts_mov_b64 exec, -1" :: "v"(cb), "v"(v), "i"(A - T_CNT) : "memory");
}
template <unsigned A> DI unsigned cpeek(unsigned cb) { return *(volatile LAS unsigned*)((LAS char*)cb + (A - T_CNT)); }
#define SPIN_HEAD "s_mov_b32 %[n], %[nb]\n1:\n\ts_sub_u32 %[n], %[n], 1\n\ts_cbranch_scc1 2f\n\t"
#define SPIN_TAIL "s_waitcnt lgkmcnt(0)\n\tv_cmp_gt_u32 vcc, %[w], %[c]\n\ts_cbranch_vccnz 1b\n2:"
#define SPIN_TAIL2 "s_waitcnt lgkmcnt(0)\n\tv_cmp_gt_u32 vcc, %[w], %[c]\n\ts_cbranch_vccz 2f\n" \
                   "3:\n\ts_sub_u32 %[n], %[n], 1\n\ts_cbranch_scc1 2f\n\tds_read_b32 %[c], %[cb] offset:%[o]\n\ts_waitcnt lgkmcnt(0)\n\t" \
                   "v_cmp_gt_u32 vcc, %[w2], %[c]\n\ts_cbranch_vccnz 3b\n\ts_branch 1b\n2:"
template <unsigned A, unsigned W, bool NAP = false> DI void cwait(unsigned cb, unsigned nb) {
  unsigned c, n;
  if (NAP)
    asm volatile(SPIN_HEAD "s_sleep 1\n\tds_read_b32 %[c], %[cb] offset:%[o]\n\t" SPIN_TAIL
                 : [c] "=&v"(c), [n] "=&s"(n) : [cb] "v"(cb), [o] "i"(A - T_CNT), [w] "i"(W), [nb] "s"(nb) : "vcc", "scc", "memory");
  else
    asm volatile(SPIN_HEAD "ds_read_b32 %[c], %[cb] offset:%[o]\n\t" SPIN_TAIL
                 : [c] "=&v"(c), [n] "=&s"(n) : [cb] "v"(cb), [o] "i"(A - T_CNT), [w] "i"(W), [nb] "s"(nb) : "vcc", "scc", "memory");
}
constexpr unsigned T_DRY = T_CNT + 256;
DI constexpr unsigned fsw(int row) { return ((row & 3) << 2) | ((row >> 1) & 2) | ((row >> 3) & 1); }
DI unsigned slot_of(int t) { return ((0x02310210u >> ((4 * t) & 31)) & 3) * 32768; }
#define DMA4(AUX, base, dst, v0, v1, v2, v3) \
  asm volatile("s_mov_b32 m0, %5\n\ts_waitcnt vmcnt(31)\n\tglobal_load_lds_dwordx4 %0, %4 " AUX "\n\t" \
               "s_waitcnt vmcnt(31)\n\tglobal_load_lds_dwordx4 %1, %4 offset:1024 " AUX "\n\t" \
               "s_waitcnt vmcnt(31)\n\tglobal_load_lds_dwordx4 %2, %4 offset:2048 " AUX "\n\t" \
               "s_waitcnt vmcnt(31)\n\tglobal_load_lds_dwordx4 %3, %4 offset:3072 " AUX \
               :: "v"(v0), "v"(v1), "v"(v2), "v"(v3), "s"(base), "s"(dst) : "memory")

typedef float f32x32 __attribute__((ext_vector_type(32)));
DI void pv_loop(f32x32& o0, f32x32& o1, const unsigned (&a)[8], unsigned pa, unsigned cb, unsigned one, unsigned nb) {
  unsigned n, k;
  u64 m0, m1, ret;
  asm volatile(
      "s_branch 300f\n"
      "200:\n"
      "v_readfirstlane_b32 %[n], v20\n"
      "v_lshrrev_b32 v32, 4, %[pa]\n"
      "v_and_b32 v32, 48, v32\n"
      "v_add_u32 v32, %[k], v32\n"
      "v_add_u32 v32, %[fac], v32\n"
      "ds_read_b128 v[36:39], v32 offset:64\n"
      "ds_read_b128 v[32:35], v32\n"
      "s_waitcnt lgkmcnt(0)\n"
      "s_bitcmp1_b32 %[n], 0\n"
      "s_cbranch_scc0 93f\n"
      "v_pk_mul_f32 v[40:41], v[40:41], v[32:33]\n"
      "v_pk_mul_f32 v[42:43], v[42:43], v[34:35]\n"
      "v_pk_mul_f32 v[44:45], v[44:45], v[32:33]\n"
      "v_pk_mul_f32 v[46:47], v[46:47], v[34:35]\n"
      "v_pk_mul_f32 v[48:49], v[48:49], v[32:33]\n"
      "v_pk_mul_f32 v[50:51], v[50:51], v[34:35]\n"
      "v_pk_mul_f32 v[52:53], v[52:53], v[32:33]\n"
      "v_pk_mul_f32 v[54:55], v[54:55], v[34:35]\n"
      "v_pk_mul_f32 v[56:57], v[56:57], v[32:33]\n"
      "v_pk_mul_f32 v[58:59], v[58:59], v[34:35]\n"
      "v_pk_mul_f32 v[60:61], v[60:61], v[32:33]\n"
      "v_pk_mul_f32 v[62:63], v[62:63], v[34:35]\n"
      "v_pk_mul_f32 v[64:65], v[64:65], v[32:33]\n"
      "v_pk_mul_f32 v[66:67], v[66:67], v[34:35]\n"
      "v_pk_mul_f32 v[68:69], v[68:69], v[32:33]\n"
      "v_pk_mul_f32 v[70:71], v[70:71], v[34:35]\n"
      "93:\n"
      "s_bitcmp1_b32 %[n], 1\n"
      "s_cbranch_scc0 93f\n"
      "v_pk_mul_f32 v[72:73], v[72:73], v[36:37]\n"
      "v_pk_mul_f32 v[74:75], v[74:75], v[38:39]\n"
      "v_pk_mul_f32 v[76:77], v[76:77], v[36:37]\n"
      "v_pk_mul_f32 v[78:79], v[78:79], v[38:39]\n"
      "v_pk_mul_f32 v[80:81], v[80:81], v[36:37]\n"
      "v_pk_mul_f32 v[82:83], v[82:83], v[38:39]\n"
      "v_pk_mul_f32 v[84:85], v[84:85], v[36:37]\n"
      "v_pk_mul_f32 v[86:87], v[86:87], v[38:39]\n"
      "v_pk_mul_f32 v[88:89], v[88:89], v[36:37]\n"
      "v_pk_mul_f32 v[90:91], v[90:91], v[38:39]\n"
      "v_pk_mul_f32 v[92:93], v[92:93], v[36:37]\n"
      "v_pk_mul_f32 v[94:95], v[94:95], v[38:39]\n"
      "v_pk_mul_f32 v[96:97], v[96:97], v[36:37]\n"
      "v_pk_mul_f32 v[98:99], v[98:99], v[38:39]\n"
      "v_pk_mul_f32 v[100:101], v[100:101], v[36:37]\n"
      "v_pk_mul_f32 v[102:103], v[102:103], v[38:39]\n"
      "93:\n"
      "s_nop 4\n"
      "s_setpc_b64 %[ret]\n"
      "201:\n"
      "v_readfirstlane_b32 %[n], v22\n"
      "v_lshrrev_b32 v24, 4, %[pa]\n"
      "v_and_b32 v24, 48, v24\n"
      "v_add_u32 v24, %[k], v24\n"
      "v_add_u32 v24, %[fac], v24\n"
      "ds_read_b128 v[28:31], v24 offset:64\n"
      "ds_read_b128 v[24:27], v24\n"
      "s_waitcnt lgkmcnt(0)\n"
      "s_bitcmp1_b32 %[n], 0\n"
      "s_cbranch_scc0 93f\n"
      "v_pk_mul_f32 v[40:41], v[40:41], v[24:25]\n"
      "v_pk_mul_f32 v[42:43], v[42:43], v[26:27]\n"
      "v_pk_mul_f32 v[44:45], v[44:45], v[24:25]\n"
      "v_pk_mul_f32 v[46:47], v[46:47], v[26:27]\n"
      "v_pk_mul_f32 v[48:49], v[48:49], v[24:25]\n"
      "v_pk_mul_f32 v[50:51], v[50:51], v[26:27]\n"
      "v_pk_mul_f32 v[52:53], v[52:53], v[24:25]\n"
      "v_pk_mul_f32 v[54:55], v[54:55], v[26:27]\n"
      "v_pk_mul_f32 v[56:57], v[56:57], v[24:25]\n"
      "v_pk_mul_f32 v[58:59], v[58:59], v[26:27]\n"
      "v_pk_mul_f32 v[60:61], v[60:61], v[24:25]\n"
      "v_pk_mul_f32 v[62:63], v[62:63], v[26:27]\n"
      "v_pk_mul_f32 v[64:65], v[64:65], v[24:25]\n"
      "v_pk_mul_f32 v[66:67], v[66:67], v[26:27]\n"
      "v_pk_mul_f32 v[68:69], v[68:69], v[24:25]\n"
      "v_pk_mul_f32 v[70:71], v[70:71], v[26:27]\n"
      "93:\n"
      "s_bitcmp1_b32 %[n], 1\n"
      "s_cbranch_scc0 93f\n"
      "v_pk_mul_f32 v[72:73], v[72:73], v[28:29]\n"
      "v_pk_mul_f32 v[74:75], v[74:75], v[30:31]\n"
      "v_pk_mul_f32 v[76:77], v[76:77], v[28:29]\n"
      "v_pk_mul_f32 v[78:79], v[78:79], v[30:31]\n"
      "v_pk_mul_f32 v[80:81], v[80:81], v[28:29]\n"
      "v_pk_mul_f32 v[82:83], v[82:83], v[30:31]\n"
      "v_pk_mul_f32 v[84:85], v[84:85], v[28:29]\n"
      "v_pk_mul_f32 v[86:87], v[86:87], v[30:31]\n"
      "v_pk_mul_f32 v[88:89], v[88:89], v[28:29]\n"
      "v_pk_mul_f32 v[90:91], v[90:91], v[30:31]\n"
      "v_pk_mul_f32 v[92:93], v[92:93], v[28:29]\n"
      "v_pk_mul_f32 v[94:95], v[94:95], v[30:31]\n"
      "v_pk_mul_f32 v[96:97], v[96:97], v[28:29]\n"
      "v_pk_mul_f32 v[98:99], v[98:99], v[30:31]\n"
      "v_pk_mul_f32 v[100:101], v[100:101], v[28:29]\n"
      "v_pk_mul_f32 v[102:103], v[102:103], v[30:31]\n"
      "93:\n"
      "s_nop 4\n"
      "s_setpc_b64 %[ret]\n"
      "110:\n"
      "s_mov_b32 %[n], %[nb]\n"
      "91:\n"
      "s_sub_u32 %[n], %[n], 1\n"
      "s_cbranch_scc1 92f\n"
      "ds_read_b32 v22, %[cb] offset:60\n"
      "s_waitcnt lgkmcnt(0)\n"
      "v_cmp_gt_u32 vcc, 8, v22\n"
      "s_cbranch_vccnz 91b\n"
      "92:\n"
      "ds_read_b32 v22, %[cb] offset:60\n"
      "ds_read_b128 v[32:35], %[pa] offset:2048\n"
      "ds_read_b128 v[36:39], %[pa] offset:3072\n"
      "ds_read_b32 v23, %[cb] offset:80\n"
      "s_waitcnt lgkmcnt(0)\n"
      "v_cmp_lt_u32 vcc, 8, v22\n"
      "s_cbranch_vccz 111f\n"
      "114:\n"
      "s_movk_i32 %[k], 128\n"
      "s_call_b64 %[ret], 201b\n"
      "s_branch 111f\n"
      "120:\n"
      "s_mov_b32 %[n], %[nb]\n"
      "91:\n"
      "s_sub_u32 %[n], %[n], 1\n"
      "s_cbranch_scc1 92f\n"
      "ds_read_b32 v20, %[cb] offset:76\n"
      "s_waitcnt lgkmcnt(0)\n"
      "v_cmp_gt_u32 vcc, 8, v20\n"
      "s_cbranch_vccnz 91b\n"
      "92:\n"
      "ds_read_b32 v20, %[cb] offset:76\n"
      "ds_read_b128 v[24:27], %[pa] offset:4096\n"
      "ds_read_b128 v[28:31], %[pa] offset:5120\n"
      "ds_read_b32 v21, %[cb] offset:96\n"
      "s_waitcnt lgkmcnt(0)\n"
      "v_cmp_lt_u32 vcc, 8, v20\n"
      "s_cbranch_vccz 121f\n"
      "124:\n"
      "s_movk_i32 %[k], 256\n"
      "s_call_b64 %[ret], 200b\n"
      "s_branch 121f\n"
      "123:\n"
      "s_mov_b32 %[n], %[nb]\n"
      "91:\n"
      "s_sub_u32 %[n], %[n], 1\n"
      "s_cbranch_scc1 92f\n"
      "ds_read_b32 v22, %[cb] offset:80\n"
      "s_waitcnt lgkmcnt(0)\n"
      "v_cmp_gt_u32 vcc, 1, v22\n"
      "s_cbranch_vccnz 91b\n"
      "92:\n"
      "ds_read_b64_tr_b16 v[136:137], %[a0] offset:0\n"
      "ds_read_b64_tr_b16 v[138:139], %[a0] offset:16384\n"
      "ds_read_b64_tr_b16 v[140:141], %[a1] offset:0\n"
      "ds_read_b64_tr_b16 v[142:143], %[a1] offset:16384\n"
      "ds_read_b64_tr_b16 v[144:145], %[a2] offset:0\n"
      "ds_read_b64_tr_b16 v[146:147], %[a2] offset:16384\n"
      "ds_read_b64_tr_b16 v[148:149], %[a3] offset:0\n"
      "ds_read_b64_tr_b16 v[150:151], %[a3] offset:16384\n"
      "ds_read_b64_tr_b16 v[152:153], %[a4] offset:0\n"
      "ds_read_b64_tr_b16 v[154:155], %[a4] offset:16384\n"
      "ds_read_b64_tr_b16 v[156:157], %[a5] offset:0\n"
      "ds_read_b64_tr_b16 v[158:159], %[a5] offset:16384\n"
      "ds_read_b64_tr_b16 v[160:161], %[a6] offset:0\n"
      "ds_read_b64_tr_b16 v[162:163], %[a6] offset:16384\n"
      "ds_read_b64_tr_b16 v[164:165], %[a7] offset:0\n"
      "ds_read_b64_tr_b16 v[166:167], %[a7] offset:16384\n"
      "s_mov_b64 exec, 1\n"
      "ds_add_u32 %[cb], %[one] offset:84\n"
      "s_mov_b64 exec, -1\n"
      "130:\n"
      "s_mov_b32 %[n], %[nb]\n"
      "91:\n"
      "s_sub_u32 %[n], %[n], 1\n"
      "s_cbranch_scc1 92f\n"
      "ds_read_b32 v22, %[cb] offset:92\n"
      "s_waitcnt lgkmcnt(0)\n"
      "v_cmp_gt_u32 vcc, 8, v22\n"
      "s_cbranch_vccnz 91b\n"
      "92:\n"
      "ds_read_b32 v22, %[cb] offset:92\n"
      "ds_read_b128 v[32:35], %[pa] offset:6144\n"
      "ds_read_b128 v[36:39], %[pa] offset:7168\n"
      "ds_read_b32 v23, %[cb] offset:112\n"
      "s_waitcnt lgkmcnt(0)\n"
      "v_cmp_lt_u32 vcc, 8, v22\n"
      "s_cbranch_vccz 131f\n"
      "134:\n"
      "s_movk_i32 %[k], 384\n"
      "s_call_b64 %[ret], 201b\n"
      "s_branch 131f\n"
      "133:\n"
      "s_mov_b32 %[n], %[nb]\n"
      "91:\n"
      "s_sub_u32 %[n], %[n], 1\n"
      "s_cbranch_scc1 92f\n"
      "ds_read_b32 v20, %[cb] offset:96\n"
      "s_waitcnt lgkmcnt(0)\n"
      "v_cmp_gt_u32 vcc, 1, v20\n"
      "s_cbranch_vccnz 91b\n"
      "92:\n"
      "ds_read_b64_tr_b16 v[104:105], %[a0] offset:32768\n"
      "ds_read_b64_tr_b16 v[106:107], %[a0] offset:49152\n"
      "ds_read_b64_tr_b16 v[108:109], %[a1] offset:32768\n"
      "ds_read_b64_tr_b16 v[110:111], %[a1] offset:49152\n"
      "ds_read_b64_tr_b16 v[112:113], %[a2] offset:32768\n"
      "ds_read_b64_tr_b16 v[114:115], %[a2] offset:49152\n"
      "ds_read_b64_tr_b16 v[116:117], %[a3] offset:32768\n"
      "ds_read_b64_tr_b16 v[118:119], %[a3] offset:49152\n"
      "ds_read_b64_tr_b16 v[120:121], %[a4] offset:32768\n"
      "ds_read_b64_tr_b16 v[122:123], %[a4] offset:49152\n"
      "ds_read_b64_tr_b16 v[124:125], %[a5] offset:32768\n"
      "ds_read_b64_tr_b16 v[126:127], %[a5] offset:49152\n"
      "ds_read_b64_tr_b16 v[128:129], %[a6] offset:32768\n"
      "ds_read_b64_tr_b16 v[130:131], %[a6] offset:49152\n"
      "ds_read_b64_tr_b16 v[132:133], %[a7] offset:32768\n"
      "ds_read_b64_tr_b16 v[134:135], %[a7] offset:49152\n"
      "s_mov_b64 exec, 1\n"
      "ds_add_u32 %[cb], %[one] offset:100\n"
      "s_mov_b64 exec, -1\n"
      "140:\n"
      "s_mov_b32 %[n], %[nb]\n"
      "91:\n"
      "s_sub_u32 %[n], %[n], 1\n"
      "s_cbranch_scc1 92f\n"
      "ds_read_b32 v20, %[cb] offset:108\n"
      "s_waitcnt lgkmcnt(0)\n"
      "v_cmp_gt_u32 vcc, 8, v20\n"
      "s_cbranch_vccnz 91b\n"
      "92:\n"
      "ds_read_b32 v20, %[cb] offset:108\n"
      "ds_read_b128 v[24:27], %[pa] offset:0\n"
      "ds_read_b128 v[28:31], %[pa] offset:1024\n"
      "ds_read_b32 v21, %[cb] offset:128\n"
      "s_waitcnt lgkmcnt(0)\n"
      "v_cmp_lt_u32 vcc, 8, v20\n"
      "s_cbranch_vccz 141f\n"
      "144:\n"
      "s_movk_i32 %[k], 512\n"
      "s_call_b64 %[ret], 200b\n"
      "s_branch 141f\n"
      "143:\n"
      "s_mov_b32 %[n], %[nb]\n"
      "91:\n"
      "s_sub_u32 %[n], %[n], 1\n"
      "s_cbranch_scc1 92f\n"
      "ds_read_b32 v22, %[cb] offset:112\n"
      "s_waitcnt lgkmcnt(0)\n"
      "v_cmp_gt_u32 vcc, 1, v22\n"
      "s_cbranch_vccnz 91b\n"
      "92:\n"
      "v_add_u32 v138, 0x10000, %[a0]\n"
      "ds_read_b64_tr_b16 v[136:137], v138 offset:32768\n"
      "ds_read_b64_tr_b16 v[138:139], v138 offset:49152\n"
      "v_add_u32 v142, 0x10000, %[a1]\n"
      "ds_read_b64_tr_b16 v[140:141], v142 offset:32768\n"
      "ds_read_b64_tr_b16 v[142:143], v142 offset:49152\n"
      "v_add_u32 v146, 0x10000, %[a2]\n"
      "ds_read_b64_tr_b16 v[144:145], v146 offset:32768\n"
      "ds_read_b64_tr_b16 v[146:147], v146 offset:49152\n"
      "v_add_u32 v150, 0x10000, %[a3]\n"
      "ds_read_b64_tr_b16 v[148:149], v150 offset:32768\n"
      "ds_read_b64_tr_b16 v[150:151], v150 offset:49152\n"
      "v_add_u32 v154, 0x10000, %[a4]\n"
      "ds_read_b64_tr_b16 v[152:153], v154 offset:32768\n"
      "ds_read_b64_tr_b16 v[154:155], v154 offset:49152\n"
      "v_add_u32 v158, 0x10000, %[a5]\n"
      "ds_read_b64_tr_b16 v[156:157], v158 offset:32768\n"
      "ds_read_b64_tr_b16 v[158:159], v158 offset:49152\n"
      "v_add_u32 v162, 0x10000, %[a6]\n"
      "ds_read_b64_tr_b16 v[160:161], v162 offset:32768\n"
      "ds_read_b64_tr_b16 v[162:163], v162 offset:49152\n"
      "v_add_u32 v166, 0x10000, %[a7]\n"
      "ds_read_b64_tr_b16 v[164:165], v166 offset:32768\n"
      "ds_read_b64_tr_b16 v[166:167], v166 offset:49152\n"
      "s_mov_b64 exec, 1\n"
      "ds_add_u32 %[cb], %[one] offset:116\n"
      "s_mov_b64 exec, -1\n"
      "150:\n"
      "s_mov_b32 %[n], %[nb]\n"
      "91:\n"
      "s_sub_u32 %[n], %[n], 1\n"
      "s_cbranch_scc1 92f\n"
      "ds_read_b32 v22, %[cb] offset:124\n"
      "s_waitcnt lgkmcnt(0)\n"
      "v_cmp_gt_u32 vcc, 8, v22\n"
      "s_cbranch_vccnz 91b\n"
      "92:\n"
      "ds_read_b32 v22, %[cb] offset:124\n"
      "ds_read_b128 v[32:35], %[pa] offset:2048\n"
      "ds_read_b128 v[36:39], %[pa] offset:3072\n"
      "ds_read_b32 v23, %[cb] offset:144\n"
      "s_waitcnt lgkmcnt(0)\n"
      "v_cmp_lt_u32 vcc, 8, v22\n"
      "s_cbranch_vccz 151f\n"
      "154:\n"
      "s_movk_i32 %[k], 640\n"
      "s_call_b64 %[ret], 201b\n"
      "s_branch 151f\n"
      "153:\n"
      "s_mov_b32 %[n], %[nb]\n"
      "91:\n"
      "s_sub_u32 %[n], %[n], 1\n"
      "s_cbranch_scc1 92f\n"
      "ds_read_b32 v20, %[cb] offset:128\n"
      "s_waitcnt lgkmcnt(0)\n"
      "v_cmp_gt_u32 vcc, 1, v20\n"
      "s_cbranch_vccnz 91b\n"
      "92:\n"
      "v_add_u32 v106, 0x10000, %[a0]\n"
      "ds_read_b64_tr_b16 v[104:105], v106 offset:0\n"
      "ds_read_b64_tr_b16 v[106:107], v106 offset:16384\n"
      "v_add_u32 v110, 0x10000, %[a1]\n"
      "ds_read_b64_tr_b16 v[108:109], v110 offset:0\n"
      "ds_read_b64_tr_b16 v[110:111], v110 offset:16384\n"
      "v_add_u32 v114, 0x10000, %[a2]\n"
      "ds_read_b64_tr_b16 v[112:113], v114 offset:0\n"
      "ds_read_b64_tr_b16 v[114:115], v114 offset:16384\n"
      "v_add_u32 v118, 0x10000, %[a3]\n"
      "ds_read_b64_tr_b16 v[116:117], v118 offset:0\n"
      "ds_read_b64_tr_b16 v[118:119], v118 offset:16384\n"
      "v_add_u32 v122, 0x10000, %[a4]\n"
      "ds_read_b64_tr_b16 v[120:121], v122 offset:0\n"
      "ds_read_b64_tr_b16 v[122:123], v122 offset:16384\n"
      "v_add_u32 v126, 0x10000, %[a5]\n"
      "ds_read_b64_tr_b16 v[124:125], v126 offset:0\n"
      "ds_read_b64_tr_b16 v[126:127], v126 offset:16384\n"
      "v_add_u32 v130, 0x10000, %[a6]\n"
      "ds_read_b64_tr_b16 v[128:129], v130 offset:0\n"
      "ds_read_b64_tr_b16 v[130:131], v130 offset:16384\n"
      "v_add_u32 v134, 0x10000, %[a7]\n"
      "ds_read_b64_tr_b16 v[132:133], v134 offset:0\n"
      "ds_read_b64_tr_b16 v[134:135], v134 offset:16384\n"
      "160:\n"
      "s_mov_b32 %[n], %[nb]\n"
      "91:\n"
      "s_sub_u32 %[n], %[n], 1\n"
      "s_cbranch_scc1 92f\n"
      "ds_read_b32 v20, %[cb] offset:140\n"
      "s_waitcnt lgkmcnt(0)\n"
      "v_cmp_gt_u32 vcc, 8, v20\n"
      "s_cbranch_vccnz 91b\n"
      "92:\n"
      "ds_read_b32 v20, %[cb] offset:140\n"
      "ds_read_b128 v[24:27], %[pa] offset:4096\n"
      "ds_read_b128 v[28:31], %[pa] offset:5120\n"
      "s_waitcnt lgkmcnt(0)\n"
      "v_cmp_lt_u32 vcc, 8, v20\n"
      "s_cbranch_vccz 161f\n"
      "164:\n"
      "s_movk_i32 %[k], 768\n"
      "s_call_b64 %[ret], 200b\n"
      "s_branch 161f\n"
      "174:\n"
      "s_movk_i32 %[k], 896\n"
      "s_call_b64 %[ret], 201b\n"
      "s_branch 171f\n"
      "300:\n"
      "s_mov_b32 %[n], %[nb]\n"
      "91:\n"
      "s_sub_u32 %[n], %[n], 1\n"
      "s_cbranch_scc1 92f\n"
      "s_sleep 1\n"
      "ds_read_b32 v20, %[cb] offset:32\n"
      "s_waitcnt lgkmcnt(0)\n"
      "v_cmp_gt_u32 vcc, 1, v20\n"
      "s_cbranch_vccnz 91b\n"
      "92:\n"
      "ds_read_b64_tr_b16 v[72:73], %[a0] offset:0\n"
      "ds_read_b64_tr_b16 v[74:75], %[a0] offset:16384\n"
      "ds_read_b64_tr_b16 v[76:77], %[a1] offset:0\n"
      "ds_read_b64_tr_b16 v[78:79], %[a1] offset:16384\n"
      "ds_read_b64_tr_b16 v[80:81], %[a2] offset:0\n"
      "ds_read_b64_tr_b16 v[82:83], %[a2] offset:16384\n"
      "ds_read_b64_tr_b16 v[84:85], %[a3] offset:0\n"
      "ds_read_b64_tr_b16 v[86:87], %[a3] offset:16384\n"
      "ds_read_b64_tr_b16 v[88:89], %[a4] offset:0\n"
      "ds_read_b64_tr_b16 v[90:91], %[a4] offset:16384\n"
      "ds_read_b64_tr_b16 v[92:93], %[a5] offset:0\n"
      "ds_read_b64_tr_b16 v[94:95], %[a5] offset:16384\n"
      "ds_read_b64_tr_b16 v[96:97], %[a6] offset:0\n"
      "ds_read_b64_tr_b16 v[98:99], %[a6] offset:16384\n"
      "ds_read_b64_tr_b16 v[100:101], %[a7] offset:0\n"
      "ds_read_b64_tr_b16 v[102:103], %[a7] offset:16384\n"
      "s_mov_b64 exec, 1\n"
      "ds_add_u32 %[cb], %[one] offset:36\n"
      "s_mov_b64 exec, -1\n"
      "s_mov_b32 %[n], %[nb]\n"
      "91:\n"
      "s_sub_u32 %[n], %[n], 1\n"
      "s_cbranch_scc1 92f\n"
      "s_sleep 1\n"
      "ds_read_b32 v20, %[cb] offset:48\n"
      "s_waitcnt lgkmcnt(0)\n"
      "v_cmp_gt_u32 vcc, 1, v20\n"
      "s_cbranch_vccnz 91b\n"
      "92:\n"
      "ds_read_b64_tr_b16 v[136:137], %[a0] offset:32768\n"
      "ds_read_b64_tr_b16 v[138:139], %[a0] offset:49152\n"
      "ds_read_b64_tr_b16 v[140:141], %[a1] offset:32768\n"
      "ds_read_b64_tr_b16 v[142:143], %[a1] offset:49152\n"
      "ds_read_b64_tr_b16 v[144:145], %[a2] offset:32768\n"
      "ds_read_b64_tr_b16 v[146:147], %[a2] offset:49152\n"
      "ds_read_b64_tr_b16 v[148:149], %[a3] offset:32768\n"
      "ds_read_b64_tr_b16 v[150:151], %[a3] offset:49152\n"
      "ds_read_b64_tr_b16 v[152:153], %[a4] offset:32768\n"
      "ds_read_b64_tr_b16 v[154:155], %[a4] offset:49152\n"
      "ds_read_b64_tr_b16 v[156:157], %[a5] offset:32768\n"
      "ds_read_b64_tr_b16 v[158:159], %[a5] offset:49152\n"
      "ds_read_b64_tr_b16 v[160:161], %[a6] offset:32768\n"
      "ds_read_b64_tr_b16 v[162:163], %[a6] offset:49152\n"
      "ds_read_b64_tr_b16 v[164:165], %[a7] offset:32768\n"
      "ds_read_b64_tr_b16 v[166:167], %[a7] offset:49152\n"
      "s_mov_b64 exec, 1\n"
      "ds_add_u32 %[cb], %[one] offset:52\n"
      "s_mov_b64 exec, -1\n"
      "s_mov_b32 %[n], %[nb]\n"
      "91:\n"
      "s_sub_u32 %[n], %[n], 1\n"
      "s_cbranch_scc1 92f\n"
      "s_sleep 1\n"
      "ds_read_b32 v20, %[cb] offset:64\n"
      "s_waitcnt lgkmcnt(0)\n"
      "v_cmp_gt_u32 vcc, 1, v20\n"
      "s_cbranch_vccnz 91b\n"
      "92:\n"
      "v_add_u32 v106, 0x10000, %[a0]\n"
      "ds_read_b64_tr_b16 v[104:105], v106 offset:0\n"
      "ds_read_b64_tr_b16 v[106:107], v106 offset:16384\n"
      "v_add_u32 v110, 0x10000, %[a1]\n"
      "ds_read_b64_tr_b16 v[108:109], v110 offset:0\n"
      "ds_read_b64_tr_b16 v[110:111], v110 offset:16384\n"
      "v_add_u32 v114, 0x10000, %[a2]\n"
      "ds_read_b64_tr_b16 v[112:113], v114 offset:0\n"
      "ds_read_b64_tr_b16 v[114:115], v114 offset:16384\n"
      "v_add_u32 v118, 0x10000, %[a3]\n"
      "ds_read_b64_tr_b16 v[116:117], v118 offset:0\n"
      "ds_read_b64_tr_b16 v[118:119], v118 offset:16384\n"
      "v_add_u32 v122, 0x10000, %[a4]\n"
      "ds_read_b64_tr_b16 v[120:121], v122 offset:0\n"
      "ds_read_b64_tr_b16 v[122:123], v122 offset:16384\n"
      "v_add_u32 v126, 0x10000, %[a5]\n"
      "ds_read_b64_tr_b16 v[124:125], v126 offset:0\n"
      "ds_read_b64_tr_b16 v[126:127], v126 offset:16384\n"
      "v_add_u32 v130, 0x10000, %[a6]\n"
      "ds_read_b64_tr_b16 v[128:129], v130 offset:0\n"
      "ds_read_b64_tr_b16 v[130:131], v130 offset:16384\n"
      "v_add_u32 v134, 0x10000, %[a7]\n"
      "ds_read_b64_tr_b16 v[132:133], v134 offset:0\n"
      "ds_read_b64_tr_b16 v[134:135], v134 offset:16384\n"
      "s_mov_b64 exec, 1\n"
      "ds_add_u32 %[cb], %[one] offset:68\n"
      "s_mov_b64 exec, -1\n"
      "s_mov_b64 %[m0], 0\n"
      "s_mov_b32 %[n], %[nb]\n"
      "91:\n"
      "s_sub_u32 %[n], %[n], 1\n"
      "s_cbranch_scc1 92f\n"
      "s_sleep 1\n"
      "ds_read_b32 v20, %[cb] offset:40\n"
      "s_waitcnt lgkmcnt(0)\n"
      "v_cmp_gt_u32 vcc, 1, v20\n"
      "s_cbranch_vccnz 91b\n"
      "92:\n"
      "s_mov_b32 %[n], %[nb]\n"
      "91:\n"
      "s_sub_u32 %[n], %[n], 1\n"
      "s_cbranch_scc1 92f\n"
      "ds_read_b32 v20, %[cb] offset:44\n"
      "s_waitcnt lgkmcnt(0)\n"
      "v_cmp_gt_u32 vcc, 8, v20\n"
      "s_cbranch_vccnz 91b\n"
      "92:\n"
      "ds_read_b32 v20, %[cb] offset:44\n"
      "ds_read_b128 v[24:27], %[pa] offset:0\n"
      "ds_read_b128 v[28:31], %[pa] offset:1024\n"
      "ds_read_b32 v21, %[cb] offset:64\n"
      "s_waitcnt lgkmcnt(0)\n"
      "v_mfma_f32_16x16x32_bf16 v[40:43], v[24:27], v[72:75], 0\n"
      "v_mfma_f32_16x16x32_bf16 v[72:75], v[28:31], v[72:75], 0\n"
      "v_mfma_f32_16x16x32_bf16 v[44:47], v[24:27], v[76:79], 0\n"
      "v_mfma_f32_16x16x32_bf16 v[76:79], v[28:31], v[76:79], 0\n"
      "v_mfma_f32_16x16x32_bf16 v[48:51], v[24:27], v[80:83], 0\n"
      "v_mfma_f32_16x16x32_bf16 v[80:83], v[28:31], v[80:83], 0\n"
      "v_mfma_f32_16x16x32_bf16 v[52:55], v[24:27], v[84:87], 0\n"
      "v_mfma_f32_16x16x32_bf16 v[84:87], v[28:31], v[84:87], 0\n"
      "ds_read_b32 v22, %[cb] offset:60\n"
      "ds_read_b128 v[32:35], %[pa] offset:2048\n"
      "ds_read_b128 v[36:39], %[pa] offset:3072\n"
      "ds_read_b32 v23, %[cb] offset:80\n"
      "v_mfma_f32_16x16x32_bf16 v[56:59], v[24:27], v[88:91], 0\n"
      "v_mfma_f32_16x16x32_bf16 v[88:91], v[28:31], v[88:91], 0\n"
      "v_mfma_f32_16x16x32_bf16 v[60:63], v[24:27], v[92:95], 0\n"
      "v_mfma_f32_16x16x32_bf16 v[92:95], v[28:31], v[92:95], 0\n"
      "v_mfma_f32_16x16x32_bf16 v[64:67], v[24:27], v[96:99], 0\n"
      "v_mfma_f32_16x16x32_bf16 v[96:99], v[28:31], v[96:99], 0\n"
      "v_mfma_f32_16x16x32_bf16 v[68:71], v[24:27], v[100:103], 0\n"
      "v_mfma_f32_16x16x32_bf16 v[100:103], v[28:31], v[100:103], 0\n"
      "s_waitcnt lgkmcnt(0)\n"
      "v_cmp_ne_u32 vcc, 8, v22\n"
      "s_cbranch_vccnz 110b\n"
      "111:\n"
      "v_cmp_eq_u32 %[m1], 0, v23\n"
      "v_mfma_f32_16x16x32_bf16 v[40:43], v[32:35], v[136:139], v[40:43]\n"
      "v_mfma_f32_16x16x32_bf16 v[72:75], v[36:39], v[136:139], v[72:75]\n"
      "ds_read_b64_tr_b16 v[136:137], %[a0] offset:0\n"
      "ds_read_b64_tr_b16 v[138:139], %[a0] offset:16384\n"
      "v_mfma_f32_16x16x32_bf16 v[44:47], v[32:35], v[140:143], v[44:47]\n"
      "v_mfma_f32_16x16x32_bf16 v[76:79], v[36:39], v[140:143], v[76:79]\n"
      "ds_read_b64_tr_b16 v[140:141], %[a1] offset:0\n"
      "ds_read_b64_tr_b16 v[142:143], %[a1] offset:16384\n"
      "v_mfma_f32_16x16x32_bf16 v[48:51], v[32:35], v[144:147], v[48:51]\n"
      "v_mfma_f32_16x16x32_bf16 v[80:83], v[36:39], v[144:147], v[80:83]\n"
      "ds_read_b64_tr_b16 v[144:145], %[a2] offset:0\n"
      "ds_read_b64_tr_b16 v[146:147], %[a2] offset:16384\n"
      "v_mfma_f32_16x16x32_bf16 v[52:55], v[32:35], v[148:151], v[52:55]\n"
      "v_mfma_f32_16x16x32_bf16 v[84:87], v[36:39], v[148:151], v[84:87]\n"
      "ds_read_b64_tr_b16 v[148:149], %[a3] offset:0\n"
      "ds_read_b64_tr_b16 v[150:151], %[a3] offset:16384\n"
      "ds_read_b32 v20, %[cb] offset:76\n"
      "ds_read_b128 v[24:27], %[pa] offset:4096\n"
      "ds_read_b128 v[28:31], %[pa] offset:5120\n"
      "ds_read_b32 v21, %[cb] offset:96\n"
      "v_mfma_f32_16x16x32_bf16 v[56:59], v[32:35], v[152:155], v[56:59]\n"
      "v_mfma_f32_16x16x32_bf16 v[88:91], v[36:39], v[152:155], v[88:91]\n"
      "ds_read_b64_tr_b16 v[152:153], %[a4] offset:0\n"
      "ds_read_b64_tr_b16 v[154:155], %[a4] offset:16384\n"
      "v_mfma_f32_16x16x32_bf16 v[60:63], v[32:35], v[156:159], v[60:63]\n"
      "v_mfma_f32_16x16x32_bf16 v[92:95], v[36:39], v[156:159], v[92:95]\n"
      "ds_read_b64_tr_b16 v[156:157], %[a5] offset:0\n"
      "ds_read_b64_tr_b16 v[158:159], %[a5] offset:16384\n"
      "v_mfma_f32_16x16x32_bf16 v[64:67], v[32:35], v[160:163], v[64:67]\n"
      "v_mfma_f32_16x16x32_bf16 v[96:99], v[36:39], v[160:163], v[96:99]\n"
      "ds_read_b64_tr_b16 v[160:161], %[a6] offset:0\n"
      "ds_read_b64_tr_b16 v[162:163], %[a6] offset:16384\n"
      "v_mfma_f32_16x16x32_bf16 v[68:71], v[32:35], v[164:167], v[68:71]\n"
      "v_mfma_f32_16x16x32_bf16 v[100:103], v[36:39], v[164:167], v[100:103]\n"
      "ds_read_b64_tr_b16 v[164:165], %[a7] offset:0\n"
      "ds_read_b64_tr_b16 v[166:167], %[a7] offset:16384\n"
      "s_cmp_lg_u64 %[m1], 0\n"
      "s_cbranch_scc1 112f\n"
      "s_mov_b64 exec, 1\n"
      "ds_add_u32 %[cb], %[one] offset:84\n"
      "s_mov_b64 exec, -1\n"
      "112:\n"
      "s_waitcnt lgkmcnt(8)\n"
      "v_cmp_ne_u32 vcc, 8, v20\n"
      "s_cbranch_vccnz 120b\n"
      "121:\n"
      "v_cmp_eq_u32 %[m0], 0, v21\n"
      "v_mfma_f32_16x16x32_bf16 v[40:43], v[24:27], v[104:107], v[40:43]\n"
      "v_mfma_f32_16x16x32_bf16 v[72:75], v[28:31], v[104:107], v[72:75]\n"
      "ds_read_b64_tr_b16 v[104:105], %[a0] offset:32768\n"
      "ds_read_b64_tr_b16 v[106:107], %[a0] offset:49152\n"
      "v_mfma_f32_16x16x32_bf16 v[44:47], v[24:27], v[108:111], v[44:47]\n"
      "v_mfma_f32_16x16x32_bf16 v[76:79], v[28:31], v[108:111], v[76:79]\n"
      "ds_read_b64_tr_b16 v[108:109], %[a1] offset:32768\n"
      "ds_read_b64_tr_b16 v[110:111], %[a1] offset:49152\n"
      "v_mfma_f32_16x16x32_bf16 v[48:51], v[24:27], v[112:115], v[48:51]\n"
      "v_mfma_f32_16x16x32_bf16 v[80:83], v[28:31], v[112:115], v[80:83]\n"
      "ds_read_b64_tr_b16 v[112:113], %[a2] offset:32768\n"
      "ds_read_b64_tr_b16 v[114:115], %[a2] offset:49152\n"
      "v_mfma_f32_16x16x32_bf16 v[52:55], v[24:27], v[116:119], v[52:55]\n"
      "v_mfma_f32_16x16x32_bf16 v[84:87], v[28:31], v[116:119], v[84:87]\n"
      "ds_read_b64_tr_b16 v[116:117], %[a3] offset:32768\n"
      "ds_read_b64_tr_b16 v[118:119], %[a3] offset:49152\n"
      "ds_read_b32 v22, %[cb] offset:92\n"
      "ds_read_b128 v[32:35], %[pa] offset:6144\n"
      "ds_read_b128 v[36:39], %[pa] offset:7168\n"
      "ds_read_b32 v23, %[cb] offset:112\n"
      "v_mfma_f32_16x16x32_bf16 v[56:59], v[24:27], v[120:123], v[56:59]\n"
      "v_mfma_f32_16x16x32_bf16 v[88:91], v[28:31], v[120:123], v[88:91]\n"
      "ds_read_b64_tr_b16 v[120:121], %[a4] offset:32768\n"
      "ds_read_b64_tr_b16 v[122:123], %[a4] offset:49152\n"
      "v_mfma_f32_16x16x32_bf16 v[60:63], v[24:27], v[124:127], v[60:63]\n"
      "v_mfma_f32_16x16x32_bf16 v[92:95], v[28:31], v[124:127], v[92:95]\n"
      "ds_read_b64_tr_b16 v[124:125], %[a5] offset:32768\n"
      "ds_read_b64_tr_b16 v[126:127], %[a5] offset:49152\n"
      "v_mfma_f32_16x16x32_bf16 v[64:67], v[24:27], v[128:131], v[64:67]\n"
      "v_mfma_f32_16x16x32_bf16 v[96:99], v[28:31], v[128:131], v[96:99]\n"
      "ds_read_b64_tr_b16 v[128:129], %[a6] offset:32768\n"
      "ds_read_b64_tr_b16 v[130:131], %[a6] offset:49152\n"
      "v_mfma_f32_16x16x32_bf16 v[68:71], v[24:27], v[132:135], v[68:71]\n"
      "v_mfma_f32_16x16x32_bf16 v[100:103], v[28:31], v[132:135], v[100:103]\n"
      "ds_read_b64_tr_b16 v[132:133], %[a7] offset:32768\n"
      "ds_read_b64_tr_b16 v[134:135], %[a7] offset:49152\n"
      "s_cmp_lg_u64 %[m0], 0\n"
      "s_cbranch_scc1 122f\n"
      "s_mov_b64 exec, 1\n"
      "ds_add_u32 %[cb], %[one] offset:100\n"
      "s_mov_b64 exec, -1\n"
      "122:\n"
      "s_cmp_lg_u64 %[m1], 0\n"
      "s_cbranch_scc1 123b\n"
      "s_waitcnt lgkmcnt(8)\n"
      "v_cmp_ne_u32 vcc, 8, v22\n"
      "s_cbranch_vccnz 130b\n"
      "131:\n"
      "v_cmp_eq_u32 %[m1], 0, v23\n"
      "v_mfma_f32_16x16x32_bf16 v[40:43], v[32:35], v[136:139], v[40:43]\n"
      "v_mfma_f32_16x16x32_bf16 v[72:75], v[36:39], v[136:139], v[72:75]\n"
      "v_add_u32 v138, 0x10000, %[a0]\n"
      "ds_read_b64_tr_b16 v[136:137], v138 offset:32768\n"
      "ds_read_b64_tr_b16 v[138:139], v138 offset:49152\n"
      "v_mfma_f32_16x16x32_bf16 v[44:47], v[32:35], v[140:143], v[44:47]\n"
      "v_mfma_f32_16x16x32_bf16 v[76:79], v[36:39], v[140:143], v[76:79]\n"
      "v_add_u32 v142, 0x10000, %[a1]\n"
      "ds_read_b64_tr_b16 v[140:141], v142 offset:32768\n"
      "ds_read_b64_tr_b16 v[142:143], v142 offset:49152\n"
      "v_mfma_f32_16x16x32_bf16 v[48:51], v[32:35], v[144:147], v[48:51]\n"
      "v_mfma_f32_16x16x32_bf16 v[80:83], v[36:39], v[144:147], v[80:83]\n"
      "v_add_u32 v146, 0x10000, %[a2]\n"
      "ds_read_b64_tr_b16 v[144:145], v146 offset:32768\n"
      "ds_read_b64_tr_b16 v[146:147], v146 offset:49152\n"
      "v_mfma_f32_16x16x32_bf16 v[52:55], v[32:35], v[148:151], v[52:55]\n"
      "v_mfma_f32_16x16x32_bf16 v[84:87], v[36:39], v[148:151], v[84:87]\n"
      "v_add_u32 v150, 0x10000, %[a3]\n"
      "ds_read_b64_tr_b16 v[148:149], v150 offset:32768\n"
      "ds_read_b64_tr_b16 v[150:151], v150 offset:49152\n"
      "ds_read_b32 v20, %[cb] offset:108\n"
      "ds_read_b128 v[24:27], %[pa] offset:0\n"
      "ds_read_b128 v[28:31], %[pa] offset:1024\n"
      "ds_read_b32 v21, %[cb] offset:128\n"
      "v_mfma_f32_16x16x32_bf16 v[56:59], v[32:35], v[152:155], v[56:59]\n"
      "v_mfma_f32_16x16x32_bf16 v[88:91], v[36:39], v[152:155], v[88:91]\n"
      "v_add_u32 v154, 0x10000, %[a4]\n"
      "ds_read_b64_tr_b16 v[152:153], v154 offset:32768\n"
      "ds_read_b64_tr_b16 v[154:155], v154 offset:49152\n"
      "v_mfma_f32_16x16x32_bf16 v[60:63], v[32:35], v[156:159], v[60:63]\n"
      "v_mfma_f32_16x16x32_bf16 v[92:95], v[36:39], v[156:159], v[92:95]\n"
      "v_add_u32 v158, 0x10000, %[a5]\n"
      "ds_read_b64_tr_b16 v[156:157], v158 offset:32768\n"
      "ds_read_b64_tr_b16 v[158:159], v158 offset:49152\n"
      "v_mfma_f32_16x16x32_bf16 v[64:67], v[32:35], v[160:163], v[64:67]\n"
      "v_mfma_f32_16x16x32_bf16 v[96:99], v[36:39], v[160:163], v[96:99]\n"
      "v_add_u32 v162, 0x10000, %[a6]\n"
      "ds_read_b64_tr_b16 v[160:161], v162 offset:32768\n"
      "ds_read_b64_tr_b16 v[162:163], v162 offset:49152\n"
      "v_mfma_f32_16x16x32_bf16 v[68:71], v[32:35], v[164:167], v[68:71]\n"
      "v_mfma_f32_16x16x32_bf16 v[100:103], v[36:39], v[164:167], v[100:103]\n"
      "v_add_u32 v166, 0x10000, %[a7]\n"
      "ds_read_b64_tr_b16 v[164:165], v166 offset:32768\n"
      "ds_read_b64_tr_b16 v[166:167], v166 offset:49152\n"
      "s_cmp_lg_u64 %[m1], 0\n"
      "s_cbranch_scc1 132f\n"
      "s_mov_b64 exec, 1\n"
      "ds_add_u32 %[cb], %[one] offset:116\n"
      "s_mov_b64 exec, -1\n"
      "132:\n"
      "s_cmp_lg_u64 %[m0], 0\n"
      "s_cbranch_scc1 133b\n"
      "s_waitcnt lgkmcnt(8)\n"
      "v_cmp_ne_u32 vcc, 8, v20\n"
      "s_cbranch_vccnz 140b\n"
      "141:\n"
      "v_cmp_eq_u32 %[m0], 0, v21\n"
      "v_mfma_f32_16x16x32_bf16 v[40:43], v[24:27], v[104:107], v[40:43]\n"
      "v_mfma_f32_16x16x32_bf16 v[72:75], v[28:31], v[104:107], v[72:75]\n"
      "v_add_u32 v106, 0x10000, %[a0]\n"
      "ds_read_b64_tr_b16 v[104:105], v106 offset:0\n"
      "ds_read_b64_tr_b16 v[106:107], v106 offset:16384\n"
      "v_mfma_f32_16x16x32_bf16 v[44:47], v[24:27], v[108:111], v[44:47]\n"
      "v_mfma_f32_16x16x32_bf16 v[76:79], v[28:31], v[108:111], v[76:79]\n"
      "v_add_u32 v110, 0x10000, %[a1]\n"
      "ds_read_b64_tr_b16 v[108:109], v110 offset:0\n"
      "ds_read_b64_tr_b16 v[110:111], v110 offset:16384\n"
      "v_mfma_f32_16x16x32_bf16 v[48:51], v[24:27], v[112:115], v[48:51]\n"
      "v_mfma_f32_16x16x32_bf16 v[80:83], v[28:31], v[112:115], v[80:83]\n"
      "v_add_u32 v114, 0x10000, %[a2]\n"
      "ds_read_b64_tr_b16 v[112:113], v114 offset:0\n"
      "ds_read_b64_tr_b16 v[114:115], v114 offset:16384\n"
      "v_mfma_f32_16x16x32_bf16 v[52:55], v[24:27], v[116:119], v[52:55]\n"
      "v_mfma_f32_16x16x32_bf16 v[84:87], v[28:31], v[116:119], v[84:87]\n"
      "v_add_u32 v118, 0x10000, %[a3]\n"
      "ds_read_b64_tr_b16 v[116:117], v118 offset:0\n"
      "ds_read_b64_tr_b16 v[118:119], v118 offset:16384\n"
      "ds_read_b32 v22, %[cb] offset:124\n"
      "ds_read_b128 v[32:35], %[pa] offset:2048\n"
      "ds_read_b128 v[36:39], %[pa] offset:3072\n"
      "ds_read_b32 v23, %[cb] offset:144\n"
      "v_mfma_f32_16x16x32_bf16 v[56:59], v[24:27], v[120:123], v[56:59]\n"
      "v_mfma_f32_16x16x32_bf16 v[88:91], v[28:31], v[120:123], v[88:91]\n"
      "v_add_u32 v122, 0x10000, %[a4]\n"
      "ds_read_b64_tr_b16 v[120:121], v122 offset:0\n"
      "ds_read_b64_tr_b16 v[122:123], v122 offset:16384\n"
      "v_mfma_f32_16x16x32_bf16 v[60:63], v[24:27], v[124:127], v[60:63]\n"
      "v_mfma_f32_16x16x32_bf16 v[92:95], v[28:31], v[124:127], v[92:95]\n"
      "v_add_u32 v126, 0x10000, %[a5]\n"
      "ds_read_b64_tr_b16 v[124:125], v126 offset:0\n"
      "ds_read_b64_tr_b16 v[126:127], v126 offset:16384\n"
      "v_mfma_f32_16x16x32_bf16 v[64:67], v[24:27], v[128:131], v[64:67]\n"
      "v_mfma_f32_16x16x32_bf16 v[96:99], v[28:31], v[128:131], v[96:99]\n"
      "v_add_u32 v130, 0x10000, %[a6]\n"
      "ds_read_b64_tr_b16 v[128:129], v130 offset:0\n"
      "ds_read_b64_tr_b16 v[130:131], v130 offset:16384\n"
      "v_mfma_f32_16x16x32_bf16 v[68:71], v[24:27], v[132:135], v[68:71]\n"
      "v_mfma_f32_16x16x32_bf16 v[100:103], v[28:31], v[132:135], v[100:103]\n"
      "v_add_u32 v134, 0x10000, %[a7]\n"
      "ds_read_b64_tr_b16 v[132:133], v134 offset:0\n"
      "ds_read_b64_tr_b16 v[134:135], v134 offset:16384\n"
      "s_cmp_lg_u64 %[m1], 0\n"
      "s_cbranch_scc1 143b\n"
      "s_waitcnt lgkmcnt(8)\n"
      "v_cmp_ne_u32 vcc, 8, v22\n"
      "s_cbranch_vccnz 150b\n"
      "151:\n"
      "v_cmp_eq_u32 %[m1], 0, v23\n"
      "v_mfma_f32_16x16x32_bf16 v[40:43], v[32:35], v[136:139], v[40:43]\n"
      "v_mfma_f32_16x16x32_bf16 v[72:75], v[36:39], v[136:139], v[72:75]\n"
      "ds_read_b64_tr_b16 v[136:137], %[a0] offset:0\n"
      "ds_read_b64_tr_b16 v[138:139], %[a0] offset:16384\n"
      "v_mfma_f32_16x16x32_bf16 v[44:47], v[32:35], v[140:143], v[44:47]\n"
      "v_mfma_f32_16x16x32_bf16 v[76:79], v[36:39], v[140:143], v[76:79]\n"
      "ds_read_b64_tr_b16 v[140:141], %[a1] offset:0\n"
      "ds_read_b64_tr_b16 v[142:143], %[a1] offset:16384\n"
      "v_mfma_f32_16x16x32_bf16 v[48:51], v[32:35], v[144:147], v[48:51]\n"
      "v_mfma_f32_16x16x32_bf16 v[80:83], v[36:39], v[144:147], v[80:83]\n"
      "ds_read_b64_tr_b16 v[144:145], %[a2] offset:0\n"
      "ds_read_b64_tr_b16 v[146:147], %[a2] offset:16384\n"
      "v_mfma_f32_16x16x32_bf16 v[52:55], v[32:35], v[148:151], v[52:55]\n"
      "v_mfma_f32_16x16x32_bf16 v[84:87], v[36:39], v[148:151], v[84:87]\n"
      "ds_read_b64_tr_b16 v[148:149], %[a3] offset:0\n"
      "ds_read_b64_tr_b16 v[150:151], %[a3] offset:16384\n"
      "ds_read_b32 v20, %[cb] offset:140\n"
      "ds_read_b128 v[24:27], %[pa] offset:4096\n"
      "ds_read_b128 v[28:31], %[pa] offset:5120\n"
      "v_mfma_f32_16x16x32_bf16 v[56:59], v[32:35], v[152:155], v[56:59]\n"
      "v_mfma_f32_16x16x32_bf16 v[88:91], v[36:39], v[152:155], v[88:91]\n"
      "ds_read_b64_tr_b16 v[152:153], %[a4] offset:0\n"
      "ds_read_b64_tr_b16 v[154:155], %[a4] offset:16384\n"
      "v_mfma_f32_16x16x32_bf16 v[60:63], v[32:35], v[156:159], v[60:63]\n"
      "v_mfma_f32_16x16x32_bf16 v[92:95], v[36:39], v[156:159], v[92:95]\n"
      "ds_read_b64_tr_b16 v[156:157], %[a5] offset:0\n"
      "ds_read_b64_tr_b16 v[158:159], %[a5] offset:16384\n"
      "v_mfma_f32_16x16x32_bf16 v[64:67], v[32:35], v[160:163], v[64:67]\n"
      "v_mfma_f32_16x16x32_bf16 v[96:99], v[36:39], v[160:163], v[96:99]\n"
      "ds_read_b64_tr_b16 v[160:161], %[a6] offset:0\n"
      "ds_read_b64_tr_b16 v[162:163], %[a6] offset:16384\n"
      "v_mfma_f32_16x16x32_bf16 v[68:71], v[32:35], v[164:167], v[68:71]\n"
      "v_mfma_f32_16x16x32_bf16 v[100:103], v[36:39], v[164:167], v[100:103]\n"
      "ds_read_b64_tr_b16 v[164:165], %[a7] offset:0\n"
      "ds_read_b64_tr_b16 v[166:167], %[a7] offset:16384\n"
      "s_cmp_lg_u64 %[m0], 0\n"
      "s_cbranch_scc1 153b\n"
      "s_waitcnt lgkmcnt(8)\n"
      "v_cmp_ne_u32 vcc, 8, v20\n"
      "s_cbranch_vccnz 160b\n"
      "161:\n"
      "v_mfma_f32_16x16x32_bf16 v[40:43], v[24:27], v[104:107], v[40:43]\n"
      "v_mfma_f32_16x16x32_bf16 v[72:75], v[28:31], v[104:107], v[72:75]\n"
      "v_mfma_f32_16x16x32_bf16 v[44:47], v[24:27], v[108:111], v[44:47]\n"
      "v_mfma_f32_16x16x32_bf16 v[76:79], v[28:31], v[108:111], v[76:79]\n"
      "v_mfma_f32_16x16x32_bf16 v[48:51], v[24:27], v[112:115], v[48:51]\n"
      "v_mfma_f32_16x16x32_bf16 v[80:83], v[28:31], v[112:115], v[80:83]\n"
      "v_mfma_f32_16x16x32_bf16 v[52:55], v[24:27], v[116:119], v[52:55]\n"
      "v_mfma_f32_16x16x32_bf16 v[84:87], v[28:31], v[116:119], v[84:87]\n"
      "v_mfma_f32_16x16x32_bf16 v[56:59], v[24:27], v[120:123], v[56:59]\n"
      "v_mfma_f32_16x16x32_bf16 v[88:91], v[28:31], v[120:123], v[88:91]\n"
      "v_mfma_f32_16x16x32_bf16 v[60:63], v[24:27], v[124:127], v[60:63]\n"
      "v_mfma_f32_16x16x32_bf16 v[92:95], v[28:31], v[124:127], v[92:95]\n"
      "v_mfma_f32_16x16x32_bf16 v[64:67], v[24:27], v[128:131], v[64:67]\n"
      "v_mfma_f32_16x16x32_bf16 v[96:99], v[28:31], v[128:131], v[96:99]\n"
      "v_mfma_f32_16x16x32_bf16 v[68:71], v[24:27], v[132:135], v[68:71]\n"
      "v_mfma_f32_16x16x32_bf16 v[100:103], v[28:31], v[132:135], v[100:103]\n"
      "s_cmp_lg_u64 %[m1], 0\n"
      "s_cbranch_scc0 170f\n"
      "s_mov_b32 %[n], %[nb]\n"
      "91:\n"
      "s_sub_u32 %[n], %[n], 1\n"
      "s_cbranch_scc1 92f\n"
      "ds_read_b32 v22, %[cb] offset:144\n"
      "s_waitcnt lgkmcnt(0)\n"
      "v_cmp_gt_u32 vcc, 1, v22\n"
      "s_cbranch_vccnz 91b\n"
      "92:\n"
      "ds_read_b64_tr_b16 v[136:137], %[a0] offset:0\n"
      "ds_read_b64_tr_b16 v[138:139], %[a0] offset:16384\n"
      "ds_read_b64_tr_b16 v[140:141], %[a1] offset:0\n"
      "ds_read_b64_tr_b16 v[142:143], %[a1] offset:16384\n"
      "ds_read_b64_tr_b16 v[144:145], %[a2] offset:0\n"
      "ds_read_b64_tr_b16 v[146:147], %[a2] offset:16384\n"
      "ds_read_b64_tr_b16 v[148:149], %[a3] offset:0\n"
      "ds_read_b64_tr_b16 v[150:151], %[a3] offset:16384\n"
      "ds_read_b64_tr_b16 v[152:153], %[a4] offset:0\n"
      "ds_read_b64_tr_b16 v[154:155], %[a4] offset:16384\n"
      "ds_read_b64_tr_b16 v[156:157], %[a5] offset:0\n"
      "ds_read_b64_tr_b16 v[158:159], %[a5] offset:16384\n"
      "ds_read_b64_tr_b16 v[160:161], %[a6] offset:0\n"
      "ds_read_b64_tr_b16 v[162:163], %[a6] offset:16384\n"
      "ds_read_b64_tr_b16 v[164:165], %[a7] offset:0\n"
      "ds_read_b64_tr_b16 v[166:167], %[a7] offset:16384\n"
      "170:\n"
      "s_mov_b32 %[n], %[nb]\n"
      "91:\n"
      "s_sub_u32 %[n], %[n], 1\n"
      "s_cbranch_scc1 92f\n"
      "ds_read_b32 v22, %[cb] offset:152\n"
      "s_waitcnt lgkmcnt(0)\n"
      "v_cmp_gt_u32 vcc, 4, v22\n"
      "s_cbranch_vccnz 91b\n"
      "92:\n"
      "s_mov_b32 %[n], %[nb]\n"
      "91:\n"
      "s_sub_u32 %[n], %[n], 1\n"
      "s_cbranch_scc1 92f\n"
      "ds_read_b32 v22, %[cb] offset:156\n"
      "ds_read_b128 v[32:35], %[pa] offset:6144\n"
      "ds_read_b128 v[36:39], %[pa] offset:7168\n"
      "s_waitcnt lgkmcnt(0)\n"
      "v_cmp_gt_u32 vcc, 8, v22\n"
      "s_cbranch_vccnz 91b\n"
      "92:\n"
      "v_cmp_lt_u32 vcc, 8, v22\n"
      "s_cbranch_vccnz 174b\n"
      "171:\n"
      "v_mfma_f32_16x16x32_bf16 v[40:43], v[32:35], v[136:139], v[40:43]\n"
      "v_mfma_f32_16x16x32_bf16 v[44:47], v[32:35], v[140:143], v[44:47]\n"
      "v_mfma_f32_16x16x32_bf16 v[48:51], v[32:35], v[144:147], v[48:51]\n"
      "v_mfma_f32_16x16x32_bf16 v[52:55], v[32:35], v[148:151], v[52:55]\n"
      "v_mfma_f32_16x16x32_bf16 v[56:59], v[32:35], v[152:155], v[56:59]\n"
      "v_mfma_f32_16x16x32_bf16 v[60:63], v[32:35], v[156:159], v[60:63]\n"
      "v_mfma_f32_16x16x32_bf16 v[64:67], v[32:35], v[160:163], v[64:67]\n"
      "v_mfma_f32_16x16x32_bf16 v[68:71], v[32:35], v[164:167], v[68:71]\n"
      "v_mfma_f32_16x16x32_bf16 v[72:75], v[36:39], v[136:139], v[72:75]\n"
      "v_mfma_f32_16x16x32_bf16 v[76:79], v[36:39], v[140:143], v[76:79]\n"
      "v_mfma_f32_16x16x32_bf16 v[80:83], v[36:39], v[144:147], v[80:83]\n"
      "v_mfma_f32_16x16x32_bf16 v[84:87], v[36:39], v[148:151], v[84:87]\n"
      "v_mfma_f32_16x16x32_bf16 v[88:91], v[36:39], v[152:155], v[88:91]\n"
      "v_mfma_f32_16x16x32_bf16 v[92:95], v[36:39], v[156:159], v[92:95]\n"
      "v_mfma_f32_16x16x32_bf16 v[96:99], v[36:39], v[160:163], v[96:99]\n"
      "v_mfma_f32_16x16x32_bf16 v[100:103], v[36:39], v[164:167], v[100:103]\n"
      : "={v[40:71]}"(o0), "={v[72:103]}"(o1), [n] "=&s"(n), [k] "=&s"(k), [m0] "=&s"(m0), [m1] "=&s"(m1), [ret] "=&s"(ret)
      : [a0] "{v9}"(a[0]), [a1] "{v10}"(a[1]), [a2] "{v11}"(a[2]), [a3] "{v12}"(a[3]), [a4] "{v13}"(a[4]), [a5] "{v14}"(a[5]), [a6] "{v15}"(a[6]),
        [a7] "{v16}"(a[7]), [pa] "{v17}"(pa), [cb] "{v18}"(cb), [one] "{v19}"(one), [fac] "s"(T_FAC), [nb] "s"(nb)
      : "vcc", "scc", "memory", "v20", "v21", "v22", "v23", "v24", "v25", "v26", "v27", "v28", "v29", "v30", "v31", "v32", "v33", "v34", "v35", "v36", "v37", "v38", "v39", "v104", "v105", "v106", "v107", "v108", "v109", "v110", "v111", "v112", "v113", "v114", "v115", "v116", "v117", "v118", "v119", "v120", "v121", "v122", "v123", "v124", "v125", "v126", "v127", "v128", "v129", "v130", "v131", "v132", "v133", "v134", "v135", "v136", "v137", "v138", "v139", "v140", "v141", "v142", "v143", "v144", "v145", "v146", "v147", "v148", "v149", "v150", "v151", "v152", "v153", "v154", "v155", "v156", "v157", "v158", "v159", "v160", "v161", "v162", "v163", "v164", "v165", "v166", "v167");
}
DI void ffn_part(const Params& p, int b, int s, int wave, unsigned t0, u64 tag);
DI void attn_wg(const Params& p, int b, int s, int wave, unsigned t0) {
  unsigned char* ws = p.ws;
  const rsrc_t rs = __builtin_amdgcn_make_buffer_rsrc(ws, 0, WS_BYTES, 0x00020000);
  const int w = wave & 3;
  const f32x4 z4 = {0.f, 0.f, 0.f, 0.f};

  if (wave < 2) { const int lane = fresh_lane(); *(LAS unsigned*)(T_CNT + wave * 256 + lane * 4) = wave == 0 && lane < 8 ? 8u : 0u; }
  bar();
  u64 tag = 0;
  if (wave != 8) tag = get_tag(ws);
  if (wave == 8) {
    const int lane = fresh_lane();
    unsigned vo[16];
#pragma unroll
    for (int r = 0; r < 16; ++r) vo[r] = 16 * (lane ^ fsw(r));
    const unsigned char* kv = (const unsigned char*)p.ckv + (size_t)(b * 4096 + s * 32) * 1024;
    nap<1>();
    wait_until(t0 + T_PREF);
    __builtin_amdgcn_s_setprio(PRIO_L);
    bool watch = true;
    STAMP(62); STAMPD(31);
#pragma unroll 1
    for (int t = 0; t < 8; ++t) {
      bool told = false;
      if (t >= 3) {
        const unsigned c = t == 5 ? C_QRD : C_FREE + 16 * (t < 5 ? t - 3 : t - 4), want = t == 5 ? 4 : 8;
        auto free_now = [&]() { return __builtin_amdgcn_readfirstlane(lpeek(c)) >= want; };
        bool ok = free_now();
        if (!ok) { VMCNT(24); ok = free_now(); }
        if (!ok) { VMCNT(16); ok = free_now(); }
        if (!ok) { VMCNT(8); ok = free_now(); }
        if (!ok) { VMCNT(0); lsig(C_LAND + 16 * t - 16); told = true; lwait<true>(c, want); }
      }
      const unsigned dst = slot_of(t);
#pragma unroll
      for (int i = 0; i < 8; ++i) {
        DMA4("nt", kv + i * 4096, dst + i * 4096, vo[4 * (i & 3)], vo[4 * (i & 3) + 1], vo[4 * (i & 3) + 2], vo[4 * (i & 3) + 3]);
        if (watch) {
          const unsigned hold = __builtin_amdgcn_readfirstlane(lpeek(C_HOLD));
          if (hold) { lwait<true>(C_HOLD, 2); watch = false; }
        }
      }
      if (!told) lsig(C_LAND + 16 * t - 16);
      kv += 16 * 32768;
    }
    STAMP(22);

    VMCNT(0);
    lsig(C_LAND + 16 * 7);
    STAMP(23);
    if (s >= 8 || b == 7) { DUMP(62); DUMP(31); DUMP(22); DUMP(23); }
    ROLE_END();
    return;
  }
  __builtin_amdgcn_s_setprio(2);
  if (wave == 9) {
    const int lane = fresh_lane();
    unsigned vo[16];
#pragma unroll
    for (int r = 0; r < 16; ++r) { vo[r] = (lane >> 5) * ASTR + (r & 3) * (4 * ASTR - 1024) + 16 * ((lane & 31) ^ fsw(r)); pin(vo[r]); }
    const unsigned char* qa = ws + WS_ARENA + 2 * ASTR + A_E + b * 512;
    wait_until(t0 + T_FLAG);
    wait_flags(rs, WS_FLAG1 + lane * 16, tag);
    STAMP(17); STAMPD(47);
    const unsigned tq = latest_time32<128>(ws + WS_HQ + (16 * b + s) * 512, t0, (unsigned)tag);
    STAMPV(47);
    wait_until(tq + T_QA - T_HOLD);
    lsig(C_HOLD);
    wait_until(tq + T_QA);
    STAMP(18);
#pragma unroll
    for (int i = 0; i < 8; ++i)
      DMA4("sc1", qa + i * (16 * ASTR), 3 * 32768 + i * 4096, vo[4 * (i & 3)], vo[4 * (i & 3) + 1], vo[4 * (i & 3) + 2], vo[4 * (i & 3) + 3]);
    lsig(C_HOLD);
    VMCNT(0);
    lsig(C_QST);
    STAMP(38);

    if (s >= 8 || b == 7) { lwait<true>(C_PRDY + 16 * 7, 8); nap<30>(); DUMP(17); DUMP(18); DUMP(38); DUMP(47); }
    ROLE_END();
    return;
  }
  if (wave >= 10) {
    __builtin_amdgcn_s_setprio(3);
    const int hh = wave - 10, lane = fresh_lane(), l15 = lane & 15, lq = lane >> 4;
#pragma unroll 1
    for (int real = DRY ? 0 : 1; real < 2; ++real) {
    const unsigned nb = real ? SPIN_L : 1;
    unsigned sa = T_SP + hh * 1024 + lane * 16, cb = real ? T_CNT : T_DRY, four = 4u;
    int mlo = real && lq == 0 ? arena(16 * b + s) + A_ML + hh * 256 + l15 * 16 : OOB;
    pin(sa); pin(cb); pin(four); pin(mlo);
    float ref = 0.f, l = 0.f;
    cwait<C_QST, 1, true>(cb, nb);
    static_for<0, 8>([&](auto tc) {
      constexpr int t = decltype(tc)::value;
      const unsigned nb_ = nb;
      if constexpr (t >= 4) cwait<C_FREE + 16 * (t == 4 ? 3 : t - 2), 8>(cb, nb);
      f32x4 p0, p1, p2, p3;
      unsigned c, n;
      asm volatile(SPIN_HEAD "ds_read_b32 %[c], %[cb] offset:%[o]\n\tds_read_b128 %[p0], %[sa] offset:%[s0]\n\tds_read_b128 %[p2], %[sa] offset:%[s2]\n\t"
                   "ds_read_b128 %[p1], %[sa] offset:%[s1]\n\tds_read_b128 %[p3], %[sa] offset:%[s3]\n\t" SPIN_TAIL2
                   : [c] "=&v"(c), [n] "=&s"(n), [p0] "=&v"(p0), [p1] "=&v"(p1), [p2] "=&v"(p2), [p3] "=&v"(p3)
                   : [cb] "v"(cb), [sa] "v"(sa), [o] "i"(C_SRDY + 16 * t - T_CNT), [w] "i"(4), [w2] "i"(t == 7 ? 1 : 4), [s0] "i"((t & 1) * 8192),
                     [s1] "i"((t & 1) * 8192 + 2048), [s2] "i"((t & 1) * 8192 + 4096), [s3] "i"((t & 1) * 8192 + 6144), [nb] "s"(nb_)
                   : "vcc", "scc", "memory");
      f32x4 a = (p0 - ref) + p2, c4 = (p1 - ref) + p3;
      const float m = fmaxf(fmaxf(fmaxf(a.x, a.y), fmaxf(a.z, a.w)), fmaxf(fmaxf(c4.x, c4.y), fmaxf(c4.z, c4.w)));
      unsigned sig = four;
      if constexpr (t == 0) {
        ref = xmax32(xmax16(m));
        a -= ref; c4 -= ref;
      } else if (__builtin_expect(__any(m > BUMP), 0)) {
        const float up = fmaxf(0.f, xmax32(xmax16(m)));
        const float fa = __builtin_amdgcn_exp2f(-up);
        *(LAS float*)(T_FAC + t * 128 + hh * 64 + l15 * 4) = fa;
        l *= fa;
        ref += up;
        a = (p0 - ref) + p2; c4 = (p1 - ref) + p3;
        sig = 4u + (1u << hh);
      }
      const v4u pf = {pk(__builtin_amdgcn_exp2f(a.x), __builtin_amdgcn_exp2f(a.y)), pk(__builtin_amdgcn_exp2f(a.z), __builtin_amdgcn_exp2f(a.w)),
                      pk(__builtin_amdgcn_exp2f(c4.x), __builtin_amdgcn_exp2f(c4.y)), pk(__builtin_amdgcn_exp2f(c4.z), __builtin_amdgcn_exp2f(c4.w))};
      *(LAS v4u*)((LAS char*)sa + (T_P - T_SP) + (t & 3) * 2048) = pf;
      csig<C_PRDY + 16 * t>(cb, sig);
#pragma unroll
      for (int e = 0; e < 4; ++e) {
        const unsigned u = pf[e];
        l = __builtin_amdgcn_fdot2_f32_bf16(__builtin_bit_cast(bf16x2, u), __builtin_bit_cast(bf16x2, 0x3F803F80u), l, false);
      }
    });
    stg(rs, mlo, v4u{__float_as_uint(ref), (unsigned)tag, __float_as_uint(xsum32(xsum16(l))), (unsigned)(tag >> 32)});
    }
    STAMP(24);
    if (hh == 0 && (s >= 8 || b == 7)) DUMP(24);
    ROLE_END();
    return;
  }
  if (wave < 4) {
    __builtin_amdgcn_s_setprio(PRIO_S);
    const int kh = wave & 1, ch = wave >> 1;
    STAMPD(19); STAMPD(20);
    int rqa = 0;
#pragma unroll 1
    for (int real = DRY ? 0 : 1; real < 2; ++real) {
    const unsigned nb = real ? SPIN_L : 1;
    const int lane = fresh_lane(), l15 = lane & 15, lq = lane >> 4;
    const int sg = (0x2130 >> (4 * lq)) & 3;
    unsigned ao[4];
#pragma unroll
    for (int k = 0; k < 4; ++k) ao[k] = l15 * 1024 + ch * 512 + 16 * ((4 * k + sg) ^ fsw(l15));
    unsigned sa = T_SP + wave * 2048 + lane * 16, cb = real ? T_CNT : T_DRY, one = 1u;
    unsigned al[4], ah[4];
#pragma unroll
    for (int k = 0; k < 4; ++k) { al[k] = ao[k] + kh * 16384; ah[k] = al[k] + 65536; pin(al[k]); pin(ah[k]); }
    int aro = real && wave == 0 && lane < 32 ? WS_ARR + (4 * lane + (b >> 1)) * 512 + (s & 3) * 128 + (b & 1) * 16 + (s >> 2) * 4 : OOB;
    pin(sa); pin(cb); pin(one); pin(aro);
    v4u af[2][8], qf[2][8];
    auto rd = [&](unsigned sl, int ks) { return lds128(sl < 65536 ? al[ks & 3] : ah[ks & 3], (sl & 32768) + (ks >> 2) * 256); };
    static_for<0, 2>([&](auto tc) {
      constexpr int t = decltype(tc)::value;
      cwait<C_LAND + 16 * t, 1, true>(cb, nb);
#pragma unroll
      for (int ks = 0; ks < 8; ++ks) af[t][ks] = rd(t * 32768, ks);
      csig<C_FREE + 16 * t>(cb, one);
    });
    unsigned qa[4];
#pragma unroll
    for (int k = 0; k < 4; ++k) { qa[k] = 3 * 32768 + ao[k]; pin(qa[k]); }
    cwait<C_QST, 1>(cb, nb);
    STAMPV(19);
    unsigned nx2 = cpeek<C_LAND + 32>(cb), nxt = cpeek<C_LAND + 48>(cb);
    f32x4 s0, s1;
#pragma unroll 1
    for (int sp = 0; sp < SPIN_G; ++sp) {
      rqa = sp + 1;
#pragma unroll
      for (int ks = 0; ks < 8; ++ks) {
#pragma unroll
        for (int hh = 0; hh < 2; ++hh) qf[hh][ks] = lds128(qa[ks & 3], hh * 16384 + (ks >> 2) * 256);
        __builtin_amdgcn_sched_barrier(0);
      }
      s0 = z4; s1 = z4;
#pragma unroll
      for (int ks = 0; ks < 8; ++ks) { s0 = mfma16(af[0][ks], qf[0][ks], s0); s1 = mfma16(af[0][ks], qf[1][ks], s1); }
      if (__builtin_expect(!__any(__builtin_isunordered(s0.x, s1.x)), 1) || !real || !(any_sent<8>(qf[0]) || any_sent<8>(qf[1]))) break;
#pragma unroll 1
      for (unsigned m = ((unsigned)__ballot(s0.x != s0.x) & 0xFFFFu) | ((unsigned)__ballot(s1.x != s1.x) << 16); m; m &= m - 1) {
        const int r = __builtin_ctz(m);
        asm volatile("s_mov_b32 m0, %2\n\ts_nop 0\n\tglobal_load_lds_dwordx4 %0, %1 sc1"
                     :: "v"((lane >> 5) * ASTR + 16 * ((lane & 31) ^ fsw(r))), "s"(ws + WS_ARENA + 2 * ASTR + A_E + b * 512 + r * (4 * ASTR)), "s"(3 * 32768 + r * 1024) : "memory");
      }
      VMCNT(0);
    }
    *(LAS f32x4*)sa = s0;
    *(LAS f32x4*)((LAS char*)sa + 1024) = s1;
    csig<C_SRDY>(cb, one);
    csig<C_QRD>(cb, one);
    STAMPV(20);
    auto refill = [&](v4u& a, auto slc, auto ksc) {
      constexpr unsigned sl = decltype(slc)::value;
      constexpr int ks = decltype(ksc)::value;
      rd_inplace<(sl & 32768) + (ks >> 2) * 256>(a, sl < 65536 ? al[ks & 3] : ah[ks & 3]);
    };
    static_for<0, 8>([&](auto k) { refill(af[0][decltype(k)::value], IntC<slot_c(2)>{}, k); });
    bool miss = __any(nx2 < 1);
    if (!miss) csig<C_FREE + 32>(cb, one);
    static_for<1, 8>([&](auto tc) {
      constexpr int t = decltype(tc)::value;
      const bool was = miss;
      if constexpr (t < 6) miss = __any(nxt < 1);
      unsigned use;
      peek2<C_PRDY + 16 * (t - 2) - T_CNT, C_LAND + 16 * (t < 5 ? t + 3 : 8) - T_CNT>(use, nxt, cb);
      static_for<0, 8>([&](auto k) {
        constexpr int ks = decltype(k)::value;
        score_pair<(t < 6 ? 9 : 9 - ks), ks == 0>(s0, s1, af[t & 1][ks], qf[0][ks], qf[1][ks]);
        if constexpr (t < 6) refill(af[t & 1][ks], IntC<slot_c(t + 2)>{}, k);
      });
      wait2<(t < 6 ? 8 : 0)>(use, nxt);
      if (__builtin_expect(__any(use < 8), 0)) cwait<C_PRDY + 16 * (t - 2), 8>(cb, nb);
      put_scores<(t & 1) * 8192>(sa, s0, s1);
      csig<C_SRDY + 16 * t>(cb, one);
      if constexpr (t == 7 && !ARR_EARLY) st32(rs, aro, now32() + (unsigned)tag);
      if constexpr (t < 4) { if (!miss) csig<C_FREE + 16 * (t + 2)>(cb, one); }
      if constexpr (t < 7) {
        if (was) {
          cwait<C_LAND + 16 * (t + 1), 1>(cb, nb);
          static_for<0, 8>([&](auto k) { refill(af[(t + 1) & 1][decltype(k)::value], IntC<slot_c(t + 1)>{}, k); });
          if constexpr (t < 5) csig<C_FREE + 16 * (t + 1)>(cb, one);
        }
      }
      if constexpr (t == 6 && ARR_EARLY) { if (wave == 0) st32(rs, aro, now32() + (unsigned)tag); }
    });
    asm volatile("s_waitcnt lgkmcnt(0)" ::: "memory");
    }
    STAMP(21);
    if (wave == 0 && (s >= 8 || b == 7)) { DUMP(19); DUMP(20); DUMP(21); DUMPC(50, rqa); }
  } else {
    __builtin_amdgcn_s_setprio(PRIO_PV);
#pragma unroll 1
    for (int real = DRY ? 0 : 1; real < 2; ++real) {
    const int lane = fresh_lane(), l15 = lane & 15, lq = lane >> 4;
    unsigned vl[8];
#pragma unroll
    for (int ct = 0; ct < 8; ++ct)
      vl[ct] = ((lane >> 2) * 1024 + w * 256 + 16 * ((2 * (ct & 1) + ((lane >> 1) & 1)) ^ fsw(lane >> 2)) + 8 * (lane & 1)) ^ ((ct >> 1) * 64);
    int po = real ? arena(16 * lq + w) + A_PART + (b * 16 + s) * 256 + l15 * 16 : OOB - 80 * ASTR;
    int cko = (lane & 2) || !real ? OOB : arena(16 * b + s) + A_CK + w * 512 + (2 * (lane & 8) + 4 * lq + ((lane >> 1) & 2) + (lane & 1)) * 16;
    pin(po); pin(cko);
    f32x32 acc[2];
    pv_loop(acc[0], acc[1], vl, T_P + lane * 16, real ? T_CNT : T_DRY, 1u, real ? SPIN_L : 1);
    unsigned ce[8], co[8];
#pragma unroll
    for (int ht = 0; ht < 2; ++ht)
#pragma unroll
      for (int r = 0; r < 4; ++r) {
        if (ht == 1 && r == 0) asm volatile("s_nop 1" : "+{v[72:103]}"(acc[1]) :: "memory");
        const v4u v = {pk(acc[ht][r], acc[ht][4 + r]), pk(acc[ht][8 + r], acc[ht][12 + r]), pk(acc[ht][16 + r], acc[ht][20 + r]),
                       pk(acc[ht][24 + r], acc[ht][28 + r])};
        stg(rs, po + (16 * ht + r) * 4 * ASTR, v);
        ce[4 * ht + r] = v.x ^ v.z; co[4 * ht + r] = v.y ^ v.w;
      }
    {
      unsigned e0, e1, o0, o1;
      xor_rows8(ce, e0, e1);
      xor_rows8(co, o0, o1);
      const bool odd = cko & 16;
      stg(rs, cko, v4u{odd ? e1 : e0, (unsigned)tag, odd ? o1 : o0, (unsigned)(tag >> 32)});
    }
    }
    STAMP(25);
  }
  if (b == 7 || s >= 8) { ROLE_END(); return; }
  ffn_part(p, b, s, wave, t0, tag);
  ROLE_END();
}

DI void ffn_part(const Params& p, int b, int s, int wave, unsigned t0, u64 tag) {
  const int lane = fresh_lane();
  unsigned char* ws = p.ws;
  const rsrc_t rs = __builtin_amdgcn_make_buffer_rsrc(ws, 0, WS_BYTES, 0x00020000);
  const int l15 = lane & 15, lq = lane >> 4, w = wave & 3;
  const f32x4 z4 = {0.f, 0.f, 0.f, 0.f};
  __builtin_amdgcn_s_setprio(2);
  const int fg = 8 * b + s, b8 = lane & 7, p4 = 4 * (lane & 3);
  const bool bvalid = l15 < 8;
  const int ch = l15 ^ ((lq << 2) | w), row0 = 4 * w + lq;
  bar();
  STAMP(52);
  nap<D_F>();
  v4u mm[4];
  const int mo = arena(32 + 4 * wave) + A_S + (4 * (l15 >> 3) + lq) * 128 + b8 * 16;
  auto load_mod = [&]() {
    FENCE();
#pragma unroll
    for (int i = 0; i < 4; ++i) mm[i] = ldg(rs, mo, ((i >> 1) * 32 + (i & 1)) * ASTR);
    FENCE();
  };
  load_mod();
  if (wave < 4) {
    dma_run<false>(p.w1, (row0 + (ch >> 2) * 128) * 3584 + fg * 64 + (ch & 3) * 16, W0 + w * 1024, 8, 16 * 3584, 4096);
  } else {
    const unsigned u = (32 * fg + row0) * 1024 + ch * 16;
    dma_run<true>(p.w2, u, W1 + w * 1024, 4, 256, 8192);
    dma_run<true>(p.w2, u + 16 * 1024, W1 + w * 1024 + 4096, 4, 256, 8192);
  }
#pragma unroll 1
  for (int sp = 0; sp < SPIN_G; ++sp) {
    if (!any_sent<4>(mm)) break;
    load_mod();
  }
  f32x4 sb[2], cb[2];
  sb[0] = up4(mm[0].xy) + up4(mm[1].xy); sb[1] = up4(mm[0].zw) + up4(mm[1].zw);
  cb[0] = up4(mm[2].xy) + up4(mm[3].xy) + 1.f; cb[1] = up4(mm[2].zw) + up4(mm[3].zw) + 1.f;
  pin(sb[0]); pin(sb[1]); pin(cb[0]); pin(cb[1]);
  if (wave == 7) wait_flags(rs, WS_FLAG2 + lane * 16, tag);
  VMCNT(0);
  bar();
  STAMP(28);
  v4u f1[2][2], f2[8];
#pragma unroll
  for (int tt = 0; tt < 2; ++tt) {
    const int c4 = 32 * (wave >> 1) + 2 * p4 + 4 * tt;
    const unsigned a0 = tr_addr(W0 + (wave & 1) * 16384, lane, c4, 0), a1 = tr_addr(W0 + (wave & 1) * 16384, lane, c4, 1);
    f1[0][tt] = tr_frag(a0, a1, 0); f1[1][tt] = tr_frag(a0, a1, 1);
    pin(f1[0][tt]); pin(f1[1][tt]);
  }
#pragma unroll
  for (int i = 0; i < 8; ++i) {
    const int c4 = 32 * (i >> 1) + 2 * p4 + 4 * (i & 1);
    f2[i] = tr_frag(tr_addr(W1 + w * 8192, lane, c4, 0), tr_addr(W1 + w * 8192, lane, c4, 1), 0);
    pin(f2[i]);
  }
  int xo = arena(wave) + A_X1 + (4 * (l15 >> 3) + lq) * 128 + b8 * 16;
  unsigned zw = L_A5 + wave * 1024 + lane * 16, zr = L_A5 + lane * 16;
  unsigned sw = lane < 8 ? L_A6 + lane * 80 + wave * 8 : L_JUNK, sr = L_A6 + b8 * 80;
  pin(xo); pin(zw); pin(zr); pin(sw); pin(sr);
  STAMP(55);
  STAMPD(40); STAMPD(15); STAMPD(36); STAMPD(37);
  int rx = 0;
  auto first = [&](v4u u) {
    const f32x4 d00 = mfma16(f1[0][0], u, z4), d10 = mfma16(f1[1][0], u, z4), d01 = mfma16(f1[0][1], u, z4), d11 = mfma16(f1[1][1], u, z4);
    f32x4 zz;
#pragma unroll
    for (int r = 0; r < 4; ++r) zz[r] = (bvalid ? d00[r] : d11[r]) + dppf<0x128>(bvalid ? d01[r] : d10[r]);
    return zz;
  };
  *(LAS f32x4*)((LAS char*)zw + 16384) = first(pk8(cb[0], cb[1]));
  *(LAS f32x4*)((LAS char*)zw + 24576) = first(pk8(sb[0], sb[1]));
  bar();
  f32x4 v1 = z4, v2 = z4;
  int yo = arena(fg) + A_YP + (16 * w + 4 * (l15 >> 3) + lq) * 128 + b8 * 16;
  if (wave >= 4) {
#pragma unroll
    for (int i = 0; i < 8; ++i) { v1 += u2f(lds128(zr, 16384 + i * 1024)); v2 += u2f(lds128(zr, 24576 + i * 1024)); }
  }
  pin(v1); pin(v2); pin(yo);
  STAMPV(40);
  if (wave == 0) wait_until(latest_time(ws + WS_HB + (16 * b + s) * 128, t0, (unsigned)tag) + T_C);
  bar();
  {
    v4u xv[1];
#pragma unroll 1
    for (int sp = 0; sp < SPIN_G; ++sp) { load_n<1>(rs, xo, 0, xv); rx = sp + 1; if (!any_sent<1>(xv)) break; }
    STAMPV(15);
    const f32x4 x0 = up4(xv[0].xy), x1 = up4(xv[0].zw);
    *(LAS f32x4*)zw = first(pk8(x0 * cb[0], x1 * cb[1]));
    const f32x4 s4 = x0 + x1, q4 = x0 * x0 + x1 * x1;
    float sx = (s4.x + s4.y) + (s4.z + s4.w), sq = (q4.x + q4.y) + (q4.z + q4.w);
    sx += dppf<0x128>(sx); sq += dppf<0x128>(sq);
    {
      auto y = __builtin_amdgcn_permlane16_swap(__float_as_uint(sx), __float_as_uint(sq), false, false);
      const float t = xsum32(__uint_as_float(y[0]) + __uint_as_float(y[1]));
      auto z = __builtin_amdgcn_permlane16_swap(__float_as_uint(t), __float_as_uint(t), false, false);
      *(LAS v2u*)sw = v2u{z[0], z[1]};
    }
    STAMPV(36);
    bar();
    if (wave >= 4) {
      STAMPV(37);
      f32x4 zp[8], st[4];
#pragma unroll
      for (int i = 0; i < 4; ++i) st[i] = u2f(lds128(sr, i * 16));
#pragma unroll
      for (int i = 0; i < 8; ++i) zp[i] = u2f(lds128(zr, i * 1024));
      __builtin_amdgcn_sched_barrier(0);
      const f32x4 ss = (st[0] + st[1]) + (st[2] + st[3]);
      const float mean = (ss.x + ss.z) * (1.f / 512), msq = (ss.y + ss.w) * (1.f / 512);
      const float rstd = __builtin_amdgcn_rsqf(fmaxf(msq - mean * mean, 0.f) + EPS);
      const f32x4 zs = ((zp[0] + zp[1]) + (zp[2] + zp[3])) + ((zp[4] + zp[5]) + (zp[6] + zp[7]));
      const f32x4 z = (zs - v1 * mean) * rstd + v2;
      const unsigned g0 = pk(z.x < 0.f ? 0.f : z.x, z.y < 0.f ? 0.f : z.y), g1 = pk(z.z < 0.f ? 0.f : z.z, z.w < 0.f ? 0.f : z.w);
      const unsigned o0 = (unsigned)__builtin_amdgcn_update_dpp(0, (int)g0, 0x128, 0xF, 0xF, true), o1 = (unsigned)__builtin_amdgcn_update_dpp(0, (int)g1, 0x128, 0xF, 0xF, true);
      const v4u bf = {bvalid ? g0 : o0, bvalid ? g1 : o1, bvalid ? o0 : g0, bvalid ? o1 : g1};
#pragma unroll
      for (int i = 0; i < 2; ++i) {
        const f32x4 a0 = mfma16(f2[4 * i], bf, z4), a1 = mfma16(f2[4 * i + 1], bf, z4), c0 = mfma16(f2[4 * i + 2], bf, z4), c1 = mfma16(f2[4 * i + 3], bf, z4);
        stg(rs, yo + i * 1024, pick(bvalid, pk8(a0, a1), pk8(c0, c1)));
      }
    }
  }
}

DI void first_requests() {
  u64 px, pc, pw;
  unsigned a, kp, wv, v0, v1, v2;
  asm volatile(
      "s_load_dwordx2 %[px], %[k], 0x0\n\ts_load_dwordx2 %[pc], %[k], 0x40\n\ts_load_dwordx2 %[pw], %[k], 0x48\n\t"
      "v_readfirstlane_b32 %[wv], %[t]\n\ts_and_b32 %[a], %[bx], 0x90\n\ts_cmpk_eq_u32 %[a], 0x90\n\ts_cbranch_scc0 3f\n\t"
      "s_bfe_u32 %[a], %[bx], 0x20005\n\ts_bfe_u32 %[kp], %[bx], 0x10003\n\ts_lshl1_add_u32 %[kp], %[a], %[kp]\n\t"
      "s_lshr_b32 %[wv], %[wv], 6\n\ts_cmp_gt_u32 %[wv], 3\n\ts_cbranch_scc1 2f\n\t"
      "v_bfe_u32 %[v0], %[t], 4, 2\n\tv_lshl_or_b32 %[v1], %[v0], 2, %[wv]\n\tv_xor_b32 %[v1], %[v1], %[t]\n\t"
      "v_mul_u32_u24 %[v0], 0x1800, %[v0]\n\tv_and_b32 %[v2], 7, %[v1]\n\tv_lshl_add_u32 %[v0], %[v2], 4, %[v0]\n\t"
      "v_bfe_u32 %[v2], %[v1], 3, 1\n\tv_lshl_add_u32 %[v0], %[v2], 10, %[v0]\n\t"
      "s_lshl4_add_u32 %[kp], %[kp], %[wv]\n\ts_mul_i32 %[kp], %[kp], 0x6000\n\ts_and_b32 %[a], %[bx], 7\n\ts_lshl_b32 %[a], %[a], 7\n\t"
      "s_add_u32 %[a], %[a], %[kp]\n\tv_add_u32 %[v0], %[a], %[v0]\n\ts_lshl_b32 %[wv], %[wv], 10\n\ts_add_u32 m0, %[wv], %[w2]\n\t"
      "v_add_u32 %[v1], 0x18000, %[v0]\n\tv_add_u32 %[v2], 0x30000, %[v0]\n\ts_waitcnt lgkmcnt(0)\n\t"
      "global_load_lds_dwordx4 %[v0], %[pw] nt\n\ts_add_u32 m0, m0, 0x1000\n\tv_add_u32 %[v0], 0x48000, %[v0]\n\t"
      "global_load_lds_dwordx4 %[v1], %[pw] nt\n\ts_add_u32 m0, m0, 0x1000\n\ts_nop 0\n\t"
      "global_load_lds_dwordx4 %[v2], %[pw] nt\n\ts_add_u32 m0, m0, 0x1000\n\ts_nop 0\n\t"
      "global_load_lds_dwordx4 %[v0], %[pw] nt\n\ts_branch 3f\n"
      "2:\n\tv_add_u32 %[v0], 0xffffff00, %[t]\n\tv_lshlrev_b32 %[v0], 4, %[v0]\n\tv_and_b32 %[v1], 0xfffffc70, %[v0]\n\t"
      "s_lshl_b32 %[kp], %[kp], 7\n\tv_or_b32 %[v1], %[kp], %[v1]\n\ts_lshl_b32 %[wv], %[wv], 11\n\ts_add_u32 m0, %[wv], %[raw]\n\t"
      "s_waitcnt lgkmcnt(0)\n\tglobal_load_lds_dwordx4 %[v1], %[pc]\n\ts_sub_u32 m0, m0, 0x400\n\ts_nop 0\n\t"
      "global_load_lds_dwordx4 %[v0], %[px]\n"
      "3:\n\ts_waitcnt lgkmcnt(0)"
      : [px] "=&s"(px), [pc] "=&s"(pc), [pw] "=&s"(pw), [a] "=&s"(a), [kp] "=&s"(kp), [wv] "=&s"(wv), [v0] "=&v"(v0), [v1] "=&v"(v1), [v2] "=&v"(v2)
      : [k] "s"(__builtin_amdgcn_kernarg_segment_ptr()), [bx] "s"(blockIdx.x), [t] "v"(threadIdx.x), [w2] "i"(W2), [raw] "i"(M_RAW + 1024 - 4 * 2048)
      : "scc", "memory");
}

__global__ __launch_bounds__(768) void adaln_mla_ffn_kernel(Params p) {
  STAMP(0);
  first_requests();
  const unsigned t0 = now32();
  const int g = blockIdx.x & 7, idx = blockIdx.x >> 3;
  const int wave = __builtin_amdgcn_readfirstlane(threadIdx.x >> 6), lane = threadIdx.x & 63;
  if (wave == 0 && idx < 16) {
    u64 pc;
    unsigned left;
    asm volatile("s_getpc_b64 %0\n99:\n\ts_mov_b32 %1, 98f-99b-4\n\t.subsection 2\n98:\n\t.subsection 0" : "=s"(pc), "=s"(left));
    asm volatile("s_mov_b32 m0, %2\n\ts_nop 0\n\tglobal_load_lds_dword %0, %1" :: "v"(min((idx + 16 * min(lane >> 1, 21)) * 128u + (lane & 1) * 64, left)), "s"(pc), "s"(L_JUNK) : "memory");
  }
  const int wi = idx & 15, l15 = lane & 15, lq = lane >> 4, b8 = lane & 7;
  const int kp = 2 * (wi >> 2) + (wi & 1);
  const unsigned vx = lane * 16;
  asm volatile("" :: "s"(p.x), "s"(p.ckv), "s"(p.wq), "s"(p.wuk), "s"(p.wuv), "s"(p.wo), "s"(p.w1), "s"(p.w2), "s"(p.c), "s"(p.wmod),
               "s"(p.out), "s"(p.ws));
  STAMP(39);
  STAMPD(2); STAMPD(3); STAMPD(32); STAMPD(1);
  const rsrc_t rs = __builtin_amdgcn_make_buffer_rsrc(p.ws, 0, WS_BYTES, 0x00020000);
  const int my = arena(16 * g + wi);
  if (idx >= 16 && wave == 3) {
#pragma unroll
    for (int n = 0; n < 5; ++n) stg(rs, my + vx + n * 1024, v4u{SENT, SENT, SENT, SENT});
  }
  if ((idx & 18) == 18) {
    const int r = wave - 4;
    if (wave >= 4) {
      unsigned raw = M_RAW + r * 2048 + vx, d = (lane >> 3) == g ? M_LNX + r * 256 + b8 * 32 : L_JUNK, sc = M_SC + r * 144 + lane * 2;
      unsigned rc = M_RAW + r * 2048 + 1024 + lane * 2;
      pin(raw); pin(d); pin(sc); pin(rc);
      VMCNT(0);
      const v4u xr = lds128(raw);
      const float cv = bflo(*(LAS u16*)rc);
      *(LAS u16*)sc = (u16)pk(cv * __builtin_amdgcn_rcpf(1.f + __expf(-cv)), 0.f);
      bar();
      const f32x4 v0 = up4(xr.xy), v2 = up4(xr.zw), sv4 = v0 + v2, qv4 = v0 * v0 + v2 * v2;
      const float mean = wave_sum((sv4.x + sv4.y) + (sv4.z + sv4.w)) * (1.f / 512);
      const float msq = wave_sum((qv4.x + qv4.y) + (qv4.z + qv4.w)) * (1.f / 512);
      const float rstd = __builtin_amdgcn_rsqf(fmaxf(msq - mean * mean, 0.f) + EPS);
      *(LAS f32x4*)d = (v0 - mean) * rstd;
      *(LAS f32x4*)((LAS char*)d + 16) = (v2 - mean) * rstd;
      STAMPV(32);
      bar();
    } else {
      const int p4 = 4 * (lane & 3);
      unsigned s0 = tr_addr(W2, lane, 16 * wave + p4, 0), s1 = tr_addr(W2, lane, 16 * wave + p4, 1), c0 = s0 ^ 128, c1 = s1 ^ 128;
      unsigned ba = M_SC + b8 * 144 + lq * 16, la = M_LNX + b8 * 256 + (16 * wave + 4 * lq) * 4;
      int o = l15 < 8 ? my + wave * 256 + (lq >> 1) * 128 + l15 * 16 + (lq & 1) * 8 : OOB;
      int hh = wave == 3 && lane < 32 ? WS_HH + (4 * lane + (g >> 2)) * 512 + (g & 3) * 128 + kp * 4 : OOB;
      const float one = kp == 0 ? 1.f : 0.f;
      *(LAS unsigned*)L_CNT = 0u;
      f32x4 sa = {0.f, 0.f, 0.f, 0.f}, ca = {one, one, one, one};
      pin(s0); pin(s1); pin(c0); pin(c1); pin(ba); pin(la); pin(o); pin(hh); pin(ca);
      u64 tag = 0;
      int fo = wave == 3 ? (lane < 32 ? WS_EFL + (4 * lane + (g >> 2)) * 256 + (g & 3) * 64 + kp * 8 : lane == 32 ? WS_FLAG1 + (16 * g + wi) * 8 : OOB) : OOB;
      pin(fo);
      if (wave == 3) { tag = get_tag(p.ws); VMCNT(5); } else { VMCNT(0); }
      STAMPV(32);
      bar();
      const unsigned tn = now32();
      const v4u b0 = lds128(ba), f0 = tr_frag(s0, s1, 0), g0 = tr_frag(c0, c1, 0), b1 = lds128(ba, 64), f1 = tr_frag(s0, s1, 1), g1 = tr_frag(c0, c1, 1);
      __builtin_amdgcn_sched_barrier(0);
      st32(rs, hh, tn + (unsigned)tag);
      STAMPV(2);
      sa = mfma16(f1, b1, mfma16(f0, b0, sa));
      ca = mfma16(g1, b1, mfma16(g0, b0, ca));
      if (wave == 3) {
        VMCNT(1);
        __builtin_amdgcn_raw_buffer_store_b64(v2u{(unsigned)tag, (unsigned)(tag >> 32)}, rs, fo, 0, SC1);
        STAMPV(1);
      }
      bar();
      sa += u2f(lds128(la)) * ca;
      __builtin_amdgcn_raw_buffer_store_b64(v2u{pk(sa.x, sa.y), pk(sa.z, sa.w)}, rs, o, 0, SC1);
      STAMPV(3);
    }
    bar();
  } else if (idx >= 16 && wave == 3) {
    const u64 tag = get_tag(p.ws);
    VMCNT(0);
    __builtin_amdgcn_raw_buffer_store_b64(v2u{(unsigned)tag, (unsigned)(tag >> 32)}, rs,
                                          lane < 2 ? WS_EFL + (16 * g + (wi | 2) - (wi & 1) + lane) * 256 + (wi & 1) * 8 : lane == 2 ? WS_FLAG1 + (16 * g + wi) * 8 : OOB, 0, SC1);
  } else if (wave >= 8) {
    const int w = wave & 3, hq = idx >= 16 ? wi >> 2 : wi >> 2, row = idx >= 16 ? 256 * (wi & 1) : 256 * ((wi >> 1) & 1) + 128 + 64 * (wi & 1);
    const unsigned vq = (row + 4 * w + lq) * 8192 + (4 * g + hq) * 256 + (l15 ^ ((lq << 2) | w)) * 16;
    wait_until(t0 + T_W);
    dma_run<true>(p.wq, vq, (idx >= 16 ? W0 : T_CNT + 1024) + w * 1024, idx >= 16 ? 16 : 4, 16 * 8192, idx >= 16 ? 4096 : 0);
    if (idx < 16) dma_run<true>(p.wuk, PF_UK == 4 ? vq : vq + (128 * (wi & 3) - row) * 8192, T_CNT + 1024 + w * 1024, PF_UK, 16 * 8192, 0);
  }
  FENCE();
  if (idx < 16) attn_wg(p, g, idx, wave, t0);
  else { weight_wg(p, g, wi, wave, fresh_lane(), t0); ROLE_END(); }
}

extern "C" void kernel_launch(void* const* d_in, const int* in_sizes, int n_in, void* d_out, int out_size, void* d_ws, size_t ws_size,
                              hipStream_t stream) {
  Params p;
  p.x = (const u16*)d_in[0]; p.ckv = (const u16*)d_in[1]; p.wq = (const u16*)d_in[2]; p.wuk = (const u16*)d_in[3];
  p.wuv = (const u16*)d_in[4]; p.wo = (const u16*)d_in[5]; p.w1 = (const u16*)d_in[7]; p.w2 = (const u16*)d_in[8];
  p.c = (const u16*)d_in[9]; p.wmod = (const u16*)d_in[10];
  p.out = (u16*)d_out; p.ws = (unsigned char*)d_ws + WS_BASE;
  (void)hipFuncSetAttribute((const void*)adaln_mla_ffn_kernel, hipFuncAttributeMaxDynamicSharedMemorySize, LDS_BYTES);
  adaln_mla_ffn_kernel<<<256, 768, LDS_BYTES, stream>>>(p);
}
```

```cpp
#include <hip/hip_runtime.h>
#include <hip/hip_bf16.h>
#pragma clang diagnostic ignored "-Wint-to-pointer-cast"
#pragma clang diagnostic ignored "-Wsometimes-uninitialized"

typedef unsigned short u16;
typedef unsigned long long u64;
typedef unsigned v4u __attribute__((ext_vector_type(4)));
typedef unsigned v2u __attribute__((ext_vector_type(2)));
typedef float f32x2 __attribute__((ext_vector_type(2)));
typedef float f32x4 __attribute__((ext_vector_type(4)));
typedef float f32x16 __attribute__((ext_vector_type(16)));
typedef short s16x4 __attribute__((ext_vector_type(4)));
typedef short bf16x8 __attribute__((ext_vector_type(8)));
typedef __bf16 bf16x2 __attribute__((ext_vector_type(2)));
#define GAS __attribute__((address_space(1)))
#define LAS __attribute__((address_space(3)))
#define DI __device__ __forceinline__
#define RLX_AGENT __ATOMIC_RELAXED, __HIP_MEMORY_SCOPE_AGENT
typedef __amdgpu_buffer_rsrc_t rsrc_t;

#define ATT_DELAY 0
#define T_H 70
#define D_UK 8
#define T_QP 135
#define T_QA 122
#define T_B 86
#define T_C 170
#define T_D 312
#define ARR_EARLY 0
#define T_ML 62
#define T_PART 126
#define T_B6 68
#define D_F 0
#define D_ML 2
#define FLAG_AT 1
#define D_PREF 16
#define PRIO_S 2
#define PRIO_PV 1
#define PF_NT false
#define D_W 0
#define T_PREF 0
#define T_W 40
#define PF_EVEN 24
#define T_FILL 0
#define T_FLAG 300
#define WS_BASE 0
#define FILL_AUX 16
#define WWIN 7
#define DRY 0
#define T_EF 250
#define T_LQ 190
#define T_LQ2 100
#define PF_UK 8
#define T_UK 190
#define T_FP 540
#define T_RP 560
#define T_HOLD 45
#define HELP 0
#define PRIO_L 3
#define WO_MODE 1
#define T_WO 170
#define FP_ALL 0
#define FP_W2 1
#define T_L 240
#define FILL_EARLY 1
#define D_FILL 0
#define PREF 1
#define WO_NT 1

constexpr int WS_FLAG1 = 4096, WS_FLAG2 = 8192;
constexpr int WS_EFL = 16384  , WS_HP = 81920, WS_HB = 98304  ,
              WS_HH = 131072  , WS_HQ = 196608  , WS_ARR = 262144  ;
constexpr int WS_ARENA = 327680, ASTR = 58624;
constexpr int A_S = 0, A_E = 1024, A_ML = 5120, A_X1 = 6144, A_OUTP = 7168, A_YP = 15360, A_PART = 23552, A_CK = 56320;
constexpr int WS_BYTES = WS_ARENA + 128 * ASTR;
constexpr unsigned SENT = 0xFFFFFFFFu;
constexpr int OOB = 0x7FFFFF00;
constexpr int SC1 = 16;
constexpr int SPIN_H = 1 << 11;
constexpr int SPIN_G = 1 << 19;
constexpr int SPIN_L = 1 << 23;
constexpr int LDS_BYTES = 163840;
constexpr float EPS = 1e-5f;
constexpr float QSCALE = 0.08838834764831845f * 1.4426950408889634f;
constexpr float BUMP = 40.f;

#define STAMP(k) do {} while (0)
#define STAMPV(k) do {} while (0)
#define STAMPD(k) do {} while (0)
#define STV(k) 0
#define DUMP(k) do {} while (0)
#define DUMPC(k, c) do {} while (0)

DI int fresh_lane() {
  int l;
  asm volatile("v_mbcnt_lo_u32_b32 %0, -1, 0\n\tv_mbcnt_hi_u32_b32 %0, -1, %0" : "=v"(l));
  return l;
}
template <class T> DI void pin(T& v) { asm volatile("" : "+v"(v)); }
DI float bflo(unsigned u) { return __uint_as_float(u << 16); }
DI float bfhi(unsigned u) { return __uint_as_float(u & 0xFFFF0000u); }
DI unsigned pk(float a, float b) { f32x2 v = {a, b}; return __builtin_bit_cast(unsigned, __builtin_convertvector(v, bf16x2)); }
DI f32x4 mfma16(v4u a, v4u b, f32x4 c) {
  return __builtin_amdgcn_mfma_f32_16x16x32_bf16(__builtin_bit_cast(bf16x8, a), __builtin_bit_cast(bf16x8, b), c, 0, 0, 0);
}
DI f32x16 mfma32(v4u a, v4u b, f32x16 c) {
  return __builtin_amdgcn_mfma_f32_32x32x16_bf16(__builtin_bit_cast(bf16x8, a), __builtin_bit_cast(bf16x8, b), c, 0, 0, 0);
}
DI v4u ldg(rsrc_t rs, int off, int so = 0) { return __builtin_amdgcn_raw_buffer_load_b128(rs, off, so, SC1); }
DI void stg(rsrc_t rs, int off, v4u v) { __builtin_amdgcn_raw_buffer_store_b128(v, rs, off, 0, SC1); }
DI unsigned umax4(v4u v) { return max(max(v.x, v.y), max(v.z, v.w)); }
DI v4u f2u(f32x4 v) { return __builtin_bit_cast(v4u, v); }
DI f32x4 u2f(v4u v) { return __builtin_bit_cast(f32x4, v); }
DI bool nan4(f32x4 v) { return __builtin_isunordered(v.x, v.y) | __builtin_isunordered(v.z, v.w); }
DI v4u lds128(unsigned a, int o = 0) { return *(LAS v4u*)((LAS char*)a + o); }
DI v2u ldstr(unsigned a, int o = 0) { return __builtin_bit_cast(v2u, __builtin_amdgcn_ds_read_tr16_b64_v4i16((LAS s16x4*)((LAS char*)a + o))); }
DI void bar() {
  __builtin_amdgcn_sched_barrier(0);
  asm volatile("s_waitcnt lgkmcnt(0)\n\ts_barrier" ::: "memory");
  __builtin_amdgcn_sched_barrier(0);
}
#define VMCNT_(n) asm volatile("s_waitcnt vmcnt(" #n ")" ::: "memory")
#define VMCNT(n) VMCNT_(n)
#define FENCE() asm volatile("" ::: "memory")
#define ROLE_END() __builtin_amdgcn_s_waitcnt(0x0F70)
template <bool NT> DI void dma16(const void* base, unsigned voff, unsigned dst) {
  if (NT) asm volatile("s_mov_b32 m0, %2\n\ts_nop 0\n\tglobal_load_lds_dwordx4 %0, %1 nt" :: "v"(voff), "s"(base), "s"(dst) : "memory");
  else asm volatile("s_mov_b32 m0, %2\n\ts_nop 0\n\tglobal_load_lds_dwordx4 %0, %1" :: "v"(voff), "s"(base), "s"(dst) : "memory");
}
template <bool NT> DI void dma_run(const void* base, unsigned voff, unsigned dst, int n, int gstr, int lstr) {
  const unsigned char* b = (const unsigned char*)base;
#pragma unroll 1
  for (int i = 0; i < n; ++i) { VMCNT(WWIN); dma16<NT>(b, voff, dst); b += gstr; dst += lstr; }
}
template <int N> DI void nap() { if (N > 0) __builtin_amdgcn_s_sleep(N); }
DI void lsig(unsigned a, unsigned v = 1u) { asm volatile("s_mov_b64 exec, 1\n\tds_add_u32 %0, %1\n\ts_mov_b64 exec, -1" :: "v"(a), "v"(v) : "memory"); }
DI unsigned lpeek(unsigned a) { return *(volatile LAS unsigned*)a; }
template <bool NAP = false> DI void lwait(unsigned a, unsigned want) {
  unsigned v, n = SPIN_L;
  if (NAP)
    asm volatile("1:\n\tds_read_b32 %0, %2\n\ts_waitcnt lgkmcnt(0)\n\tv_cmp_le_u32 vcc, %3, %0\n\ts_cbranch_vccnz 2f\n\t"
                 "s_sleep 1\n\ts_sub_u32 %1, %1, 1\n\ts_cbranch_scc0 1b\n2:" : "=&v"(v), "+s"(n) : "v"(a), "s"(want) : "vcc", "scc", "memory");
  else
    asm volatile("1:\n\tds_read_b32 %0, %2\n\ts_waitcnt lgkmcnt(0)\n\tv_cmp_le_u32 vcc, %3, %0\n\ts_cbranch_vccnz 2f\n\t"
                 "s_sub_u32 %1, %1, 1\n\ts_cbranch_scc0 1b\n2:" : "=&v"(v), "+s"(n) : "v"(a), "s"(want) : "vcc", "scc", "memory");
}
template <int CTRL> DI float dppf(float v) {
  return __builtin_bit_cast(float, __builtin_amdgcn_update_dpp(0, __builtin_bit_cast(int, v), CTRL, 0xF, 0xF, true));
}
DI float row16_max(float v) {
  asm volatile("s_nop 1\n\tv_max_f32_dpp %0, %0, %0 row_ror:8 row_mask:0xf bank_mask:0xf\n\ts_nop 1\n\tv_max_f32_dpp %0, %0, %0 row_ror:4 row_mask:0xf bank_mask:0xf\n\t"
               "s_nop 1\n\tv_max_f32_dpp %0, %0, %0 row_ror:2 row_mask:0xf bank_mask:0xf\n\ts_nop 1\n\tv_max_f32_dpp %0, %0, %0 row_ror:1 row_mask:0xf bank_mask:0xf" : "+v"(v));
  return v;
}
DI float row16_sum(float v) { v += dppf<0x128>(v); v += dppf<0x124>(v); v += dppf<0x122>(v); return v + dppf<0x121>(v); }
DI float xsum16(float v) { auto r = __builtin_amdgcn_permlane16_swap(__float_as_uint(v), __float_as_uint(v), false, false); return __uint_as_float(r[0]) + __uint_as_float(r[1]); }
DI float xsum32(float v) { auto r = __builtin_amdgcn_permlane32_swap(__float_as_uint(v), __float_as_uint(v), false, false); return __uint_as_float(r[0]) + __uint_as_float(r[1]); }
DI float xmax16(float v) { auto r = __builtin_amdgcn_permlane16_swap(__float_as_uint(v), __float_as_uint(v), false, false); return fmaxf(__uint_as_float(r[0]), __uint_as_float(r[1])); }
DI float xmax32(float v) { auto r = __builtin_amdgcn_permlane32_swap(__float_as_uint(v), __float_as_uint(v), false, false); return fmaxf(__uint_as_float(r[0]), __uint_as_float(r[1])); }
DI float wave_sum(float v) { return xsum32(xsum16(row16_sum(v))); }

#define XS8(d, a, b) "v_xor_b32_dpp " d ", " a ", " a " row_ror:8 row_mask:0xf bank_mask:0x3\n\tv_xor_b32_dpp " d ", " b ", " b " row_ror:8 row_mask:0xf bank_mask:0xc\n\t"
#define XS4(d, a, b) "v_xor_b32_dpp " d ", " a ", " a " row_half_mirror row_mask:0xf bank_mask:0x5\n\tv_xor_b32_dpp " d ", " b ", " b " row_half_mirror row_mask:0xf bank_mask:0xa\n\t"
#define XQ(d, p) "v_xor_b32_dpp " d ", " d ", " d " quad_perm:" p " row_mask:0xf bank_mask:0xf\n\t"
DI void xor_rows8(const unsigned (&v)[8], unsigned& r0, unsigned& r1) {
  unsigned t0, t1, t2, t3;
  asm volatile("s_nop 1\n\t" XS8("%2", "%6", "%10") XS8("%4", "%8", "%12") XS8("%3", "%7", "%11") XS8("%5", "%9", "%13")
               XS4("%0", "%2", "%4") XS4("%1", "%3", "%5") XQ("%0", "[2,3,0,1]") "s_nop 0\n\t" XQ("%1", "[2,3,0,1]") XQ("%0", "[1,0,3,2]")
               "s_nop 0\n\t" XQ("%1", "[1,0,3,2]")
               : "=&v"(r0), "=&v"(r1), "=&v"(t0), "=&v"(t1), "=&v"(t2), "=&v"(t3)
               : "v"(v[0]), "v"(v[1]), "v"(v[2]), "v"(v[3]), "v"(v[4]), "v"(v[5]), "v"(v[6]), "v"(v[7]));
}
DI unsigned xor_rows2(unsigned a, unsigned b) {
  unsigned r;
  asm volatile("s_nop 1\n\t" XS8("%0", "%1", "%2") "s_nop 1\n\tv_xor_b32_dpp %0, %0, %0 row_half_mirror row_mask:0xf bank_mask:0xf\n\ts_nop 1\n\t"
               XQ("%0", "[2,3,0,1]") "s_nop 1\n\t" XQ("%0", "[1,0,3,2]") "s_nop 1" : "=&v"(r) : "v"(a), "v"(b));
  return r;
}
template <int N> DI bool any_sent(const v4u (&r)[N]) {
  unsigned m = 0;
#pragma unroll
  for (int i = 0; i < N; ++i) m = max(m, umax4(r[i]));
  return __any(m == SENT);
}
template <int N> DI void load_n(rsrc_t rs, int off0, int str, v4u (&r)[N]) {
  FENCE();
#pragma unroll
  for (int i = 0; i < N; ++i) r[i] = ldg(rs, off0, i * str);
  __builtin_amdgcn_sched_barrier(0);
}
template <int N, int NAP = 0> DI void gather(rsrc_t rs, int off0, int str, v4u (&r)[N]) {
#pragma unroll 1
  for (int sp = 0; sp < SPIN_G; ++sp) {
    load_n<N>(rs, off0, str, r);
    if (!any_sent<N>(r)) break;
    nap<NAP>();
  }
}
DI u64 get_tag(const unsigned char* ws) {
  u64 e;
  asm volatile("s_load_dwordx2 %0, %1, 0x0\n\ts_waitcnt lgkmcnt(0)" : "=s"(e) : "s"(ws) : "memory");
  return e * 6364136223846793005ull + 1442695040888963407ull;
}
DI bool flags_up(v4u f, u64 tag) {
  const unsigned lo = (unsigned)tag, hi = (unsigned)(tag >> 32);
  return __all(f.x == lo && f.y == hi && f.z == lo && f.w == hi);
}
DI void wait_flags(rsrc_t rs, int off, u64 tag) {
#pragma unroll 1
  for (int sp = 0; sp < SPIN_G; ++sp) {
    FENCE();
    if (flags_up(ldg(rs, off), tag)) break;
    __builtin_amdgcn_s_sleep(8);
  }
  FENCE();
}
typedef unsigned s8u __attribute__((ext_vector_type(8)));
DI s8u sload8(const unsigned char* a) {
  s8u v;
  asm volatile("s_load_dwordx8 %0, %1, 0x0 glc\n\ts_waitcnt lgkmcnt(0)" : "=s"(v) : "s"(a) : "memory");
  return v;
}
DI unsigned max8(s8u v) { return max(max(max(v[0], v[1]), max(v[2], v[3])), max(max(v[4], v[5]), max(v[6], v[7]))); }
DI unsigned now32() { return (unsigned)__builtin_amdgcn_s_memrealtime(); }
constexpr unsigned HWIN = 1u << 20;
DI unsigned latest_time(const unsigned char* a, unsigned t0, unsigned key) {
  unsigned m = 0;
#pragma unroll 1
  for (int sp = 0; sp < SPIN_H; ++sp) {
    m = max8(sload8(a) - (key + t0 - HWIN));
    if (m < 2 * HWIN) return m + t0 - HWIN;
  }
  return t0;
}
template <int STR> DI unsigned latest_time32(const unsigned char* a, unsigned t0, unsigned key) {
  const unsigned kb = key + t0 - HWIN;
#pragma unroll 1
  for (int sp = 0; sp < SPIN_H; ++sp) {
    s8u u, v, x, y;
    asm volatile("s_load_dwordx8 %0, %4, 0x0 glc\n\ts_load_dwordx8 %1, %4, %5 glc\n\ts_load_dwordx8 %2, %4, %6 glc\n\t"
                 "s_load_dwordx8 %3, %4, %7 glc\n\ts_waitcnt lgkmcnt(0)"
                 : "=&s"(u), "=&s"(v), "=&s"(x), "=&s"(y) : "s"(a), "i"(STR), "i"(2 * STR), "i"(3 * STR) : "memory");
    const unsigned m = max(max(max8(u - kb), max8(v - kb)), max(max8(x - kb), max8(y - kb)));
    if (m < 2 * HWIN) return m + t0 - HWIN;
  }
  return t0;
}
DI void wait_until(unsigned t) {
#pragma unroll 1
  for (int sp = 0; sp < SPIN_H; ++sp) if ((int)(now32() - t) >= 0) break;
}
DI int arena(int wid) { return WS_ARENA + wid * ASTR; }

DI unsigned tr_addr(unsigned region, int lane, int col4, int t) {
  const int g = lane >> 4, q = (lane & 15) >> 2;
  return region + (8 * g + 4 * t + q) * 256 + 16 * ((col4 >> 3) ^ ((q << 2) | ((2 * g + t) & 3))) + 8 * ((col4 >> 2) & 1);
}
DI v4u tr_frag(unsigned a0, unsigned a1, int ks) {
  const v2u lo = ldstr(a0, ks * 8192), hi = ldstr(a1, ks * 8192);
  return v4u{lo.x, lo.y, hi.x, hi.y};
}
DI void panel_frags(unsigned region, int lane, int col_a, int col_b, v4u (&f)[2][4]) {
  const unsigned a0 = tr_addr(region, lane, col_a, 0), a1 = tr_addr(region, lane, col_a, 1);
  const unsigned b0 = tr_addr(region, lane, col_b, 0), b1 = tr_addr(region, lane, col_b, 1);
#pragma unroll
  for (int ks = 0; ks < 4; ++ks) { f[0][ks] = tr_frag(a0, a1, ks); f[1][ks] = tr_frag(b0, b1, ks); }
}
DI void gemv2(const v4u (&f)[2][4], unsigned act, f32x4& d0, f32x4& d1) {
  v4u bf[4];
#pragma unroll
  for (int ks = 0; ks < 4; ++ks) bf[ks] = lds128(act, ks * 64);
  __builtin_amdgcn_sched_barrier(0);
#pragma unroll
  for (int ks = 0; ks < 4; ++ks) { d0 = mfma16(f[0][ks], bf[ks], d0); d1 = mfma16(f[1][ks], bf[ks], d1); }
}
DI f32x4 up4(v2u v) { return f32x4{bflo(v.x), bfhi(v.x), bflo(v.y), bfhi(v.y)}; }
DI v4u pk8(f32x4 a, f32x4 b) { return v4u{pk(a.x, a.y), pk(a.z, a.w), pk(b.x, b.y), pk(b.z, b.w)}; }
DI v4u silu8(v4u cv) {
  v4u o;
#pragma unroll
  for (int e = 0; e < 4; ++e) {
    const float a = bflo(cv[e]), c = bfhi(cv[e]);
    o[e] = pk(a * __builtin_amdgcn_rcpf(1.f + __expf(-a)), c * __builtin_amdgcn_rcpf(1.f + __expf(-c)));
  }
  return o;
}

struct Params {
  const u16 *x, *ckv, *wq, *wuk, *wuv, *wo, *w1, *w2, *c, *wmod;
  u16* out;
  unsigned char* ws;
};

constexpr unsigned W0 = 0, W1 = 32768, W2 = 65536, L_SC = 98304  , L_A1 = L_SC + 4224  ,
                   L_A3 = L_A1 + 4224  , L_A4 = L_A3 + 2176, L_CNT = L_A4 + 2176,
                   M_RAW = 114688  , M_SC = M_RAW + 16384  , M_LNX = M_SC + 1152  ;
constexpr unsigned T_SP = 131072, T_P = T_SP + 16384, T_FAC = T_P + 8192, T_CNT = T_FAC + 1024,
                   L_A5 = 98304  , L_A6 = L_A5 + 8320  ;
constexpr unsigned C_LAND = T_CNT + 32, C_FREE = C_LAND + 4, C_SRDY = C_LAND + 8, C_PRDY = C_LAND + 12,
                   C_QST = T_CNT + 192, C_QRD = T_CNT + 196, C_HOLD = T_CNT + 200;
constexpr unsigned L_JUNK = LDS_BYTES - 256;

template <int N> DI f32x4 reduce_rows(rsrc_t rs, int off0, int offl, v4u sel, int& rounds) {
  f32x4 a;
#pragma unroll 1
  for (int sp = 0; sp < SPIN_G; ++sp) {
    v4u d[N];
    FENCE();
#pragma unroll
    for (int i = 0; i < N; ++i) d[i] = ldg(rs, i == N - 1 ? offl : off0, i * 16 * ASTR);
    __builtin_amdgcn_sched_barrier(0);
    f32x4 b = {0.f, 0.f, 0.f, 0.f};
    a = b;
#pragma unroll
    for (int i = 0; i < N; i += 2) { a = mfma16(sel, d[i], a); b = mfma16(sel, d[i + 1], b); }
    a += b;
    rounds = sp + 1;
    if (!__any(a.x != a.x) || !any_sent<N>(d)) break;
  }
#pragma unroll
  for (int r = 0; r < 4; ++r) { a[r] += dppf<0x128>(a[r]); a[r] += dppf<0x124>(a[r]); }
  return a;
}
DI v4u pick(bool lo, v4u a, v4u b) { return v4u{lo ? a.x : b.x, lo ? a.y : b.y, lo ? a.z : b.z, lo ? a.w : b.w}; }
DI void st32(rsrc_t rs, int off, unsigned v) { __builtin_amdgcn_raw_buffer_store_b32(v, rs, off, 0, SC1); }

DI void weight_wg(const Params& p, int g, int wi, int wave, int lane, unsigned t0) {
  unsigned char* ws = p.ws;
  const rsrc_t rs = __builtin_amdgcn_make_buffer_rsrc(ws, 0, WS_BYTES, 0x00020000);
  const int h = 4 * g + (wi >> 2), j = wi & 3, wid = 16 * g + wi, my = arena(wid);
  const int l15 = lane & 15, lq = lane >> 4, b8 = lane & 7;
  const bool bvalid = l15 < 8, isq = j < 2;
  const int w = wave & 3;
  const int ch = l15 ^ ((lq << 2) | w);
  const unsigned dst0 = w * 1024;
  const unsigned vuv = (128 * j + 16 * ((4 * w + lq) & 7) + (w >> 1)) * 8192 + h * 256 + ch * 16;
  const f32x4 z4 = {0.f, 0.f, 0.f, 0.f};

  if (wave >= 8) {
    if (!isq) wait_until(t0 + T_UK);
    if (!isq) dma_run<true>(p.wuk, (256 * (j - 2) + 4 * w + lq) * 8192 + h * 256 + ch * 16, W0 + dst0, 16, 16 * 8192, 4096);
    const unsigned key = (unsigned)get_tag(ws);
    int hp = isq && lane < 8 ? WS_HP + (4 * h + 2 + (lane & 1)) * 128 + j * 4 + (lane >> 1) * 8 : OOB;
    pin(hp);
    VMCNT(0);
    STAMP(44);
    if (!isq) lsig(L_CNT);
    bar();
    const unsigned tb1 = now32();
    nap<3>();
    if (w == 0) st32(rs, hp, tb1 + key);
    bar();
    v4u wo[4][8];
    const GAS unsigned char* wb = (const GAS unsigned char*)p.wo + (h * 128 + 8 * lq) * 1024 + w * 256 + (4 * (l15 & 3) + (l15 >> 2)) * 16;
    bar();
    wait_until(tb1 + (isq ? T_WO + T_QP : T_WO));
#pragma unroll
    for (int ks = 0; ks < 4; ++ks) {
      const GAS unsigned char* wk = wb + ((ks + j) & 3) * 32768;
#pragma unroll
      for (int r = 0; r < 8; ++r) wo[ks][r] = ks == 0 ? __builtin_nontemporal_load((const GAS v4u*)(wk + r * 1024)) : *(const GAS v4u*)(wk + r * 1024);
      if (ks == 0) VMCNT(0);
    }
    bar();
    __builtin_amdgcn_sched_barrier(0);
#pragma unroll
    for (int ks = 0; ks < 4; ++ks) {
      v4u o[8];
#pragma unroll
      for (int dd = 0; dd < 4; ++dd)
#pragma unroll
        for (int rp = 0; rp < 4; ++rp) {
          o[2 * dd][rp] = __builtin_amdgcn_perm(wo[ks][2 * rp + 1][dd], wo[ks][2 * rp][dd], 0x05040100u);
          o[2 * dd + 1][rp] = __builtin_amdgcn_perm(wo[ks][2 * rp + 1][dd], wo[ks][2 * rp][dd], 0x07060302u);
        }
#pragma unroll
      for (int t = 0; t < 8; ++t) { wo[ks][t] = o[t]; pin(wo[ks][t]); }
    }
    STAMP(10);
    unsigned ba[4];
#pragma unroll
    for (int ks = 0; ks < 4; ++ks) { ba[ks] = L_A4 + b8 * 272 + ((ks + (WO_MODE ? j : 2 * j)) & 3) * 64 + lq * 16; pin(ba[ks]); }
    int oo = my + A_OUTP + (16 * w + 4 * (l15 >> 3) + lq) * 128 + b8 * 16;
    pin(oo);
    __builtin_amdgcn_s_setprio(2);
    bar();
    bar();
    v4u bf[4];
#pragma unroll
    for (int ks = 0; ks < 4; ++ks) bf[ks] = lds128(ba[ks]);
    v4u r0, r1;
#pragma unroll
    for (int tp = 0; tp < 4; ++tp) {
      f32x4 e = z4, o = z4;
#pragma unroll
      for (int ks = 0; ks < 4; ++ks) { e = mfma16(wo[ks][2 * tp], bf[ks], e); o = mfma16(wo[ks][2 * tp + 1], bf[ks], o); }
      r0[tp] = bvalid ? pk(e[0], o[0]) : pk(e[1], o[1]);
      r1[tp] = bvalid ? pk(e[2], o[2]) : pk(e[3], o[3]);
    }
    stg(rs, oo, r0);
    stg(rs, oo + 1024, r1);
    STAMP(14);
    if (wave == 8) { DUMP(10); DUMP(14); if (isq) DUMP(44); else { const unsigned stv60 = STV(44); DUMP(60); } }
    ROLE_END();
  } else if (wave >= 4) {
    __builtin_amdgcn_s_setprio(2);
    const int t = w * 64 + lane;
    const u64 tag = get_tag(ws);
    v4u msk;
    {
      const unsigned half = (lane & 1) ? 0xFFFF0000u : 0x0000FFFFu;
      const int dw = b8 >> 1;
      msk = v4u{dw == 0 ? half : 0u, dw == 1 ? half : 0u, dw == 2 ? half : 0u, dw == 3 ? half : 0u};
    }
    v4u sela = msk & (bvalid ? 0x3F803F80u : 0u), selb = msk & (bvalid ? 0u : 0x3F803F80u);
    int go = isq ? arena(64 * j + 16 * w + 4 * (lq >> 1) + 2 + (lq & 1)) + A_S + l15 * 16 : arena(4 * h) + A_E + t * 16;
    unsigned gd = isq ? L_A1 + b8 * 528 + (64 * w + 16 * (lq >> 1) + 8 * (l15 >> 3) + 4 * (lq & 1)) * 2
                      : L_A1 + b8 * 528 + (t >> 3) * 8;
    pin(msk); pin(sela); pin(selb); pin(go); pin(gd);
    STAMPD(34); STAMPD(46);
    if (w == 2) {
      wait_until(t0 + T_EF);
      wait_flags(rs, WS_EFL + wid * 256 + (isq ? l15 * 16 : 0), tag);
      STAMPV(46);
    }
    if (w == 3) {
      unsigned tr;
      if (isq) {
        tr = latest_time32<128>(ws + WS_HH + wid * 512, t0, (unsigned)tag) + T_H;
      } else {
        tr = latest_time(ws + WS_HP + wid * 128, t0, (unsigned)tag) + T_QP;
        lwait(L_CNT, 4);
        const unsigned tn = now32() + 8;
        if ((int)(tn - tr) > 0) tr = tn;
        st32(rs, WS_HQ + (2 * lane + j - 2) * 512 + (h & 3) * 128 + g * 4, tr + (unsigned)tag);
      }
      STAMPV(34);
      wait_until(tr);
    }
    bar();
    int rh = 0;
    if (isq) {
      f32x4 h0, h1;
#pragma unroll 1
      for (int sp = 0; sp < SPIN_G; ++sp) {
        v4u r[8];
        FENCE();
#pragma unroll
        for (int i = 0; i < 8; ++i) r[i] = ldg(rs, go + (i & 3) * 256, (i >> 2) * 8 * ASTR);
        __builtin_amdgcn_sched_barrier(0);
        h0 = mfma16(selb, r[5], mfma16(sela, r[4], mfma16(selb, r[1], mfma16(sela, r[0], z4))));
        h1 = mfma16(selb, r[7], mfma16(sela, r[6], mfma16(selb, r[3], mfma16(sela, r[2], z4))));
        rh = sp + 1;
        if (!__any(__builtin_isunordered(h0.x, h1.x)) || !any_sent<8>(r)) break;
      }
      *(LAS v2u*)gd = v2u{pk(h0.x, h0.y), pk(h0.z, h0.w)};
      *(LAS v2u*)((LAS char*)gd + 64) = v2u{pk(h1.x, h1.y), pk(h1.z, h1.w)};
    } else {
      f32x4 q;
#pragma unroll 1
      for (int sp = 0; sp < SPIN_G; ++sp) {
        v4u r[2];
        load_n<2>(rs, go, ASTR, r);
        q = u2f(r[0]) + u2f(r[1]);
        rh = sp + 1;
        if (!__any(nan4(q)) || !any_sent<2>(r)) break;
      }
      *(LAS v2u*)gd = v2u{pk(q.x, q.y), pk(q.z, q.w)};
    }
    STAMP(5);
    bar();
    bar();
    if (isq)
      *(LAS v4u*)(L_SC + (2 * w + (lane >> 5)) * 528 + (lane & 31) * 16) =
          silu8(*(const GAS v4u*)(p.c + (2 * w + (lane >> 5)) * 512 + j * 256 + (lane & 31) * 8));
    if (w == 3) {
      nap<D_FILL>();
#pragma unroll 1
      for (int n = wid < 8 ? 6 : 7; n < (wid < 56 ? 23 : 15); ++n)
        stg(rs, my + lane * 16 + n * 1024, v4u{SENT, SENT, SENT, SENT});
      VMCNT(0);
      if (lane == 0) __hip_atomic_store((GAS u64*)(ws + WS_FLAG2 + wid * 8), tag, RLX_AGENT);
    }
    bar();
    const int el = (4 * (wi & 1) + (lane & 3)) * 512 + 8 * (wid >> 1) + 4 * (lq & 1), pc = wi * 64 + (lane & 3) * 16;
    int gmo = w == 0 ? arena(4 * g) + A_S + pc + (lq & 1) * 8 : OOB - 100 * ASTR;
    pin(gmo);
    int mo = arena(32 * w + (lane & 31)) + A_ML + h * 16;
    int po = my + A_PART + w * 8192 + lane * 16;
    unsigned o3 = L_A3 + (2 * w + (lane >> 5)) * 272 + l15 * 16 + (lq & 1) * 8;
    int hb = lane < 16 ? WS_HB + (16 * lane + ((wi - 5 * g) & 15)) * 128 + g * 4 : OOB;
    int co = lane < 32 ? mo + (A_CK - A_ML) + j * 512 : OOB;
    int bp = 4 * lq + (bvalid ? 0 : 128);
    unsigned tlo = lane < 32 ? (unsigned)tag : 0u, thi = lane < 32 ? (unsigned)(tag >> 32) : 0u;
    pin(mo); pin(po); pin(o3); pin(hb); pin(co); pin(bp); pin(tlo); pin(thi);
    const unsigned ta = latest_time32<128>(ws + WS_ARR + (4 * h + w) * 512, t0, (unsigned)tag);
    STAMP(11);
    *(LAS unsigned*)(L_CNT + 16 + w * 4) = ta;
    wait_until(ta + T_ML);
    FENCE();
    v4u ml = ldg(rs, mo);
    wait_until(ta + T_PART);
    v4u d[8], ck;
    load_n<8>(rs, po, 1024, d);
    ck = ldg(rs, co);
    __builtin_amdgcn_sched_barrier(0);
    if (__builtin_expect(__any(((ml.y ^ (unsigned)tag) | (ml.w ^ (unsigned)(tag >> 32))) != 0), 0)) {
#pragma unroll 1
      for (int sp = 0; sp < SPIN_G; ++sp) {
        FENCE();
        ml = ldg(rs, mo);
        if (__all(ml.y == (unsigned)tag && ml.w == (unsigned)(tag >> 32))) break;
      }
    }
    v4u sel[8];
    {
      const float r = __uint_as_float(ml.x), e = __builtin_amdgcn_exp2f(r - row16_max(r));
      const float wgt = e * __builtin_amdgcn_rcpf(row16_sum(e * __uint_as_float(ml.z)));
      const unsigned hi = pk(wgt, wgt);
      const float rest = wgt - bflo(hi);
      const int v = lane < 32 ? hi : pk(rest, rest);
      unsigned wv[8];
#pragma unroll
      for (int i = 0; i < 8; ++i) wv[i] = __builtin_amdgcn_ds_bpermute(bp + 16 * i, v);
      __builtin_amdgcn_sched_barrier(0);
#pragma unroll
      for (int i = 0; i < 8; ++i) { sel[i] = msk & wv[i]; pin(sel[i]); }
    }
    f32x4 oa, ob;
    int rp = 0;
#pragma unroll 1
    for (int sp = 0; sp < SPIN_G; ++sp) {
      oa = z4; ob = z4;
      unsigned xe = 0, xo = 0;
#pragma unroll
      for (int i = 0; i < 8; ++i) {
        if (i < 4) oa = mfma16(sel[i], d[i], oa); else ob = mfma16(sel[i], d[i], ob);
        xe = __builtin_amdgcn_bitop3_b32(xe, d[i].x, d[i].z, 0x96);
        xo = __builtin_amdgcn_bitop3_b32(xo, d[i].y, d[i].w, 0x96);
        __builtin_amdgcn_sched_barrier(0);
      }
      rp = sp + 1;
      unsigned x = xor_rows2(xe ^ ck.x, xo ^ ck.z);
      auto y = __builtin_amdgcn_permlane16_swap(x, x, false, false);
      x = y[0] ^ y[1];
      y = __builtin_amdgcn_permlane32_swap(x, x, false, false);
      if (!__any(((y[0] ^ y[1]) | (ck.y ^ tlo) | (ck.w ^ thi)) != 0)) break;
      load_n<8>(rs, po, 1024, d);
      ck = ldg(rs, co);
    }
    ROLE_END();
    STAMP(12);
    {
      f32x4 s;
#pragma unroll
      for (int r = 0; r < 4; ++r) {
        auto x = __builtin_amdgcn_permlane32_swap(__float_as_uint(oa[r]), __float_as_uint(ob[r]), false, false);
        s[r] = __uint_as_float(x[0]) + __uint_as_float(x[1]);
      }
      *(LAS v2u*)o3 = v2u{pk(s.x, s.y), pk(s.z, s.w)};
    }
    bar();
    v2u gg[4], xb;
    v4u f2;
    if (w == 0) {
#pragma unroll
      for (int i = 0; i < 4; ++i) gg[i] = __builtin_amdgcn_raw_buffer_load_b64(rs, gmo, ((i >> 1) * 96 + (i & 1)) * ASTR, SC1);
      f2 = ldg(rs, WS_FLAG2 + lane * 16);
      xb = *(const GAS v2u*)(p.x + el);
    }
    unsigned tb = 0;
    if (w < 2) {
      const v4u tv = lds128(L_CNT + 16) - t0;
      tb = __builtin_amdgcn_readfirstlane(umax4(tv)) + t0 + T_PART + T_B6;
      const unsigned tn = now32() - 6;
      if ((int)(tn - tb) > 0) tb = tn;
    }
    if (w == 1) st32(rs, hb, tb + (unsigned)tag);
    bar();
    if (w == 0) {
#pragma unroll 1
      for (int sp = 0; sp < SPIN_G; ++sp) {
        unsigned m = 0;
#pragma unroll
        for (int i = 0; i < 4; ++i) m = max(m, max(gg[i].x, gg[i].y));
        if (!__any(m == SENT)) break;
        FENCE();
#pragma unroll
        for (int i = 0; i < 4; ++i) gg[i] = __builtin_amdgcn_raw_buffer_load_b64(rs, gmo, ((i >> 1) * 96 + (i & 1)) * ASTR, SC1);
      }
      if (!flags_up(f2, tag)) wait_flags(rs, WS_FLAG2 + lane * 16, tag);
      f32x4 x0 = up4(xb), ga = up4(gg[0]) + up4(gg[1]), gb = up4(gg[2]) + up4(gg[3]);
      pin(x0); pin(ga); pin(gb);
      STAMP(26);
      const bool act = lane < 32 && l15 < 4;
      v4u one = sela;
      int oo = arena(lane >> 2) + A_OUTP + g * 1024 + pc;
      int yo = oo + (A_YP - A_OUTP);
      int yl = lane < 32 ? yo : OOB - 48 * ASTR;
      int xo = act ? arena(g) + A_X1 + pc + lq * 8 : OOB;
      const rsrc_t ro = __builtin_amdgcn_make_buffer_rsrc(p.out, 0, 8192, 0x00020000);
      int fo = act ? el * 2 : OOB;
      pin(one); pin(oo); pin(yo); pin(yl); pin(xo); pin(fo);
      wait_until(tb + T_B);
      int r1 = 0, r2 = 0;
      const f32x4 x1 = x0 + ga * reduce_rows<8>(rs, oo, oo, one, r1);
      __builtin_amdgcn_raw_buffer_store_b64(v2u{pk(x1.x, x1.y), pk(x1.z, x1.w)}, rs, xo, 0, SC1);
      STAMP(27);
      wait_until(latest_time(ws + WS_HB + (128 + wid) * 128, t0, (unsigned)tag) + T_D);
      const f32x4 r = x1 + gb * reduce_rows<4>(rs, yo, yl, one, r2);
      STAMP(29);
      __builtin_amdgcn_raw_buffer_store_b64(v2u{pk(r.x, r.y), pk(r.z, r.w)}, ro, fo, 0, 0);
      if (wid == 0 && lane == 0) __hip_atomic_store((GAS u64*)ws, tag, RLX_AGENT);
      STAMP(30);
      DUMP(26); DUMP(27); DUMP(29); DUMP(30); DUMPC(42, r1); DUMPC(43, r2);
    }
    ROLE_END();
  } else {
    __builtin_amdgcn_s_setprio(2);
    const int p4 = 4 * (lane & 3);
    unsigned act = b8 * 272 + lq * 16;
    unsigned s0 = tr_addr(W2, lane, 16 * w + p4, 0), s1 = tr_addr(W2, lane, 16 * w + p4, 1);
    unsigned ba = L_SC + b8 * 528 + lq * 16;
    v4u f[2][4], f2[2][4];
    unsigned a1 = L_A1 + b8 * 528 + lq * 16;
    int o = my + A_E + (isq ? w * 1024 + (l15 >> 3) * 512 + lq * 128 + b8 * 16 : b8 * 512 + w * 128 + (l15 >> 3) * 64 + lq * 16);
    pin(o); pin(a1); pin(act);
    bar();
    const unsigned tb1 = now32();
    STAMP(4);
    if (isq) {
      panel_frags(W0, lane, 32 * w + p4, 32 * w + 16 + p4, f);
      panel_frags(W1, lane, 32 * w + p4, 32 * w + 16 + p4, f2);
      bar();
      f32x4 q0 = z4, q1 = z4;
      v4u bf[8];
#pragma unroll
      for (int ks = 0; ks < 8; ++ks) bf[ks] = lds128(a1, ks * 64);
      __builtin_amdgcn_sched_barrier(0);
#pragma unroll
      for (int ks = 0; ks < 4; ++ks) { q0 = mfma16(f[0][ks], bf[ks], q0); q1 = mfma16(f[1][ks], bf[ks], q1); }
#pragma unroll
      for (int ks = 0; ks < 4; ++ks) { q0 = mfma16(f2[0][ks], bf[4 + ks], q0); q1 = mfma16(f2[1][ks], bf[4 + ks], q1); }
      stg(rs, o, pick(bvalid, f2u(q0), f2u(q1)));
    } else {
#pragma unroll
      for (int tt = 0; tt < 2; ++tt) {
        const unsigned ka = W0 + (64 * w + 8 * (l15 >> 2) + 4 * tt + (l15 & 3)) * 256 + 16 * (lq ^ (((l15 & 3) << 2) | ((2 * (l15 >> 2) + tt) & 3)));
#pragma unroll
        for (int ks = 0; ks < 4; ++ks) { f[tt][ks] = lds128(ka ^ (ks * 64)); f2[tt][ks] = lds128(ka ^ (ks * 64), 8192); }
      }
      bar();
      f32x4 a0 = z4, a2 = z4, c0 = z4, c2 = z4;
      v4u bf[4];
#pragma unroll
      for (int ks = 0; ks < 4; ++ks) bf[ks] = lds128(a1, ks * 64);
      __builtin_amdgcn_sched_barrier(0);
#pragma unroll
      for (int ks = 0; ks < 4; ++ks) {
        a0 = mfma16(f[0][ks], bf[ks], a0); c0 = mfma16(f2[0][ks], bf[ks], c0);
        a2 = mfma16(f[1][ks], bf[ks], a2); c2 = mfma16(f2[1][ks], bf[ks], c2);
      }
      stg(rs, o, pk8(u2f(pick(bvalid, f2u(a0), f2u(c0))) * QSCALE, u2f(pick(bvalid, f2u(a2), f2u(c2))) * QSCALE));
    }
    STAMP(6);
    bar();
    wait_until(tb1 + (isq ? T_L + T_QP : T_L));
    if (HELP && !isq) {
      const unsigned char* kv = (const unsigned char*)p.ckv + (size_t)(g * 4096 + (7 * 16 + 4 * (wi >> 2) + 2 * (j - 2)) * 32) * 1024;
      dma_run<false>(kv, w * 1024 + lane * 16, W1 + dst0, 16, 4096, 0);
    }
    dma_run<true>(p.wuv, vuv, W0 + dst0, 8, 2 * 8192, 4096);
    const bool m2 = isq;
    if (m2) dma_run<true>(p.wmod, (256 * j + 4 * w + lq + (ch >> 3) * 128) * 6144 + 2048 + h * 128 + (ch & 7) * 16, W2 + dst0, 8, 16 * 6144, 4096);
    VMCNT(0);
    bar();
    STAMP(9);
    if (m2) {
      f32x4 m0 = z4, m1 = z4;
#pragma unroll 1
      for (int ks = 0; ks < 4; ++ks) {
        m0 = mfma16(tr_frag(s0 + ks * 8192, s1 + ks * 8192, 0), lds128(ba + ks * 64), m0);
        m1 = mfma16(tr_frag((s0 ^ 128) + ks * 8192, (s1 ^ 128) + ks * 8192, 0), lds128(ba + ks * 64, 256), m1);
      }
      m0 += m1;
      __builtin_amdgcn_raw_buffer_store_b64(v2u{pk(m0.x, m0.y), pk(m0.z, m0.w)}, rs, bvalid ? my + A_S + (2 * w + (lq >> 1)) * 128 + l15 * 16 + (lq & 1) * 8 : OOB, 0, SC1);
    }
    panel_frags(W0, lane, 32 * w + 2 * p4, 32 * w + 2 * p4 + 4, f);
    unsigned o4 = bvalid ? L_A4 + l15 * 272 + w * 64 + lq * 16 : L_JUNK;
    pin(o4);
    if (g < 7 && (FP_ALL || !isq)) {
      const int e = 2 * (wi >> 2) + (j & 1);
      wait_until(tb1 + (isq ? T_FP + T_QP : T_FP));
      if (!isq) dma_run<false>(p.w1, (64 * e + 16 * w + (lane >> 5)) * 3584 + g * 512 + (lane & 31) * 16, W1 + dst0, 8, 2 * 3584, 0);
      if (FP_ALL == isq && FP_W2) dma_run<false>(p.w2, (256 * g + 32 * e + 8 * w) * 1024 + lane * 16, W1 + dst0, 8, 1024, 0);
    }
    bar();
    STAMP(35);
    f32x4 c0 = z4, c1 = z4;
    gemv2(f, L_A3 + act, c0, c1);
    *(LAS v4u*)o4 = pk8(c0, c1);
    bar();
    STAMP(13);
  }
}

template <int OFF> DI void rd_inplace(v4u& a, unsigned ad) { asm volatile("ds_read_b128 %0, %1 offset:%2" : "+v"(a) : "v"(ad), "i"(OFF) : "memory"); }
template <int O0, int O1> DI void peek2(unsigned& a, unsigned& b, unsigned cb) {
  asm volatile("ds_read_b32 %0, %2 offset:%3\n\tds_read_b32 %1, %2 offset:%4" : "=&v"(a), "=&v"(b) : "v"(cb), "i"(O0), "i"(O1) : "memory");
}
template <int W> DI void wait2(unsigned& a, unsigned& b) { asm volatile("s_waitcnt lgkmcnt(%2)" : "+v"(a), "+v"(b) : "i"(W)); }
template <int W, bool FIRST> DI void score_pair(f32x4& s0, f32x4& s1, v4u a, v4u q0, v4u q1) {
  if constexpr (FIRST)
    asm volatile("s_waitcnt lgkmcnt(%5)\n\tv_mfma_f32_16x16x32_bf16 %0, %2, %3, 0\n\tv_mfma_f32_16x16x32_bf16 %1, %2, %4, 0"
                 : "=&v"(s0), "=&v"(s1) : "v"(a), "v"(q0), "v"(q1), "i"(W));
  else
    asm volatile("s_waitcnt lgkmcnt(%5)\n\tv_mfma_f32_16x16x32_bf16 %0, %2, %3, %0\n\tv_mfma_f32_16x16x32_bf16 %1, %2, %4, %1"
                 : "+v"(s0), "+v"(s1) : "v"(a), "v"(q0), "v"(q1), "i"(W));
}
template <int OFF> DI void put_scores(unsigned sa, f32x4 s0, f32x4 s1) {
  asm volatile("s_nop 4\n\tds_write_b128 %0, %1 offset:%3\n\tds_write_b128 %0, %2 offset:%4" :: "v"(sa), "v"(s0), "v"(s1), "i"(OFF), "i"(OFF + 1024) : "memory");
}
template <int V> struct IntC { static constexpr int value = V; };
template <int I, int N, class F> DI void static_for(F&& f) { if constexpr (I < N) { f(IntC<I>{}); static_for<I + 1, N>(f); } }
constexpr unsigned slot_c(int t) { return ((0x02310210u >> ((4 * t) & 31)) & 3) * 32768; }
template <unsigned A> DI void csig(unsigned cb, unsigned v) {
  asm volatile("s_mov_b64 exec, 1\n\tds_add_u32 %0, %1 offset:%2\n\ts_mov_b64 exec, -1" :: "v"(cb), "v"(v), "i"(A - T_CNT) : "memory");
}
template <unsigned A> DI unsigned cpeek(unsigned cb) { return *(volatile LAS unsigned*)((LAS char*)cb + (A - T_CNT)); }
#define SPIN_HEAD "s_mov_b32 %[n], %[nb]\n1:\n\ts_sub_u32 %[n], %[n], 1\n\ts_cbranch_scc1 2f\n\t"
#define SPIN_TAIL "s_waitcnt lgkmcnt(0)\n\tv_cmp_gt_u32 vcc, %[w], %[c]\n\ts_cbranch_vccnz 1b\n2:"
#define SPIN_TAIL2 "s_waitcnt lgkmcnt(0)\n\tv_cmp_gt_u32 vcc, %[w], %[c]\n\ts_cbranch_vccz 2f\n" \
                   "3:\n\ts_sub_u32 %[n], %[n], 1\n\ts_cbranch_scc1 2f\n\tds_read_b32 %[c], %[cb] offset:%[o]\n\ts_waitcnt lgkmcnt(0)\n\t" \
                   "v_cmp_gt_u32 vcc, %[w2], %[c]\n\ts_cbranch_vccnz 3b\n\ts_branch 1b\n2:"
template <unsigned A, unsigned W, bool NAP = false> DI void cwait(unsigned cb, unsigned nb) {
  unsigned c, n;
  if (NAP)
    asm volatile(SPIN_HEAD "s_sleep 1\n\tds_read_b32 %[c], %[cb] offset:%[o]\n\t" SPIN_TAIL
                 : [c] "=&v"(c), [n] "=&s"(n) : [cb] "v"(cb), [o] "i"(A - T_CNT), [w] "i"(W), [nb] "s"(nb) : "vcc", "scc", "memory");
  else
    asm volatile(SPIN_HEAD "ds_read_b32 %[c], %[cb] offset:%[o]\n\t" SPIN_TAIL
                 : [c] "=&v"(c), [n] "=&s"(n) : [cb] "v"(cb), [o] "i"(A - T_CNT), [w] "i"(W), [nb] "s"(nb) : "vcc", "scc", "memory");
}
constexpr unsigned T_DRY = T_CNT + 256;
DI constexpr unsigned fsw(int row) { return ((row & 3) << 2) | ((row >> 1) & 2) | ((row >> 3) & 1); }
DI unsigned slot_of(int t) { return ((0x02310210u >> ((4 * t) & 31)) & 3) * 32768; }
#define DMA4(AUX, base, dst, v0, v1, v2, v3) \
  asm volatile("s_mov_b32 m0, %5\n\ts_waitcnt vmcnt(31)\n\tglobal_load_lds_dwordx4 %0, %4 " AUX "\n\t" \
               "s_waitcnt vmcnt(31)\n\tglobal_load_lds_dwordx4 %1, %4 offset:1024 " AUX "\n\t" \
               "s_waitcnt vmcnt(31)\n\tglobal_load_lds_dwordx4 %2, %4 offset:2048 " AUX "\n\t" \
               "s_waitcnt vmcnt(31)\n\tglobal_load_lds_dwordx4 %3, %4 offset:3072 " AUX \
               :: "v"(v0), "v"(v1), "v"(v2), "v"(v3), "s"(base), "s"(dst) : "memory")

typedef float f32x32 __attribute__((ext_vector_type(32)));
DI void pv_loop(f32x32& o0, f32x32& o1, const unsigned (&a)[8], unsigned pa, unsigned cb, unsigned one, unsigned nb) {
  unsigned n, k;
  u64 m0, m1, ret;
  asm volatile(
      "s_branch 300f\n"
      "200:\n"
      "v_readfirstlane_b32 %[n], v20\n"
      "v_lshrrev_b32 v32, 4, %[pa]\n"
      "v_and_b32 v32, 48, v32\n"
      "v_add_u32 v32, %[k], v32\n"
      "v_add_u32 v32, %[fac], v32\n"
      "ds_read_b128 v[36:39], v32 offset:64\n"
      "ds_read_b128 v[32:35], v32\n"
      "s_waitcnt lgkmcnt(0)\n"
      "s_bitcmp1_b32 %[n], 0\n"
      "s_cbranch_scc0 93f\n"
      "v_pk_mul_f32 v[40:41], v[40:41], v[32:33]\n"
      "v_pk_mul_f32 v[42:43], v[42:43], v[34:35]\n"
      "v_pk_mul_f32 v[44:45], v[44:45], v[32:33]\n"
      "v_pk_mul_f32 v[46:47], v[46:47], v[34:35]\n"
      "v_pk_mul_f32 v[48:49], v[48:49], v[32:33]\n"
      "v_pk_mul_f32 v[50:51], v[50:51], v[34:35]\n"
      "v_pk_mul_f32 v[52:53], v[52:53], v[32:33]\n"
      "v_pk_mul_f32 v[54:55], v[54:55], v[34:35]\n"
      "v_pk_mul_f32 v[56:57], v[56:57], v[32:33]\n"
      "v_pk_mul_f32 v[58:59], v[58:59], v[34:35]\n"
      "v_pk_mul_f32 v[60:61], v[60:61], v[32:33]\n"
      "v_pk_mul_f32 v[62:63], v[62:63], v[34:35]\n"
      "v_pk_mul_f32 v[64:65], v[64:65], v[32:33]\n"
      "v_pk_mul_f32 v[66:67], v[66:67], v[34:35]\n"
      "v_pk_mul_f32 v[68:69], v[68:69], v[32:33]\n"
      "v_pk_mul_f32 v[70:71], v[70:71], v[34:35]\n"
      "93:\n"
      "s_bitcmp1_b32 %[n], 1\n"
      "s_cbranch_scc0 93f\n"
      "v_pk_mul_f32 v[72:73], v[72:73], v[36:37]\n"
      "v_pk_mul_f32 v[74:75], v[74:75], v[38:39]\n"
      "v_pk_mul_f32 v[76:77], v[76:77], v[36:37]\n"
      "v_pk_mul_f32 v[78:79], v[78:79], v[38:39]\n"
      "v_pk_mul_f32 v[80:81], v[80:81], v[36:37]\n"
      "v_pk_mul_f32 v[82:83], v[82:83], v[38:39]\n"
      "v_pk_mul_f32 v[84:85], v[84:85], v[36:37]\n"
      "v_pk_mul_f32 v[86:87], v[86:87], v[38:39]\n"
      "v_pk_mul_f32 v[88:89], v[88:89], v[36:37]\n"
      "v_pk_mul_f32 v[90:91], v[90:91], v[38:39]\n"
      "v_pk_mul_f32 v[92:93], v[92:93], v[36:37]\n"
      "v_pk_mul_f32 v[94:95], v[94:95], v[38:39]\n"
      "v_pk_mul_f32 v[96:97], v[96:97], v[36:37]\n"
      "v_pk_mul_f32 v[98:99], v[98:99], v[38:39]\n"
      "v_pk_mul_f32 v[100:101], v[100:101], v[36:37]\n"
      "v_pk_mul_f32 v[102:103], v[102:103], v[38:39]\n"
      "93:\n"
      "s_nop 4\n"
      "s_setpc_b64 %[ret]\n"
      "201:\n"
      "v_readfirstlane_b32 %[n], v22\n"
      "v_lshrrev_b32 v24, 4, %[pa]\n"
      "v_and_b32 v24, 48, v24\n"
      "v_add_u32 v24, %[k], v24\n"
      "v_add_u32 v24, %[fac], v24\n"
      "ds_read_b128 v[28:31], v24 offset:64\n"
      "ds_read_b128 v[24:27], v24\n"
      "s_waitcnt lgkmcnt(0)\n"
      "s_bitcmp1_b32 %[n], 0\n"
      "s_cbranch_scc0 93f\n"
      "v_pk_mul_f32 v[40:41], v[40:41], v[24:25]\n"
      "v_pk_mul_f32 v[42:43], v[42:43], v[26:27]\n"
      "v_pk_mul_f32 v[44:45], v[44:45], v[24:25]\n"
      "v_pk_mul_f32 v[46:47], v[46:47], v[26:27]\n"
      "v_pk_mul_f32 v[48:49], v[48:49], v[24:25]\n"
      "v_pk_mul_f32 v[50:51], v[50:51], v[26:27]\n"
      "v_pk_mul_f32 v[52:53], v[52:53], v[24:25]\n"
      "v_pk_mul_f32 v[54:55], v[54:55], v[26:27]\n"
      "v_pk_mul_f32 v[56:57], v[56:57], v[24:25]\n"
      "v_pk_mul_f32 v[58:59], v[58:59], v[26:27]\n"
      "v_pk_mul_f32 v[60:61], v[60:61], v[24:25]\n"
      "v_pk_mul_f32 v[62:63], v[62:63], v[26:27]\n"
      "v_pk_mul_f32 v[64:65], v[64:65], v[24:25]\n"
      "v_pk_mul_f32 v[66:67], v[66:67], v[26:27]\n"
      "v_pk_mul_f32 v[68:69], v[68:69], v[24:25]\n"
      "v_pk_mul_f32 v[70:71], v[70:71], v[26:27]\n"
      "93:\n"
      "s_bitcmp1_b32 %[n], 1\n"
      "s_cbranch_scc0 93f\n"
      "v_pk_mul_f32 v[72:73], v[72:73], v[28:29]\n"
      "v_pk_mul_f32 v[74:75], v[74:75], v[30:31]\n"
      "v_pk_mul_f32 v[76:77], v[76:77], v[28:29]\n"
      "v_pk_mul_f32 v[78:79], v[78:79], v[30:31]\n"
      "v_pk_mul_f32 v[80:81], v[80:81], v[28:29]\n"
      "v_pk_mul_f32 v[82:83], v[82:83], v[30:31]\n"
      "v_pk_mul_f32 v[84:85], v[84:85], v[28:29]\n"
      "v_pk_mul_f32 v[86:87], v[86:87], v[30:31]\n"
      "v_pk_mul_f32 v[88:89], v[88:89], v[28:29]\n"
      "v_pk_mul_f32 v[90:91], v[90:91], v[30:31]\n"
      "v_pk_mul_f32 v[92:93], v[92:93], v[28:29]\n"
      "v_pk_mul_f32 v[94:95], v[94:95], v[30:31]\n"
      "v_pk_mul_f32 v[96:97], v[96:97], v[28:29]\n"
      "v_pk_mul_f32 v[98:99], v[98:99], v[30:31]\n"
      "v_pk_mul_f32 v[100:101], v[100:101], v[28:29]\n"
      "v_pk_mul_f32 v[102:103], v[102:103], v[30:31]\n"
      "93:\n"
      "s_nop 4\n"
      "s_setpc_b64 %[ret]\n"
      "110:\n"
      "s_mov_b32 %[n], %[nb]\n"
      "91:\n"
      "s_sub_u32 %[n], %[n], 1\n"
      "s_cbranch_scc1 92f\n"
      "ds_read_b32 v22, %[cb] offset:60\n"
      "s_waitcnt lgkmcnt(0)\n"
      "v_cmp_gt_u32 vcc, 8, v22\n"
      "s_cbranch_vccnz 91b\n"
      "92:\n"
      "ds_read_b32 v22, %[cb] offset:60\n"
      "ds_read_b128 v[32:35], %[pa] offset:2048\n"
      "ds_read_b128 v[36:39], %[pa] offset:3072\n"
      "ds_read_b32 v23, %[cb] offset:80\n"
      "s_waitcnt lgkmcnt(0)\n"
      "v_cmp_lt_u32 vcc, 8, v22\n"
      "s_cbranch_vccz 111f\n"
      "114:\n"
      "s_movk_i32 %[k], 128\n"
      "s_call_b64 %[ret], 201b\n"
      "s_branch 111f\n"
      "120:\n"
      "s_mov_b32 %[n], %[nb]\n"
      "91:\n"
      "s_sub_u32 %[n], %[n], 1\n"
      "s_cbranch_scc1 92f\n"
      "ds_read_b32 v20, %[cb] offset:76\n"
      "s_waitcnt lgkmcnt(0)\n"
      "v_cmp_gt_u32 vcc, 8, v20\n"
      "s_cbranch_vccnz 91b\n"
      "92:\n"
      "ds_read_b32 v20, %[cb] offset:76\n"
      "ds_read_b128 v[24:27], %[pa] offset:4096\n"
      "ds_read_b128 v[28:31], %[pa] offset:5120\n"
      "ds_read_b32 v21, %[cb] offset:96\n"
      "s_waitcnt lgkmcnt(0)\n"
      "v_cmp_lt_u32 vcc, 8, v20\n"
      "s_cbranch_vccz 121f\n"
      "124:\n"
      "s_movk_i32 %[k], 256\n"
      "s_call_b64 %[ret], 200b\n"
      "s_branch 121f\n"
      "123:\n"
      "s_mov_b32 %[n], %[nb]\n"
      "91:\n"
      "s_sub_u32 %[n], %[n], 1\n"
      "s_cbranch_scc1 92f\n"
      "ds_read_b32 v22, %[cb] offset:80\n"
      "s_waitcnt lgkmcnt(0)\n"
      "v_cmp_gt_u32 vcc, 1, v22\n"
      "s_cbranch_vccnz 91b\n"
      "92:\n"
      "ds_read_b64_tr_b16 v[136:137], %[a0] offset:0\n"
      "ds_read_b64_tr_b16 v[138:139], %[a0] offset:16384\n"
      "ds_read_b64_tr_b16 v[140:141], %[a1] offset:0\n"
      "ds_read_b64_tr_b16 v[142:143], %[a1] offset:16384\n"
      "ds_read_b64_tr_b16 v[144:145], %[a2] offset:0\n"
      "ds_read_b64_tr_b16 v[146:147], %[a2] offset:16384\n"
      "ds_read_b64_tr_b16 v[148:149], %[a3] offset:0\n"
      "ds_read_b64_tr_b16 v[150:151], %[a3] offset:16384\n"
      "ds_read_b64_tr_b16 v[152:153], %[a4] offset:0\n"
      "ds_read_b64_tr_b16 v[154:155], %[a4] offset:16384\n"
      "ds_read_b64_tr_b16 v[156:157], %[a5] offset:0\n"
      "ds_read_b64_tr_b16 v[158:159], %[a5] offset:16384\n"
      "ds_read_b64_tr_b16 v[160:161], %[a6] offset:0\n"
      "ds_read_b64_tr_b16 v[162:163], %[a6] offset:16384\n"
      "ds_read_b64_tr_b16 v[164:165], %[a7] offset:0\n"
      "ds_read_b64_tr_b16 v[166:167], %[a7] offset:16384\n"
      "s_mov_b64 exec, 1\n"
      "ds_add_u32 %[cb], %[one] offset:84\n"
      "s_mov_b64 exec, -1\n"
      "130:\n"
      "s_mov_b32 %[n], %[nb]\n"
      "91:\n"
      "s_sub_u32 %[n], %[n], 1\n"
      "s_cbranch_scc1 92f\n"
      "ds_read_b32 v22, %[cb] offset:92\n"
      "s_waitcnt lgkmcnt(0)\n"
      "v_cmp_gt_u32 vcc, 8, v22\n"
      "s_cbranch_vccnz 91b\n"
      "92:\n"
      "ds_read_b32 v22, %[cb] offset:92\n"
      "ds_read_b128 v[32:35], %[pa] offset:6144\n"
      "ds_read_b128 v[36:39], %[pa] offset:7168\n"
      "ds_read_b32 v23, %[cb] offset:112\n"
      "s_waitcnt lgkmcnt(0)\n"
      "v_cmp_lt_u32 vcc, 8, v22\n"
      "s_cbranch_vccz 131f\n"
      "134:\n"
      "s_movk_i32 %[k], 384\n"
      "s_call_b64 %[ret], 201b\n"
      "s_branch 131f\n"
      "133:\n"
      "s_mov_b32 %[n], %[nb]\n"
      "91:\n"
      "s_sub_u32 %[n], %[n], 1\n"
      "s_cbranch_scc1 92f\n"
      "ds_read_b32 v20, %[cb] offset:96\n"
      "s_waitcnt lgkmcnt(0)\n"
      "v_cmp_gt_u32 vcc, 1, v20\n"
      "s_cbranch_vccnz 91b\n"
      "92:\n"
      "ds_read_b64_tr_b16 v[104:105], %[a0] offset:32768\n"
      "ds_read_b64_tr_b16 v[106:107], %[a0] offset:49152\n"
      "ds_read_b64_tr_b16 v[108:109], %[a1] offset:32768\n"
      "ds_read_b64_tr_b16 v[110:111], %[a1] offset:49152\n"
      "ds_read_b64_tr_b16 v[112:113], %[a2] offset:32768\n"
      "ds_read_b64_tr_b16 v[114:115], %[a2] offset:49152\n"
      "ds_read_b64_tr_b16 v[116:117], %[a3] offset:32768\n"
      "ds_read_b64_tr_b16 v[118:119], %[a3] offset:49152\n"
      "ds_read_b64_tr_b16 v[120:121], %[a4] offset:32768\n"
      "ds_read_b64_tr_b16 v[122:123], %[a4] offset:49152\n"
      "ds_read_b64_tr_b16 v[124:125], %[a5] offset:32768\n"
      "ds_read_b64_tr_b16 v[126:127], %[a5] offset:49152\n"
      "ds_read_b64_tr_b16 v[128:129], %[a6] offset:32768\n"
      "ds_read_b64_tr_b16 v[130:131], %[a6] offset:49152\n"
      "ds_read_b64_tr_b16 v[132:133], %[a7] offset:32768\n"
      "ds_read_b64_tr_b16 v[134:135], %[a7] offset:49152\n"
      "s_mov_b64 exec, 1\n"
      "ds_add_u32 %[cb], %[one] offset:100\n"
      "s_mov_b64 exec, -1\n"
      "140:\n"
      "s_mov_b32 %[n], %[nb]\n"
      "91:\n"
      "s_sub_u32 %[n], %[n], 1\n"
      "s_cbranch_scc1 92f\n"
      "ds_read_b32 v20, %[cb] offset:108\n"
      "s_waitcnt lgkmcnt(0)\n"
      "v_cmp_gt_u32 vcc, 8, v20\n"
      "s_cbranch_vccnz 91b\n"
      "92:\n"
      "ds_read_b32 v20, %[cb] offset:108\n"
      "ds_read_b128 v[24:27], %[pa] offset:0\n"
      "ds_read_b128 v[28:31], %[pa] offset:1024\n"
      "ds_read_b32 v21, %[cb] offset:128\n"
      "s_waitcnt lgkmcnt(0)\n"
      "v_cmp_lt_u32 vcc, 8, v20\n"
      "s_cbranch_vccz 141f\n"
      "144:\n"
      "s_movk_i32 %[k], 512\n"
      "s_call_b64 %[ret], 200b\n"
      "s_branch 141f\n"
      "143:\n"
      "s_mov_b32 %[n], %[nb]\n"
      "91:\n"
      "s_sub_u32 %[n], %[n], 1\n"
      "s_cbranch_scc1 92f\n"
      "ds_read_b32 v22, %[cb] offset:112\n"
      "s_waitcnt lgkmcnt(0)\n"
      "v_cmp_gt_u32 vcc, 1, v22\n"
      "s_cbranch_vccnz 91b\n"
      "92:\n"
      "v_add_u32 v138, 0x10000, %[a0]\n"
      "ds_read_b64_tr_b16 v[136:137], v138 offset:32768\n"
      "ds_read_b64_tr_b16 v[138:139], v138 offset:49152\n"
      "v_add_u32 v142, 0x10000, %[a1]\n"
      "ds_read_b64_tr_b16 v[140:141], v142 offset:32768\n"
      "ds_read_b64_tr_b16 v[142:143], v142 offset:49152\n"
      "v_add_u32 v146, 0x10000, %[a2]\n"
      "ds_read_b64_tr_b16 v[144:145], v146 offset:32768\n"
      "ds_read_b64_tr_b16 v[146:147], v146 offset:49152\n"
      "v_add_u32 v150, 0x10000, %[a3]\n"
      "ds_read_b64_tr_b16 v[148:149], v150 offset:32768\n"
      "ds_read_b64_tr_b16 v[150:151], v150 offset:49152\n"
      "v_add_u32 v154, 0x10000, %[a4]\n"
      "ds_read_b64_tr_b16 v[152:153], v154 offset:32768\n"
      "ds_read_b64_tr_b16 v[154:155], v154 offset:49152\n"
      "v_add_u32 v158, 0x10000, %[a5]\n"
      "ds_read_b64_tr_b16 v[156:157], v158 offset:32768\n"
      "ds_read_b64_tr_b16 v[158:159], v158 offset:49152\n"
      "v_add_u32 v162, 0x10000, %[a6]\n"
      "ds_read_b64_tr_b16 v[160:161], v162 offset:32768\n"
      "ds_read_b64_tr_b16 v[162:163], v162 offset:49152\n"
      "v_add_u32 v166, 0x10000, %[a7]\n"
      "ds_read_b64_tr_b16 v[164:165], v166 offset:32768\n"
      "ds_read_b64_tr_b16 v[166:167], v166 offset:49152\n"
      "s_mov_b64 exec, 1\n"
      "ds_add_u32 %[cb], %[one] offset:116\n"
      "s_mov_b64 exec, -1\n"
      "150:\n"
      "s_mov_b32 %[n], %[nb]\n"
      "91:\n"
      "s_sub_u32 %[n], %[n], 1\n"
      "s_cbranch_scc1 92f\n"
      "ds_read_b32 v22, %[cb] offset:124\n"
      "s_waitcnt lgkmcnt(0)\n"
      "v_cmp_gt_u32 vcc, 8, v22\n"
      "s_cbranch_vccnz 91b\n"
      "92:\n"
      "ds_read_b32 v22, %[cb] offset:124\n"
      "ds_read_b128 v[32:35], %[pa] offset:2048\n"
      "ds_read_b128 v[36:39], %[pa] offset:3072\n"
      "ds_read_b32 v23, %[cb] offset:144\n"
      "s_waitcnt lgkmcnt(0)\n"
      "v_cmp_lt_u32 vcc, 8, v22\n"
      "s_cbranch_vccz 151f\n"
      "154:\n"
      "s_movk_i32 %[k], 640\n"
      "s_call_b64 %[ret], 201b\n"
      "s_branch 151f\n"
      "153:\n"
      "s_mov_b32 %[n], %[nb]\n"
      "91:\n"
      "s_sub_u32 %[n], %[n], 1\n"
      "s_cbranch_scc1 92f\n"
      "ds_read_b32 v20, %[cb] offset:128\n"
      "s_waitcnt lgkmcnt(0)\n"
      "v_cmp_gt_u32 vcc, 1, v20\n"
      "s_cbranch_vccnz 91b\n"
      "92:\n"
      "v_add_u32 v106, 0x10000, %[a0]\n"
      "ds_read_b64_tr_b16 v[104:105], v106 offset:0\n"
      "ds_read_b64_tr_b16 v[106:107], v106 offset:16384\n"
      "v_add_u32 v110, 0x10000, %[a1]\n"
      "ds_read_b64_tr_b16 v[108:109], v110 offset:0\n"
      "ds_read_b64_tr_b16 v[110:111], v110 offset:16384\n"
      "v_add_u32 v114, 0x10000, %[a2]\n"
      "ds_read_b64_tr_b16 v[112:113], v114 offset:0\n"
      "ds_read_b64_tr_b16 v[114:115], v114 offset:16384\n"
      "v_add_u32 v118, 0x10000, %[a3]\n"
      "ds_read_b64_tr_b16 v[116:117], v118 offset:0\n"
      "ds_read_b64_tr_b16 v[118:119], v118 offset:16384\n"
      "v_add_u32 v122, 0x10000, %[a4]\n"
      "ds_read_b64_tr_b16 v[120:121], v122 offset:0\n"
      "ds_read_b64_tr_b16 v[122:123], v122 offset:16384\n"
      "v_add_u32 v126, 0x10000, %[a5]\n"
      "ds_read_b64_tr_b16 v[124:125], v126 offset:0\n"
      "ds_read_b64_tr_b16 v[126:127], v126 offset:16384\n"
      "v_add_u32 v130, 0x10000, %[a6]\n"
      "ds_read_b64_tr_b16 v[128:129], v130 offset:0\n"
      "ds_read_b64_tr_b16 v[130:131], v130 offset:16384\n"
      "v_add_u32 v134, 0x10000, %[a7]\n"
      "ds_read_b64_tr_b16 v[132:133], v134 offset:0\n"
      "ds_read_b64_tr_b16 v[134:135], v134 offset:16384\n"
      "160:\n"
      "s_mov_b32 %[n], %[nb]\n"
      "91:\n"
      "s_sub_u32 %[n], %[n], 1\n"
      "s_cbranch_scc1 92f\n"
      "ds_read_b32 v20, %[cb] offset:140\n"
      "s_waitcnt lgkmcnt(0)\n"
      "v_cmp_gt_u32 vcc, 8, v20\n"
      "s_cbranch_vccnz 91b\n"
      "92:\n"
      "ds_read_b32 v20, %[cb] offset:140\n"
      "ds_read_b128 v[24:27], %[pa] offset:4096\n"
      "ds_read_b128 v[28:31], %[pa] offset:5120\n"
      "s_waitcnt lgkmcnt(0)\n"
      "v_cmp_lt_u32 vcc, 8, v20\n"
      "s_cbranch_vccz 161f\n"
      "164:\n"
      "s_movk_i32 %[k], 768\n"
      "s_call_b64 %[ret], 200b\n"
      "s_branch 161f\n"
      "174:\n"
      "s_movk_i32 %[k], 896\n"
      "s_call_b64 %[ret], 201b\n"
      "s_branch 171f\n"
      "300:\n"
      "s_mov_b32 %[n], %[nb]\n"
      "91:\n"
      "s_sub_u32 %[n], %[n], 1\n"
      "s_cbranch_scc1 92f\n"
      "s_sleep 1\n"
      "ds_read_b32 v20, %[cb] offset:32\n"
      "s_waitcnt lgkmcnt(0)\n"
      "v_cmp_gt_u32 vcc, 1, v20\n"
      "s_cbranch_vccnz 91b\n"
      "92:\n"
      "ds_read_b64_tr_b16 v[72:73], %[a0] offset:0\n"
      "ds_read_b64_tr_b16 v[74:75], %[a0] offset:16384\n"
      "ds_read_b64_tr_b16 v[76:77], %[a1] offset:0\n"
      "ds_read_b64_tr_b16 v[78:79], %[a1] offset:16384\n"
      "ds_read_b64_tr_b16 v[80:81], %[a2] offset:0\n"
      "ds_read_b64_tr_b16 v[82:83], %[a2] offset:16384\n"
      "ds_read_b64_tr_b16 v[84:85], %[a3] offset:0\n"
      "ds_read_b64_tr_b16 v[86:87], %[a3] offset:16384\n"
      "ds_read_b64_tr_b16 v[88:89], %[a4] offset:0\n"
      "ds_read_b64_tr_b16 v[90:91], %[a4] offset:16384\n"
      "ds_read_b64_tr_b16 v[92:93], %[a5] offset:0\n"
      "ds_read_b64_tr_b16 v[94:95], %[a5] offset:16384\n"
      "ds_read_b64_tr_b16 v[96:97], %[a6] offset:0\n"
      "ds_read_b64_tr_b16 v[98:99], %[a6] offset:16384\n"
      "ds_read_b64_tr_b16 v[100:101], %[a7] offset:0\n"
      "ds_read_b64_tr_b16 v[102:103], %[a7] offset:16384\n"
      "s_mov_b64 exec, 1\n"
      "ds_add_u32 %[cb], %[one] offset:36\n"
      "s_mov_b64 exec, -1\n"
      "s_mov_b32 %[n], %[nb]\n"
      "91:\n"
      "s_sub_u32 %[n], %[n], 1\n"
      "s_cbranch_scc1 92f\n"
      "s_sleep 1\n"
      "ds_read_b32 v20, %[cb] offset:48\n"
      "s_waitcnt lgkmcnt(0)\n"
      "v_cmp_gt_u32 vcc, 1, v20\n"
      "s_cbranch_vccnz 91b\n"
      "92:\n"
      "ds_read_b64_tr_b16 v[136:137], %[a0] offset:32768\n"
      "ds_read_b64_tr_b16 v[138:139], %[a0] offset:49152\n"
      "ds_read_b64_tr_b16 v[140:141], %[a1] offset:32768\n"
      "ds_read_b64_tr_b16 v[142:143], %[a1] offset:49152\n"
      "ds_read_b64_tr_b16 v[144:145], %[a2] offset:32768\n"
      "ds_read_b64_tr_b16 v[146:147], %[a2] offset:49152\n"
      "ds_read_b64_tr_b16 v[148:149], %[a3] offset:32768\n"
      "ds_read_b64_tr_b16 v[150:151], %[a3] offset:49152\n"
      "ds_read_b64_tr_b16 v[152:153], %[a4] offset:32768\n"
      "ds_read_b64_tr_b16 v[154:155], %[a4] offset:49152\n"
      "ds_read_b64_tr_b16 v[156:157], %[a5] offset:32768\n"
      "ds_read_b64_tr_b16 v[158:159], %[a5] offset:49152\n"
      "ds_read_b64_tr_b16 v[160:161], %[a6] offset:32768\n"
      "ds_read_b64_tr_b16 v[162:163], %[a6] offset:49152\n"
      "ds_read_b64_tr_b16 v[164:165], %[a7] offset:32768\n"
      "ds_read_b64_tr_b16 v[166:167], %[a7] offset:49152\n"
      "s_mov_b64 exec, 1\n"
      "ds_add_u32 %[cb], %[one] offset:52\n"
      "s_mov_b64 exec, -1\n"
      "s_mov_b32 %[n], %[nb]\n"
      "91:\n"
      "s_sub_u32 %[n], %[n], 1\n"
      "s_cbranch_scc1 92f\n"
      "s_sleep 1\n"
      "ds_read_b32 v20, %[cb] offset:64\n"
      "s_waitcnt lgkmcnt(0)\n"
      "v_cmp_gt_u32 vcc, 1, v20\n"
      "s_cbranch_vccnz 91b\n"
      "92:\n"
      "v_add_u32 v106, 0x10000, %[a0]\n"
      "ds_read_b64_tr_b16 v[104:105], v106 offset:0\n"
      "ds_read_b64_tr_b16 v[106:107], v106 offset:16384\n"
      "v_add_u32 v110, 0x10000, %[a1]\n"
      "ds_read_b64_tr_b16 v[108:109], v110 offset:0\n"
      "ds_read_b64_tr_b16 v[110:111], v110 offset:16384\n"
      "v_add_u32 v114, 0x10000, %[a2]\n"
      "ds_read_b64_tr_b16 v[112:113], v114 offset:0\n"
      "ds_read_b64_tr_b16 v[114:115], v114 offset:16384\n"
      "v_add_u32 v118, 0x10000, %[a3]\n"
      "ds_read_b64_tr_b16 v[116:117], v118 offset:0\n"
      "ds_read_b64_tr_b16 v[118:119], v118 offset:16384\n"
      "v_add_u32 v122, 0x10000, %[a4]\n"
      "ds_read_b64_tr_b16 v[120:121], v122 offset:0\n"
      "ds_read_b64_tr_b16 v[122:123], v122 offset:16384\n"
      "v_add_u32 v126, 0x10000, %[a5]\n"
      "ds_read_b64_tr_b16 v[124:125], v126 offset:0\n"
      "ds_read_b64_tr_b16 v[126:127], v126 offset:16384\n"
      "v_add_u32 v130, 0x10000, %[a6]\n"
      "ds_read_b64_tr_b16 v[128:129], v130 offset:0\n"
      "ds_read_b64_tr_b16 v[130:131], v130 offset:16384\n"
      "v_add_u32 v134, 0x10000, %[a7]\n"
      "ds_read_b64_tr_b16 v[132:133], v134 offset:0\n"
      "ds_read_b64_tr_b16 v[134:135], v134 offset:16384\n"
      "s_mov_b64 exec, 1\n"
      "ds_add_u32 %[cb], %[one] offset:68\n"
      "s_mov_b64 exec, -1\n"
      "s_mov_b64 %[m0], 0\n"
      "s_mov_b32 %[n], %[nb]\n"
      "91:\n"
      "s_sub_u32 %[n], %[n], 1\n"
      "s_cbranch_scc1 92f\n"
      "s_sleep 1\n"
      "ds_read_b32 v20, %[cb] offset:40\n"
      "s_waitcnt lgkmcnt(0)\n"
      "v_cmp_gt_u32 vcc, 1, v20\n"
      "s_cbranch_vccnz 91b\n"
      "92:\n"
      "s_mov_b32 %[n], %[nb]\n"
      "91:\n"
      "s_sub_u32 %[n], %[n], 1\n"
      "s_cbranch_scc1 92f\n"
      "ds_read_b32 v20, %[cb] offset:44\n"
      "s_waitcnt lgkmcnt(0)\n"
      "v_cmp_gt_u32 vcc, 8, v20\n"
      "s_cbranch_vccnz 91b\n"
      "92:\n"
      "ds_read_b32 v20, %[cb] offset:44\n"
      "ds_read_b128 v[24:27], %[pa] offset:0\n"
      "ds_read_b128 v[28:31], %[pa] offset:1024\n"
      "ds_read_b32 v21, %[cb] offset:64\n"
      "s_waitcnt lgkmcnt(0)\n"
      "v_mfma_f32_16x16x32_bf16 v[40:43], v[24:27], v[72:75], 0\n"
      "v_mfma_f32_16x16x32_bf16 v[72:75], v[28:31], v[72:75], 0\n"
      "v_mfma_f32_16x16x32_bf16 v[44:47], v[24:27], v[76:79], 0\n"
      "v_mfma_f32_16x16x32_bf16 v[76:79], v[28:31], v[76:79], 0\n"
      "v_mfma_f32_16x16x32_bf16 v[48:51], v[24:27], v[80:83], 0\n"
      "v_mfma_f32_16x16x32_bf16 v[80:83], v[28:31], v[80:83], 0\n"
      "v_mfma_f32_16x16x32_bf16 v[52:55], v[24:27], v[84:87], 0\n"
      "v_mfma_f32_16x16x32_bf16 v[84:87], v[28:31], v[84:87], 0\n"
      "ds_read_b32 v22, %[cb] offset:60\n"
      "ds_read_b128 v[32:35], %[pa] offset:2048\n"
      "ds_read_b128 v[36:39], %[pa] offset:3072\n"
      "ds_read_b32 v23, %[cb] offset:80\n"
      "v_mfma_f32_16x16x32_bf16 v[56:59], v[24:27], v[88:91], 0\n"
      "v_mfma_f32_16x16x32_bf16 v[88:91], v[28:31], v[88:91], 0\n"
      "v_mfma_f32_16x16x32_bf16 v[60:63], v[24:27], v[92:95], 0\n"
      "v_mfma_f32_16x16x32_bf16 v[92:95], v[28:31], v[92:95], 0\n"
      "v_mfma_f32_16x16x32_bf16 v[64:67], v[24:27], v[96:99], 0\n"
      "v_mfma_f32_16x16x32_bf16 v[96:99], v[28:31], v[96:99], 0\n"
      "v_mfma_f32_16x16x32_bf16 v[68:71], v[24:27], v[100:103], 0\n"
      "v_mfma_f32_16x16x32_bf16 v[100:103], v[28:31], v[100:103], 0\n"
      "s_waitcnt lgkmcnt(0)\n"
      "v_cmp_ne_u32 vcc, 8, v22\n"
      "s_cbranch_vccnz 110b\n"
      "111:\n"
      "v_cmp_eq_u32 %[m1], 0, v23\n"
      "v_mfma_f32_16x16x32_bf16 v[40:43], v[32:35], v[136:139], v[40:43]\n"
      "v_mfma_f32_16x16x32_bf16 v[72:75], v[36:39], v[136:139], v[72:75]\n"
      "ds_read_b64_tr_b16 v[136:137], %[a0] offset:0\n"
      "ds_read_b64_tr_b16 v[138:139], %[a0] offset:16384\n"
      "v_mfma_f32_16x16x32_bf16 v[44:47], v[32:35], v[140:143], v[44:47]\n"
      "v_mfma_f32_16x16x32_bf16 v[76:79], v[36:39], v[140:143], v[76:79]\n"
      "ds_read_b64_tr_b16 v[140:141], %[a1] offset:0\n"
      "ds_read_b64_tr_b16 v[142:143], %[a1] offset:16384\n"
      "v_mfma_f32_16x16x32_bf16 v[48:51], v[32:35], v[144:147], v[48:51]\n"
      "v_mfma_f32_16x16x32_bf16 v[80:83], v[36:39], v[144:147], v[80:83]\n"
      "ds_read_b64_tr_b16 v[144:145], %[a2] offset:0\n"
      "ds_read_b64_tr_b16 v[146:147], %[a2] offset:16384\n"
      "v_mfma_f32_16x16x32_bf16 v[52:55], v[32:35], v[148:151], v[52:55]\n"
      "v_mfma_f32_16x16x32_bf16 v[84:87], v[36:39], v[148:151], v[84:87]\n"
      "ds_read_b64_tr_b16 v[148:149], %[a3] offset:0\n"
      "ds_read_b64_tr_b16 v[150:151], %[a3] offset:16384\n"
      "ds_read_b32 v20, %[cb] offset:76\n"
      "ds_read_b128 v[24:27], %[pa] offset:4096\n"
      "ds_read_b128 v[28:31], %[pa] offset:5120\n"
      "ds_read_b32 v21, %[cb] offset:96\n"
      "v_mfma_f32_16x16x32_bf16 v[56:59], v[32:35], v[152:155], v[56:59]\n"
      "v_mfma_f32_16x16x32_bf16 v[88:91], v[36:39], v[152:155], v[88:91]\n"
      "ds_read_b64_tr_b16 v[152:153], %[a4] offset:0\n"
      "ds_read_b64_tr_b16 v[154:155], %[a4] offset:16384\n"
      "v_mfma_f32_16x16x32_bf16 v[60:63], v[32:35], v[156:159], v[60:63]\n"
      "v_mfma_f32_16x16x32_bf16 v[92:95], v[36:39], v[156:159], v[92:95]\n"
      "ds_read_b64_tr_b16 v[156:157], %[a5] offset:0\n"
      "ds_read_b64_tr_b16 v[158:159], %[a5] offset:16384\n"
      "v_mfma_f32_16x16x32_bf16 v[64:67], v[32:35], v[160:163], v[64:67]\n"
      "v_mfma_f32_16x16x32_bf16 v[96:99], v[36:39], v[160:163], v[96:99]\n"
      "ds_read_b64_tr_b16 v[160:161], %[a6] offset:0\n"
      "ds_read_b64_tr_b16 v[162:163], %[a6] offset:16384\n"
      "v_mfma_f32_16x16x32_bf16 v[68:71], v[32:35], v[164:167], v[68:71]\n"
      "v_mfma_f32_16x16x32_bf16 v[100:103], v[36:39], v[164:167], v[100:103]\n"
      "ds_read_b64_tr_b16 v[164:165], %[a7] offset:0\n"
      "ds_read_b64_tr_b16 v[166:167], %[a7] offset:16384\n"
      "s_cmp_lg_u64 %[m1], 0\n"
      "s_cbranch_scc1 112f\n"
      "s_mov_b64 exec, 1\n"
      "ds_add_u32 %[cb], %[one] offset:84\n"
      "s_mov_b64 exec, -1\n"
      "112:\n"
      "s_waitcnt lgkmcnt(8)\n"
      "v_cmp_ne_u32 vcc, 8, v20\n"
      "s_cbranch_vccnz 120b\n"
      "121:\n"
      "v_cmp_eq_u32 %[m0], 0, v21\n"
      "v_mfma_f32_16x16x32_bf16 v[40:43], v[24:27], v[104:107], v[40:43]\n"
      "v_mfma_f32_16x16x32_bf16 v[72:75], v[28:31], v[104:107], v[72:75]\n"
      "ds_read_b64_tr_b16 v[104:105], %[a0] offset:32768\n"
      "ds_read_b64_tr_b16 v[106:107], %[a0] offset:49152\n"
      "v_mfma_f32_16x16x32_bf16 v[44:47], v[24:27], v[108:111], v[44:47]\n"
      "v_mfma_f32_16x16x32_bf16 v[76:79], v[28:31], v[108:111], v[76:79]\n"
      "ds_read_b64_tr_b16 v[108:109], %[a1] offset:32768\n"
      "ds_read_b64_tr_b16 v[110:111], %[a1] offset:49152\n"
      "v_mfma_f32_16x16x32_bf16 v[48:51], v[24:27], v[112:115], v[48:51]\n"
      "v_mfma_f32_16x16x32_bf16 v[80:83], v[28:31], v[112:115], v[80:83]\n"
      "ds_read_b64_tr_b16 v[112:113], %[a2] offset:32768\n"
      "ds_read_b64_tr_b16 v[114:115], %[a2] offset:49152\n"
      "v_mfma_f32_16x16x32_bf16 v[52:55], v[24:27], v[116:119], v[52:55]\n"
      "v_mfma_f32_16x16x32_bf16 v[84:87], v[28:31], v[116:119], v[84:87]\n"
      "ds_read_b64_tr_b16 v[116:117], %[a3] offset:32768\n"
      "ds_read_b64_tr_b16 v[118:119], %[a3] offset:49152\n"
      "ds_read_b32 v22, %[cb] offset:92\n"
      "ds_read_b128 v[32:35], %[pa] offset:6144\n"
      "ds_read_b128 v[36:39], %[pa] offset:7168\n"
      "ds_read_b32 v23, %[cb] offset:112\n"
      "v_mfma_f32_16x16x32_bf16 v[56:59], v[24:27], v[120:123], v[56:59]\n"
      "v_mfma_f32_16x16x32_bf16 v[88:91], v[28:31], v[120:123], v[88:91]\n"
      "ds_read_b64_tr_b16 v[120:121], %[a4] offset:32768\n"
      "ds_read_b64_tr_b16 v[122:123], %[a4] offset:49152\n"
      "v_mfma_f32_16x16x32_bf16 v[60:63], v[24:27], v[124:127], v[60:63]\n"
      "v_mfma_f32_16x16x32_bf16 v[92:95], v[28:31], v[124:127], v[92:95]\n"
      "ds_read_b64_tr_b16 v[124:125], %[a5] offset:32768\n"
      "ds_read_b64_tr_b16 v[126:127], %[a5] offset:49152\n"
      "v_mfma_f32_16x16x32_bf16 v[64:67], v[24:27], v[128:131], v[64:67]\n"
      "v_mfma_f32_16x16x32_bf16 v[96:99], v[28:31], v[128:131], v[96:99]\n"
      "ds_read_b64_tr_b16 v[128:129], %[a6] offset:32768\n"
      "ds_read_b64_tr_b16 v[130:131], %[a6] offset:49152\n"
      "v_mfma_f32_16x16x32_bf16 v[68:71], v[24:27], v[132:135], v[68:71]\n"
      "v_mfma_f32_16x16x32_bf16 v[100:103], v[28:31], v[132:135], v[100:103]\n"
      "ds_read_b64_tr_b16 v[132:133], %[a7] offset:32768\n"
      "ds_read_b64_tr_b16 v[134:135], %[a7] offset:49152\n"
      "s_cmp_lg_u64 %[m0], 0\n"
      "s_cbranch_scc1 122f\n"
      "s_mov_b64 exec, 1\n"
      "ds_add_u32 %[cb], %[one] offset:100\n"
      "s_mov_b64 exec, -1\n"
      "122:\n"
      "s_cmp_lg_u64 %[m1], 0\n"
      "s_cbranch_scc1 123b\n"
      "s_waitcnt lgkmcnt(8)\n"
      "v_cmp_ne_u32 vcc, 8, v22\n"
      "s_cbranch_vccnz 130b\n"
      "131:\n"
      "v_cmp_eq_u32 %[m1], 0, v23\n"
      "v_mfma_f32_16x16x32_bf16 v[40:43], v[32:35], v[136:139], v[40:43]\n"
      "v_mfma_f32_16x16x32_bf16 v[72:75], v[36:39], v[136:139], v[72:75]\n"
      "v_add_u32 v138, 0x10000, %[a0]\n"
      "ds_read_b64_tr_b16 v[136:137], v138 offset:32768\n"
      "ds_read_b64_tr_b16 v[138:139], v138 offset:49152\n"
      "v_mfma_f32_16x16x32_bf16 v[44:47], v[32:35], v[140:143], v[44:47]\n"
      "v_mfma_f32_16x16x32_bf16 v[76:79], v[36:39], v[140:143], v[76:79]\n"
      "v_add_u32 v142, 0x10000, %[a1]\n"
      "ds_read_b64_tr_b16 v[140:141], v142 offset:32768\n"
      "ds_read_b64_tr_b16 v[142:143], v142 offset:49152\n"
      "v_mfma_f32_16x16x32_bf16 v[48:51], v[32:35], v[144:147], v[48:51]\n"
      "v_mfma_f32_16x16x32_bf16 v[80:83], v[36:39], v[144:147], v[80:83]\n"
      "v_add_u32 v146, 0x10000, %[a2]\n"
      "ds_read_b64_tr_b16 v[144:145], v146 offset:32768\n"
      "ds_read_b64_tr_b16 v[146:147], v146 offset:49152\n"
      "v_mfma_f32_16x16x32_bf16 v[52:55], v[32:35], v[148:151], v[52:55]\n"
      "v_mfma_f32_16x16x32_bf16 v[84:87], v[36:39], v[148:151], v[84:87]\n"
      "v_add_u32 v150, 0x10000, %[a3]\n"
      "ds_read_b64_tr_b16 v[148:149], v150 offset:32768\n"
      "ds_read_b64_tr_b16 v[150:151], v150 offset:49152\n"
      "ds_read_b32 v20, %[cb] offset:108\n"
      "ds_read_b128 v[24:27], %[pa] offset:0\n"
      "ds_read_b128 v[28:31], %[pa] offset:1024\n"
      "ds_read_b32 v21, %[cb] offset:128\n"
      "v_mfma_f32_16x16x32_bf16 v[56:59], v[32:35], v[152:155], v[56:59]\n"
      "v_mfma_f32_16x16x32_bf16 v[88:91], v[36:39], v[152:155], v[88:91]\n"
      "v_add_u32 v154, 0x10000, %[a4]\n"
      "ds_read_b64_tr_b16 v[152:153], v154 offset:32768\n"
      "ds_read_b64_tr_b16 v[154:155], v154 offset:49152\n"
      "v_mfma_f32_16x16x32_bf16 v[60:63], v[32:35], v[156:159], v[60:63]\n"
      "v_mfma_f32_16x16x32_bf16 v[92:95], v[36:39], v[156:159], v[92:95]\n"
      "v_add_u32 v158, 0x10000, %[a5]\n"
      "ds_read_b64_tr_b16 v[156:157], v158 offset:32768\n"
      "ds_read_b64_tr_b16 v[158:159], v158 offset:49152\n"
      "v_mfma_f32_16x16x32_bf16 v[64:67], v[32:35], v[160:163], v[64:67]\n"
      "v_mfma_f32_16x16x32_bf16 v[96:99], v[36:39], v[160:163], v[96:99]\n"
      "v_add_u32 v162, 0x10000, %[a6]\n"
      "ds_read_b64_tr_b16 v[160:161], v162 offset:32768\n"
      "ds_read_b64_tr_b16 v[162:163], v162 offset:49152\n"
      "v_mfma_f32_16x16x32_bf16 v[68:71], v[32:35], v[164:167], v[68:71]\n"
      "v_mfma_f32_16x16x32_bf16 v[100:103], v[36:39], v[164:167], v[100:103]\n"
      "v_add_u32 v166, 0x10000, %[a7]\n"
      "ds_read_b64_tr_b16 v[164:165], v166 offset:32768\n"
      "ds_read_b64_tr_b16 v[166:167], v166 offset:49152\n"
      "s_cmp_lg_u64 %[m1], 0\n"
      "s_cbranch_scc1 132f\n"
      "s_mov_b64 exec, 1\n"
      "ds_add_u32 %[cb], %[one] offset:116\n"
      "s_mov_b64 exec, -1\n"
      "132:\n"
      "s_cmp_lg_u64 %[m0], 0\n"
      "s_cbranch_scc1 133b\n"
      "s_waitcnt lgkmcnt(8)\n"
      "v_cmp_ne_u32 vcc, 8, v20\n"
      "s_cbranch_vccnz 140b\n"
      "141:\n"
      "v_cmp_eq_u32 %[m0], 0, v21\n"
      "v_mfma_f32_16x16x32_bf16 v[40:43], v[24:27], v[104:107], v[40:43]\n"
      "v_mfma_f32_16x16x32_bf16 v[72:75], v[28:31], v[104:107], v[72:75]\n"
      "v_add_u32 v106, 0x10000, %[a0]\n"
      "ds_read_b64_tr_b16 v[104:105], v106 offset:0\n"
      "ds_read_b64_tr_b16 v[106:107], v106 offset:16384\n"
      "v_mfma_f32_16x16x32_bf16 v[44:47], v[24:27], v[108:111], v[44:47]\n"
      "v_mfma_f32_16x16x32_bf16 v[76:79], v[28:31], v[108:111], v[76:79]\n"
      "v_add_u32 v110, 0x10000, %[a1]\n"
      "ds_read_b64_tr_b16 v[108:109], v110 offset:0\n"
      "ds_read_b64_tr_b16 v[110:111], v110 offset:16384\n"
      "v_mfma_f32_16x16x32_bf16 v[48:51], v[24:27], v[112:115], v[48:51]\n"
      "v_mfma_f32_16x16x32_bf16 v[80:83], v[28:31], v[112:115], v[80:83]\n"
      "v_add_u32 v114, 0x10000, %[a2]\n"
      "ds_read_b64_tr_b16 v[112:113], v114 offset:0\n"
      "ds_read_b64_tr_b16 v[114:115], v114 offset:16384\n"
      "v_mfma_f32_16x16x32_bf16 v[52:55], v[24:27], v[116:119], v[52:55]\n"
      "v_mfma_f32_16x16x32_bf16 v[84:87], v[28:31], v[116:119], v[84:87]\n"
      "v_add_u32 v118, 0x10000, %[a3]\n"
      "ds_read_b64_tr_b16 v[116:117], v118 offset:0\n"
      "ds_read_b64_tr_b16 v[118:119], v118 offset:16384\n"
      "ds_read_b32 v22, %[cb] offset:124\n"
      "ds_read_b128 v[32:35], %[pa] offset:2048\n"
      "ds_read_b128 v[36:39], %[pa] offset:3072\n"
      "ds_read_b32 v23, %[cb] offset:144\n"
      "v_mfma_f32_16x16x32_bf16 v[56:59], v[24:27], v[120:123], v[56:59]\n"
      "v_mfma_f32_16x16x32_bf16 v[88:91], v[28:31], v[120:123], v[88:91]\n"
      "v_add_u32 v122, 0x10000, %[a4]\n"
      "ds_read_b64_tr_b16 v[120:121], v122 offset:0\n"
      "ds_read_b64_tr_b16 v[122:123], v122 offset:16384\n"
      "v_mfma_f32_16x16x32_bf16 v[60:63], v[24:27], v[124:127], v[60:63]\n"
      "v_mfma_f32_16x16x32_bf16 v[92:95], v[28:31], v[124:127], v[92:95]\n"
      "v_add_u32 v126, 0x10000, %[a5]\n"
      "ds_read_b64_tr_b16 v[124:125], v126 offset:0\n"
      "ds_read_b64_tr_b16 v[126:127], v126 offset:16384\n"
      "v_mfma_f32_16x16x32_bf16 v[64:67], v[24:27], v[128:131], v[64:67]\n"
      "v_mfma_f32_16x16x32_bf16 v[96:99], v[28:31], v[128:131], v[96:99]\n"
      "v_add_u32 v130, 0x10000, %[a6]\n"
      "ds_read_b64_tr_b16 v[128:129], v130 offset:0\n"
      "ds_read_b64_tr_b16 v[130:131], v130 offset:16384\n"
      "v_mfma_f32_16x16x32_bf16 v[68:71], v[24:27], v[132:135], v[68:71]\n"
      "v_mfma_f32_16x16x32_bf16 v[100:103], v[28:31], v[132:135], v[100:103]\n"
      "v_add_u32 v134, 0x10000, %[a7]\n"
      "ds_read_b64_tr_b16 v[132:133], v134 offset:0\n"
      "ds_read_b64_tr_b16 v[134:135], v134 offset:16384\n"
      "s_cmp_lg_u64 %[m1], 0\n"
      "s_cbranch_scc1 143b\n"
      "s_waitcnt lgkmcnt(8)\n"
      "v_cmp_ne_u32 vcc, 8, v22\n"
      "s_cbranch_vccnz 150b\n"
      "151:\n"
      "v_cmp_eq_u32 %[m1], 0, v23\n"
      "v_mfma_f32_16x16x32_bf16 v[40:43], v[32:35], v[136:139], v[40:43]\n"
      "v_mfma_f32_16x16x32_bf16 v[72:75], v[36:39], v[136:139], v[72:75]\n"
      "ds_read_b64_tr_b16 v[136:137], %[a0] offset:0\n"
      "ds_read_b64_tr_b16 v[138:139], %[a0] offset:16384\n"
      "v_mfma_f32_16x16x32_bf16 v[44:47], v[32:35], v[140:143], v[44:47]\n"
      "v_mfma_f32_16x16x32_bf16 v[76:79], v[36:39], v[140:143], v[76:79]\n"
      "ds_read_b64_tr_b16 v[140:141], %[a1] offset:0\n"
      "ds_read_b64_tr_b16 v[142:143], %[a1] offset:16384\n"
      "v_mfma_f32_16x16x32_bf16 v[48:51], v[32:35], v[144:147], v[48:51]\n"
      "v_mfma_f32_16x16x32_bf16 v[80:83], v[36:39], v[144:147], v[80:83]\n"
      "ds_read_b64_tr_b16 v[144:145], %[a2] offset:0\n"
      "ds_read_b64_tr_b16 v[146:147], %[a2] offset:16384\n"
      "v_mfma_f32_16x16x32_bf16 v[52:55], v[32:35], v[148:151], v[52:55]\n"
      "v_mfma_f32_16x16x32_bf16 v[84:87], v[36:39], v[148:151], v[84:87]\n"
      "ds_read_b64_tr_b16 v[148:149], %[a3] offset:0\n"
      "ds_read_b64_tr_b16 v[150:151], %[a3] offset:16384\n"
      "ds_read_b32 v20, %[cb] offset:140\n"
      "ds_read_b128 v[24:27], %[pa] offset:4096\n"
      "ds_read_b128 v[28:31], %[pa] offset:5120\n"
      "v_mfma_f32_16x16x32_bf16 v[56:59], v[32:35], v[152:155], v[56:59]\n"
      "v_mfma_f32_16x16x32_bf16 v[88:91], v[36:39], v[152:155], v[88:91]\n"
      "ds_read_b64_tr_b16 v[152:153], %[a4] offset:0\n"
      "ds_read_b64_tr_b16 v[154:155], %[a4] offset:16384\n"
      "v_mfma_f32_16x16x32_bf16 v[60:63], v[32:35], v[156:159], v[60:63]\n"
      "v_mfma_f32_16x16x32_bf16 v[92:95], v[36:39], v[156:159], v[92:95]\n"
      "ds_read_b64_tr_b16 v[156:157], %[a5] offset:0\n"
      "ds_read_b64_tr_b16 v[158:159], %[a5] offset:16384\n"
      "v_mfma_f32_16x16x32_bf16 v[64:67], v[32:35], v[160:163], v[64:67]\n"
      "v_mfma_f32_16x16x32_bf16 v[96:99], v[36:39], v[160:163], v[96:99]\n"
      "ds_read_b64_tr_b16 v[160:161], %[a6] offset:0\n"
      "ds_read_b64_tr_b16 v[162:163], %[a6] offset:16384\n"
      "v_mfma_f32_16x16x32_bf16 v[68:71], v[32:35], v[164:167], v[68:71]\n"
      "v_mfma_f32_16x16x32_bf16 v[100:103], v[36:39], v[164:167], v[100:103]\n"
      "ds_read_b64_tr_b16 v[164:165], %[a7] offset:0\n"
      "ds_read_b64_tr_b16 v[166:167], %[a7] offset:16384\n"
      "s_cmp_lg_u64 %[m0], 0\n"
      "s_cbranch_scc1 153b\n"
      "s_waitcnt lgkmcnt(8)\n"
      "v_cmp_ne_u32 vcc, 8, v20\n"
      "s_cbranch_vccnz 160b\n"
      "161:\n"
      "v_mfma_f32_16x16x32_bf16 v[40:43], v[24:27], v[104:107], v[40:43]\n"
      "v_mfma_f32_16x16x32_bf16 v[72:75], v[28:31], v[104:107], v[72:75]\n"
      "v_mfma_f32_16x16x32_bf16 v[44:47], v[24:27], v[108:111], v[44:47]\n"
      "v_mfma_f32_16x16x32_bf16 v[76:79], v[28:31], v[108:111], v[76:79]\n"
      "v_mfma_f32_16x16x32_bf16 v[48:51], v[24:27], v[112:115], v[48:51]\n"
      "v_mfma_f32_16x16x32_bf16 v[80:83], v[28:31], v[112:115], v[80:83]\n"
      "v_mfma_f32_16x16x32_bf16 v[52:55], v[24:27], v[116:119], v[52:55]\n"
      "v_mfma_f32_16x16x32_bf16 v[84:87], v[28:31], v[116:119], v[84:87]\n"
      "v_mfma_f32_16x16x32_bf16 v[56:59], v[24:27], v[120:123], v[56:59]\n"
      "v_mfma_f32_16x16x32_bf16 v[88:91], v[28:31], v[120:123], v[88:91]\n"
      "v_mfma_f32_16x16x32_bf16 v[60:63], v[24:27], v[124:127], v[60:63]\n"
      "v_mfma_f32_16x16x32_bf16 v[92:95], v[28:31], v[124:127], v[92:95]\n"
      "v_mfma_f32_16x16x32_bf16 v[64:67], v[24:27], v[128:131], v[64:67]\n"
      "v_mfma_f32_16x16x32_bf16 v[96:99], v[28:31], v[128:131], v[96:99]\n"
      "v_mfma_f32_16x16x32_bf16 v[68:71], v[24:27], v[132:135], v[68:71]\n"
      "v_mfma_f32_16x16x32_bf16 v[100:103], v[28:31], v[132:135], v[100:103]\n"
      "s_cmp_lg_u64 %[m1], 0\n"
      "s_cbranch_scc0 170f\n"
      "s_mov_b32 %[n], %[nb]\n"
      "91:\n"
      "s_sub_u32 %[n], %[n], 1\n"
      "s_cbranch_scc1 92f\n"
      "ds_read_b32 v22, %[cb] offset:144\n"
      "s_waitcnt lgkmcnt(0)\n"
      "v_cmp_gt_u32 vcc, 1, v22\n"
      "s_cbranch_vccnz 91b\n"
      "92:\n"
      "ds_read_b64_tr_b16 v[136:137], %[a0] offset:0\n"
      "ds_read_b64_tr_b16 v[138:139], %[a0] offset:16384\n"
      "ds_read_b64_tr_b16 v[140:141], %[a1] offset:0\n"
      "ds_read_b64_tr_b16 v[142:143], %[a1] offset:16384\n"
      "ds_read_b64_tr_b16 v[144:145], %[a2] offset:0\n"
      "ds_read_b64_tr_b16 v[146:147], %[a2] offset:16384\n"
      "ds_read_b64_tr_b16 v[148:149], %[a3] offset:0\n"
      "ds_read_b64_tr_b16 v[150:151], %[a3] offset:16384\n"
      "ds_read_b64_tr_b16 v[152:153], %[a4] offset:0\n"
      "ds_read_b64_tr_b16 v[154:155], %[a4] offset:16384\n"
      "ds_read_b64_tr_b16 v[156:157], %[a5] offset:0\n"
      "ds_read_b64_tr_b16 v[158:159], %[a5] offset:16384\n"
      "ds_read_b64_tr_b16 v[160:161], %[a6] offset:0\n"
      "ds_read_b64_tr_b16 v[162:163], %[a6] offset:16384\n"
      "ds_read_b64_tr_b16 v[164:165], %[a7] offset:0\n"
      "ds_read_b64_tr_b16 v[166:167], %[a7] offset:16384\n"
      "170:\n"
      "s_mov_b32 %[n], %[nb]\n"
      "91:\n"
      "s_sub_u32 %[n], %[n], 1\n"
      "s_cbranch_scc1 92f\n"
      "ds_read_b32 v22, %[cb] offset:152\n"
      "s_waitcnt lgkmcnt(0)\n"
      "v_cmp_gt_u32 vcc, 4, v22\n"
      "s_cbranch_vccnz 91b\n"
      "92:\n"
      "s_mov_b32 %[n], %[nb]\n"
      "91:\n"
      "s_sub_u32 %[n], %[n], 1\n"
      "s_cbranch_scc1 92f\n"
      "ds_read_b32 v22, %[cb] offset:156\n"
      "ds_read_b128 v[32:35], %[pa] offset:6144\n"
      "ds_read_b128 v[36:39], %[pa] offset:7168\n"
      "s_waitcnt lgkmcnt(0)\n"
      "v_cmp_gt_u32 vcc, 8, v22\n"
      "s_cbranch_vccnz 91b\n"
      "92:\n"
      "v_cmp_lt_u32 vcc, 8, v22\n"
      "s_cbranch_vccnz 174b\n"
      "171:\n"
      "v_mfma_f32_16x16x32_bf16 v[40:43], v[32:35], v[136:139], v[40:43]\n"
      "v_mfma_f32_16x16x32_bf16 v[44:47], v[32:35], v[140:143], v[44:47]\n"
      "v_mfma_f32_16x16x32_bf16 v[48:51], v[32:35], v[144:147], v[48:51]\n"
      "v_mfma_f32_16x16x32_bf16 v[52:55], v[32:35], v[148:151], v[52:55]\n"
      "v_mfma_f32_16x16x32_bf16 v[56:59], v[32:35], v[152:155], v[56:59]\n"
      "v_mfma_f32_16x16x32_bf16 v[60:63], v[32:35], v[156:159], v[60:63]\n"
      "v_mfma_f32_16x16x32_bf16 v[64:67], v[32:35], v[160:163], v[64:67]\n"
      "v_mfma_f32_16x16x32_bf16 v[68:71], v[32:35], v[164:167], v[68:71]\n"
      "v_mfma_f32_16x16x32_bf16 v[72:75], v[36:39], v[136:139], v[72:75]\n"
      "v_mfma_f32_16x16x32_bf16 v[76:79], v[36:39], v[140:143], v[76:79]\n"
      "v_mfma_f32_16x16x32_bf16 v[80:83], v[36:39], v[144:147], v[80:83]\n"
      "v_mfma_f32_16x16x32_bf16 v[84:87], v[36:39], v[148:151], v[84:87]\n"
      "v_mfma_f32_16x16x32_bf16 v[88:91], v[36:39], v[152:155], v[88:91]\n"
      "v_mfma_f32_16x16x32_bf16 v[92:95], v[36:39], v[156:159], v[92:95]\n"
      "v_mfma_f32_16x16x32_bf16 v[96:99], v[36:39], v[160:163], v[96:99]\n"
      "v_mfma_f32_16x16x32_bf16 v[100:103], v[36:39], v[164:167], v[100:103]\n"
      : "={v[40:71]}"(o0), "={v[72:103]}"(o1), [n] "=&s"(n), [k] "=&s"(k), [m0] "=&s"(m0), [m1] "=&s"(m1), [ret] "=&s"(ret)
      : [a0] "{v9}"(a[0]), [a1] "{v10}"(a[1]), [a2] "{v11}"(a[2]), [a3] "{v12}"(a[3]), [a4] "{v13}"(a[4]), [a5] "{v14}"(a[5]), [a6] "{v15}"(a[6]),
        [a7] "{v16}"(a[7]), [pa] "{v17}"(pa), [cb] "{v18}"(cb), [one] "{v19}"(one), [fac] "s"(T_FAC), [nb] "s"(nb)
      : "vcc", "scc", "memory", "v20", "v21", "v22", "v23", "v24", "v25", "v26", "v27", "v28", "v29", "v30", "v31", "v32", "v33", "v34", "v35", "v36", "v37", "v38", "v39", "v104", "v105", "v106", "v107", "v108", "v109", "v110", "v111", "v112", "v113", "v114", "v115", "v116", "v117", "v118", "v119", "v120", "v121", "v122", "v123", "v124", "v125", "v126", "v127", "v128", "v129", "v130", "v131", "v132", "v133", "v134", "v135", "v136", "v137", "v138", "v139", "v140", "v141", "v142", "v143", "v144", "v145", "v146", "v147", "v148", "v149", "v150", "v151", "v152", "v153", "v154", "v155", "v156", "v157", "v158", "v159", "v160", "v161", "v162", "v163", "v164", "v165", "v166", "v167");
}
DI void ffn_part(const Params& p, int b, int s, int wave, unsigned t0, u64 tag);
DI void attn_wg(const Params& p, int b, int s, int wave, unsigned t0) {
  unsigned char* ws = p.ws;
  const rsrc_t rs = __builtin_amdgcn_make_buffer_rsrc(ws, 0, WS_BYTES, 0x00020000);
  const int w = wave & 3;
  const f32x4 z4 = {0.f, 0.f, 0.f, 0.f};

  if (wave < 2) { const int lane = fresh_lane(); *(LAS unsigned*)(T_CNT + wave * 256 + lane * 4) = wave == 0 && lane < 8 ? 8u : 0u; }
  bar();
  u64 tag = 0;
  if (wave != 8) tag = get_tag(ws);
  if (wave == 8) {
    const int lane = fresh_lane();
    unsigned vo[16];
#pragma unroll
    for (int r = 0; r < 16; ++r) vo[r] = 16 * (lane ^ fsw(r));
    const unsigned char* kv = (const unsigned char*)p.ckv + (size_t)(b * 4096 + s * 32) * 1024;
    nap<1>();
    wait_until(t0 + T_PREF);
    __builtin_amdgcn_s_setprio(PRIO_L);
    bool watch = true;
    STAMP(62); STAMPD(31);
#pragma unroll 1
    for (int t = 0; t < 8; ++t) {
      bool told = false;
      if (t >= 3) {
        const unsigned c = t == 5 ? C_QRD : C_FREE + 16 * (t < 5 ? t - 3 : t - 4), want = t == 5 ? 4 : 8;
        auto free_now = [&]() { return __builtin_amdgcn_readfirstlane(lpeek(c)) >= want; };
        bool ok = free_now();
        if (!ok) { VMCNT(24); ok = free_now(); }
        if (!ok) { VMCNT(16); ok = free_now(); }
        if (!ok) { VMCNT(8); ok = free_now(); }
        if (!ok) { VMCNT(0); lsig(C_LAND + 16 * t - 16); told = true; lwait<true>(c, want); }
      }
      const unsigned dst = slot_of(t);
#pragma unroll
      for (int i = 0; i < 8; ++i) {
        DMA4("nt", kv + i * 4096, dst + i * 4096, vo[4 * (i & 3)], vo[4 * (i & 3) + 1], vo[4 * (i & 3) + 2], vo[4 * (i & 3) + 3]);
        if (watch) {
          const unsigned hold = __builtin_amdgcn_readfirstlane(lpeek(C_HOLD));
          if (hold) { lwait<true>(C_HOLD, 2); watch = false; }
        }
      }
      if (!told) lsig(C_LAND + 16 * t - 16);
      kv += 16 * 32768;
    }
    STAMP(22);

    VMCNT(0);
    lsig(C_LAND + 16 * 7);
    STAMP(23);
    if (s >= 8 || b == 7) { DUMP(62); DUMP(31); DUMP(22); DUMP(23); }
    ROLE_END();
    return;
  }
  __builtin_amdgcn_s_setprio(2);
  if (wave == 9) {
    const int lane = fresh_lane();
    unsigned vo[16];
#pragma unroll
    for (int r = 0; r < 16; ++r) { vo[r] = (lane >> 5) * ASTR + (r & 3) * (4 * ASTR - 1024) + 16 * ((lane & 31) ^ fsw(r)); pin(vo[r]); }
    const unsigned char* qa = ws + WS_ARENA + 2 * ASTR + A_E + b * 512;
    wait_until(t0 + T_FLAG);
    wait_flags(rs, WS_FLAG1 + lane * 16, tag);
    STAMP(17); STAMPD(47);
    const unsigned tq = latest_time32<128>(ws + WS_HQ + (16 * b + s) * 512, t0, (unsigned)tag);
    STAMPV(47);
    wait_until(tq + T_QA - T_HOLD);
    lsig(C_HOLD);
    wait_until(tq + T_QA);
    STAMP(18);
#pragma unroll
    for (int i = 0; i < 8; ++i)
      DMA4("sc1", qa + i * (16 * ASTR), 3 * 32768 + i * 4096, vo[4 * (i & 3)], vo[4 * (i & 3) + 1], vo[4 * (i & 3) + 2], vo[4 * (i & 3) + 3]);
    lsig(C_HOLD);
    VMCNT(0);
    lsig(C_QST);
    STAMP(38);

    if (s >= 8 || b == 7) { lwait<true>(C_PRDY + 16 * 7, 8); nap<30>(); DUMP(17); DUMP(18); DUMP(38); DUMP(47); }
    ROLE_END();
    return;
  }
  if (wave >= 10) {
    __builtin_amdgcn_s_setprio(3);
    const int hh = wave - 10, lane = fresh_lane(), l15 = lane & 15, lq = lane >> 4;
#pragma unroll 1
    for (int real = DRY ? 0 : 1; real < 2; ++real) {
    const unsigned nb = real ? SPIN_L : 1;
    unsigned sa = T_SP + hh * 1024 + lane * 16, cb = real ? T_CNT : T_DRY, four = 4u;
    int mlo = real && lq == 0 ? arena(16 * b + s) + A_ML + hh * 256 + l15 * 16 : OOB;
    pin(sa); pin(cb); pin(four); pin(mlo);
    float ref = 0.f, l = 0.f;
    cwait<C_QST, 1, true>(cb, nb);
    static_for<0, 8>([&](auto tc) {
      constexpr int t = decltype(tc)::value;
      const unsigned nb_ = nb;
      if constexpr (t >= 4) cwait<C_FREE + 16 * (t == 4 ? 3 : t - 2), 8>(cb, nb);
      f32x4 p0, p1, p2, p3;
      unsigned c, n;
      asm volatile(SPIN_HEAD "ds_read_b32 %[c], %[cb] offset:%[o]\n\tds_read_b128 %[p0], %[sa] offset:%[s0]\n\tds_read_b128 %[p2], %[sa] offset:%[s2]\n\t"
                   "ds_read_b128 %[p1], %[sa] offset:%[s1]\n\tds_read_b128 %[p3], %[sa] offset:%[s3]\n\t" SPIN_TAIL2
                   : [c] "=&v"(c), [n] "=&s"(n), [p0] "=&v"(p0), [p1] "=&v"(p1), [p2] "=&v"(p2), [p3] "=&v"(p3)
                   : [cb] "v"(cb), [sa] "v"(sa), [o] "i"(C_SRDY + 16 * t - T_CNT), [w] "i"(4), [w2] "i"(t == 7 ? 1 : 4), [s0] "i"((t & 1) * 8192),
                     [s1] "i"((t & 1) * 8192 + 2048), [s2] "i"((t & 1) * 8192 + 4096), [s3] "i"((t & 1) * 8192 + 6144), [nb] "s"(nb_)
                   : "vcc", "scc", "memory");
      f32x4 a = (p0 - ref) + p2, c4 = (p1 - ref) + p3;
      const float m = fmaxf(fmaxf(fmaxf(a.x, a.y), fmaxf(a.z, a.w)), fmaxf(fmaxf(c4.x, c4.y), fmaxf(c4.z, c4.w)));
      unsigned sig = four;
      if constexpr (t == 0) {
        ref = xmax32(xmax16(m));
        a -= ref; c4 -= ref;
      } else if (__builtin_expect(__any(m > BUMP), 0)) {
        const float up = fmaxf(0.f, xmax32(xmax16(m)));
        const float fa = __builtin_amdgcn_exp2f(-up);
        *(LAS float*)(T_FAC + t * 128 + hh * 64 + l15 * 4) = fa;
        l *= fa;
        ref += up;
        a = (p0 - ref) + p2; c4 = (p1 - ref) + p3;
        sig = 4u + (1u << hh);
      }
      const v4u pf = {pk(__builtin_amdgcn_exp2f(a.x), __builtin_amdgcn_exp2f(a.y)), pk(__builtin_amdgcn_exp2f(a.z), __builtin_amdgcn_exp2f(a.w)),
                      pk(__builtin_amdgcn_exp2f(c4.x), __builtin_amdgcn_exp2f(c4.y)), pk(__builtin_amdgcn_exp2f(c4.z), __builtin_amdgcn_exp2f(c4.w))};
      *(LAS v4u*)((LAS char*)sa + (T_P - T_SP) + (t & 3) * 2048) = pf;
      csig<C_PRDY + 16 * t>(cb, sig);
#pragma unroll
      for (int e = 0; e < 4; ++e) {
        const unsigned u = pf[e];
        l = __builtin_amdgcn_fdot2_f32_bf16(__builtin_bit_cast(bf16x2, u), __builtin_bit_cast(bf16x2, 0x3F803F80u), l, false);
      }
    });
    stg(rs, mlo, v4u{__float_as_uint(ref), (unsigned)tag, __float_as_uint(xsum32(xsum16(l))), (unsigned)(tag >> 32)});
    }
    STAMP(24);
    if (hh == 0 && (s >= 8 || b == 7)) DUMP(24);
    ROLE_END();
    return;
  }
  if (wave < 4) {
    __builtin_amdgcn_s_setprio(PRIO_S);
    const int kh = wave & 1, ch = wave >> 1;
    STAMPD(19); STAMPD(20);
    int rqa = 0;
#pragma unroll 1
    for (int real = DRY ? 0 : 1; real < 2; ++real) {
    const unsigned nb = real ? SPIN_L : 1;
    const int lane = fresh_lane(), l15 = lane & 15, lq = lane >> 4;
    const int sg = (0x2130 >> (4 * lq)) & 3;
    unsigned ao[4];
#pragma unroll
    for (int k = 0; k < 4; ++k) ao[k] = l15 * 1024 + ch * 512 + 16 * ((4 * k + sg) ^ fsw(l15));
    unsigned sa = T_SP + wave * 2048 + lane * 16, cb = real ? T_CNT : T_DRY, one = 1u;
    unsigned al[4], ah[4];
#pragma unroll
    for (int k = 0; k < 4; ++k) { al[k] = ao[k] + kh * 16384; ah[k] = al[k] + 65536; pin(al[k]); pin(ah[k]); }
    int aro = real && wave == 0 && lane < 32 ? WS_ARR + (4 * lane + (b >> 1)) * 512 + (s & 3) * 128 + (b & 1) * 16 + (s >> 2) * 4 : OOB;
    pin(sa); pin(cb); pin(one); pin(aro);
    v4u af[2][8], qf[2][8];
    auto rd = [&](unsigned sl, int ks) { return lds128(sl < 65536 ? al[ks & 3] : ah[ks & 3], (sl & 32768) + (ks >> 2) * 256); };
    static_for<0, 2>([&](auto tc) {
      constexpr int t = decltype(tc)::value;
      cwait<C_LAND + 16 * t, 1, true>(cb, nb);
#pragma unroll
      for (int ks = 0; ks < 8; ++ks) af[t][ks] = rd(t * 32768, ks);
      csig<C_FREE + 16 * t>(cb, one);
    });
    unsigned qa[4];
#pragma unroll
    for (int k = 0; k < 4; ++k) { qa[k] = 3 * 32768 + ao[k]; pin(qa[k]); }
    cwait<C_QST, 1>(cb, nb);
    STAMPV(19);
    unsigned nx2 = cpeek<C_LAND + 32>(cb), nxt = cpeek<C_LAND + 48>(cb);
    f32x4 s0, s1;
#pragma unroll 1
    for (int sp = 0; sp < SPIN_G; ++sp) {
      rqa = sp + 1;
#pragma unroll
      for (int ks = 0; ks < 8; ++ks) {
#pragma unroll
        for (int hh = 0; hh < 2; ++hh) qf[hh][ks] = lds128(qa[ks & 3], hh * 16384 + (ks >> 2) * 256);
        __builtin_amdgcn_sched_barrier(0);
      }
      s0 = z4; s1 = z4;
#pragma unroll
      for (int ks = 0; ks < 8; ++ks) { s0 = mfma16(af[0][ks], qf[0][ks], s0); s1 = mfma16(af[0][ks], qf[1][ks], s1); }
      if (__builtin_expect(!__any(__builtin_isunordered(s0.x, s1.x)), 1) || !real || !(any_sent<8>(qf[0]) || any_sent<8>(qf[1]))) break;
#pragma unroll 1
      for (unsigned m = ((unsigned)__ballot(s0.x != s0.x) & 0xFFFFu) | ((unsigned)__ballot(s1.x != s1.x) << 16); m; m &= m - 1) {
        const int r = __builtin_ctz(m);
        asm volatile("s_mov_b32 m0, %2\n\ts_nop 0\n\tglobal_load_lds_dwordx4 %0, %1 sc1"
                     :: "v"((lane >> 5) * ASTR + 16 * ((lane & 31) ^ fsw(r))), "s"(ws + WS_ARENA + 2 * ASTR + A_E + b * 512 + r * (4 * ASTR)), "s"(3 * 32768 + r * 1024) : "memory");
      }
      VMCNT(0);
    }
    *(LAS f32x4*)sa = s0;
    *(LAS f32x4*)((LAS char*)sa + 1024) = s1;
    csig<C_SRDY>(cb, one);
    csig<C_QRD>(cb, one);
    STAMPV(20);
    auto refill = [&](v4u& a, auto slc, auto ksc) {
      constexpr unsigned sl = decltype(slc)::value;
      constexpr int ks = decltype(ksc)::value;
      rd_inplace<(sl & 32768) + (ks >> 2) * 256>(a, sl < 65536 ? al[ks & 3] : ah[ks & 3]);
    };
    static_for<0, 8>([&](auto k) { refill(af[0][decltype(k)::value], IntC<slot_c(2)>{}, k); });
    bool miss = __any(nx2 < 1);
    if (!miss) csig<C_FREE + 32>(cb, one);
    static_for<1, 8>([&](auto tc) {
      constexpr int t = decltype(tc)::value;
      const bool was = miss;
      if constexpr (t < 6) miss = __any(nxt < 1);
      unsigned use;
      peek2<C_PRDY + 16 * (t - 2) - T_CNT, C_LAND + 16 * (t < 5 ? t + 3 : 8) - T_CNT>(use, nxt, cb);
      static_for<0, 8>([&](auto k) {
        constexpr int ks = decltype(k)::value;
        score_pair<(t < 6 ? 9 : 9 - ks), ks == 0>(s0, s1, af[t & 1][ks], qf[0][ks], qf[1][ks]);
        if constexpr (t < 6) refill(af[t & 1][ks], IntC<slot_c(t + 2)>{}, k);
      });
      wait2<(t < 6 ? 8 : 0)>(use, nxt);
      if (__builtin_expect(__any(use < 8), 0)) cwait<C_PRDY + 16 * (t - 2), 8>(cb, nb);
      put_scores<(t & 1) * 8192>(sa, s0, s1);
      csig<C_SRDY + 16 * t>(cb, one);
      if constexpr (t == 7 && !ARR_EARLY) st32(rs, aro, now32() + (unsigned)tag);
      if constexpr (t < 4) { if (!miss) csig<C_FREE + 16 * (t + 2)>(cb, one); }
      if constexpr (t < 7) {
        if (was) {
          cwait<C_LAND + 16 * (t + 1), 1>(cb, nb);
          static_for<0, 8>([&](auto k) { refill(af[(t + 1) & 1][decltype(k)::value], IntC<slot_c(t + 1)>{}, k); });
          if constexpr (t < 5) csig<C_FREE + 16 * (t + 1)>(cb, one);
        }
      }
      if constexpr (t == 6 && ARR_EARLY) { if (wave == 0) st32(rs, aro, now32() + (unsigned)tag); }
    });
    asm volatile("s_waitcnt lgkmcnt(0)" ::: "memory");
    }
    STAMP(21);
    if (wave == 0 && (s >= 8 || b == 7)) { DUMP(19); DUMP(20); DUMP(21); DUMPC(50, rqa); }
  } else {
    __builtin_amdgcn_s_setprio(PRIO_PV);
#pragma unroll 1
    for (int real = DRY ? 0 : 1; real < 2; ++real) {
    const int lane = fresh_lane(), l15 = lane & 15, lq = lane >> 4;
    unsigned vl[8];
#pragma unroll
    for (int ct = 0; ct < 8; ++ct)
      vl[ct] = ((lane >> 2) * 1024 + w * 256 + 16 * ((2 * (ct & 1) + ((lane >> 1) & 1)) ^ fsw(lane >> 2)) + 8 * (lane & 1)) ^ ((ct >> 1) * 64);
    int po = real ? arena(16 * lq + w) + A_PART + (b * 16 + s) * 256 + l15 * 16 : OOB - 80 * ASTR;
    int cko = (lane & 2) || !real ? OOB : arena(16 * b + s) + A_CK + w * 512 + (2 * (lane & 8) + 4 * lq + ((lane >> 1) & 2) + (lane & 1)) * 16;
    pin(po); pin(cko);
    f32x32 acc[2];
    pv_loop(acc[0], acc[1], vl, T_P + lane * 16, real ? T_CNT : T_DRY, 1u, real ? SPIN_L : 1);
    unsigned ce[8], co[8];
#pragma unroll
    for (int ht = 0; ht < 2; ++ht)
#pragma unroll
      for (int r = 0; r < 4; ++r) {
        if (ht == 1 && r == 0) asm volatile("s_nop 1" : "+{v[72:103]}"(acc[1]) :: "memory");
        const v4u v = {pk(acc[ht][r], acc[ht][4 + r]), pk(acc[ht][8 + r], acc[ht][12 + r]), pk(acc[ht][16 + r], acc[ht][20 + r]),
                       pk(acc[ht][24 + r], acc[ht][28 + r])};
        stg(rs, po + (16 * ht + r) * 4 * ASTR, v);
        ce[4 * ht + r] = v.x ^ v.z; co[4 * ht + r] = v.y ^ v.w;
      }
    {
      unsigned e0, e1, o0, o1;
      xor_rows8(ce, e0, e1);
      xor_rows8(co, o0, o1);
      const bool odd = cko & 16;
      stg(rs, cko, v4u{odd ? e1 : e0, (unsigned)tag, odd ? o1 : o0, (unsigned)(tag >> 32)});
    }
    }
    STAMP(25);
  }
  if (b == 7 || s >= 8) { ROLE_END(); return; }
  ffn_part(p, b, s, wave, t0, tag);
  ROLE_END();
}

DI void ffn_part(const Params& p, int b, int s, int wave, unsigned t0, u64 tag) {
  const int lane = fresh_lane();
  unsigned char* ws = p.ws;
  const rsrc_t rs = __builtin_amdgcn_make_buffer_rsrc(ws, 0, WS_BYTES, 0x00020000);
  const int l15 = lane & 15, lq = lane >> 4, w = wave & 3;
  const f32x4 z4 = {0.f, 0.f, 0.f, 0.f};
  __builtin_amdgcn_s_setprio(2);
  const int fg = 8 * b + s, b8 = lane & 7, p4 = 4 * (lane & 3);
  const bool bvalid = l15 < 8;
  const int ch = l15 ^ ((lq << 2) | w), row0 = 4 * w + lq;
  bar();
  STAMP(52);
  nap<D_F>();
  v4u mm[4];
  const int mo = arena(32 + 4 * wave) + A_S + (4 * (l15 >> 3) + lq) * 128 + b8 * 16;
  auto load_mod = [&]() {
    FENCE();
#pragma unroll
    for (int i = 0; i < 4; ++i) mm[i] = ldg(rs, mo, ((i >> 1) * 32 + (i & 1)) * ASTR);
    FENCE();
  };
  load_mod();
  if (wave < 4) {
    dma_run<false>(p.w1, (row0 + (ch >> 2) * 128) * 3584 + fg * 64 + (ch & 3) * 16, W0 + w * 1024, 8, 16 * 3584, 4096);
  } else {
    const unsigned u = (32 * fg + row0) * 1024 + ch * 16;
    dma_run<true>(p.w2, u, W1 + w * 1024, 4, 256, 8192);
    dma_run<true>(p.w2, u + 16 * 1024, W1 + w * 1024 + 4096, 4, 256, 8192);
  }
#pragma unroll 1
  for (int sp = 0; sp < SPIN_G; ++sp) {
    if (!any_sent<4>(mm)) break;
    load_mod();
  }
  f32x4 sb[2], cb[2];
  sb[0] = up4(mm[0].xy) + up4(mm[1].xy); sb[1] = up4(mm[0].zw) + up4(mm[1].zw);
  cb[0] = up4(mm[2].xy) + up4(mm[3].xy) + 1.f; cb[1] = up4(mm[2].zw) + up4(mm[3].zw) + 1.f;
  pin(sb[0]); pin(sb[1]); pin(cb[0]); pin(cb[1]);
  if (wave == 7) wait_flags(rs, WS_FLAG2 + lane * 16, tag);
  VMCNT(0);
  bar();
  STAMP(28);
  v4u f1[2][2], f2[8];
#pragma unroll
  for (int tt = 0; tt < 2; ++tt) {
    const int c4 = 32 * (wave >> 1) + 2 * p4 + 4 * tt;
    const unsigned a0 = tr_addr(W0 + (wave & 1) * 16384, lane, c4, 0), a1 = tr_addr(W0 + (wave & 1) * 16384, lane, c4, 1);
    f1[0][tt] = tr_frag(a0, a1, 0); f1[1][tt] = tr_frag(a0, a1, 1);
    pin(f1[0][tt]); pin(f1[1][tt]);
  }
#pragma unroll
  for (int i = 0; i < 8; ++i) {
    const int c4 = 32 * (i >> 1) + 2 * p4 + 4 * (i & 1);
    f2[i] = tr_frag(tr_addr(W1 + w * 8192, lane, c4, 0), tr_addr(W1 + w * 8192, lane, c4, 1), 0);
    pin(f2[i]);
  }
  int xo = arena(wave) + A_X1 + (4 * (l15 >> 3) + lq) * 128 + b8 * 16;
  unsigned zw = L_A5 + wave * 1024 + lane * 16, zr = L_A5 + lane * 16;
  unsigned sw = lane < 8 ? L_A6 + lane * 80 + wave * 8 : L_JUNK, sr = L_A6 + b8 * 80;
  pin(xo); pin(zw); pin(zr); pin(sw); pin(sr);
  STAMP(55);
  STAMPD(40); STAMPD(15); STAMPD(36); STAMPD(37);
  int rx = 0;
  auto first = [&](v4u u) {
    const f32x4 d00 = mfma16(f1[0][0], u, z4), d10 = mfma16(f1[1][0], u, z4), d01 = mfma16(f1[0][1], u, z4), d11 = mfma16(f1[1][1], u, z4);
    f32x4 zz;
#pragma unroll
    for (int r = 0; r < 4; ++r) zz[r] = (bvalid ? d00[r] : d11[r]) + dppf<0x128>(bvalid ? d01[r] : d10[r]);
    return zz;
  };
  *(LAS f32x4*)((LAS char*)zw + 16384) = first(pk8(cb[0], cb[1]));
  *(LAS f32x4*)((LAS char*)zw + 24576) = first(pk8(sb[0], sb[1]));
  bar();
  f32x4 v1 = z4, v2 = z4;
  int yo = arena(fg) + A_YP + (16 * w + 4 * (l15 >> 3) + lq) * 128 + b8 * 16;
  if (wave >= 4) {
#pragma unroll
    for (int i = 0; i < 8; ++i) { v1 += u2f(lds128(zr, 16384 + i * 1024)); v2 += u2f(lds128(zr, 24576 + i * 1024)); }
  }
  pin(v1); pin(v2); pin(yo);
  STAMPV(40);
  if (wave == 0) wait_until(latest_time(ws + WS_HB + (16 * b + s) * 128, t0, (unsigned)tag) + T_C);
  bar();
  {
    v4u xv[1];
#pragma unroll 1
    for (int sp = 0; sp < SPIN_G; ++sp) { load_n<1>(rs, xo, 0, xv); rx = sp + 1; if (!any_sent<1>(xv)) break; }
    STAMPV(15);
    const f32x4 x0 = up4(xv[0].xy), x1 = up4(xv[0].zw);
    *(LAS f32x4*)zw = first(pk8(x0 * cb[0], x1 * cb[1]));
    const f32x4 s4 = x0 + x1, q4 = x0 * x0 + x1 * x1;
    float sx = (s4.x + s4.y) + (s4.z + s4.w), sq = (q4.x + q4.y) + (q4.z + q4.w);
    sx += dppf<0x128>(sx); sq += dppf<0x128>(sq);
    {
      auto y = __builtin_amdgcn_permlane16_swap(__float_as_uint(sx), __float_as_uint(sq), false, false);
      const float t = xsum32(__uint_as_float(y[0]) + __uint_as_float(y[1]));
      auto z = __builtin_amdgcn_permlane16_swap(__float_as_uint(t), __float_as_uint(t), false, false);
      *(LAS v2u*)sw = v2u{z[0], z[1]};
    }
    STAMPV(36);
    bar();
    if (wave >= 4) {
      STAMPV(37);
      f32x4 zp[8], st[4];
#pragma unroll
      for (int i = 0; i < 4; ++i) st[i] = u2f(lds128(sr, i * 16));
#pragma unroll
      for (int i = 0; i < 8; ++i) zp[i] = u2f(lds128(zr, i * 1024));
      __builtin_amdgcn_sched_barrier(0);
      const f32x4 ss = (st[0] + st[1]) + (st[2] + st[3]);
      const float mean = (ss.x + ss.z) * (1.f / 512), msq = (ss.y + ss.w) * (1.f / 512);
      const float rstd = __builtin_amdgcn_rsqf(fmaxf(msq - mean * mean, 0.f) + EPS);
      const f32x4 zs = ((zp[0] + zp[1]) + (zp[2] + zp[3])) + ((zp[4] + zp[5]) + (zp[6] + zp[7]));
      const f32x4 z = (zs - v1 * mean) * rstd + v2;
      const unsigned g0 = pk(z.x < 0.f ? 0.f : z.x, z.y < 0.f ? 0.f : z.y), g1 = pk(z.z < 0.f ? 0.f : z.z, z.w < 0.f ? 0.f : z.w);
      const unsigned o0 = (unsigned)__builtin_amdgcn_update_dpp(0, (int)g0, 0x128, 0xF, 0xF, true), o1 = (unsigned)__builtin_amdgcn_update_dpp(0, (int)g1, 0x128, 0xF, 0xF, true);
      const v4u bf = {bvalid ? g0 : o0, bvalid ? g1 : o1, bvalid ? o0 : g0, bvalid ? o1 : g1};
#pragma unroll
      for (int i = 0; i < 2; ++i) {
        const f32x4 a0 = mfma16(f2[4 * i], bf, z4), a1 = mfma16(f2[4 * i + 1], bf, z4), c0 = mfma16(f2[4 * i + 2], bf, z4), c1 = mfma16(f2[4 * i + 3], bf, z4);
        stg(rs, yo + i * 1024, pick(bvalid, pk8(a0, a1), pk8(c0, c1)));
      }
    }
  }
}

DI void first_requests() {
  u64 px, pc, pw;
  unsigned a, kp, wv, v0, v1, v2;
  asm volatile(
      "s_load_dwordx2 %[px], %[k], 0x0\n\ts_load_dwordx2 %[pc], %[k], 0x40\n\ts_load_dwordx2 %[pw], %[k], 0x48\n\t"
      "v_readfirstlane_b32 %[wv], %[t]\n\ts_and_b32 %[a], %[bx], 0x90\n\ts_cmpk_eq_u32 %[a], 0x90\n\ts_cbranch_scc0 3f\n\t"
      "s_bfe_u32 %[a], %[bx], 0x20005\n\ts_bfe_u32 %[kp], %[bx], 0x10003\n\ts_lshl1_add_u32 %[kp], %[a], %[kp]\n\t"
      "s_lshr_b32 %[wv], %[wv], 6\n\ts_cmp_gt_u32 %[wv], 3\n\ts_cbranch_scc1 2f\n\t"
      "v_bfe_u32 %[v0], %[t], 4, 2\n\tv_lshl_or_b32 %[v1], %[v0], 2, %[wv]\n\tv_xor_b32 %[v1], %[v1], %[t]\n\t"
      "v_mul_u32_u24 %[v0], 0x1800, %[v0]\n\tv_and_b32 %[v2], 7, %[v1]\n\tv_lshl_add_u32 %[v0], %[v2], 4, %[v0]\n\t"
      "v_bfe_u32 %[v2], %[v1], 3, 1\n\tv_lshl_add_u32 %[v0], %[v2], 10, %[v0]\n\t"
      "s_lshl4_add_u32 %[kp], %[kp], %[wv]\n\ts_mul_i32 %[kp], %[kp], 0x6000\n\ts_and_b32 %[a], %[bx], 7\n\ts_lshl_b32 %[a], %[a], 7\n\t"
      "s_add_u32 %[a], %[a], %[kp]\n\tv_add_u32 %[v0], %[a], %[v0]\n\ts_lshl_b32 %[wv], %[wv], 10\n\ts_add_u32 m0, %[wv], %[w2]\n\t"
      "v_add_u32 %[v1], 0x18000, %[v0]\n\tv_add_u32 %[v2], 0x30000, %[v0]\n\ts_waitcnt lgkmcnt(0)\n\t"
      "global_load_lds_dwordx4 %[v0], %[pw] nt\n\ts_add_u32 m0, m0, 0x1000\n\tv_add_u32 %[v0], 0x48000, %[v0]\n\t"
      "global_load_lds_dwordx4 %[v1], %[pw] nt\n\ts_add_u32 m0, m0, 0x1000\n\ts_nop 0\n\t"
      "global_load_lds_dwordx4 %[v2], %[pw] nt\n\ts_add_u32 m0, m0, 0x1000\n\ts_nop 0\n\t"
      "global_load_lds_dwordx4 %[v0], %[pw] nt\n\ts_branch 3f\n"
      "2:\n\tv_add_u32 %[v0], 0xffffff00, %[t]\n\tv_lshlrev_b32 %[v0], 4, %[v0]\n\tv_and_b32 %[v1], 0xfffffc70, %[v0]\n\t"
      "s_lshl_b32 %[kp], %[kp], 7\n\tv_or_b32 %[v1], %[kp], %[v1]\n\ts_lshl_b32 %[wv], %[wv], 11\n\ts_add_u32 m0, %[wv], %[raw]\n\t"
      "s_waitcnt lgkmcnt(0)\n\tglobal_load_lds_dwordx4 %[v1], %[pc]\n\ts_sub_u32 m0, m0, 0x400\n\ts_nop 0\n\t"
      "global_load_lds_dwordx4 %[v0], %[px]\n"
      "3:\n\ts_waitcnt lgkmcnt(0)"
      : [px] "=&s"(px), [pc] "=&s"(pc), [pw] "=&s"(pw), [a] "=&s"(a), [kp] "=&s"(kp), [wv] "=&s"(wv), [v0] "=&v"(v0), [v1] "=&v"(v1), [v2] "=&v"(v2)
      : [k] "s"(__builtin_amdgcn_kernarg_segment_ptr()), [bx] "s"(blockIdx.x), [t] "v"(threadIdx.x), [w2] "i"(W2), [raw] "i"(M_RAW + 1024 - 4 * 2048)
      : "scc", "memory");
}

__global__ __launch_bounds__(768) void adaln_mla_ffn_kernel(Params p) {
  STAMP(0);
  first_requests();
  const unsigned t0 = now32();
  const int g = blockIdx.x & 7, idx = blockIdx.x >> 3;
  const int wave = __builtin_amdgcn_readfirstlane(threadIdx.x >> 6), lane = threadIdx.x & 63;
  if (wave == 0 && idx < 16) {
    u64 pc;
    unsigned left;
    asm volatile("s_getpc_b64 %0\n99:\n\ts_mov_b32 %1, 98f-99b-4\n\t.subsection 2\n98:\n\t.subsection 0" : "=s"(pc), "=s"(left));
    asm volatile("s_mov_b32 m0, %2\n\ts_nop 0\n\tglobal_load_lds_dword %0, %1" :: "v"(min((idx + 16 * min(lane >> 1, 21)) * 128u + (lane & 1) * 64, left)), "s"(pc), "s"(L_JUNK) : "memory");
  }
  const int wi = idx & 15, l15 = lane & 15, lq = lane >> 4, b8 = lane & 7;
  const int kp = 2 * (wi >> 2) + (wi & 1);
  const unsigned vx = lane * 16;
  asm volatile("" :: "s"(p.x), "s"(p.ckv), "s"(p.wq), "s"(p.wuk), "s"(p.wuv), "s"(p.wo), "s"(p.w1), "s"(p.w2), "s"(p.c), "s"(p.wmod),
               "s"(p.out), "s"(p.ws));
  STAMP(39);
  STAMPD(2); STAMPD(3); STAMPD(32); STAMPD(1);
  const rsrc_t rs = __builtin_amdgcn_make_buffer_rsrc(p.ws, 0, WS_BYTES, 0x00020000);
  const int my = arena(16 * g + wi);
  if (idx >= 16 && wave == 3) {
#pragma unroll
    for (int n = 0; n < 5; ++n) stg(rs, my + vx + n * 1024, v4u{SENT, SENT, SENT, SENT});
  }
  if ((idx & 18) == 18) {
    const int r = wave - 4;
    if (wave >= 4) {
      unsigned raw = M_RAW + r * 2048 + vx, d = (lane >> 3) == g ? M_LNX + r * 256 + b8 * 32 : L_JUNK, sc = M_SC + r * 144 + lane * 2;
      unsigned rc = M_RAW + r * 2048 + 1024 + lane * 2;
      pin(raw); pin(d); pin(sc); pin(rc);
      VMCNT(0);
      const v4u xr = lds128(raw);
      const float cv = bflo(*(LAS u16*)rc);
      *(LAS u16*)sc = (u16)pk(cv * __builtin_amdgcn_rcpf(1.f + __expf(-cv)), 0.f);
      bar();
      const f32x4 v0 = up4(xr.xy), v2 = up4(xr.zw), sv4 = v0 + v2, qv4 = v0 * v0 + v2 * v2;
      const float mean = wave_sum((sv4.x + sv4.y) + (sv4.z + sv4.w)) * (1.f / 512);
      const float msq = wave_sum((qv4.x + qv4.y) + (qv4.z + qv4.w)) * (1.f / 512);
      const float rstd = __builtin_amdgcn_rsqf(fmaxf(msq - mean * mean, 0.f) + EPS);
      *(LAS f32x4*)d = (v0 - mean) * rstd;
      *(LAS f32x4*)((LAS char*)d + 16) = (v2 - mean) * rstd;
      STAMPV(32);
      bar();
    } else {
      const int p4 = 4 * (lane & 3);
      unsigned s0 = tr_addr(W2, lane, 16 * wave + p4, 0), s1 = tr_addr(W2, lane, 16 * wave + p4, 1), c0 = s0 ^ 128, c1 = s1 ^ 128;
      unsigned ba = M_SC + b8 * 144 + lq * 16, la = M_LNX + b8 * 256 + (16 * wave + 4 * lq) * 4;
      int o = l15 < 8 ? my + wave * 256 + (lq >> 1) * 128 + l15 * 16 + (lq & 1) * 8 : OOB;
      int hh = wave == 3 && lane < 32 ? WS_HH + (4 * lane + (g >> 2)) * 512 + (g & 3) * 128 + kp * 4 : OOB;
      const float one = kp == 0 ? 1.f : 0.f;
      *(LAS unsigned*)L_CNT = 0u;
      f32x4 sa = {0.f, 0.f, 0.f, 0.f}, ca = {one, one, one, one};
      pin(s0); pin(s1); pin(c0); pin(c1); pin(ba); pin(la); pin(o); pin(hh); pin(ca);
      u64 tag = 0;
      int fo = wave == 3 ? (lane < 32 ? WS_EFL + (4 * lane + (g >> 2)) * 256 + (g & 3) * 64 + kp * 8 : lane == 32 ? WS_FLAG1 + (16 * g + wi) * 8 : OOB) : OOB;
      pin(fo);
      if (wave == 3) { tag = get_tag(p.ws); VMCNT(5); } else { VMCNT(0); }
      STAMPV(32);
      bar();
      const unsigned tn = now32();
      const v4u b0 = lds128(ba), f0 = tr_frag(s0, s1, 0), g0 = tr_frag(c0, c1, 0), b1 = lds128(ba, 64), f1 = tr_frag(s0, s1, 1), g1 = tr_frag(c0, c1, 1);
      __builtin_amdgcn_sched_barrier(0);
      st32(rs, hh, tn + (unsigned)tag);
      STAMPV(2);
      sa = mfma16(f1, b1, mfma16(f0, b0, sa));
      ca = mfma16(g1, b1, mfma16(g0, b0, ca));
      if (wave == 3) {
        VMCNT(1);
        __builtin_amdgcn_raw_buffer_store_b64(v2u{(unsigned)tag, (unsigned)(tag >> 32)}, rs, fo, 0, SC1);
        STAMPV(1);
      }
      bar();
      sa += u2f(lds128(la)) * ca;
      __builtin_amdgcn_raw_buffer_store_b64(v2u{pk(sa.x, sa.y), pk(sa.z, sa.w)}, rs, o, 0, SC1);
      STAMPV(3);
    }
    bar();
  } else if (idx >= 16 && wave == 3) {
    const u64 tag = get_tag(p.ws);
    VMCNT(0);
    __builtin_amdgcn_raw_buffer_store_b64(v2u{(unsigned)tag, (unsigned)(tag >> 32)}, rs,
                                          lane < 2 ? WS_EFL + (16 * g + (wi | 2) - (wi & 1) + lane) * 256 + (wi & 1) * 8 : lane == 2 ? WS_FLAG1 + (16 * g + wi) * 8 : OOB, 0, SC1);
  } else if (wave >= 8) {
    const int w = wave & 3, hq = idx >= 16 ? wi >> 2 : wi >> 2, row = idx >= 16 ? 256 * (wi & 1) : 256 * ((wi >> 1) & 1) + 128 + 64 * (wi & 1);
    const unsigned vq = (row + 4 * w + lq) * 8192 + (4 * g + hq) * 256 + (l15 ^ ((lq << 2) | w)) * 16;
    wait_until(t0 + T_W);
    dma_run<true>(p.wq, vq, (idx >= 16 ? W0 : T_CNT + 1024) + w * 1024, idx >= 16 ? 16 : 4, 16 * 8192, idx >= 16 ? 4096 : 0);
    if (idx < 16) dma_run<true>(p.wuk, PF_UK == 4 ? vq : vq + (128 * (wi & 3) - row) * 8192, T_CNT + 1024 + w * 1024, PF_UK, 16 * 8192, 0);
  }
  FENCE();
  if (idx < 16) attn_wg(p, g, idx, wave, t0);
  else { weight_wg(p, g, wi, wave, fresh_lane(), t0); ROLE_END(); }
}

extern "C" void kernel_launch(void* const* d_in, const int* in_sizes, int n_in, void* d_out, int out_size, void* d_ws, size_t ws_size,
                              hipStream_t stream) {
  Params p;
  p.x = (const u16*)d_in[0]; p.ckv = (const u16*)d_in[1]; p.wq = (const u16*)d_in[2]; p.wuk = (const u16*)d_in[3];
  p.wuv = (const u16*)d_in[4]; p.wo = (const u16*)d_in[5]; p.w1 = (const u16*)d_in[7]; p.w2 = (const u16*)d_in[8];
  p.c = (const u16*)d_in[9]; p.wmod = (const u16*)d_in[10];
  p.out = (u16*)d_out; p.ws = (unsigned char*)d_ws + WS_BASE;
  (void)hipFuncSetAttribute((const void*)adaln_mla_ffn_kernel, hipFuncAttributeMaxDynamicSharedMemorySize, LDS_BYTES);
  adaln_mla_ffn_kernel<<<256, 768, LDS_BYTES, stream>>>(p);
}
```

```cpp
#include <hip/hip_runtime.h>
#include <hip/hip_bf16.h>
#pragma clang diagnostic ignored "-Wint-to-pointer-cast"
#pragma clang diagnostic ignored "-Wsometimes-uninitialized"

typedef unsigned short u16;
typedef unsigned long long u64;
typedef unsigned v4u __attribute__((ext_vector_type(4)));
typedef unsigned v2u __attribute__((ext_vector_type(2)));
typedef float f32x2 __attribute__((ext_vector_type(2)));
typedef float f32x4 __attribute__((ext_vector_type(4)));
typedef float f32x16 __attribute__((ext_vector_type(16)));
typedef short s16x4 __attribute__((ext_vector_type(4)));
typedef short bf16x8 __attribute__((ext_vector_type(8)));
typedef __bf16 bf16x2 __attribute__((ext_vector_type(2)));
#define GAS __attribute__((address_space(1)))
#define LAS __attribute__((address_space(3)))
#define DI __device__ __forceinline__
#define RLX_AGENT __ATOMIC_RELAXED, __HIP_MEMORY_SCOPE_AGENT
typedef __amdgpu_buffer_rsrc_t rsrc_t;

#define ATT_DELAY 0
#define T_H 62
#define D_UK 8
#define T_QP 125
#define T_QA 118
#define T_B 80
#define T_C 158
#define T_D 286
#define ARR_EARLY 0
#define T_ML 62
#define T_PART 118
#define T_B6 80
#define D_F 0
#define D_ML 2
#define FLAG_AT 1
#define D_PREF 16
#define PRIO_S 2
#define PRIO_PV 1
#define PF_NT false
#define D_W 0
#define T_PREF 0
#define T_W 30
#define PF_EVEN 24
#define T_FILL 0
#define T_FLAG 260
#define WS_BASE 0
#define FILL_AUX 16
#define WWIN 7
#define DRY 0
#define T_EF 220
#define T_LQ 190
#define T_LQ2 100
#define PF_UK 8
#define T_UK 190
#define T_FP 150
#define T_RP 560
#define T_HOLD 45
#define ILV 8
#define PRIO_L 3
#define FP_AT 0
#define T_WO 380
#define FP_ALL 1
#define FP_W2 1
#define PART_AUX 16
#define LWS "37"
#define T_L 150
#define FILL_EARLY 1
#define D_FILL 0
#define PREF 1
#define WO_NT 1

constexpr int WS_FLAG1 = 4096, WS_FLAG2 = 8192;
constexpr int WS_EFL = 16384  , WS_HP = 81920, WS_HB = 98304  ,
              WS_HH = 131072  , WS_HQ = 196608  , WS_ARR = 262144  ;
constexpr int WS_ARENA = 327680, ASTR = 58624;
constexpr int A_S = 0, A_E = 1024, A_ML = 5120, A_X1 = 6144, A_OUTP = 7168, A_YP = 15360, A_PART = 23552, A_CK = 56320;
constexpr int NROT = 16, WS_BYTES = WS_ARENA + NROT * 128 * ASTR;
constexpr unsigned SENT = 0xFFFFFFFFu;
constexpr int OOB = 0x7FFFFF00;
constexpr int SC1 = 16;
constexpr int SPIN_H = 1 << 10;
constexpr int SPIN_G = 1 << 19;
constexpr int SPIN_L = 1 << 23;
constexpr int LDS_BYTES = 163840;
constexpr float EPS = 1e-5f;
constexpr float QSCALE = 0.08838834764831845f * 1.4426950408889634f;
constexpr float BUMP = 40.f;

#define STAMP(k) do {} while (0)
#define STAMPV(k) do {} while (0)
#define STAMPD(k) do {} while (0)
#define STV(k) 0
#define DUMP(k) do {} while (0)
#define DUMPC(k, c) do {} while (0)

DI int fresh_lane() {
  int l;
  asm volatile("v_mbcnt_lo_u32_b32 %0, -1, 0\n\tv_mbcnt_hi_u32_b32 %0, -1, %0" : "=v"(l));
  return l;
}
template <class T> DI void pin(T& v) { asm volatile("" : "+v"(v)); }
DI float bflo(unsigned u) { return __uint_as_float(u << 16); }
DI float bfhi(unsigned u) { return __uint_as_float(u & 0xFFFF0000u); }
DI unsigned pk(float a, float b) { f32x2 v = {a, b}; return __builtin_bit_cast(unsigned, __builtin_convertvector(v, bf16x2)); }
DI f32x4 mfma16(v4u a, v4u b, f32x4 c) {
  return __builtin_amdgcn_mfma_f32_16x16x32_bf16(__builtin_bit_cast(bf16x8, a), __builtin_bit_cast(bf16x8, b), c, 0, 0, 0);
}
DI f32x16 mfma32(v4u a, v4u b, f32x16 c) {
  return __builtin_amdgcn_mfma_f32_32x32x16_bf16(__builtin_bit_cast(bf16x8, a), __builtin_bit_cast(bf16x8, b), c, 0, 0, 0);
}
DI v4u ldg(rsrc_t rs, int off, int so = 0) { return __builtin_amdgcn_raw_buffer_load_b128(rs, off, so, SC1); }
DI void stg(rsrc_t rs, int off, v4u v) { __builtin_amdgcn_raw_buffer_store_b128(v, rs, off, 0, SC1); }
DI unsigned umax4(v4u v) { return max(max(v.x, v.y), max(v.z, v.w)); }
DI v4u f2u(f32x4 v) { return __builtin_bit_cast(v4u, v); }
DI f32x4 u2f(v4u v) { return __builtin_bit_cast(f32x4, v); }
DI bool nan4(f32x4 v) { return __builtin_isunordered(v.x, v.y) | __builtin_isunordered(v.z, v.w); }
DI v4u lds128(unsigned a, int o = 0) { return *(LAS v4u*)((LAS char*)a + o); }
DI v2u ldstr(unsigned a, int o = 0) { return __builtin_bit_cast(v2u, __builtin_amdgcn_ds_read_tr16_b64_v4i16((LAS s16x4*)((LAS char*)a + o))); }
DI void bar() {
  __builtin_amdgcn_sched_barrier(0);
  asm volatile("s_waitcnt lgkmcnt(0)\n\ts_barrier" ::: "memory");
  __builtin_amdgcn_sched_barrier(0);
}
#define VMCNT_(n) asm volatile("s_waitcnt vmcnt(" #n ")" ::: "memory")
#define VMCNT(n) VMCNT_(n)
#define FENCE() asm volatile("" ::: "memory")
#define ROLE_END() __builtin_amdgcn_s_waitcnt(0x0F70)
template <bool NT> DI void dma16(const void* base, unsigned voff, unsigned dst) {
  if (NT) asm volatile("s_mov_b32 m0, %2\n\ts_nop 0\n\tglobal_load_lds_dwordx4 %0, %1 nt" :: "v"(voff), "s"(base), "s"(dst) : "memory");
  else asm volatile("s_mov_b32 m0, %2\n\ts_nop 0\n\tglobal_load_lds_dwordx4 %0, %1" :: "v"(voff), "s"(base), "s"(dst) : "memory");
}
template <bool NT> DI void dma_run(const void* base, unsigned voff, unsigned dst, int n, int gstr, int lstr) {
  const unsigned char* b = (const unsigned char*)base;
#pragma unroll 1
  for (int i = 0; i < n; ++i) { VMCNT(WWIN); dma16<NT>(b, voff, dst); b += gstr; dst += lstr; }
}
template <int N> DI void nap() { if (N > 0) __builtin_amdgcn_s_sleep(N); }
DI void lsig(unsigned a, unsigned v = 1u) { asm volatile("s_mov_b64 exec, 1\n\tds_add_u32 %0, %1\n\ts_mov_b64 exec, -1" :: "v"(a), "v"(v) : "memory"); }
DI unsigned lpeek(unsigned a) { return *(volatile LAS unsigned*)a; }
template <bool NAP = false> DI void lwait(unsigned a, unsigned want) {
  unsigned v, n = SPIN_L;
  if (NAP)
    asm volatile("1:\n\tds_read_b32 %0, %2\n\ts_waitcnt lgkmcnt(0)\n\tv_cmp_le_u32 vcc, %3, %0\n\ts_cbranch_vccnz 2f\n\t"
                 "s_sleep 1\n\ts_sub_u32 %1, %1, 1\n\ts_cbranch_scc0 1b\n2:" : "=&v"(v), "+s"(n) : "v"(a), "s"(want) : "vcc", "scc", "memory");
  else
    asm volatile("1:\n\tds_read_b32 %0, %2\n\ts_waitcnt lgkmcnt(0)\n\tv_cmp_le_u32 vcc, %3, %0\n\ts_cbranch_vccnz 2f\n\t"
                 "s_sub_u32 %1, %1, 1\n\ts_cbranch_scc0 1b\n2:" : "=&v"(v), "+s"(n) : "v"(a), "s"(want) : "vcc", "scc", "memory");
}
template <int CTRL> DI float dppf(float v) {
  return __builtin_bit_cast(float, __builtin_amdgcn_update_dpp(0, __builtin_bit_cast(int, v), CTRL, 0xF, 0xF, true));
}
DI float row16_max(float v) {
  asm volatile("s_nop 1\n\tv_max_f32_dpp %0, %0, %0 row_ror:8 row_mask:0xf bank_mask:0xf\n\ts_nop 1\n\tv_max_f32_dpp %0, %0, %0 row_ror:4 row_mask:0xf bank_mask:0xf\n\t"
               "s_nop 1\n\tv_max_f32_dpp %0, %0, %0 row_ror:2 row_mask:0xf bank_mask:0xf\n\ts_nop 1\n\tv_max_f32_dpp %0, %0, %0 row_ror:1 row_mask:0xf bank_mask:0xf" : "+v"(v));
  return v;
}
DI float row16_sum(float v) { v += dppf<0x128>(v); v += dppf<0x124>(v); v += dppf<0x122>(v); return v + dppf<0x121>(v); }
DI float xsum16(float v) { auto r = __builtin_amdgcn_permlane16_swap(__float_as_uint(v), __float_as_uint(v), false, false); return __uint_as_float(r[0]) + __uint_as_float(r[1]); }
DI float xsum32(float v) { auto r = __builtin_amdgcn_permlane32_swap(__float_as_uint(v), __float_as_uint(v), false, false); return __uint_as_float(r[0]) + __uint_as_float(r[1]); }
DI float xmax16(float v) { auto r = __builtin_amdgcn_permlane16_swap(__float_as_uint(v), __float_as_uint(v), false, false); return fmaxf(__uint_as_float(r[0]), __uint_as_float(r[1])); }
DI float xmax32(float v) { auto r = __builtin_amdgcn_permlane32_swap(__float_as_uint(v), __float_as_uint(v), false, false); return fmaxf(__uint_as_float(r[0]), __uint_as_float(r[1])); }
DI float wave_sum(float v) { return xsum32(xsum16(row16_sum(v))); }

#define XS8(d, a, b) "v_xor_b32_dpp " d ", " a ", " a " row_ror:8 row_mask:0xf bank_mask:0x3\n\tv_xor_b32_dpp " d ", " b ", " b " row_ror:8 row_mask:0xf bank_mask:0xc\n\t"
#define XS4(d, a, b) "v_xor_b32_dpp " d ", " a ", " a " row_half_mirror row_mask:0xf bank_mask:0x5\n\tv_xor_b32_dpp " d ", " b ", " b " row_half_mirror row_mask:0xf bank_mask:0xa\n\t"
#define XQ(d, p) "v_xor_b32_dpp " d ", " d ", " d " quad_perm:" p " row_mask:0xf bank_mask:0xf\n\t"
DI void xor_rows8(const unsigned (&v)[8], unsigned& r0, unsigned& r1) {
  unsigned t0, t1, t2, t3;
  asm volatile("s_nop 1\n\t" XS8("%2", "%6", "%10") XS8("%4", "%8", "%12") XS8("%3", "%7", "%11") XS8("%5", "%9", "%13")
               XS4("%0", "%2", "%4") XS4("%1", "%3", "%5") XQ("%0", "[2,3,0,1]") "s_nop 0\n\t" XQ("%1", "[2,3,0,1]") XQ("%0", "[1,0,3,2]")
               "s_nop 0\n\t" XQ("%1", "[1,0,3,2]")
               : "=&v"(r0), "=&v"(r1), "=&v"(t0), "=&v"(t1), "=&v"(t2), "=&v"(t3)
               : "v"(v[0]), "v"(v[1]), "v"(v[2]), "v"(v[3]), "v"(v[4]), "v"(v[5]), "v"(v[6]), "v"(v[7]));
}
DI unsigned xor_rows2(unsigned a, unsigned b) {
  unsigned r;
  asm volatile("s_nop 1\n\t" XS8("%0", "%1", "%2") "s_nop 1\n\tv_xor_b32_dpp %0, %0, %0 row_half_mirror row_mask:0xf bank_mask:0xf\n\ts_nop 1\n\t"
               XQ("%0", "[2,3,0,1]") "s_nop 1\n\t" XQ("%0", "[1,0,3,2]") "s_nop 1" : "=&v"(r) : "v"(a), "v"(b));
  return r;
}
template <int N> DI bool any_sent(const v4u (&r)[N]) {
  unsigned m = 0;
#pragma unroll
  for (int i = 0; i < N; ++i) m = max(m, umax4(r[i]));
  return __any(m == SENT);
}
template <int N, int AUX = SC1> DI void load_n(rsrc_t rs, int off0, int str, v4u (&r)[N]) {
  FENCE();
#pragma unroll
  for (int i = 0; i < N; ++i) r[i] = __builtin_amdgcn_raw_buffer_load_b128(rs, off0, i * str, AUX);
  __builtin_amdgcn_sched_barrier(0);
}
template <int N, int NAP = 0> DI void gather(rsrc_t rs, int off0, int str, v4u (&r)[N]) {
#pragma unroll 1
  for (int sp = 0; sp < SPIN_G; ++sp) {
    load_n<N>(rs, off0, str, r);
    if (!any_sent<N>(r)) break;
    nap<NAP>();
  }
}
DI u64 get_tag(const unsigned char* ws) {
  u64 e;
  asm volatile("s_load_dwordx2 %0, %1, 0x0\n\ts_waitcnt lgkmcnt(0)" : "=s"(e) : "s"(ws) : "memory");
  return e * 6364136223846793005ull + 1442695040888963407ull;
}
DI bool flags_up(v4u f, u64 tag) {
  const unsigned lo = (unsigned)tag, hi = (unsigned)(tag >> 32);
  return __all(f.x == lo && f.y == hi && f.z == lo && f.w == hi);
}
DI void wait_flags(rsrc_t rs, int off, u64 tag) {
#pragma unroll 1
  for (int sp = 0; sp < SPIN_G; ++sp) {
    FENCE();
    if (flags_up(ldg(rs, off), tag)) break;
    __builtin_amdgcn_s_sleep(8);
  }
  FENCE();
}
typedef unsigned s8u __attribute__((ext_vector_type(8)));
DI s8u sload8(const unsigned char* a) {
  s8u v;
  asm volatile("s_load_dwordx8 %0, %1, 0x0 glc\n\ts_waitcnt lgkmcnt(0)" : "=s"(v) : "s"(a) : "memory");
  return v;
}
DI unsigned max8(s8u v) { return max(max(max(v[0], v[1]), max(v[2], v[3])), max(max(v[4], v[5]), max(v[6], v[7]))); }
DI unsigned now32() { return (unsigned)__builtin_amdgcn_s_memrealtime(); }
constexpr unsigned HWIN = 1u << 12;
DI unsigned latest_time(const unsigned char* a, unsigned t0, unsigned key) {
  unsigned m = 0;
#pragma unroll 1
  for (int sp = 0; sp < SPIN_H; ++sp) {
    m = max8(sload8(a) - (key + t0 - HWIN));
    if (m < 2 * HWIN) return m + t0 - HWIN;
  }
  return t0;
}
template <int STR> DI unsigned latest_time32(const unsigned char* a, unsigned t0, unsigned key) {
  const unsigned kb = key + t0 - HWIN;
#pragma unroll 1
  for (int sp = 0; sp < SPIN_H; ++sp) {
    s8u u, v, x, y;
    asm volatile("s_load_dwordx8 %0, %4, 0x0 glc\n\ts_load_dwordx8 %1, %4, %5 glc\n\ts_load_dwordx8 %2, %4, %6 glc\n\t"
                 "s_load_dwordx8 %3, %4, %7 glc\n\ts_waitcnt lgkmcnt(0)"
                 : "=&s"(u), "=&s"(v), "=&s"(x), "=&s"(y) : "s"(a), "i"(STR), "i"(2 * STR), "i"(3 * STR) : "memory");
    const unsigned m = max(max(max8(u - kb), max8(v - kb)), max(max8(x - kb), max8(y - kb)));
    if (m < 2 * HWIN) return m + t0 - HWIN;
  }
  return t0;
}
DI void wait_until(unsigned t) {
#pragma unroll 1
  for (int sp = 0; sp < SPIN_H; ++sp) if ((int)(now32() - t) >= 0) break;
}
DI int arena(int wid) { return WS_ARENA + wid * ASTR; }
DI int rarena(int wid, u64 tag) { return WS_ARENA + ((int)(tag & (NROT - 1)) * 128 + wid) * ASTR; }

DI unsigned tr_addr(unsigned region, int lane, int col4, int t) {
  const int g = lane >> 4, q = (lane & 15) >> 2;
  return region + (8 * g + 4 * t + q) * 256 + 16 * ((col4 >> 3) ^ ((q << 2) | ((2 * g + t) & 3))) + 8 * ((col4 >> 2) & 1);
}
DI v4u tr_frag(unsigned a0, unsigned a1, int ks) {
  const v2u lo = ldstr(a0, ks * 8192), hi = ldstr(a1, ks * 8192);
  return v4u{lo.x, lo.y, hi.x, hi.y};
}
DI void panel_frags(unsigned region, int lane, int col_a, int col_b, v4u (&f)[2][4]) {
  const unsigned a0 = tr_addr(region, lane, col_a, 0), a1 = tr_addr(region, lane, col_a, 1);
  const unsigned b0 = tr_addr(region, lane, col_b, 0), b1 = tr_addr(region, lane, col_b, 1);
#pragma unroll
  for (int ks = 0; ks < 4; ++ks) { f[0][ks] = tr_frag(a0, a1, ks); f[1][ks] = tr_frag(b0, b1, ks); }
}
DI void gemv2(const v4u (&f)[2][4], unsigned act, f32x4& d0, f32x4& d1) {
  v4u bf[4];
#pragma unroll
  for (int ks = 0; ks < 4; ++ks) bf[ks] = lds128(act, ks * 64);
  __builtin_amdgcn_sched_barrier(0);
#pragma unroll
  for (int ks = 0; ks < 4; ++ks) { d0 = mfma16(f[0][ks], bf[ks], d0); d1 = mfma16(f[1][ks], bf[ks], d1); }
}
DI f32x4 up4(v2u v) { return f32x4{bflo(v.x), bfhi(v.x), bflo(v.y), bfhi(v.y)}; }
DI v4u pk8(f32x4 a, f32x4 b) { return v4u{pk(a.x, a.y), pk(a.z, a.w), pk(b.x, b.y), pk(b.z, b.w)}; }
DI v4u silu8(v4u cv) {
  v4u o;
#pragma unroll
  for (int e = 0; e < 4; ++e) {
    const float a = bflo(cv[e]), c = bfhi(cv[e]);
    o[e] = pk(a * __builtin_amdgcn_rcpf(1.f + __expf(-a)), c * __builtin_amdgcn_rcpf(1.f + __expf(-c)));
  }
  return o;
}

struct Params {
  const u16 *x, *ckv, *wq, *wuk, *wuv, *wo, *w1, *w2, *c, *wmod;
  u16* out;
  unsigned char* ws;
};

constexpr unsigned W0 = 0, W1 = 32768, W2 = 65536, L_SC = 98304  , L_A1 = L_SC + 4224  ,
                   L_A3 = L_A1 + 4224  , L_A4 = L_A3 + 2176, L_CNT = L_A4 + 2176,
                   M_RAW = 114688  , M_SC = M_RAW + 16384  , M_LNX = M_SC + 1152  ;
constexpr unsigned T_SP = 131072, T_P = T_SP + 16384, T_FAC = T_P + 8192, T_CNT = T_FAC + 1024,
                   L_A5 = 98304  , L_A6 = L_A5 + 8320  ;
constexpr unsigned C_LAND = T_CNT + 32, C_FREE = C_LAND + 4, C_SRDY = C_LAND + 8, C_PRDY = C_LAND + 12,
                   C_QST = T_CNT + 192, C_QRD = T_CNT + 196, C_HOLD = T_CNT + 200;
constexpr unsigned L_JUNK = LDS_BYTES - 256;

template <int N> DI f32x4 reduce_rows(rsrc_t rs, int off0, int offl, v4u sel, int& rounds) {
  f32x4 a;
#pragma unroll 1
  for (int sp = 0; sp < SPIN_G; ++sp) {
    v4u d[N];
    FENCE();
#pragma unroll
    for (int i = 0; i < N; ++i) d[i] = ldg(rs, i == N - 1 ? offl : off0, i * 16 * ASTR);
    __builtin_amdgcn_sched_barrier(0);
    f32x4 b = {0.f, 0.f, 0.f, 0.f};
    a = b;
#pragma unroll
    for (int i = 0; i < N; i += 2) { a = mfma16(sel, d[i], a); b = mfma16(sel, d[i + 1], b); }
    a += b;
    rounds = sp + 1;
    if (!__any(a.x != a.x) || !any_sent<N>(d)) break;
  }
#pragma unroll
  for (int r = 0; r < 4; ++r) { a[r] += dppf<0x128>(a[r]); a[r] += dppf<0x124>(a[r]); }
  return a;
}
DI v4u pick(bool lo, v4u a, v4u b) { return v4u{lo ? a.x : b.x, lo ? a.y : b.y, lo ? a.z : b.z, lo ? a.w : b.w}; }
DI void st32(rsrc_t rs, int off, unsigned v) { __builtin_amdgcn_raw_buffer_store_b32(v, rs, off, 0, SC1); }

DI void weight_wg(const Params& p, int g, int wi, int wave, int lane, unsigned t0) {
  unsigned char* ws = p.ws;
  const rsrc_t rs = __builtin_amdgcn_make_buffer_rsrc(ws, 0, WS_BYTES, 0x00020000);
  const int h = 4 * g + (wi >> 2), j = wi & 3, wid = 16 * g + wi, my = arena(wid);
  const int l15 = lane & 15, lq = lane >> 4, b8 = lane & 7;
  const bool bvalid = l15 < 8, isq = j < 2;
  const int w = wave & 3;
  const int ch = l15 ^ ((lq << 2) | w);
  const unsigned dst0 = w * 1024;
  const unsigned vuv = (128 * j + 16 * ((4 * w + lq) & 7) + (w >> 1)) * 8192 + h * 256 + ch * 16;
  const f32x4 z4 = {0.f, 0.f, 0.f, 0.f};

  if (wave >= 8) {
    if (!isq) wait_until(t0 + T_UK);
    if (!isq) dma_run<true>(p.wuk, (256 * (j - 2) + 4 * w + lq) * 8192 + h * 256 + ch * 16, W0 + dst0, 16, 16 * 8192, 4096);
    const unsigned key = (unsigned)get_tag(ws);
    int hp = isq && lane < 8 ? WS_HP + (4 * h + 2 + (lane & 1)) * 128 + j * 4 + (lane >> 1) * 8 : OOB;
    pin(hp);
    VMCNT(0);
    STAMP(44);
    if (!isq) lsig(L_CNT);
    bar();
    const unsigned tb1 = now32();
    nap<3>();
    if (w == 0) st32(rs, hp, tb1 + key);
    bar();
    v4u wo[4][8];
    const GAS unsigned char* wb = (const GAS unsigned char*)p.wo + (h * 128 + 8 * lq) * 1024 + w * 256 + (4 * (l15 & 3) + (l15 >> 2)) * 16;
    bar();
    if (FP_AT == 0 && g < 7 && (FP_ALL || !isq)) {
      const int e = 2 * (wi >> 2) + (j & 1);
      wait_until(tb1 + (isq ? T_FP + T_QP : T_FP));
      if (!isq) dma_run<false>(p.w1, (64 * e + 16 * w + (lane >> 5)) * 3584 + g * 512 + (lane & 31) * 16, W1 + dst0, 8, 2 * 3584, 0);
      if (FP_ALL == isq && FP_W2) dma_run<false>(p.w2, (256 * g + 32 * e + 8 * w) * 1024 + lane * 16, W1 + dst0, 8, 1024, 0);
    }
    wait_until(tb1 + (isq ? T_WO + T_QP : T_WO));
#pragma unroll
    for (int ks = 0; ks < 4; ++ks) {
      const GAS unsigned char* wk = wb + ((ks + j) & 3) * 32768;
#pragma unroll
      for (int r = 0; r < 8; ++r) wo[ks][r] = ks == 0 ? __builtin_nontemporal_load((const GAS v4u*)(wk + r * 1024)) : *(const GAS v4u*)(wk + r * 1024);
      if (ks == 0) VMCNT(0);
    }
    if (FP_AT == 2 && g < 7 && (FP_ALL || !isq)) {
      const int e = 2 * (wi >> 2) + (j & 1);
      wait_until(tb1 + (isq ? T_FP + T_QP : T_FP));
      if (!isq) dma_run<false>(p.w1, (64 * e + 16 * w + (lane >> 5)) * 3584 + g * 512 + (lane & 31) * 16, W1 + dst0, 8, 2 * 3584, 0);
      if (FP_ALL == isq && FP_W2) dma_run<false>(p.w2, (256 * g + 32 * e + 8 * w) * 1024 + lane * 16, W1 + dst0, 8, 1024, 0);
    }

    __builtin_amdgcn_sched_barrier(0);
#pragma unroll
    for (int ks = 0; ks < 4; ++ks) {
      v4u o[8];
#pragma unroll
      for (int dd = 0; dd < 4; ++dd)
#pragma unroll
        for (int rp = 0; rp < 4; ++rp) {
          o[2 * dd][rp] = __builtin_amdgcn_perm(wo[ks][2 * rp + 1][dd], wo[ks][2 * rp][dd], 0x05040100u);
          o[2 * dd + 1][rp] = __builtin_amdgcn_perm(wo[ks][2 * rp + 1][dd], wo[ks][2 * rp][dd], 0x07060302u);
        }
#pragma unroll
      for (int t = 0; t < 8; ++t) { wo[ks][t] = o[t]; pin(wo[ks][t]); }
    }
    STAMP(10);
    unsigned ba[4];
#pragma unroll
    for (int ks = 0; ks < 4; ++ks) { ba[ks] = L_A4 + b8 * 272 + ((ks + j) & 3) * 64 + lq * 16; pin(ba[ks]); }
    int oo = my + A_OUTP + (16 * w + 4 * (l15 >> 3) + lq) * 128 + b8 * 16;
    pin(oo);
    __builtin_amdgcn_s_setprio(2);
    bar();
    bar();
    v4u bf[4];
#pragma unroll
    for (int ks = 0; ks < 4; ++ks) bf[ks] = lds128(ba[ks]);
    v4u r0, r1;
#pragma unroll
    for (int tp = 0; tp < 4; ++tp) {
      f32x4 e = z4, o = z4;
#pragma unroll
      for (int ks = 0; ks < 4; ++ks) { e = mfma16(wo[ks][2 * tp], bf[ks], e); o = mfma16(wo[ks][2 * tp + 1], bf[ks], o); }
      r0[tp] = bvalid ? pk(e[0], o[0]) : pk(e[1], o[1]);
      r1[tp] = bvalid ? pk(e[2], o[2]) : pk(e[3], o[3]);
    }
    stg(rs, oo, r0);
    stg(rs, oo + 1024, r1);
    STAMP(14);
    if (wave == 8) { DUMP(10); DUMP(14); if (isq) DUMP(44); else { const unsigned stv60 = STV(44); DUMP(60); } }
    ROLE_END();
  } else if (wave >= 4) {
    __builtin_amdgcn_s_setprio(2);
    const int t = w * 64 + lane;
    const u64 tag = get_tag(ws);
    *(LAS unsigned*)(L_CNT + 4) = 0u;
    v4u msk;
    {
      const unsigned half = (lane & 1) ? 0xFFFF0000u : 0x0000FFFFu;
      const int dw = b8 >> 1;
      msk = v4u{dw == 0 ? half : 0u, dw == 1 ? half : 0u, dw == 2 ? half : 0u, dw == 3 ? half : 0u};
    }
    v4u sela = msk & (bvalid ? 0x3F803F80u : 0u), selb = msk & (bvalid ? 0u : 0x3F803F80u);
    int go = isq ? arena(64 * j + 16 * w + 4 * (lq >> 1) + 2 + (lq & 1)) + A_S + l15 * 16 : arena(4 * h) + A_E + t * 16;
    unsigned gd = isq ? L_A1 + b8 * 528 + (64 * w + 16 * (lq >> 1) + 8 * (l15 >> 3) + 4 * (lq & 1)) * 2
                      : L_A1 + b8 * 528 + (t >> 3) * 8;
    pin(msk); pin(sela); pin(selb); pin(go); pin(gd);
    STAMPD(34); STAMPD(46);
    if (w == 2) {
      wait_until(t0 + T_EF);
      wait_flags(rs, WS_EFL + wid * 256 + (isq ? l15 * 16 : 0), tag);
      STAMPV(46);
    }
    if (w == 3) {
      unsigned tr;
      if (isq) {
        tr = latest_time32<128>(ws + WS_HH + wid * 512, t0, (unsigned)tag) + T_H;
      } else {
        tr = latest_time(ws + WS_HP + wid * 128, t0, (unsigned)tag) + T_QP;
        lwait(L_CNT, 4);
        const unsigned tn = now32() + 8;
        if ((int)(tn - tr) > 0) tr = tn;
        st32(rs, WS_HQ + (2 * lane + j - 2) * 512 + (h & 3) * 128 + g * 4, tr + (unsigned)tag);
      }
      STAMPV(34);
      wait_until(tr);
    }
    bar();
    int rh = 0;
    if (isq) {
      f32x4 h0, h1;
#pragma unroll 1
      for (int sp = 0; sp < SPIN_G; ++sp) {
        v4u r[8];
        FENCE();
#pragma unroll
        for (int i = 0; i < 8; ++i) r[i] = ldg(rs, go + (i & 3) * 256, (i >> 2) * 8 * ASTR);
        __builtin_amdgcn_sched_barrier(0);
        h0 = mfma16(selb, r[5], mfma16(sela, r[4], mfma16(selb, r[1], mfma16(sela, r[0], z4))));
        h1 = mfma16(selb, r[7], mfma16(sela, r[6], mfma16(selb, r[3], mfma16(sela, r[2], z4))));
        rh = sp + 1;
        if (!__any(__builtin_isunordered(h0.x, h1.x)) || !any_sent<8>(r)) break;
      }
      *(LAS v2u*)gd = v2u{pk(h0.x, h0.y), pk(h0.z, h0.w)};
      *(LAS v2u*)((LAS char*)gd + 64) = v2u{pk(h1.x, h1.y), pk(h1.z, h1.w)};
    } else {
      f32x4 q;
#pragma unroll 1
      for (int sp = 0; sp < SPIN_G; ++sp) {
        v4u r[2];
        load_n<2>(rs, go, ASTR, r);
        q = u2f(r[0]) + u2f(r[1]);
        rh = sp + 1;
        if (!__any(nan4(q)) || !any_sent<2>(r)) break;
      }
      *(LAS v2u*)gd = v2u{pk(q.x, q.y), pk(q.z, q.w)};
    }
    STAMP(5);
    bar();
    bar();
    if (isq)
      *(LAS v4u*)(L_SC + (2 * w + (lane >> 5)) * 528 + (lane & 31) * 16) =
          silu8(*(const GAS v4u*)(p.c + (2 * w + (lane >> 5)) * 512 + j * 256 + (lane & 31) * 8));
    lsig(L_CNT + 4);
    if (w == 3) {
      nap<D_FILL>();
#pragma unroll 1
      for (int n = wid < 8 ? 6 : 7; n < (wid < 56 ? 23 : 15); ++n)
        stg(rs, my + lane * 16 + n * 1024, v4u{SENT, SENT, SENT, SENT});
      VMCNT(0);
      if (lane == 0) __hip_atomic_store((GAS u64*)(ws + WS_FLAG2 + wid * 8), tag, RLX_AGENT);
    }
    const int el = (4 * (wi & 1) + (lane & 3)) * 512 + 8 * (wid >> 1) + 4 * (lq & 1), pc = wi * 64 + (lane & 3) * 16;
    int gmo = w == 0 ? arena(4 * g) + A_S + pc + (lq & 1) * 8 : OOB - 100 * ASTR;
    pin(gmo);
    int mo = rarena(32 * w + (lane & 31), tag) + A_ML + h * 16;
    int po = rarena(wid, tag) + A_PART + w * 8192 + lane * 16;
    unsigned o3 = L_A3 + (2 * w + (lane >> 5)) * 272 + l15 * 16 + (lq & 1) * 8;
    int hb = lane < 16 ? WS_HB + (16 * lane + ((wi - 5 * g) & 15)) * 128 + g * 4 : OOB;
    int co = lane < 32 ? mo + (A_CK - A_ML) + j * 512 : OOB;
    int bp = 4 * lq + (bvalid ? 0 : 128);
    unsigned tlo = lane < 32 ? (unsigned)tag : 0u, thi = lane < 32 ? (unsigned)(tag >> 32) : 0u;
    pin(mo); pin(po); pin(o3); pin(hb); pin(co); pin(bp); pin(tlo); pin(thi);
    const unsigned ta = latest_time32<128>(ws + WS_ARR + (4 * h + w) * 512, t0, (unsigned)tag);
    STAMP(11);
    *(LAS unsigned*)(L_CNT + 16 + w * 4) = ta;
    wait_until(ta + T_ML);
    FENCE();
    v4u ml = ldg(rs, mo);
    wait_until(ta + T_PART);
    v4u d[8], ck;
    load_n<8, PART_AUX>(rs, po, 1024, d);
    ck = ldg(rs, co);
    __builtin_amdgcn_sched_barrier(0);
    if (__builtin_expect(__any(((ml.y ^ (unsigned)tag) | (ml.w ^ (unsigned)(tag >> 32))) != 0), 0)) {
#pragma unroll 1
      for (int sp = 0; sp < SPIN_G; ++sp) {
        FENCE();
        ml = ldg(rs, mo);
        if (__all(ml.y == (unsigned)tag && ml.w == (unsigned)(tag >> 32))) break;
      }
    }
    v4u sel[8];
    {
      const float r = __uint_as_float(ml.x), e = __builtin_amdgcn_exp2f(r - row16_max(r));
      const float wgt = e * __builtin_amdgcn_rcpf(row16_sum(e * __uint_as_float(ml.z)));
      const unsigned hi = pk(wgt, wgt);
      const float rest = wgt - bflo(hi);
      const int v = lane < 32 ? hi : pk(rest, rest);
      unsigned wv[8];
#pragma unroll
      for (int i = 0; i < 8; ++i) wv[i] = __builtin_amdgcn_ds_bpermute(bp + 16 * i, v);
      __builtin_amdgcn_sched_barrier(0);
#pragma unroll
      for (int i = 0; i < 8; ++i) { sel[i] = msk & wv[i]; pin(sel[i]); }
    }
    f32x4 oa, ob;
    int rp = 0;
#pragma unroll 1
    for (int sp = 0; sp < SPIN_G; ++sp) {
      oa = z4; ob = z4;
      unsigned xe = 0, xo = 0;
#pragma unroll
      for (int i = 0; i < 8; ++i) {
        if (i < 4) oa = mfma16(sel[i], d[i], oa); else ob = mfma16(sel[i], d[i], ob);
        xe = __builtin_amdgcn_bitop3_b32(xe, d[i].x, d[i].z, 0x96);
        xo = __builtin_amdgcn_bitop3_b32(xo, d[i].y, d[i].w, 0x96);
        __builtin_amdgcn_sched_barrier(0);
      }
      rp = sp + 1;
      unsigned x = xor_rows2(xe ^ ck.x, xo ^ ck.z);
      auto y = __builtin_amdgcn_permlane16_swap(x, x, false, false);
      x = y[0] ^ y[1];
      y = __builtin_amdgcn_permlane32_swap(x, x, false, false);
      if (!__any(((y[0] ^ y[1]) | (ck.y ^ tlo) | (ck.w ^ thi)) != 0)) break;
      load_n<8, PART_AUX>(rs, po, 1024, d);
      ck = ldg(rs, co);
    }
    ROLE_END();
    STAMP(12);
    {
      f32x4 s;
#pragma unroll
      for (int r = 0; r < 4; ++r) {
        auto x = __builtin_amdgcn_permlane32_swap(__float_as_uint(oa[r]), __float_as_uint(ob[r]), false, false);
        s[r] = __uint_as_float(x[0]) + __uint_as_float(x[1]);
      }
      *(LAS v2u*)o3 = v2u{pk(s.x, s.y), pk(s.z, s.w)};
    }
    bar();
    v2u gg[4], xb;
    v4u f2;
    if (w == 0) {
#pragma unroll
      for (int i = 0; i < 4; ++i) gg[i] = __builtin_amdgcn_raw_buffer_load_b64(rs, gmo, ((i >> 1) * 96 + (i & 1)) * ASTR, SC1);
      f2 = ldg(rs, WS_FLAG2 + lane * 16);
      xb = *(const GAS v2u*)(p.x + el);
    }
    unsigned tb = 0;
    if (w < 2) {
      const v4u tv = lds128(L_CNT + 16) - t0;
      tb = __builtin_amdgcn_readfirstlane(umax4(tv)) + t0 + T_PART + T_B6;
      const unsigned tn = now32() - 6;
      if ((int)(tn - tb) > 0) tb = tn;
    }
    if (w == 1) st32(rs, hb, tb + (unsigned)tag);
    bar();
    if (w == 0) {
#pragma unroll 1
      for (int sp = 0; sp < SPIN_G; ++sp) {
        unsigned m = 0;
#pragma unroll
        for (int i = 0; i < 4; ++i) m = max(m, max(gg[i].x, gg[i].y));
        if (!__any(m == SENT)) break;
        FENCE();
#pragma unroll
        for (int i = 0; i < 4; ++i) gg[i] = __builtin_amdgcn_raw_buffer_load_b64(rs, gmo, ((i >> 1) * 96 + (i & 1)) * ASTR, SC1);
      }
      if (!flags_up(f2, tag)) wait_flags(rs, WS_FLAG2 + lane * 16, tag);
      f32x4 x0 = up4(xb), ga = up4(gg[0]) + up4(gg[1]), gb = up4(gg[2]) + up4(gg[3]);
      pin(x0); pin(ga); pin(gb);
      STAMP(26);
      const bool act = lane < 32 && l15 < 4;
      v4u one = sela;
      int oo = arena(lane >> 2) + A_OUTP + g * 1024 + pc;
      int yo = oo + (A_YP - A_OUTP);
      int yl = lane < 32 ? yo : OOB - 48 * ASTR;
      int xo = act ? arena(g) + A_X1 + pc + lq * 8 : OOB;
      const rsrc_t ro = __builtin_amdgcn_make_buffer_rsrc(p.out, 0, 8192, 0x00020000);
      int fo = act ? el * 2 : OOB;
      pin(one); pin(oo); pin(yo); pin(yl); pin(xo); pin(fo);
      wait_until(tb + T_B);
      int r1 = 0, r2 = 0;
      const f32x4 x1 = x0 + ga * reduce_rows<8>(rs, oo, oo, one, r1);
      __builtin_amdgcn_raw_buffer_store_b64(v2u{pk(x1.x, x1.y), pk(x1.z, x1.w)}, rs, xo, 0, SC1);
      STAMP(27);
      wait_until(latest_time(ws + WS_HB + (128 + wid) * 128, t0, (unsigned)tag) + T_D);
      const f32x4 r = x1 + gb * reduce_rows<4>(rs, yo, yl, one, r2);
      STAMP(29);
      __builtin_amdgcn_raw_buffer_store_b64(v2u{pk(r.x, r.y), pk(r.z, r.w)}, ro, fo, 0, 0);
      if (wid == 0 && lane == 0) __hip_atomic_store((GAS u64*)ws, tag, RLX_AGENT);
      STAMP(30);
      DUMP(26); DUMP(27); DUMP(29); DUMP(30); DUMPC(42, r1); DUMPC(43, r2);
    }
    ROLE_END();
  } else {
    __builtin_amdgcn_s_setprio(2);
    const int p4 = 4 * (lane & 3);
    unsigned act = b8 * 272 + lq * 16;
    unsigned s0 = tr_addr(W2, lane, 16 * w + p4, 0), s1 = tr_addr(W2, lane, 16 * w + p4, 1);
    unsigned ba = L_SC + b8 * 528 + lq * 16;
    v4u f[2][4], f2[2][4];
    unsigned a1 = L_A1 + b8 * 528 + lq * 16;
    int o = my + A_E + (isq ? w * 1024 + (l15 >> 3) * 512 + lq * 128 + b8 * 16 : b8 * 512 + w * 128 + (l15 >> 3) * 64 + lq * 16);
    pin(o); pin(a1); pin(act);
    bar();
    const unsigned tb1 = now32();
    STAMP(4);
    if (isq) {
      panel_frags(W0, lane, 32 * w + p4, 32 * w + 16 + p4, f);
      panel_frags(W1, lane, 32 * w + p4, 32 * w + 16 + p4, f2);
      bar();
      f32x4 q0 = z4, q1 = z4;
      v4u bf[8];
#pragma unroll
      for (int ks = 0; ks < 8; ++ks) bf[ks] = lds128(a1, ks * 64);
      __builtin_amdgcn_sched_barrier(0);
#pragma unroll
      for (int ks = 0; ks < 4; ++ks) { q0 = mfma16(f[0][ks], bf[ks], q0); q1 = mfma16(f[1][ks], bf[ks], q1); }
#pragma unroll
      for (int ks = 0; ks < 4; ++ks) { q0 = mfma16(f2[0][ks], bf[4 + ks], q0); q1 = mfma16(f2[1][ks], bf[4 + ks], q1); }
      stg(rs, o, pick(bvalid, f2u(q0), f2u(q1)));
    } else {
#pragma unroll
      for (int tt = 0; tt < 2; ++tt) {
        const unsigned ka = W0 + (64 * w + 8 * (l15 >> 2) + 4 * tt + (l15 & 3)) * 256 + 16 * (lq ^ (((l15 & 3) << 2) | ((2 * (l15 >> 2) + tt) & 3)));
#pragma unroll
        for (int ks = 0; ks < 4; ++ks) { f[tt][ks] = lds128(ka ^ (ks * 64)); f2[tt][ks] = lds128(ka ^ (ks * 64), 8192); }
      }
      bar();
      f32x4 a0 = z4, a2 = z4, c0 = z4, c2 = z4;
      v4u bf[4];
#pragma unroll
      for (int ks = 0; ks < 4; ++ks) bf[ks] = lds128(a1, ks * 64);
      __builtin_amdgcn_sched_barrier(0);
#pragma unroll
      for (int ks = 0; ks < 4; ++ks) {
        a0 = mfma16(f[0][ks], bf[ks], a0); c0 = mfma16(f2[0][ks], bf[ks], c0);
        a2 = mfma16(f[1][ks], bf[ks], a2); c2 = mfma16(f2[1][ks], bf[ks], c2);
      }
      stg(rs, o, pk8(u2f(pick(bvalid, f2u(a0), f2u(c0))) * QSCALE, u2f(pick(bvalid, f2u(a2), f2u(c2))) * QSCALE));
    }
    STAMP(6);
    bar();
    wait_until(tb1 + (isq ? T_L + T_QP : T_L));
    dma_run<true>(p.wuv, vuv, W0 + dst0, 8, 2 * 8192, 4096);
    const bool m2 = isq;
    if (m2) dma_run<true>(p.wmod, (256 * j + 4 * w + lq + (ch >> 3) * 128) * 6144 + 2048 + h * 128 + (ch & 7) * 16, W2 + dst0, 8, 16 * 6144, 4096);
    VMCNT(0);
    lsig(L_CNT + 4);
    if (FP_AT == 1 && g < 7 && (FP_ALL || !isq)) {
      const int e = 2 * (wi >> 2) + (j & 1);
      wait_until(tb1 + (isq ? T_FP + T_QP : T_FP));
      if (!isq) dma_run<false>(p.w1, (64 * e + 16 * w + (lane >> 5)) * 3584 + g * 512 + (lane & 31) * 16, W1 + dst0, 8, 2 * 3584, 0);
      if (FP_ALL == isq && FP_W2) dma_run<false>(p.w2, (256 * g + 32 * e + 8 * w) * 1024 + lane * 16, W1 + dst0, 8, 1024, 0);
    }
    lwait<true>(L_CNT + 4, 8);
    STAMP(9);
    if (m2) {
      f32x4 m0 = z4, m1 = z4;
#pragma unroll 1
      for (int ks = 0; ks < 4; ++ks) {
        m0 = mfma16(tr_frag(s0 + ks * 8192, s1 + ks * 8192, 0), lds128(ba + ks * 64), m0);
        m1 = mfma16(tr_frag((s0 ^ 128) + ks * 8192, (s1 ^ 128) + ks * 8192, 0), lds128(ba + ks * 64, 256), m1);
      }
      m0 += m1;
      __builtin_amdgcn_raw_buffer_store_b64(v2u{pk(m0.x, m0.y), pk(m0.z, m0.w)}, rs, bvalid ? my + A_S + (2 * w + (lq >> 1)) * 128 + l15 * 16 + (lq & 1) * 8 : OOB, 0, SC1);
    }
    panel_frags(W0, lane, 32 * w + 2 * p4, 32 * w + 2 * p4 + 4, f);
    unsigned o4 = bvalid ? L_A4 + l15 * 272 + w * 64 + lq * 16 : L_JUNK;
    pin(o4);
    bar();
    STAMP(35);
    f32x4 c0 = z4, c1 = z4;
    gemv2(f, L_A3 + act, c0, c1);
    *(LAS v4u*)o4 = pk8(c0, c1);
    bar();
    STAMP(13);
  }
}

template <int OFF> DI void rd_inplace(v4u& a, unsigned ad) { asm volatile("ds_read_b128 %0, %1 offset:%2" : "+v"(a) : "v"(ad), "i"(OFF) : "memory"); }
template <int O0, int O1> DI void peek2(unsigned& a, unsigned& b, unsigned cb) {
  asm volatile("ds_read_b32 %0, %2 offset:%3\n\tds_read_b32 %1, %2 offset:%4" : "=&v"(a), "=&v"(b) : "v"(cb), "i"(O0), "i"(O1) : "memory");
}
template <int W> DI void wait2(unsigned& a, unsigned& b) { asm volatile("s_waitcnt lgkmcnt(%2)" : "+v"(a), "+v"(b) : "i"(W)); }
template <int W, bool FIRST> DI void score_pair(f32x4& s0, f32x4& s1, v4u a, v4u q0, v4u q1) {
  if constexpr (FIRST)
    asm volatile("s_waitcnt lgkmcnt(%5)\n\tv_mfma_f32_16x16x32_bf16 %0, %2, %3, 0\n\tv_mfma_f32_16x16x32_bf16 %1, %2, %4, 0"
                 : "=&v"(s0), "=&v"(s1) : "v"(a), "v"(q0), "v"(q1), "i"(W));
  else
    asm volatile("s_waitcnt lgkmcnt(%5)\n\tv_mfma_f32_16x16x32_bf16 %0, %2, %3, %0\n\tv_mfma_f32_16x16x32_bf16 %1, %2, %4, %1"
                 : "+v"(s0), "+v"(s1) : "v"(a), "v"(q0), "v"(q1), "i"(W));
}
template <int OFF> DI void put_scores(unsigned sa, f32x4 s0, f32x4 s1) {
  asm volatile("s_nop 4\n\tds_write_b128 %0, %1 offset:%3\n\tds_write_b128 %0, %2 offset:%4" :: "v"(sa), "v"(s0), "v"(s1), "i"(OFF), "i"(OFF + 1024) : "memory");
}
template <int V> struct IntC { static constexpr int value = V; };
template <int I, int N, class F> DI void static_for(F&& f) { if constexpr (I < N) { f(IntC<I>{}); static_for<I + 1, N>(f); } }
constexpr unsigned slot_c(int t) { return ((0x02310210u >> ((4 * t) & 31)) & 3) * 32768; }
template <unsigned A> DI void csig(unsigned cb, unsigned v) {
  asm volatile("s_mov_b64 exec, 1\n\tds_add_u32 %0, %1 offset:%2\n\ts_mov_b64 exec, -1" :: "v"(cb), "v"(v), "i"(A - T_CNT) : "memory");
}
template <unsigned A> DI unsigned cpeek(unsigned cb) { return *(volatile LAS unsigned*)((LAS char*)cb + (A - T_CNT)); }
#define SPIN_HEAD "s_mov_b32 %[n], %[nb]\n1:\n\ts_sub_u32 %[n], %[n], 1\n\ts_cbranch_scc1 2f\n\t"
#define SPIN_TAIL "s_waitcnt lgkmcnt(0)\n\tv_cmp_gt_u32 vcc, %[w], %[c]\n\ts_cbranch_vccnz 1b\n2:"
#define SPIN_TAIL2 "s_waitcnt lgkmcnt(0)\n\tv_cmp_gt_u32 vcc, %[w], %[c]\n\ts_cbranch_vccz 2f\n" \
                   "3:\n\ts_sub_u32 %[n], %[n], 1\n\ts_cbranch_scc1 2f\n\tds_read_b32 %[c], %[cb] offset:%[o]\n\ts_waitcnt lgkmcnt(0)\n\t" \
                   "v_cmp_gt_u32 vcc, %[w2], %[c]\n\ts_cbranch_vccnz 3b\n\ts_branch 1b\n2:"
template <unsigned A, unsigned W, bool NAP = false> DI void cwait(unsigned cb, unsigned nb) {
  unsigned c, n;
  if (NAP)
    asm volatile(SPIN_HEAD "s_sleep 1\n\tds_read_b32 %[c], %[cb] offset:%[o]\n\t" SPIN_TAIL
                 : [c] "=&v"(c), [n] "=&s"(n) : [cb] "v"(cb), [o] "i"(A - T_CNT), [w] "i"(W), [nb] "s"(nb) : "vcc", "scc", "memory");
  else
    asm volatile(SPIN_HEAD "ds_read_b32 %[c], %[cb] offset:%[o]\n\t" SPIN_TAIL
                 : [c] "=&v"(c), [n] "=&s"(n) : [cb] "v"(cb), [o] "i"(A - T_CNT), [w] "i"(W), [nb] "s"(nb) : "vcc", "scc", "memory");
}
constexpr unsigned T_DRY = T_CNT + 256;
DI constexpr unsigned fsw(int row) { return ((row & 3) << 2) | ((row >> 1) & 2) | ((row >> 3) & 1); }
DI unsigned slot_of(int t) { return ((0x02310210u >> ((4 * t) & 31)) & 3) * 32768; }
#define DMA4(AUX, base, dst, v0, v1, v2, v3) \
  asm volatile("s_mov_b32 m0, %5\n\ts_waitcnt vmcnt(" LWS ")\n\tglobal_load_lds_dwordx4 %0, %4 " AUX "\n\t" \
               "s_waitcnt vmcnt(" LWS ")\n\tglobal_load_lds_dwordx4 %1, %4 offset:1024 " AUX "\n\t" \
               "s_waitcnt vmcnt(" LWS ")\n\tglobal_load_lds_dwordx4 %2, %4 offset:2048 " AUX "\n\t" \
               "s_waitcnt vmcnt(" LWS ")\n\tglobal_load_lds_dwordx4 %3, %4 offset:3072 " AUX \
               :: "v"(v0), "v"(v1), "v"(v2), "v"(v3), "s"(base), "s"(dst) : "memory")

typedef float f32x32 __attribute__((ext_vector_type(32)));
DI void pv_loop(f32x32& o0, f32x32& o1, const unsigned (&a)[8], unsigned pa, unsigned cb, unsigned one, unsigned nb) {
  unsigned n, k;
  u64 m0, m1, ret;
  asm volatile(
      "s_branch 300f\n"
      "200:\n"
      "v_readfirstlane_b32 %[n], v20\n"
      "v_lshrrev_b32 v32, 4, %[pa]\n"
      "v_and_b32 v32, 48, v32\n"
      "v_add_u32 v32, %[k], v32\n"
      "v_add_u32 v32, %[fac], v32\n"
      "ds_read_b128 v[36:39], v32 offset:64\n"
      "ds_read_b128 v[32:35], v32\n"
      "s_waitcnt lgkmcnt(0)\n"
      "s_bitcmp1_b32 %[n], 0\n"
      "s_cbranch_scc0 93f\n"
      "v_pk_mul_f32 v[40:41], v[40:41], v[32:33]\n"
      "v_pk_mul_f32 v[42:43], v[42:43], v[34:35]\n"
      "v_pk_mul_f32 v[44:45], v[44:45], v[32:33]\n"
      "v_pk_mul_f32 v[46:47], v[46:47], v[34:35]\n"
      "v_pk_mul_f32 v[48:49], v[48:49], v[32:33]\n"
      "v_pk_mul_f32 v[50:51], v[50:51], v[34:35]\n"
      "v_pk_mul_f32 v[52:53], v[52:53], v[32:33]\n"
      "v_pk_mul_f32 v[54:55], v[54:55], v[34:35]\n"
      "v_pk_mul_f32 v[56:57], v[56:57], v[32:33]\n"
      "v_pk_mul_f32 v[58:59], v[58:59], v[34:35]\n"
      "v_pk_mul_f32 v[60:61], v[60:61], v[32:33]\n"
      "v_pk_mul_f32 v[62:63], v[62:63], v[34:35]\n"
      "v_pk_mul_f32 v[64:65], v[64:65], v[32:33]\n"
      "v_pk_mul_f32 v[66:67], v[66:67], v[34:35]\n"
      "v_pk_mul_f32 v[68:69], v[68:69], v[32:33]\n"
      "v_pk_mul_f32 v[70:71], v[70:71], v[34:35]\n"
      "93:\n"
      "s_bitcmp1_b32 %[n], 1\n"
      "s_cbranch_scc0 93f\n"
      "v_pk_mul_f32 v[72:73], v[72:73], v[36:37]\n"
      "v_pk_mul_f32 v[74:75], v[74:75], v[38:39]\n"
      "v_pk_mul_f32 v[76:77], v[76:77], v[36:37]\n"
      "v_pk_mul_f32 v[78:79], v[78:79], v[38:39]\n"
      "v_pk_mul_f32 v[80:81], v[80:81], v[36:37]\n"
      "v_pk_mul_f32 v[82:83], v[82:83], v[38:39]\n"
      "v_pk_mul_f32 v[84:85], v[84:85], v[36:37]\n"
      "v_pk_mul_f32 v[86:87], v[86:87], v[38:39]\n"
      "v_pk_mul_f32 v[88:89], v[88:89], v[36:37]\n"
      "v_pk_mul_f32 v[90:91], v[90:91], v[38:39]\n"
      "v_pk_mul_f32 v[92:93], v[92:93], v[36:37]\n"
      "v_pk_mul_f32 v[94:95], v[94:95], v[38:39]\n"
      "v_pk_mul_f32 v[96:97], v[96:97], v[36:37]\n"
      "v_pk_mul_f32 v[98:99], v[98:99], v[38:39]\n"
      "v_pk_mul_f32 v[100:101], v[100:101], v[36:37]\n"
      "v_pk_mul_f32 v[102:103], v[102:103], v[38:39]\n"
      "93:\n"
      "s_nop 4\n"
      "s_setpc_b64 %[ret]\n"
      "201:\n"
      "v_readfirstlane_b32 %[n], v22\n"
      "v_lshrrev_b32 v24, 4, %[pa]\n"
      "v_and_b32 v24, 48, v24\n"
      "v_add_u32 v24, %[k], v24\n"
      "v_add_u32 v24, %[fac], v24\n"
      "ds_read_b128 v[28:31], v24 offset:64\n"
      "ds_read_b128 v[24:27], v24\n"
      "s_waitcnt lgkmcnt(0)\n"
      "s_bitcmp1_b32 %[n], 0\n"
      "s_cbranch_scc0 93f\n"
      "v_pk_mul_f32 v[40:41], v[40:41], v[24:25]\n"
      "v_pk_mul_f32 v[42:43], v[42:43], v[26:27]\n"
      "v_pk_mul_f32 v[44:45], v[44:45], v[24:25]\n"
      "v_pk_mul_f32 v[46:47], v[46:47], v[26:27]\n"
      "v_pk_mul_f32 v[48:49], v[48:49], v[24:25]\n"
      "v_pk_mul_f32 v[50:51], v[50:51], v[26:27]\n"
      "v_pk_mul_f32 v[52:53], v[52:53], v[24:25]\n"
      "v_pk_mul_f32 v[54:55], v[54:55], v[26:27]\n"
      "v_pk_mul_f32 v[56:57], v[56:57], v[24:25]\n"
      "v_pk_mul_f32 v[58:59], v[58:59], v[26:27]\n"
      "v_pk_mul_f32 v[60:61], v[60:61], v[24:25]\n"
      "v_pk_mul_f32 v[62:63], v[62:63], v[26:27]\n"
      "v_pk_mul_f32 v[64:65], v[64:65], v[24:25]\n"
      "v_pk_mul_f32 v[66:67], v[66:67], v[26:27]\n"
      "v_pk_mul_f32 v[68:69], v[68:69], v[24:25]\n"
      "v_pk_mul_f32 v[70:71], v[70:71], v[26:27]\n"
      "93:\n"
      "s_bitcmp1_b32 %[n], 1\n"
      "s_cbranch_scc0 93f\n"
      "v_pk_mul_f32 v[72:73], v[72:73], v[28:29]\n"
      "v_pk_mul_f32 v[74:75], v[74:75], v[30:31]\n"
      "v_pk_mul_f32 v[76:77], v[76:77], v[28:29]\n"
      "v_pk_mul_f32 v[78:79], v[78:79], v[30:31]\n"
      "v_pk_mul_f32 v[80:81], v[80:81], v[28:29]\n"
      "v_pk_mul_f32 v[82:83], v[82:83], v[30:31]\n"
      "v_pk_mul_f32 v[84:85], v[84:85], v[28:29]\n"
      "v_pk_mul_f32 v[86:87], v[86:87], v[30:31]\n"
      "v_pk_mul_f32 v[88:89], v[88:89], v[28:29]\n"
      "v_pk_mul_f32 v[90:91], v[90:91], v[30:31]\n"
      "v_pk_mul_f32 v[92:93], v[92:93], v[28:29]\n"
      "v_pk_mul_f32 v[94:95], v[94:95], v[30:31]\n"
      "v_pk_mul_f32 v[96:97], v[96:97], v[28:29]\n"
      "v_pk_mul_f32 v[98:99], v[98:99], v[30:31]\n"
      "v_pk_mul_f32 v[100:101], v[100:101], v[28:29]\n"
      "v_pk_mul_f32 v[102:103], v[102:103], v[30:31]\n"
      "93:\n"
      "s_nop 4\n"
      "s_setpc_b64 %[ret]\n"
      "110:\n"
      "s_mov_b32 %[n], %[nb]\n"
      "91:\n"
      "s_sub_u32 %[n], %[n], 1\n"
      "s_cbranch_scc1 92f\n"
      "ds_read_b32 v22, %[cb] offset:60\n"
      "s_waitcnt lgkmcnt(0)\n"
      "v_cmp_gt_u32 vcc, 8, v22\n"
      "s_cbranch_vccnz 91b\n"
      "92:\n"
      "ds_read_b32 v22, %[cb] offset:60\n"
      "ds_read_b128 v[32:35], %[pa] offset:2048\n"
      "ds_read_b128 v[36:39], %[pa] offset:3072\n"
      "ds_read_b32 v23, %[cb] offset:80\n"
      "s_waitcnt lgkmcnt(0)\n"
      "v_cmp_lt_u32 vcc, 8, v22\n"
      "s_cbranch_vccz 111f\n"
      "114:\n"
      "s_movk_i32 %[k], 128\n"
      "s_call_b64 %[ret], 201b\n"
      "s_branch 111f\n"
      "120:\n"
      "s_mov_b32 %[n], %[nb]\n"
      "91:\n"
      "s_sub_u32 %[n], %[n], 1\n"
      "s_cbranch_scc1 92f\n"
      "ds_read_b32 v20, %[cb] offset:76\n"
      "s_waitcnt lgkmcnt(0)\n"
      "v_cmp_gt_u32 vcc, 8, v20\n"
      "s_cbranch_vccnz 91b\n"
      "92:\n"
      "ds_read_b32 v20, %[cb] offset:76\n"
      "ds_read_b128 v[24:27], %[pa] offset:4096\n"
      "ds_read_b128 v[28:31], %[pa] offset:5120\n"
      "ds_read_b32 v21, %[cb] offset:96\n"
      "s_waitcnt lgkmcnt(0)\n"
      "v_cmp_lt_u32 vcc, 8, v20\n"
      "s_cbranch_vccz 121f\n"
      "124:\n"
      "s_movk_i32 %[k], 256\n"
      "s_call_b64 %[ret], 200b\n"
      "s_branch 121f\n"
      "123:\n"
      "s_mov_b32 %[n], %[nb]\n"
      "91:\n"
      "s_sub_u32 %[n], %[n], 1\n"
      "s_cbranch_scc1 92f\n"
      "ds_read_b32 v22, %[cb] offset:80\n"
      "s_waitcnt lgkmcnt(0)\n"
      "v_cmp_gt_u32 vcc, 1, v22\n"
      "s_cbranch_vccnz 91b\n"
      "92:\n"
      "ds_read_b64_tr_b16 v[136:137], %[a0] offset:0\n"
      "ds_read_b64_tr_b16 v[138:139], %[a0] offset:16384\n"
      "ds_read_b64_tr_b16 v[140:141], %[a1] offset:0\n"
      "ds_read_b64_tr_b16 v[142:143], %[a1] offset:16384\n"
      "ds_read_b64_tr_b16 v[144:145], %[a2] offset:0\n"
      "ds_read_b64_tr_b16 v[146:147], %[a2] offset:16384\n"
      "ds_read_b64_tr_b16 v[148:149], %[a3] offset:0\n"
      "ds_read_b64_tr_b16 v[150:151], %[a3] offset:16384\n"
      "ds_read_b64_tr_b16 v[152:153], %[a4] offset:0\n"
      "ds_read_b64_tr_b16 v[154:155], %[a4] offset:16384\n"
      "ds_read_b64_tr_b16 v[156:157], %[a5] offset:0\n"
      "ds_read_b64_tr_b16 v[158:159], %[a5] offset:16384\n"
      "ds_read_b64_tr_b16 v[160:161], %[a6] offset:0\n"
      "ds_read_b64_tr_b16 v[162:163], %[a6] offset:16384\n"
      "ds_read_b64_tr_b16 v[164:165], %[a7] offset:0\n"
      "ds_read_b64_tr_b16 v[166:167], %[a7] offset:16384\n"
      "s_mov_b64 exec, 1\n"
      "ds_add_u32 %[cb], %[one] offset:84\n"
      "s_mov_b64 exec, -1\n"
      "130:\n"
      "s_mov_b32 %[n], %[nb]\n"
      "91:\n"
      "s_sub_u32 %[n], %[n], 1\n"
      "s_cbranch_scc1 92f\n"
      "ds_read_b32 v22, %[cb] offset:92\n"
      "s_waitcnt lgkmcnt(0)\n"
      "v_cmp_gt_u32 vcc, 8, v22\n"
      "s_cbranch_vccnz 91b\n"
      "92:\n"
      "ds_read_b32 v22, %[cb] offset:92\n"
      "ds_read_b128 v[32:35], %[pa] offset:6144\n"
      "ds_read_b128 v[36:39], %[pa] offset:7168\n"
      "ds_read_b32 v23, %[cb] offset:112\n"
      "s_waitcnt lgkmcnt(0)\n"
      "v_cmp_lt_u32 vcc, 8, v22\n"
      "s_cbranch_vccz 131f\n"
      "134:\n"
      "s_movk_i32 %[k], 384\n"
      "s_call_b64 %[ret], 201b\n"
      "s_branch 131f\n"
      "133:\n"
      "s_mov_b32 %[n], %[nb]\n"
      "91:\n"
      "s_sub_u32 %[n], %[n], 1\n"
      "s_cbranch_scc1 92f\n"
      "ds_read_b32 v20, %[cb] offset:96\n"
      "s_waitcnt lgkmcnt(0)\n"
      "v_cmp_gt_u32 vcc, 1, v20\n"
      "s_cbranch_vccnz 91b\n"
      "92:\n"
      "ds_read_b64_tr_b16 v[104:105], %[a0] offset:32768\n"
      "ds_read_b64_tr_b16 v[106:107], %[a0] offset:49152\n"
      "ds_read_b64_tr_b16 v[108:109], %[a1] offset:32768\n"
      "ds_read_b64_tr_b16 v[110:111], %[a1] offset:49152\n"
      "ds_read_b64_tr_b16 v[112:113], %[a2] offset:32768\n"
      "ds_read_b64_tr_b16 v[114:115], %[a2] offset:49152\n"
      "ds_read_b64_tr_b16 v[116:117], %[a3] offset:32768\n"
      "ds_read_b64_tr_b16 v[118:119], %[a3] offset:49152\n"
      "ds_read_b64_tr_b16 v[120:121], %[a4] offset:32768\n"
      "ds_read_b64_tr_b16 v[122:123], %[a4] offset:49152\n"
      "ds_read_b64_tr_b16 v[124:125], %[a5] offset:32768\n"
      "ds_read_b64_tr_b16 v[126:127], %[a5] offset:49152\n"
      "ds_read_b64_tr_b16 v[128:129], %[a6] offset:32768\n"
      "ds_read_b64_tr_b16 v[130:131], %[a6] offset:49152\n"
      "ds_read_b64_tr_b16 v[132:133], %[a7] offset:32768\n"
      "ds_read_b64_tr_b16 v[134:135], %[a7] offset:49152\n"
      "s_mov_b64 exec, 1\n"
      "ds_add_u32 %[cb], %[one] offset:100\n"
      "s_mov_b64 exec, -1\n"
      "140:\n"
      "s_mov_b32 %[n], %[nb]\n"
      "91:\n"
      "s_sub_u32 %[n], %[n], 1\n"
      "s_cbranch_scc1 92f\n"
      "ds_read_b32 v20, %[cb] offset:108\n"
      "s_waitcnt lgkmcnt(0)\n"
      "v_cmp_gt_u32 vcc, 8, v20\n"
      "s_cbranch_vccnz 91b\n"
      "92:\n"
      "ds_read_b32 v20, %[cb] offset:108\n"
      "ds_read_b128 v[24:27], %[pa] offset:0\n"
      "ds_read_b128 v[28:31], %[pa] offset:1024\n"
      "ds_read_b32 v21, %[cb] offset:128\n"
      "s_waitcnt lgkmcnt(0)\n"
      "v_cmp_lt_u32 vcc, 8, v20\n"
      "s_cbranch_vccz 141f\n"
      "144:\n"
      "s_movk_i32 %[k], 512\n"
      "s_call_b64 %[ret], 200b\n"
      "s_branch 141f\n"
      "143:\n"
      "s_mov_b32 %[n], %[nb]\n"
      "91:\n"
      "s_sub_u32 %[n], %[n], 1\n"
      "s_cbranch_scc1 92f\n"
      "ds_read_b32 v22, %[cb] offset:112\n"
      "s_waitcnt lgkmcnt(0)\n"
      "v_cmp_gt_u32 vcc, 1, v22\n"
      "s_cbranch_vccnz 91b\n"
      "92:\n"
      "v_add_u32 v138, 0x10000, %[a0]\n"
      "ds_read_b64_tr_b16 v[136:137], v138 offset:32768\n"
      "ds_read_b64_tr_b16 v[138:139], v138 offset:49152\n"
      "v_add_u32 v142, 0x10000, %[a1]\n"
      "ds_read_b64_tr_b16 v[140:141], v142 offset:32768\n"
      "ds_read_b64_tr_b16 v[142:143], v142 offset:49152\n"
      "v_add_u32 v146, 0x10000, %[a2]\n"
      "ds_read_b64_tr_b16 v[144:145], v146 offset:32768\n"
      "ds_read_b64_tr_b16 v[146:147], v146 offset:49152\n"
      "v_add_u32 v150, 0x10000, %[a3]\n"
      "ds_read_b64_tr_b16 v[148:149], v150 offset:32768\n"
      "ds_read_b64_tr_b16 v[150:151], v150 offset:49152\n"
      "v_add_u32 v154, 0x10000, %[a4]\n"
      "ds_read_b64_tr_b16 v[152:153], v154 offset:32768\n"
      "ds_read_b64_tr_b16 v[154:155], v154 offset:49152\n"
      "v_add_u32 v158, 0x10000, %[a5]\n"
      "ds_read_b64_tr_b16 v[156:157], v158 offset:32768\n"
      "ds_read_b64_tr_b16 v[158:159], v158 offset:49152\n"
      "v_add_u32 v162, 0x10000, %[a6]\n"
      "ds_read_b64_tr_b16 v[160:161], v162 offset:32768\n"
      "ds_read_b64_tr_b16 v[162:163], v162 offset:49152\n"
      "v_add_u32 v166, 0x10000, %[a7]\n"
      "ds_read_b64_tr_b16 v[164:165], v166 offset:32768\n"
      "ds_read_b64_tr_b16 v[166:167], v166 offset:49152\n"
      "s_mov_b64 exec, 1\n"
      "ds_add_u32 %[cb], %[one] offset:116\n"
      "s_mov_b64 exec, -1\n"
      "150:\n"
      "s_mov_b32 %[n], %[nb]\n"
      "91:\n"
      "s_sub_u32 %[n], %[n], 1\n"
      "s_cbranch_scc1 92f\n"
      "ds_read_b32 v22, %[cb] offset:124\n"
      "s_waitcnt lgkmcnt(0)\n"
      "v_cmp_gt_u32 vcc, 8, v22\n"
      "s_cbranch_vccnz 91b\n"
      "92:\n"
      "ds_read_b32 v22, %[cb] offset:124\n"
      "ds_read_b128 v[32:35], %[pa] offset:2048\n"
      "ds_read_b128 v[36:39], %[pa] offset:3072\n"
      "ds_read_b32 v23, %[cb] offset:144\n"
      "s_waitcnt lgkmcnt(0)\n"
      "v_cmp_lt_u32 vcc, 8, v22\n"
      "s_cbranch_vccz 151f\n"
      "154:\n"
      "s_movk_i32 %[k], 640\n"
      "s_call_b64 %[ret], 201b\n"
      "s_branch 151f\n"
      "153:\n"
      "s_mov_b32 %[n], %[nb]\n"
      "91:\n"
      "s_sub_u32 %[n], %[n], 1\n"
      "s_cbranch_scc1 92f\n"
      "ds_read_b32 v20, %[cb] offset:128\n"
      "s_waitcnt lgkmcnt(0)\n"
      "v_cmp_gt_u32 vcc, 1, v20\n"
      "s_cbranch_vccnz 91b\n"
      "92:\n"
      "v_add_u32 v106, 0x10000, %[a0]\n"
      "ds_read_b64_tr_b16 v[104:105], v106 offset:0\n"
      "ds_read_b64_tr_b16 v[106:107], v106 offset:16384\n"
      "v_add_u32 v110, 0x10000, %[a1]\n"
      "ds_read_b64_tr_b16 v[108:109], v110 offset:0\n"
      "ds_read_b64_tr_b16 v[110:111], v110 offset:16384\n"
      "v_add_u32 v114, 0x10000, %[a2]\n"
      "ds_read_b64_tr_b16 v[112:113], v114 offset:0\n"
      "ds_read_b64_tr_b16 v[114:115], v114 offset:16384\n"
      "v_add_u32 v118, 0x10000, %[a3]\n"
      "ds_read_b64_tr_b16 v[116:117], v118 offset:0\n"
      "ds_read_b64_tr_b16 v[118:119], v118 offset:16384\n"
      "v_add_u32 v122, 0x10000, %[a4]\n"
      "ds_read_b64_tr_b16 v[120:121], v122 offset:0\n"
      "ds_read_b64_tr_b16 v[122:123], v122 offset:16384\n"
      "v_add_u32 v126, 0x10000, %[a5]\n"
      "ds_read_b64_tr_b16 v[124:125], v126 offset:0\n"
      "ds_read_b64_tr_b16 v[126:127], v126 offset:16384\n"
      "v_add_u32 v130, 0x10000, %[a6]\n"
      "ds_read_b64_tr_b16 v[128:129], v130 offset:0\n"
      "ds_read_b64_tr_b16 v[130:131], v130 offset:16384\n"
      "v_add_u32 v134, 0x10000, %[a7]\n"
      "ds_read_b64_tr_b16 v[132:133], v134 offset:0\n"
      "ds_read_b64_tr_b16 v[134:135], v134 offset:16384\n"
      "160:\n"
      "s_mov_b32 %[n], %[nb]\n"
      "91:\n"
      "s_sub_u32 %[n], %[n], 1\n"
      "s_cbranch_scc1 92f\n"
      "ds_read_b32 v20, %[cb] offset:140\n"
      "s_waitcnt lgkmcnt(0)\n"
      "v_cmp_gt_u32 vcc, 8, v20\n"
      "s_cbranch_vccnz 91b\n"
      "92:\n"
      "ds_read_b32 v20, %[cb] offset:140\n"
      "ds_read_b128 v[24:27], %[pa] offset:4096\n"
      "ds_read_b128 v[28:31], %[pa] offset:5120\n"
      "s_waitcnt lgkmcnt(0)\n"
      "v_cmp_lt_u32 vcc, 8, v20\n"
      "s_cbranch_vccz 161f\n"
      "164:\n"
      "s_movk_i32 %[k], 768\n"
      "s_call_b64 %[ret], 200b\n"
      "s_branch 161f\n"
      "174:\n"
      "s_movk_i32 %[k], 896\n"
      "s_call_b64 %[ret], 201b\n"
      "s_branch 171f\n"
      "300:\n"
      "s_mov_b32 %[n], %[nb]\n"
      "91:\n"
      "s_sub_u32 %[n], %[n], 1\n"
      "s_cbranch_scc1 92f\n"
      "s_sleep 1\n"
      "ds_read_b32 v20, %[cb] offset:32\n"
      "s_waitcnt lgkmcnt(0)\n"
      "v_cmp_gt_u32 vcc, 1, v20\n"
      "s_cbranch_vccnz 91b\n"
      "92:\n"
      "ds_read_b64_tr_b16 v[72:73], %[a0] offset:0\n"
      "ds_read_b64_tr_b16 v[74:75], %[a0] offset:16384\n"
      "ds_read_b64_tr_b16 v[76:77], %[a1] offset:0\n"
      "ds_read_b64_tr_b16 v[78:79], %[a1] offset:16384\n"
      "ds_read_b64_tr_b16 v[80:81], %[a2] offset:0\n"
      "ds_read_b64_tr_b16 v[82:83], %[a2] offset:16384\n"
      "ds_read_b64_tr_b16 v[84:85], %[a3] offset:0\n"
      "ds_read_b64_tr_b16 v[86:87], %[a3] offset:16384\n"
      "ds_read_b64_tr_b16 v[88:89], %[a4] offset:0\n"
      "ds_read_b64_tr_b16 v[90:91], %[a4] offset:16384\n"
      "ds_read_b64_tr_b16 v[92:93], %[a5] offset:0\n"
      "ds_read_b64_tr_b16 v[94:95], %[a5] offset:16384\n"
      "ds_read_b64_tr_b16 v[96:97], %[a6] offset:0\n"
      "ds_read_b64_tr_b16 v[98:99], %[a6] offset:16384\n"
      "ds_read_b64_tr_b16 v[100:101], %[a7] offset:0\n"
      "ds_read_b64_tr_b16 v[102:103], %[a7] offset:16384\n"
      "s_mov_b64 exec, 1\n"
      "ds_add_u32 %[cb], %[one] offset:36\n"
      "s_mov_b64 exec, -1\n"
      "s_mov_b32 %[n], %[nb]\n"
      "91:\n"
      "s_sub_u32 %[n], %[n], 1\n"
      "s_cbranch_scc1 92f\n"
      "s_sleep 1\n"
      "ds_read_b32 v20, %[cb] offset:48\n"
      "s_waitcnt lgkmcnt(0)\n"
      "v_cmp_gt_u32 vcc, 1, v20\n"
      "s_cbranch_vccnz 91b\n"
      "92:\n"
      "ds_read_b64_tr_b16 v[136:137], %[a0] offset:32768\n"
      "ds_read_b64_tr_b16 v[138:139], %[a0] offset:49152\n"
      "ds_read_b64_tr_b16 v[140:141], %[a1] offset:32768\n"
      "ds_read_b64_tr_b16 v[142:143], %[a1] offset:49152\n"
      "ds_read_b64_tr_b16 v[144:145], %[a2] offset:32768\n"
      "ds_read_b64_tr_b16 v[146:147], %[a2] offset:49152\n"
      "ds_read_b64_tr_b16 v[148:149], %[a3] offset:32768\n"
      "ds_read_b64_tr_b16 v[150:151], %[a3] offset:49152\n"
      "ds_read_b64_tr_b16 v[152:153], %[a4] offset:32768\n"
      "ds_read_b64_tr_b16 v[154:155], %[a4] offset:49152\n"
      "ds_read_b64_tr_b16 v[156:157], %[a5] offset:32768\n"
      "ds_read_b64_tr_b16 v[158:159], %[a5] offset:49152\n"
      "ds_read_b64_tr_b16 v[160:161], %[a6] offset:32768\n"
      "ds_read_b64_tr_b16 v[162:163], %[a6] offset:49152\n"
      "ds_read_b64_tr_b16 v[164:165], %[a7] offset:32768\n"
      "ds_read_b64_tr_b16 v[166:167], %[a7] offset:49152\n"
      "s_mov_b64 exec, 1\n"
      "ds_add_u32 %[cb], %[one] offset:52\n"
      "s_mov_b64 exec, -1\n"
      "s_mov_b32 %[n], %[nb]\n"
      "91:\n"
      "s_sub_u32 %[n], %[n], 1\n"
      "s_cbranch_scc1 92f\n"
      "s_sleep 1\n"
      "ds_read_b32 v20, %[cb] offset:64\n"
      "s_waitcnt lgkmcnt(0)\n"
      "v_cmp_gt_u32 vcc, 1, v20\n"
      "s_cbranch_vccnz 91b\n"
      "92:\n"
      "v_add_u32 v106, 0x10000, %[a0]\n"
      "ds_read_b64_tr_b16 v[104:105], v106 offset:0\n"
      "ds_read_b64_tr_b16 v[106:107], v106 offset:16384\n"
      "v_add_u32 v110, 0x10000, %[a1]\n"
      "ds_read_b64_tr_b16 v[108:109], v110 offset:0\n"
      "ds_read_b64_tr_b16 v[110:111], v110 offset:16384\n"
      "v_add_u32 v114, 0x10000, %[a2]\n"
      "ds_read_b64_tr_b16 v[112:113], v114 offset:0\n"
      "ds_read_b64_tr_b16 v[114:115], v114 offset:16384\n"
      "v_add_u32 v118, 0x10000, %[a3]\n"
      "ds_read_b64_tr_b16 v[116:117], v118 offset:0\n"
      "ds_read_b64_tr_b16 v[118:119], v118 offset:16384\n"
      "v_add_u32 v122, 0x10000, %[a4]\n"
      "ds_read_b64_tr_b16 v[120:121], v122 offset:0\n"
      "ds_read_b64_tr_b16 v[122:123], v122 offset:16384\n"
      "v_add_u32 v126, 0x10000, %[a5]\n"
      "ds_read_b64_tr_b16 v[124:125], v126 offset:0\n"
      "ds_read_b64_tr_b16 v[126:127], v126 offset:16384\n"
      "v_add_u32 v130, 0x10000, %[a6]\n"
      "ds_read_b64_tr_b16 v[128:129], v130 offset:0\n"
      "ds_read_b64_tr_b16 v[130:131], v130 offset:16384\n"
      "v_add_u32 v134, 0x10000, %[a7]\n"
      "ds_read_b64_tr_b16 v[132:133], v134 offset:0\n"
      "ds_read_b64_tr_b16 v[134:135], v134 offset:16384\n"
      "s_mov_b64 exec, 1\n"
      "ds_add_u32 %[cb], %[one] offset:68\n"
      "s_mov_b64 exec, -1\n"
      "s_mov_b64 %[m0], 0\n"
      "s_mov_b32 %[n], %[nb]\n"
      "91:\n"
      "s_sub_u32 %[n], %[n], 1\n"
      "s_cbranch_scc1 92f\n"
      "s_sleep 1\n"
      "ds_read_b32 v20, %[cb] offset:40\n"
      "s_waitcnt lgkmcnt(0)\n"
      "v_cmp_gt_u32 vcc, 1, v20\n"
      "s_cbranch_vccnz 91b\n"
      "92:\n"
      "s_mov_b32 %[n], %[nb]\n"
      "91:\n"
      "s_sub_u32 %[n], %[n], 1\n"
      "s_cbranch_scc1 92f\n"
      "ds_read_b32 v20, %[cb] offset:44\n"
      "s_waitcnt lgkmcnt(0)\n"
      "v_cmp_gt_u32 vcc, 8, v20\n"
      "s_cbranch_vccnz 91b\n"
      "92:\n"
      "ds_read_b32 v20, %[cb] offset:44\n"
      "ds_read_b128 v[24:27], %[pa] offset:0\n"
      "ds_read_b128 v[28:31], %[pa] offset:1024\n"
      "ds_read_b32 v21, %[cb] offset:64\n"
      "s_waitcnt lgkmcnt(0)\n"
      "v_mfma_f32_16x16x32_bf16 v[40:43], v[24:27], v[72:75], 0\n"
      "v_mfma_f32_16x16x32_bf16 v[72:75], v[28:31], v[72:75], 0\n"
      "v_mfma_f32_16x16x32_bf16 v[44:47], v[24:27], v[76:79], 0\n"
      "v_mfma_f32_16x16x32_bf16 v[76:79], v[28:31], v[76:79], 0\n"
      "v_mfma_f32_16x16x32_bf16 v[48:51], v[24:27], v[80:83], 0\n"
      "v_mfma_f32_16x16x32_bf16 v[80:83], v[28:31], v[80:83], 0\n"
      "v_mfma_f32_16x16x32_bf16 v[52:55], v[24:27], v[84:87], 0\n"
      "v_mfma_f32_16x16x32_bf16 v[84:87], v[28:31], v[84:87], 0\n"
      "ds_read_b32 v22, %[cb] offset:60\n"
      "ds_read_b128 v[32:35], %[pa] offset:2048\n"
      "ds_read_b128 v[36:39], %[pa] offset:3072\n"
      "ds_read_b32 v23, %[cb] offset:80\n"
      "v_mfma_f32_16x16x32_bf16 v[56:59], v[24:27], v[88:91], 0\n"
      "v_mfma_f32_16x16x32_bf16 v[88:91], v[28:31], v[88:91], 0\n"
      "v_mfma_f32_16x16x32_bf16 v[60:63], v[24:27], v[92:95], 0\n"
      "v_mfma_f32_16x16x32_bf16 v[92:95], v[28:31], v[92:95], 0\n"
      "v_mfma_f32_16x16x32_bf16 v[64:67], v[24:27], v[96:99], 0\n"
      "v_mfma_f32_16x16x32_bf16 v[96:99], v[28:31], v[96:99], 0\n"
      "v_mfma_f32_16x16x32_bf16 v[68:71], v[24:27], v[100:103], 0\n"
      "v_mfma_f32_16x16x32_bf16 v[100:103], v[28:31], v[100:103], 0\n"
      "s_waitcnt lgkmcnt(0)\n"
      "v_cmp_ne_u32 vcc, 8, v22\n"
      "s_cbranch_vccnz 110b\n"
      "111:\n"
      "v_cmp_eq_u32 %[m1], 0, v23\n"
      "v_mfma_f32_16x16x32_bf16 v[40:43], v[32:35], v[136:139], v[40:43]\n"
      "v_mfma_f32_16x16x32_bf16 v[72:75], v[36:39], v[136:139], v[72:75]\n"
      "ds_read_b64_tr_b16 v[136:137], %[a0] offset:0\n"
      "ds_read_b64_tr_b16 v[138:139], %[a0] offset:16384\n"
      "v_mfma_f32_16x16x32_bf16 v[44:47], v[32:35], v[140:143], v[44:47]\n"
      "v_mfma_f32_16x16x32_bf16 v[76:79], v[36:39], v[140:143], v[76:79]\n"
      "ds_read_b64_tr_b16 v[140:141], %[a1] offset:0\n"
      "ds_read_b64_tr_b16 v[142:143], %[a1] offset:16384\n"
      "v_mfma_f32_16x16x32_bf16 v[48:51], v[32:35], v[144:147], v[48:51]\n"
      "v_mfma_f32_16x16x32_bf16 v[80:83], v[36:39], v[144:147], v[80:83]\n"
      "ds_read_b64_tr_b16 v[144:145], %[a2] offset:0\n"
      "ds_read_b64_tr_b16 v[146:147], %[a2] offset:16384\n"
      "v_mfma_f32_16x16x32_bf16 v[52:55], v[32:35], v[148:151], v[52:55]\n"
      "v_mfma_f32_16x16x32_bf16 v[84:87], v[36:39], v[148:151], v[84:87]\n"
      "ds_read_b64_tr_b16 v[148:149], %[a3] offset:0\n"
      "ds_read_b64_tr_b16 v[150:151], %[a3] offset:16384\n"
      "ds_read_b32 v20, %[cb] offset:76\n"
      "ds_read_b128 v[24:27], %[pa] offset:4096\n"
      "ds_read_b128 v[28:31], %[pa] offset:5120\n"
      "ds_read_b32 v21, %[cb] offset:96\n"
      "v_mfma_f32_16x16x32_bf16 v[56:59], v[32:35], v[152:155], v[56:59]\n"
      "v_mfma_f32_16x16x32_bf16 v[88:91], v[36:39], v[152:155], v[88:91]\n"
      "ds_read_b64_tr_b16 v[152:153], %[a4] offset:0\n"
      "ds_read_b64_tr_b16 v[154:155], %[a4] offset:16384\n"
      "v_mfma_f32_16x16x32_bf16 v[60:63], v[32:35], v[156:159], v[60:63]\n"
      "v_mfma_f32_16x16x32_bf16 v[92:95], v[36:39], v[156:159], v[92:95]\n"
      "ds_read_b64_tr_b16 v[156:157], %[a5] offset:0\n"
      "ds_read_b64_tr_b16 v[158:159], %[a5] offset:16384\n"
      "v_mfma_f32_16x16x32_bf16 v[64:67], v[32:35], v[160:163], v[64:67]\n"
      "v_mfma_f32_16x16x32_bf16 v[96:99], v[36:39], v[160:163], v[96:99]\n"
      "ds_read_b64_tr_b16 v[160:161], %[a6] offset:0\n"
      "ds_read_b64_tr_b16 v[162:163], %[a6] offset:16384\n"
      "v_mfma_f32_16x16x32_bf16 v[68:71], v[32:35], v[164:167], v[68:71]\n"
      "v_mfma_f32_16x16x32_bf16 v[100:103], v[36:39], v[164:167], v[100:103]\n"
      "ds_read_b64_tr_b16 v[164:165], %[a7] offset:0\n"
      "ds_read_b64_tr_b16 v[166:167], %[a7] offset:16384\n"
      "s_cmp_lg_u64 %[m1], 0\n"
      "s_cbranch_scc1 112f\n"
      "s_mov_b64 exec, 1\n"
      "ds_add_u32 %[cb], %[one] offset:84\n"
      "s_mov_b64 exec, -1\n"
      "112:\n"
      "s_waitcnt lgkmcnt(8)\n"
      "v_cmp_ne_u32 vcc, 8, v20\n"
      "s_cbranch_vccnz 120b\n"
      "121:\n"
      "v_cmp_eq_u32 %[m0], 0, v21\n"
      "v_mfma_f32_16x16x32_bf16 v[40:43], v[24:27], v[104:107], v[40:43]\n"
      "v_mfma_f32_16x16x32_bf16 v[72:75], v[28:31], v[104:107], v[72:75]\n"
      "ds_read_b64_tr_b16 v[104:105], %[a0] offset:32768\n"
      "ds_read_b64_tr_b16 v[106:107], %[a0] offset:49152\n"
      "v_mfma_f32_16x16x32_bf16 v[44:47], v[24:27], v[108:111], v[44:47]\n"
      "v_mfma_f32_16x16x32_bf16 v[76:79], v[28:31], v[108:111], v[76:79]\n"
      "ds_read_b64_tr_b16 v[108:109], %[a1] offset:32768\n"
      "ds_read_b64_tr_b16 v[110:111], %[a1] offset:49152\n"
      "v_mfma_f32_16x16x32_bf16 v[48:51], v[24:27], v[112:115], v[48:51]\n"
      "v_mfma_f32_16x16x32_bf16 v[80:83], v[28:31], v[112:115], v[80:83]\n"
      "ds_read_b64_tr_b16 v[112:113], %[a2] offset:32768\n"
      "ds_read_b64_tr_b16 v[114:115], %[a2] offset:49152\n"
      "v_mfma_f32_16x16x32_bf16 v[52:55], v[24:27], v[116:119], v[52:55]\n"
      "v_mfma_f32_16x16x32_bf16 v[84:87], v[28:31], v[116:119], v[84:87]\n"
      "ds_read_b64_tr_b16 v[116:117], %[a3] offset:32768\n"
      "ds_read_b64_tr_b16 v[118:119], %[a3] offset:49152\n"
      "ds_read_b32 v22, %[cb] offset:92\n"
      "ds_read_b128 v[32:35], %[pa] offset:6144\n"
      "ds_read_b128 v[36:39], %[pa] offset:7168\n"
      "ds_read_b32 v23, %[cb] offset:112\n"
      "v_mfma_f32_16x16x32_bf16 v[56:59], v[24:27], v[120:123], v[56:59]\n"
      "v_mfma_f32_16x16x32_bf16 v[88:91], v[28:31], v[120:123], v[88:91]\n"
      "ds_read_b64_tr_b16 v[120:121], %[a4] offset:32768\n"
      "ds_read_b64_tr_b16 v[122:123], %[a4] offset:49152\n"
      "v_mfma_f32_16x16x32_bf16 v[60:63], v[24:27], v[124:127], v[60:63]\n"
      "v_mfma_f32_16x16x32_bf16 v[92:95], v[28:31], v[124:127], v[92:95]\n"
      "ds_read_b64_tr_b16 v[124:125], %[a5] offset:32768\n"
      "ds_read_b64_tr_b16 v[126:127], %[a5] offset:49152\n"
      "v_mfma_f32_16x16x32_bf16 v[64:67], v[24:27], v[128:131], v[64:67]\n"
      "v_mfma_f32_16x16x32_bf16 v[96:99], v[28:31], v[128:131], v[96:99]\n"
      "ds_read_b64_tr_b16 v[128:129], %[a6] offset:32768\n"
      "ds_read_b64_tr_b16 v[130:131], %[a6] offset:49152\n"
      "v_mfma_f32_16x16x32_bf16 v[68:71], v[24:27], v[132:135], v[68:71]\n"
      "v_mfma_f32_16x16x32_bf16 v[100:103], v[28:31], v[132:135], v[100:103]\n"
      "ds_read_b64_tr_b16 v[132:133], %[a7] offset:32768\n"
      "ds_read_b64_tr_b16 v[134:135], %[a7] offset:49152\n"
      "s_cmp_lg_u64 %[m0], 0\n"
      "s_cbranch_scc1 122f\n"
      "s_mov_b64 exec, 1\n"
      "ds_add_u32 %[cb], %[one] offset:100\n"
      "s_mov_b64 exec, -1\n"
      "122:\n"
      "s_cmp_lg_u64 %[m1], 0\n"
      "s_cbranch_scc1 123b\n"
      "s_waitcnt lgkmcnt(8)\n"
      "v_cmp_ne_u32 vcc, 8, v22\n"
      "s_cbranch_vccnz 130b\n"
      "131:\n"
      "v_cmp_eq_u32 %[m1], 0, v23\n"
      "v_mfma_f32_16x16x32_bf16 v[40:43], v[32:35], v[136:139], v[40:43]\n"
      "v_mfma_f32_16x16x32_bf16 v[72:75], v[36:39], v[136:139], v[72:75]\n"
      "v_add_u32 v138, 0x10000, %[a0]\n"
      "ds_read_b64_tr_b16 v[136:137], v138 offset:32768\n"
      "ds_read_b64_tr_b16 v[138:139], v138 offset:49152\n"
      "v_mfma_f32_16x16x32_bf16 v[44:47], v[32:35], v[140:143], v[44:47]\n"
      "v_mfma_f32_16x16x32_bf16 v[76:79], v[36:39], v[140:143], v[76:79]\n"
      "v_add_u32 v142, 0x10000, %[a1]\n"
      "ds_read_b64_tr_b16 v[140:141], v142 offset:32768\n"
      "ds_read_b64_tr_b16 v[142:143], v142 offset:49152\n"
      "v_mfma_f32_16x16x32_bf16 v[48:51], v[32:35], v[144:147], v[48:51]\n"
      "v_mfma_f32_16x16x32_bf16 v[80:83], v[36:39], v[144:147], v[80:83]\n"
      "v_add_u32 v146, 0x10000, %[a2]\n"
      "ds_read_b64_tr_b16 v[144:145], v146 offset:32768\n"
      "ds_read_b64_tr_b16 v[146:147], v146 offset:49152\n"
      "v_mfma_f32_16x16x32_bf16 v[52:55], v[32:35], v[148:151], v[52:55]\n"
      "v_mfma_f32_16x16x32_bf16 v[84:87], v[36:39], v[148:151], v[84:87]\n"
      "v_add_u32 v150, 0x10000, %[a3]\n"
      "ds_read_b64_tr_b16 v[148:149], v150 offset:32768\n"
      "ds_read_b64_tr_b16 v[150:151], v150 offset:49152\n"
      "ds_read_b32 v20, %[cb] offset:108\n"
      "ds_read_b128 v[24:27], %[pa] offset:0\n"
      "ds_read_b128 v[28:31], %[pa] offset:1024\n"
      "ds_read_b32 v21, %[cb] offset:128\n"
      "v_mfma_f32_16x16x32_bf16 v[56:59], v[32:35], v[152:155], v[56:59]\n"
      "v_mfma_f32_16x16x32_bf16 v[88:91], v[36:39], v[152:155], v[88:91]\n"
      "v_add_u32 v154, 0x10000, %[a4]\n"
      "ds_read_b64_tr_b16 v[152:153], v154 offset:32768\n"
      "ds_read_b64_tr_b16 v[154:155], v154 offset:49152\n"
      "v_mfma_f32_16x16x32_bf16 v[60:63], v[32:35], v[156:159], v[60:63]\n"
      "v_mfma_f32_16x16x32_bf16 v[92:95], v[36:39], v[156:159], v[92:95]\n"
      "v_add_u32 v158, 0x10000, %[a5]\n"
      "ds_read_b64_tr_b16 v[156:157], v158 offset:32768\n"
      "ds_read_b64_tr_b16 v[158:159], v158 offset:49152\n"
      "v_mfma_f32_16x16x32_bf16 v[64:67], v[32:35], v[160:163], v[64:67]\n"
      "v_mfma_f32_16x16x32_bf16 v[96:99], v[36:39], v[160:163], v[96:99]\n"
      "v_add_u32 v162, 0x10000, %[a6]\n"
      "ds_read_b64_tr_b16 v[160:161], v162 offset:32768\n"
      "ds_read_b64_tr_b16 v[162:163], v162 offset:49152\n"
      "v_mfma_f32_16x16x32_bf16 v[68:71], v[32:35], v[164:167], v[68:71]\n"
      "v_mfma_f32_16x16x32_bf16 v[100:103], v[36:39], v[164:167], v[100:103]\n"
      "v_add_u32 v166, 0x10000, %[a7]\n"
      "ds_read_b64_tr_b16 v[164:165], v166 offset:32768\n"
      "ds_read_b64_tr_b16 v[166:167], v166 offset:49152\n"
      "s_cmp_lg_u64 %[m1], 0\n"
      "s_cbranch_scc1 132f\n"
      "s_mov_b64 exec, 1\n"
      "ds_add_u32 %[cb], %[one] offset:116\n"
      "s_mov_b64 exec, -1\n"
      "132:\n"
      "s_cmp_lg_u64 %[m0], 0\n"
      "s_cbranch_scc1 133b\n"
      "s_waitcnt lgkmcnt(8)\n"
      "v_cmp_ne_u32 vcc, 8, v20\n"
      "s_cbranch_vccnz 140b\n"
      "141:\n"
      "v_cmp_eq_u32 %[m0], 0, v21\n"
      "v_mfma_f32_16x16x32_bf16 v[40:43], v[24:27], v[104:107], v[40:43]\n"
      "v_mfma_f32_16x16x32_bf16 v[72:75], v[28:31], v[104:107], v[72:75]\n"
      "v_add_u32 v106, 0x10000, %[a0]\n"
      "ds_read_b64_tr_b16 v[104:105], v106 offset:0\n"
      "ds_read_b64_tr_b16 v[106:107], v106 offset:16384\n"
      "v_mfma_f32_16x16x32_bf16 v[44:47], v[24:27], v[108:111], v[44:47]\n"
      "v_mfma_f32_16x16x32_bf16 v[76:79], v[28:31], v[108:111], v[76:79]\n"
      "v_add_u32 v110, 0x10000, %[a1]\n"
      "ds_read_b64_tr_b16 v[108:109], v110 offset:0\n"
      "ds_read_b64_tr_b16 v[110:111], v110 offset:16384\n"
      "v_mfma_f32_16x16x32_bf16 v[48:51], v[24:27], v[112:115], v[48:51]\n"
      "v_mfma_f32_16x16x32_bf16 v[80:83], v[28:31], v[112:115], v[80:83]\n"
      "v_add_u32 v114, 0x10000, %[a2]\n"
      "ds_read_b64_tr_b16 v[112:113], v114 offset:0\n"
      "ds_read_b64_tr_b16 v[114:115], v114 offset:16384\n"
      "v_mfma_f32_16x16x32_bf16 v[52:55], v[24:27], v[116:119], v[52:55]\n"
      "v_mfma_f32_16x16x32_bf16 v[84:87], v[28:31], v[116:119], v[84:87]\n"
      "v_add_u32 v118, 0x10000, %[a3]\n"
      "ds_read_b64_tr_b16 v[116:117], v118 offset:0\n"
      "ds_read_b64_tr_b16 v[118:119], v118 offset:16384\n"
      "ds_read_b32 v22, %[cb] offset:124\n"
      "ds_read_b128 v[32:35], %[pa] offset:2048\n"
      "ds_read_b128 v[36:39], %[pa] offset:3072\n"
      "ds_read_b32 v23, %[cb] offset:144\n"
      "v_mfma_f32_16x16x32_bf16 v[56:59], v[24:27], v[120:123], v[56:59]\n"
      "v_mfma_f32_16x16x32_bf16 v[88:91], v[28:31], v[120:123], v[88:91]\n"
      "v_add_u32 v122, 0x10000, %[a4]\n"
      "ds_read_b64_tr_b16 v[120:121], v122 offset:0\n"
      "ds_read_b64_tr_b16 v[122:123], v122 offset:16384\n"
      "v_mfma_f32_16x16x32_bf16 v[60:63], v[24:27], v[124:127], v[60:63]\n"
      "v_mfma_f32_16x16x32_bf16 v[92:95], v[28:31], v[124:127], v[92:95]\n"
      "v_add_u32 v126, 0x10000, %[a5]\n"
      "ds_read_b64_tr_b16 v[124:125], v126 offset:0\n"
      "ds_read_b64_tr_b16 v[126:127], v126 offset:16384\n"
      "v_mfma_f32_16x16x32_bf16 v[64:67], v[24:27], v[128:131], v[64:67]\n"
      "v_mfma_f32_16x16x32_bf16 v[96:99], v[28:31], v[128:131], v[96:99]\n"
      "v_add_u32 v130, 0x10000, %[a6]\n"
      "ds_read_b64_tr_b16 v[128:129], v130 offset:0\n"
      "ds_read_b64_tr_b16 v[130:131], v130 offset:16384\n"
      "v_mfma_f32_16x16x32_bf16 v[68:71], v[24:27], v[132:135], v[68:71]\n"
      "v_mfma_f32_16x16x32_bf16 v[100:103], v[28:31], v[132:135], v[100:103]\n"
      "v_add_u32 v134, 0x10000, %[a7]\n"
      "ds_read_b64_tr_b16 v[132:133], v134 offset:0\n"
      "ds_read_b64_tr_b16 v[134:135], v134 offset:16384\n"
      "s_cmp_lg_u64 %[m1], 0\n"
      "s_cbranch_scc1 143b\n"
      "s_waitcnt lgkmcnt(8)\n"
      "v_cmp_ne_u32 vcc, 8, v22\n"
      "s_cbranch_vccnz 150b\n"
      "151:\n"
      "v_cmp_eq_u32 %[m1], 0, v23\n"
      "v_mfma_f32_16x16x32_bf16 v[40:43], v[32:35], v[136:139], v[40:43]\n"
      "v_mfma_f32_16x16x32_bf16 v[72:75], v[36:39], v[136:139], v[72:75]\n"
      "ds_read_b64_tr_b16 v[136:137], %[a0] offset:0\n"
      "ds_read_b64_tr_b16 v[138:139], %[a0] offset:16384\n"
      "v_mfma_f32_16x16x32_bf16 v[44:47], v[32:35], v[140:143], v[44:47]\n"
      "v_mfma_f32_16x16x32_bf16 v[76:79], v[36:39], v[140:143], v[76:79]\n"
      "ds_read_b64_tr_b16 v[140:141], %[a1] offset:0\n"
      "ds_read_b64_tr_b16 v[142:143], %[a1] offset:16384\n"
      "v_mfma_f32_16x16x32_bf16 v[48:51], v[32:35], v[144:147], v[48:51]\n"
      "v_mfma_f32_16x16x32_bf16 v[80:83], v[36:39], v[144:147], v[80:83]\n"
      "ds_read_b64_tr_b16 v[144:145], %[a2] offset:0\n"
      "ds_read_b64_tr_b16 v[146:147], %[a2] offset:16384\n"
      "v_mfma_f32_16x16x32_bf16 v[52:55], v[32:35], v[148:151], v[52:55]\n"
      "v_mfma_f32_16x16x32_bf16 v[84:87], v[36:39], v[148:151], v[84:87]\n"
      "ds_read_b64_tr_b16 v[148:149], %[a3] offset:0\n"
      "ds_read_b64_tr_b16 v[150:151], %[a3] offset:16384\n"
      "ds_read_b32 v20, %[cb] offset:140\n"
      "ds_read_b128 v[24:27], %[pa] offset:4096\n"
      "ds_read_b128 v[28:31], %[pa] offset:5120\n"
      "v_mfma_f32_16x16x32_bf16 v[56:59], v[32:35], v[152:155], v[56:59]\n"
      "v_mfma_f32_16x16x32_bf16 v[88:91], v[36:39], v[152:155], v[88:91]\n"
      "ds_read_b64_tr_b16 v[152:153], %[a4] offset:0\n"
      "ds_read_b64_tr_b16 v[154:155], %[a4] offset:16384\n"
      "v_mfma_f32_16x16x32_bf16 v[60:63], v[32:35], v[156:159], v[60:63]\n"
      "v_mfma_f32_16x16x32_bf16 v[92:95], v[36:39], v[156:159], v[92:95]\n"
      "ds_read_b64_tr_b16 v[156:157], %[a5] offset:0\n"
      "ds_read_b64_tr_b16 v[158:159], %[a5] offset:16384\n"
      "v_mfma_f32_16x16x32_bf16 v[64:67], v[32:35], v[160:163], v[64:67]\n"
      "v_mfma_f32_16x16x32_bf16 v[96:99], v[36:39], v[160:163], v[96:99]\n"
      "ds_read_b64_tr_b16 v[160:161], %[a6] offset:0\n"
      "ds_read_b64_tr_b16 v[162:163], %[a6] offset:16384\n"
      "v_mfma_f32_16x16x32_bf16 v[68:71], v[32:35], v[164:167], v[68:71]\n"
      "v_mfma_f32_16x16x32_bf16 v[100:103], v[36:39], v[164:167], v[100:103]\n"
      "ds_read_b64_tr_b16 v[164:165], %[a7] offset:0\n"
      "ds_read_b64_tr_b16 v[166:167], %[a7] offset:16384\n"
      "s_cmp_lg_u64 %[m0], 0\n"
      "s_cbranch_scc1 153b\n"
      "s_waitcnt lgkmcnt(8)\n"
      "v_cmp_ne_u32 vcc, 8, v20\n"
      "s_cbranch_vccnz 160b\n"
      "161:\n"
      "v_mfma_f32_16x16x32_bf16 v[40:43], v[24:27], v[104:107], v[40:43]\n"
      "v_mfma_f32_16x16x32_bf16 v[72:75], v[28:31], v[104:107], v[72:75]\n"
      "v_mfma_f32_16x16x32_bf16 v[44:47], v[24:27], v[108:111], v[44:47]\n"
      "v_mfma_f32_16x16x32_bf16 v[76:79], v[28:31], v[108:111], v[76:79]\n"
      "v_mfma_f32_16x16x32_bf16 v[48:51], v[24:27], v[112:115], v[48:51]\n"
      "v_mfma_f32_16x16x32_bf16 v[80:83], v[28:31], v[112:115], v[80:83]\n"
      "v_mfma_f32_16x16x32_bf16 v[52:55], v[24:27], v[116:119], v[52:55]\n"
      "v_mfma_f32_16x16x32_bf16 v[84:87], v[28:31], v[116:119], v[84:87]\n"
      "v_mfma_f32_16x16x32_bf16 v[56:59], v[24:27], v[120:123], v[56:59]\n"
      "v_mfma_f32_16x16x32_bf16 v[88:91], v[28:31], v[120:123], v[88:91]\n"
      "v_mfma_f32_16x16x32_bf16 v[60:63], v[24:27], v[124:127], v[60:63]\n"
      "v_mfma_f32_16x16x32_bf16 v[92:95], v[28:31], v[124:127], v[92:95]\n"
      "v_mfma_f32_16x16x32_bf16 v[64:67], v[24:27], v[128:131], v[64:67]\n"
      "v_mfma_f32_16x16x32_bf16 v[96:99], v[28:31], v[128:131], v[96:99]\n"
      "v_mfma_f32_16x16x32_bf16 v[68:71], v[24:27], v[132:135], v[68:71]\n"
      "v_mfma_f32_16x16x32_bf16 v[100:103], v[28:31], v[132:135], v[100:103]\n"
      "s_cmp_lg_u64 %[m1], 0\n"
      "s_cbranch_scc0 170f\n"
      "s_mov_b32 %[n], %[nb]\n"
      "91:\n"
      "s_sub_u32 %[n], %[n], 1\n"
      "s_cbranch_scc1 92f\n"
      "ds_read_b32 v22, %[cb] offset:144\n"
      "s_waitcnt lgkmcnt(0)\n"
      "v_cmp_gt_u32 vcc, 1, v22\n"
      "s_cbranch_vccnz 91b\n"
      "92:\n"
      "ds_read_b64_tr_b16 v[136:137], %[a0] offset:0\n"
      "ds_read_b64_tr_b16 v[138:139], %[a0] offset:16384\n"
      "ds_read_b64_tr_b16 v[140:141], %[a1] offset:0\n"
      "ds_read_b64_tr_b16 v[142:143], %[a1] offset:16384\n"
      "ds_read_b64_tr_b16 v[144:145], %[a2] offset:0\n"
      "ds_read_b64_tr_b16 v[146:147], %[a2] offset:16384\n"
      "ds_read_b64_tr_b16 v[148:149], %[a3] offset:0\n"
      "ds_read_b64_tr_b16 v[150:151], %[a3] offset:16384\n"
      "ds_read_b64_tr_b16 v[152:153], %[a4] offset:0\n"
      "ds_read_b64_tr_b16 v[154:155], %[a4] offset:16384\n"
      "ds_read_b64_tr_b16 v[156:157], %[a5] offset:0\n"
      "ds_read_b64_tr_b16 v[158:159], %[a5] offset:16384\n"
      "ds_read_b64_tr_b16 v[160:161], %[a6] offset:0\n"
      "ds_read_b64_tr_b16 v[162:163], %[a6] offset:16384\n"
      "ds_read_b64_tr_b16 v[164:165], %[a7] offset:0\n"
      "ds_read_b64_tr_b16 v[166:167], %[a7] offset:16384\n"
      "170:\n"
      "s_mov_b32 %[n], %[nb]\n"
      "91:\n"
      "s_sub_u32 %[n], %[n], 1\n"
      "s_cbranch_scc1 92f\n"
      "ds_read_b32 v22, %[cb] offset:152\n"
      "s_waitcnt lgkmcnt(0)\n"
      "v_cmp_gt_u32 vcc, 4, v22\n"
      "s_cbranch_vccnz 91b\n"
      "92:\n"
      "s_mov_b32 %[n], %[nb]\n"
      "91:\n"
      "s_sub_u32 %[n], %[n], 1\n"
      "s_cbranch_scc1 92f\n"
      "ds_read_b32 v22, %[cb] offset:156\n"
      "ds_read_b128 v[32:35], %[pa] offset:6144\n"
      "ds_read_b128 v[36:39], %[pa] offset:7168\n"
      "s_waitcnt lgkmcnt(0)\n"
      "v_cmp_gt_u32 vcc, 8, v22\n"
      "s_cbranch_vccnz 91b\n"
      "92:\n"
      "v_cmp_lt_u32 vcc, 8, v22\n"
      "s_cbranch_vccnz 174b\n"
      "171:\n"
      "v_mfma_f32_16x16x32_bf16 v[40:43], v[32:35], v[136:139], v[40:43]\n"
      "v_mfma_f32_16x16x32_bf16 v[44:47], v[32:35], v[140:143], v[44:47]\n"
      "v_mfma_f32_16x16x32_bf16 v[48:51], v[32:35], v[144:147], v[48:51]\n"
      "v_mfma_f32_16x16x32_bf16 v[52:55], v[32:35], v[148:151], v[52:55]\n"
      "v_mfma_f32_16x16x32_bf16 v[56:59], v[32:35], v[152:155], v[56:59]\n"
      "v_mfma_f32_16x16x32_bf16 v[60:63], v[32:35], v[156:159], v[60:63]\n"
      "v_mfma_f32_16x16x32_bf16 v[64:67], v[32:35], v[160:163], v[64:67]\n"
      "v_mfma_f32_16x16x32_bf16 v[68:71], v[32:35], v[164:167], v[68:71]\n"
      "v_mfma_f32_16x16x32_bf16 v[72:75], v[36:39], v[136:139], v[72:75]\n"
      "v_mfma_f32_16x16x32_bf16 v[76:79], v[36:39], v[140:143], v[76:79]\n"
      "v_mfma_f32_16x16x32_bf16 v[80:83], v[36:39], v[144:147], v[80:83]\n"
      "v_mfma_f32_16x16x32_bf16 v[84:87], v[36:39], v[148:151], v[84:87]\n"
      "v_mfma_f32_16x16x32_bf16 v[88:91], v[36:39], v[152:155], v[88:91]\n"
      "v_mfma_f32_16x16x32_bf16 v[92:95], v[36:39], v[156:159], v[92:95]\n"
      "v_mfma_f32_16x16x32_bf16 v[96:99], v[36:39], v[160:163], v[96:99]\n"
      "v_mfma_f32_16x16x32_bf16 v[100:103], v[36:39], v[164:167], v[100:103]\n"
      : "={v[40:71]}"(o0), "={v[72:103]}"(o1), [n] "=&s"(n), [k] "=&s"(k), [m0] "=&s"(m0), [m1] "=&s"(m1), [ret] "=&s"(ret)
      : [a0] "{v9}"(a[0]), [a1] "{v10}"(a[1]), [a2] "{v11}"(a[2]), [a3] "{v12}"(a[3]), [a4] "{v13}"(a[4]), [a5] "{v14}"(a[5]), [a6] "{v15}"(a[6]),
        [a7] "{v16}"(a[7]), [pa] "{v17}"(pa), [cb] "{v18}"(cb), [one] "{v19}"(one), [fac] "s"(T_FAC), [nb] "s"(nb)
      : "vcc", "scc", "memory", "v20", "v21", "v22", "v23", "v24", "v25", "v26", "v27", "v28", "v29", "v30", "v31", "v32", "v33", "v34", "v35", "v36", "v37", "v38", "v39", "v104", "v105", "v106", "v107", "v108", "v109", "v110", "v111", "v112", "v113", "v114", "v115", "v116", "v117", "v118", "v119", "v120", "v121", "v122", "v123", "v124", "v125", "v126", "v127", "v128", "v129", "v130", "v131", "v132", "v133", "v134", "v135", "v136", "v137", "v138", "v139", "v140", "v141", "v142", "v143", "v144", "v145", "v146", "v147", "v148", "v149", "v150", "v151", "v152", "v153", "v154", "v155", "v156", "v157", "v158", "v159", "v160", "v161", "v162", "v163", "v164", "v165", "v166", "v167");
}
DI void ffn_part(const Params& p, int b, int s, int wave, unsigned t0, u64 tag);
DI void attn_wg(const Params& p, int b, int s, int wave, unsigned t0) {
  unsigned char* ws = p.ws;
  const rsrc_t rs = __builtin_amdgcn_make_buffer_rsrc(ws, 0, WS_BYTES, 0x00020000);
  const int w = wave & 3;
  const f32x4 z4 = {0.f, 0.f, 0.f, 0.f};

  if (wave < 2) { const int lane = fresh_lane(); *(LAS unsigned*)(T_CNT + wave * 256 + lane * 4) = wave == 0 && lane < 8 ? 8u : 0u; }
  bar();
  u64 tag = 0;
  if (wave != 8) tag = get_tag(ws);
  if (wave == 8) {
    const int lane = fresh_lane();
    unsigned vo[16];
#pragma unroll
    for (int r = 0; r < 16; ++r) vo[r] = 16 * (lane ^ fsw(r));
    const unsigned char* kv = (const unsigned char*)p.ckv + (size_t)(b * 4096 + s * ILV) * 1024;
    nap<1>();
    wait_until(t0 + T_PREF);
    __builtin_amdgcn_s_setprio(PRIO_L);
    bool watch = true;
    STAMP(62); STAMPD(31);
#pragma unroll 1
    for (int t = 0; t < 8; ++t) {
      bool told = false;
      if (t >= 3) {
        const unsigned c = t == 5 ? C_QRD : C_FREE + 16 * (t < 5 ? t - 3 : t - 4), want = t == 5 ? 4 : 8;
        auto free_now = [&]() { return __builtin_amdgcn_readfirstlane(lpeek(c)) >= want; };
        bool ok = free_now();
        if (!ok) { VMCNT(24); ok = free_now(); }
        if (!ok) { VMCNT(16); ok = free_now(); }
        if (!ok) { VMCNT(8); ok = free_now(); }
        if (!ok) { VMCNT(0); lsig(C_LAND + 16 * t - 16); told = true; lwait<true>(c, want); }
      }
      const unsigned dst = slot_of(t);
#pragma unroll
      for (int i = 0; i < 8; ++i) {
        DMA4("nt", kv + (4 * i / ILV) * (16 * ILV * 1024) + (4 * i % ILV) * 1024, dst + i * 4096, vo[4 * (i & 3)], vo[4 * (i & 3) + 1], vo[4 * (i & 3) + 2], vo[4 * (i & 3) + 3]);
        if (watch) {
          const unsigned hold = __builtin_amdgcn_readfirstlane(lpeek(C_HOLD));
          if (hold) { lwait<true>(C_HOLD, 2); watch = false; }
        }
      }
      if (!told) { VMCNT(32); lsig(C_LAND + 16 * t - 16); }
      kv += ILV > 32 ? 32768 : 16 * 32768;
    }
    STAMP(22);

    VMCNT(0);
    lsig(C_LAND + 16 * 7);
    STAMP(23);
    if (s >= 8 || b == 7) { DUMP(62); DUMP(31); DUMP(22); DUMP(23); }
    ROLE_END();
    return;
  }
  __builtin_amdgcn_s_setprio(2);
  if (wave == 9) {
    const int lane = fresh_lane();
    unsigned vo[16];
#pragma unroll
    for (int r = 0; r < 16; ++r) { vo[r] = (lane >> 5) * ASTR + (r & 3) * (4 * ASTR - 1024) + 16 * ((lane & 31) ^ fsw(r)); pin(vo[r]); }
    const unsigned char* qa = ws + WS_ARENA + 2 * ASTR + A_E + b * 512;
    wait_until(t0 + T_FLAG);
    wait_flags(rs, WS_FLAG1 + lane * 16, tag);
    STAMP(17); STAMPD(47);
    const unsigned tq = latest_time32<128>(ws + WS_HQ + (16 * b + s) * 512, t0, (unsigned)tag);
    STAMPV(47);
    wait_until(tq + T_QA - T_HOLD);
    lsig(C_HOLD);
    wait_until(tq + T_QA);
    STAMP(18);
#pragma unroll
    for (int i = 0; i < 8; ++i)
      DMA4("sc1", qa + i * (16 * ASTR), 3 * 32768 + i * 4096, vo[4 * (i & 3)], vo[4 * (i & 3) + 1], vo[4 * (i & 3) + 2], vo[4 * (i & 3) + 3]);
    lsig(C_HOLD);
    VMCNT(0);
    lsig(C_QST);
    STAMP(38);

    if (s >= 8 || b == 7) { lwait<true>(C_PRDY + 16 * 7, 8); nap<30>(); DUMP(17); DUMP(18); DUMP(38); DUMP(47); }
    ROLE_END();
    return;
  }
  if (wave >= 10) {
    __builtin_amdgcn_s_setprio(3);
    const int hh = wave - 10, lane = fresh_lane(), l15 = lane & 15, lq = lane >> 4;
#pragma unroll 1
    for (int real = DRY ? 0 : 1; real < 2; ++real) {
    const unsigned nb = real ? SPIN_L : 1;
    unsigned sa = T_SP + hh * 1024 + lane * 16, cb = real ? T_CNT : T_DRY, four = 4u;
    int mlo = real && lq == 0 ? rarena(16 * b + s, tag) + A_ML + hh * 256 + l15 * 16 : OOB;
    pin(sa); pin(cb); pin(four); pin(mlo);
    float ref = 0.f, l = 0.f;
    cwait<C_QST, 1, true>(cb, nb);
    static_for<0, 8>([&](auto tc) {
      constexpr int t = decltype(tc)::value;
      const unsigned nb_ = nb;
      if constexpr (t >= 4) cwait<C_FREE + 16 * (t == 4 ? 3 : t - 2), 8>(cb, nb);
      f32x4 p0, p1, p2, p3;
      unsigned c, n;
      asm volatile(SPIN_HEAD "ds_read_b32 %[c], %[cb] offset:%[o]\n\tds_read_b128 %[p0], %[sa] offset:%[s0]\n\tds_read_b128 %[p2], %[sa] offset:%[s2]\n\t"
                   "ds_read_b128 %[p1], %[sa] offset:%[s1]\n\tds_read_b128 %[p3], %[sa] offset:%[s3]\n\t" SPIN_TAIL2
                   : [c] "=&v"(c), [n] "=&s"(n), [p0] "=&v"(p0), [p1] "=&v"(p1), [p2] "=&v"(p2), [p3] "=&v"(p3)
                   : [cb] "v"(cb), [sa] "v"(sa), [o] "i"(C_SRDY + 16 * t - T_CNT), [w] "i"(4), [w2] "i"(t == 7 ? 1 : 4), [s0] "i"((t & 1) * 8192),
                     [s1] "i"((t & 1) * 8192 + 2048), [s2] "i"((t & 1) * 8192 + 4096), [s3] "i"((t & 1) * 8192 + 6144), [nb] "s"(nb_)
                   : "vcc", "scc", "memory");
      f32x4 a = (p0 - ref) + p2, c4 = (p1 - ref) + p3;
      const float m = fmaxf(fmaxf(fmaxf(a.x, a.y), fmaxf(a.z, a.w)), fmaxf(fmaxf(c4.x, c4.y), fmaxf(c4.z, c4.w)));
      unsigned sig = four;
      if constexpr (t == 0) {
        ref = xmax32(xmax16(m));
        a -= ref; c4 -= ref;
      } else if (__builtin_expect(__any(m > BUMP), 0)) {
        const float up = fmaxf(0.f, xmax32(xmax16(m)));
        const float fa = __builtin_amdgcn_exp2f(-up);
        *(LAS float*)(T_FAC + t * 128 + hh * 64 + l15 * 4) = fa;
        l *= fa;
        ref += up;
        a = (p0 - ref) + p2; c4 = (p1 - ref) + p3;
        sig = 4u + (1u << hh);
      }
      const v4u pf = {pk(__builtin_amdgcn_exp2f(a.x), __builtin_amdgcn_exp2f(a.y)), pk(__builtin_amdgcn_exp2f(a.z), __builtin_amdgcn_exp2f(a.w)),
                      pk(__builtin_amdgcn_exp2f(c4.x), __builtin_amdgcn_exp2f(c4.y)), pk(__builtin_amdgcn_exp2f(c4.z), __builtin_amdgcn_exp2f(c4.w))};
      *(LAS v4u*)((LAS char*)sa + (T_P - T_SP) + (t & 3) * 2048) = pf;
      csig<C_PRDY + 16 * t>(cb, sig);
#pragma unroll
      for (int e = 0; e < 4; ++e) {
        const unsigned u = pf[e];
        l = __builtin_amdgcn_fdot2_f32_bf16(__builtin_bit_cast(bf16x2, u), __builtin_bit_cast(bf16x2, 0x3F803F80u), l, false);
      }
    });
    stg(rs, mlo, v4u{__float_as_uint(ref), (unsigned)tag, __float_as_uint(xsum32(xsum16(l))), (unsigned)(tag >> 32)});
    }
    STAMP(24);
    if (hh == 0 && (s >= 8 || b == 7)) DUMP(24);
    ROLE_END();
    return;
  }
  if (wave < 4) {
    __builtin_amdgcn_s_setprio(PRIO_S);
    const int kh = wave & 1, ch = wave >> 1;
    STAMPD(19); STAMPD(20);
    int rqa = 0;
#pragma unroll 1
    for (int real = DRY ? 0 : 1; real < 2; ++real) {
    const unsigned nb = real ? SPIN_L : 1;
    const int lane = fresh_lane(), l15 = lane & 15, lq = lane >> 4;
    const int sg = (0x2130 >> (4 * lq)) & 3;
    unsigned ao[4];
#pragma unroll
    for (int k = 0; k < 4; ++k) ao[k] = l15 * 1024 + ch * 512 + 16 * ((4 * k + sg) ^ fsw(l15));
    unsigned sa = T_SP + wave * 2048 + lane * 16, cb = real ? T_CNT : T_DRY, one = 1u;
    unsigned al[4], ah[4];
#pragma unroll
    for (int k = 0; k < 4; ++k) { al[k] = ao[k] + kh * 16384; ah[k] = al[k] + 65536; pin(al[k]); pin(ah[k]); }
    int aro = real && wave == 0 && lane < 32 ? WS_ARR + (4 * lane + (b >> 1)) * 512 + (s & 3) * 128 + (b & 1) * 16 + (s >> 2) * 4 : OOB;
    pin(sa); pin(cb); pin(one); pin(aro);
    v4u af[2][8], qf[2][8];
    auto rd = [&](unsigned sl, int ks) { return lds128(sl < 65536 ? al[ks & 3] : ah[ks & 3], (sl & 32768) + (ks >> 2) * 256); };
    static_for<0, 2>([&](auto tc) {
      constexpr int t = decltype(tc)::value;
      cwait<C_LAND + 16 * t, 1, true>(cb, nb);
#pragma unroll
      for (int ks = 0; ks < 8; ++ks) af[t][ks] = rd(t * 32768, ks);
      csig<C_FREE + 16 * t>(cb, one);
    });
    unsigned qa[4];
#pragma unroll
    for (int k = 0; k < 4; ++k) { qa[k] = 3 * 32768 + ao[k]; pin(qa[k]); }
    cwait<C_QST, 1>(cb, nb);
    STAMPV(19);
    unsigned nx2 = cpeek<C_LAND + 32>(cb), nxt = cpeek<C_LAND + 48>(cb);
    f32x4 s0, s1;
#pragma unroll 1
    for (int sp = 0; sp < SPIN_G; ++sp) {
      rqa = sp + 1;
#pragma unroll
      for (int ks = 0; ks < 8; ++ks) {
#pragma unroll
        for (int hh = 0; hh < 2; ++hh) qf[hh][ks] = lds128(qa[ks & 3], hh * 16384 + (ks >> 2) * 256);
        __builtin_amdgcn_sched_barrier(0);
      }
      s0 = z4; s1 = z4;
#pragma unroll
      for (int ks = 0; ks < 8; ++ks) { s0 = mfma16(af[0][ks], qf[0][ks], s0); s1 = mfma16(af[0][ks], qf[1][ks], s1); }
      if (__builtin_expect(!__any(__builtin_isunordered(s0.x, s1.x)), 1) || !real || !(any_sent<8>(qf[0]) || any_sent<8>(qf[1]))) break;
#pragma unroll 1
      for (unsigned m = ((unsigned)__ballot(s0.x != s0.x) & 0xFFFFu) | ((unsigned)__ballot(s1.x != s1.x) << 16); m; m &= m - 1) {
        const int r = __builtin_ctz(m);
        asm volatile("s_mov_b32 m0, %2\n\ts_nop 0\n\tglobal_load_lds_dwordx4 %0, %1 sc1"
                     :: "v"((lane >> 5) * ASTR + 16 * ((lane & 31) ^ fsw(r))), "s"(ws + WS_ARENA + 2 * ASTR + A_E + b * 512 + r * (4 * ASTR)), "s"(3 * 32768 + r * 1024) : "memory");
      }
      VMCNT(0);
    }
    *(LAS f32x4*)sa = s0;
    *(LAS f32x4*)((LAS char*)sa + 1024) = s1;
    csig<C_SRDY>(cb, one);
    csig<C_QRD>(cb, one);
    STAMPV(20);
    auto refill = [&](v4u& a, auto slc, auto ksc) {
      constexpr unsigned sl = decltype(slc)::value;
      constexpr int ks = decltype(ksc)::value;
      rd_inplace<(sl & 32768) + (ks >> 2) * 256>(a, sl < 65536 ? al[ks & 3] : ah[ks & 3]);
    };
    static_for<0, 8>([&](auto k) { refill(af[0][decltype(k)::value], IntC<slot_c(2)>{}, k); });
    bool miss = __any(nx2 < 1);
    if (!miss) csig<C_FREE + 32>(cb, one);
    static_for<1, 8>([&](auto tc) {
      constexpr int t = decltype(tc)::value;
      const bool was = miss;
      if constexpr (t < 6) miss = __any(nxt < 1);
      unsigned use;
      peek2<C_PRDY + 16 * (t - 2) - T_CNT, C_LAND + 16 * (t < 5 ? t + 3 : 8) - T_CNT>(use, nxt, cb);
      static_for<0, 8>([&](auto k) {
        constexpr int ks = decltype(k)::value;
        score_pair<(t < 6 ? 9 : 9 - ks), ks == 0>(s0, s1, af[t & 1][ks], qf[0][ks], qf[1][ks]);
        if constexpr (t < 6) refill(af[t & 1][ks], IntC<slot_c(t + 2)>{}, k);
      });
      wait2<(t < 6 ? 8 : 0)>(use, nxt);
      if (__builtin_expect(__any(use < 8), 0)) cwait<C_PRDY + 16 * (t - 2), 8>(cb, nb);
      put_scores<(t & 1) * 8192>(sa, s0, s1);
      csig<C_SRDY + 16 * t>(cb, one);
      if constexpr (t == 7 && !ARR_EARLY) st32(rs, aro, now32() + (unsigned)tag);
      if constexpr (t < 4) { if (!miss) csig<C_FREE + 16 * (t + 2)>(cb, one); }
      if constexpr (t < 7) {
        if (was) {
          cwait<C_LAND + 16 * (t + 1), 1>(cb, nb);
          static_for<0, 8>([&](auto k) { refill(af[(t + 1) & 1][decltype(k)::value], IntC<slot_c(t + 1)>{}, k); });
          if constexpr (t < 5) csig<C_FREE + 16 * (t + 1)>(cb, one);
        }
      }
      if constexpr (t == 6 && ARR_EARLY) { if (wave == 0) st32(rs, aro, now32() + (unsigned)tag); }
    });
    asm volatile("s_waitcnt lgkmcnt(0)" ::: "memory");
    }
    STAMP(21);
    if (wave == 0 && (s >= 8 || b == 7)) { DUMP(19); DUMP(20); DUMP(21); DUMPC(50, rqa); }
  } else {
    __builtin_amdgcn_s_setprio(PRIO_PV);
#pragma unroll 1
    for (int real = DRY ? 0 : 1; real < 2; ++real) {
    const int lane = fresh_lane(), l15 = lane & 15, lq = lane >> 4;
    unsigned vl[8];
#pragma unroll
    for (int ct = 0; ct < 8; ++ct)
      vl[ct] = ((lane >> 2) * 1024 + w * 256 + 16 * ((2 * (ct & 1) + ((lane >> 1) & 1)) ^ fsw(lane >> 2)) + 8 * (lane & 1)) ^ ((ct >> 1) * 64);
    int po = real ? rarena(16 * lq + w, tag) + A_PART + (b * 16 + s) * 256 + l15 * 16 : OOB - 80 * ASTR;
    int cko = (lane & 2) || !real ? OOB : rarena(16 * b + s, tag) + A_CK + w * 512 + (2 * (lane & 8) + 4 * lq + ((lane >> 1) & 2) + (lane & 1)) * 16;
    pin(po); pin(cko);
    f32x32 acc[2];
    pv_loop(acc[0], acc[1], vl, T_P + lane * 16, real ? T_CNT : T_DRY, 1u, real ? SPIN_L : 1);
    unsigned ce[8], co[8];
#pragma unroll
    for (int ht = 0; ht < 2; ++ht)
#pragma unroll
      for (int r = 0; r < 4; ++r) {
        if (ht == 1 && r == 0) asm volatile("s_nop 1" : "+{v[72:103]}"(acc[1]) :: "memory");
        const v4u v = {pk(acc[ht][r], acc[ht][4 + r]), pk(acc[ht][8 + r], acc[ht][12 + r]), pk(acc[ht][16 + r], acc[ht][20 + r]),
                       pk(acc[ht][24 + r], acc[ht][28 + r])};
        stg(rs, po + (16 * ht + r) * 4 * ASTR, v);
        ce[4 * ht + r] = v.x ^ v.z; co[4 * ht + r] = v.y ^ v.w;
      }
    {
      unsigned e0, e1, o0, o1;
      xor_rows8(ce, e0, e1);
      xor_rows8(co, o0, o1);
      const bool odd = cko & 16;
      stg(rs, cko, v4u{odd ? e1 : e0, (unsigned)tag, odd ? o1 : o0, (unsigned)(tag >> 32)});
    }
    }
    STAMP(25);
  }
  if (b == 7 || s >= 8) { ROLE_END(); return; }
  ffn_part(p, b, s, wave, t0, tag);
  ROLE_END();
}

DI void ffn_part(const Params& p, int b, int s, int wave, unsigned t0, u64 tag) {
  const int lane = fresh_lane();
  unsigned char* ws = p.ws;
  const rsrc_t rs = __builtin_amdgcn_make_buffer_rsrc(ws, 0, WS_BYTES, 0x00020000);
  const int l15 = lane & 15, lq = lane >> 4, w = wave & 3;
  const f32x4 z4 = {0.f, 0.f, 0.f, 0.f};
  __builtin_amdgcn_s_setprio(2);
  const int fg = 8 * b + s, b8 = lane & 7, p4 = 4 * (lane & 3);
  const bool bvalid = l15 < 8;
  const int ch = l15 ^ ((lq << 2) | w), row0 = 4 * w + lq;
  bar();
  STAMP(52);
  nap<D_F>();
  v4u mm[4];
  const int mo = arena(32 + 4 * wave) + A_S + (4 * (l15 >> 3) + lq) * 128 + b8 * 16;
  auto load_mod = [&]() {
    FENCE();
#pragma unroll
    for (int i = 0; i < 4; ++i) mm[i] = ldg(rs, mo, ((i >> 1) * 32 + (i & 1)) * ASTR);
    FENCE();
  };
  load_mod();
  if (wave < 4) {
    dma_run<false>(p.w1, (row0 + (ch >> 2) * 128) * 3584 + fg * 64 + (ch & 3) * 16, W0 + w * 1024, 8, 16 * 3584, 4096);
  } else {
    const unsigned u = (32 * fg + row0) * 1024 + ch * 16;
    dma_run<true>(p.w2, u, W1 + w * 1024, 4, 256, 8192);
    dma_run<true>(p.w2, u + 16 * 1024, W1 + w * 1024 + 4096, 4, 256, 8192);
  }
#pragma unroll 1
  for (int sp = 0; sp < SPIN_G; ++sp) {
    if (!any_sent<4>(mm)) break;
    load_mod();
  }
  f32x4 sb[2], cb[2];
  sb[0] = up4(mm[0].xy) + up4(mm[1].xy); sb[1] = up4(mm[0].zw) + up4(mm[1].zw);
  cb[0] = up4(mm[2].xy) + up4(mm[3].xy) + 1.f; cb[1] = up4(mm[2].zw) + up4(mm[3].zw) + 1.f;
  pin(sb[0]); pin(sb[1]); pin(cb[0]); pin(cb[1]);
  if (wave == 7) wait_flags(rs, WS_FLAG2 + lane * 16, tag);
  VMCNT(0);
  bar();
  STAMP(28);
  v4u f1[2][2], f2[8];
#pragma unroll
  for (int tt = 0; tt < 2; ++tt) {
    const int c4 = 32 * (wave >> 1) + 2 * p4 + 4 * tt;
    const unsigned a0 = tr_addr(W0 + (wave & 1) * 16384, lane, c4, 0), a1 = tr_addr(W0 + (wave & 1) * 16384, lane, c4, 1);
    f1[0][tt] = tr_frag(a0, a1, 0); f1[1][tt] = tr_frag(a0, a1, 1);
    pin(f1[0][tt]); pin(f1[1][tt]);
  }
#pragma unroll
  for (int i = 0; i < 8; ++i) {
    const int c4 = 32 * (i >> 1) + 2 * p4 + 4 * (i & 1);
    f2[i] = tr_frag(tr_addr(W1 + w * 8192, lane, c4, 0), tr_addr(W1 + w * 8192, lane, c4, 1), 0);
    pin(f2[i]);
  }
  int xo = arena(wave) + A_X1 + (4 * (l15 >> 3) + lq) * 128 + b8 * 16;
  unsigned zw = L_A5 + wave * 1024 + lane * 16, zr = L_A5 + lane * 16;
  unsigned sw = lane < 8 ? L_A6 + lane * 80 + wave * 8 : L_JUNK, sr = L_A6 + b8 * 80;
  pin(xo); pin(zw); pin(zr); pin(sw); pin(sr);
  STAMP(55);
  STAMPD(40); STAMPD(15); STAMPD(36); STAMPD(37);
  int rx = 0;
  auto first = [&](v4u u) {
    const f32x4 d00 = mfma16(f1[0][0], u, z4), d10 = mfma16(f1[1][0], u, z4), d01 = mfma16(f1[0][1], u, z4), d11 = mfma16(f1[1][1], u, z4);
    f32x4 zz;
#pragma unroll
    for (int r = 0; r < 4; ++r) zz[r] = (bvalid ? d00[r] : d11[r]) + dppf<0x128>(bvalid ? d01[r] : d10[r]);
    return zz;
  };
  *(LAS f32x4*)((LAS char*)zw + 16384) = first(pk8(cb[0], cb[1]));
  *(LAS f32x4*)((LAS char*)zw + 24576) = first(pk8(sb[0], sb[1]));
  bar();
  f32x4 v1 = z4, v2 = z4;
  int yo = arena(fg) + A_YP + (16 * w + 4 * (l15 >> 3) + lq) * 128 + b8 * 16;
  if (wave >= 4) {
#pragma unroll
    for (int i = 0; i < 8; ++i) { v1 += u2f(lds128(zr, 16384 + i * 1024)); v2 += u2f(lds128(zr, 24576 + i * 1024)); }
  }
  pin(v1); pin(v2); pin(yo);
  STAMPV(40);
  if (wave == 0) wait_until(latest_time(ws + WS_HB + (16 * b + s) * 128, t0, (unsigned)tag) + T_C);
  bar();
  {
    v4u xv[1];
#pragma unroll 1
    for (int sp = 0; sp < SPIN_G; ++sp) { load_n<1>(rs, xo, 0, xv); rx = sp + 1; if (!any_sent<1>(xv)) break; }
    STAMPV(15);
    const f32x4 x0 = up4(xv[0].xy), x1 = up4(xv[0].zw);
    *(LAS f32x4*)zw = first(pk8(x0 * cb[0], x1 * cb[1]));
    const f32x4 s4 = x0 + x1, q4 = x0 * x0 + x1 * x1;
    float sx = (s4.x + s4.y) + (s4.z + s4.w), sq = (q4.x + q4.y) + (q4.z + q4.w);
    sx += dppf<0x128>(sx); sq += dppf<0x128>(sq);
    {
      auto y = __builtin_amdgcn_permlane16_swap(__float_as_uint(sx), __float_as_uint(sq), false, false);
      const float t = xsum32(__uint_as_float(y[0]) + __uint_as_float(y[1]));
      auto z = __builtin_amdgcn_permlane16_swap(__float_as_uint(t), __float_as_uint(t), false, false);
      *(LAS v2u*)sw = v2u{z[0], z[1]};
    }
    STAMPV(36);
    bar();
    if (wave >= 4) {
      STAMPV(37);
      f32x4 zp[8], st[4];
#pragma unroll
      for (int i = 0; i < 4; ++i) st[i] = u2f(lds128(sr, i * 16));
#pragma unroll
      for (int i = 0; i < 8; ++i) zp[i] = u2f(lds128(zr, i * 1024));
      __builtin_amdgcn_sched_barrier(0);
      const f32x4 ss = (st[0] + st[1]) + (st[2] + st[3]);
      const float mean = (ss.x + ss.z) * (1.f / 512), msq = (ss.y + ss.w) * (1.f / 512);
      const float rstd = __builtin_amdgcn_rsqf(fmaxf(msq - mean * mean, 0.f) + EPS);
      const f32x4 zs = ((zp[0] + zp[1]) + (zp[2] + zp[3])) + ((zp[4] + zp[5]) + (zp[6] + zp[7]));
      const f32x4 z = (zs - v1 * mean) * rstd + v2;
      const unsigned g0 = pk(z.x < 0.f ? 0.f : z.x, z.y < 0.f ? 0.f : z.y), g1 = pk(z.z < 0.f ? 0.f : z.z, z.w < 0.f ? 0.f : z.w);
      const unsigned o0 = (unsigned)__builtin_amdgcn_update_dpp(0, (int)g0, 0x128, 0xF, 0xF, true), o1 = (unsigned)__builtin_amdgcn_update_dpp(0, (int)g1, 0x128, 0xF, 0xF, true);
      const v4u bf = {bvalid ? g0 : o0, bvalid ? g1 : o1, bvalid ? o0 : g0, bvalid ? o1 : g1};
#pragma unroll
      for (int i = 0; i < 2; ++i) {
        const f32x4 a0 = mfma16(f2[4 * i], bf, z4), a1 = mfma16(f2[4 * i + 1], bf, z4), c0 = mfma16(f2[4 * i + 2], bf, z4), c1 = mfma16(f2[4 * i + 3], bf, z4);
        stg(rs, yo + i * 1024, pick(bvalid, pk8(a0, a1), pk8(c0, c1)));
      }
    }
  }
}

DI void first_requests() {
  u64 px, pc, pw;
  unsigned a, kp, wv, v0, v1, v2;
  asm volatile(
      "s_load_dwordx2 %[px], %[k], 0x0\n\ts_load_dwordx2 %[pc], %[k], 0x40\n\ts_load_dwordx2 %[pw], %[k], 0x48\n\t"
      "v_readfirstlane_b32 %[wv], %[t]\n\ts_and_b32 %[a], %[bx], 0x90\n\ts_cmpk_eq_u32 %[a], 0x90\n\ts_cbranch_scc0 3f\n\t"
      "s_bfe_u32 %[a], %[bx], 0x20005\n\ts_bfe_u32 %[kp], %[bx], 0x10003\n\ts_lshl1_add_u32 %[kp], %[a], %[kp]\n\t"
      "s_lshr_b32 %[wv], %[wv], 6\n\ts_cmp_gt_u32 %[wv], 3\n\ts_cbranch_scc1 2f\n\t"
      "v_bfe_u32 %[v0], %[t], 4, 2\n\tv_lshl_or_b32 %[v1], %[v0], 2, %[wv]\n\tv_xor_b32 %[v1], %[v1], %[t]\n\t"
      "v_mul_u32_u24 %[v0], 0x1800, %[v0]\n\tv_and_b32 %[v2], 7, %[v1]\n\tv_lshl_add_u32 %[v0], %[v2], 4, %[v0]\n\t"
      "v_bfe_u32 %[v2], %[v1], 3, 1\n\tv_lshl_add_u32 %[v0], %[v2], 10, %[v0]\n\t"
      "s_lshl4_add_u32 %[kp], %[kp], %[wv]\n\ts_mul_i32 %[kp], %[kp], 0x6000\n\ts_and_b32 %[a], %[bx], 7\n\ts_lshl_b32 %[a], %[a], 7\n\t"
      "s_add_u32 %[a], %[a], %[kp]\n\tv_add_u32 %[v0], %[a], %[v0]\n\ts_lshl_b32 %[wv], %[wv], 10\n\ts_add_u32 m0, %[wv], %[w2]\n\t"
      "v_add_u32 %[v1], 0x18000, %[v0]\n\tv_add_u32 %[v2], 0x30000, %[v0]\n\ts_waitcnt lgkmcnt(0)\n\t"
      "global_load_lds_dwordx4 %[v0], %[pw] nt\n\ts_add_u32 m0, m0, 0x1000\n\tv_add_u32 %[v0], 0x48000, %[v0]\n\t"
      "global_load_lds_dwordx4 %[v1], %[pw] nt\n\ts_add_u32 m0, m0, 0x1000\n\ts_nop 0\n\t"
      "global_load_lds_dwordx4 %[v2], %[pw] nt\n\ts_add_u32 m0, m0, 0x1000\n\ts_nop 0\n\t"
      "global_load_lds_dwordx4 %[v0], %[pw] nt\n\ts_branch 3f\n"
      "2:\n\tv_add_u32 %[v0], 0xffffff00, %[t]\n\tv_lshlrev_b32 %[v0], 4, %[v0]\n\tv_and_b32 %[v1], 0xfffffc70, %[v0]\n\t"
      "s_lshl_b32 %[kp], %[kp], 7\n\tv_or_b32 %[v1], %[kp], %[v1]\n\ts_lshl_b32 %[wv], %[wv], 11\n\ts_add_u32 m0, %[wv], %[raw]\n\t"
      "s_waitcnt lgkmcnt(0)\n\tglobal_load_lds_dwordx4 %[v1], %[pc]\n\ts_sub_u32 m0, m0, 0x400\n\ts_nop 0\n\t"
      "global_load_lds_dwordx4 %[v0], %[px]\n"
      "3:\n\ts_waitcnt lgkmcnt(0)"
      : [px] "=&s"(px), [pc] "=&s"(pc), [pw] "=&s"(pw), [a] "=&s"(a), [kp] "=&s"(kp), [wv] "=&s"(wv), [v0] "=&v"(v0), [v1] "=&v"(v1), [v2] "=&v"(v2)
      : [k] "s"(__builtin_amdgcn_kernarg_segment_ptr()), [bx] "s"(blockIdx.x), [t] "v"(threadIdx.x), [w2] "i"(W2), [raw] "i"(M_RAW + 1024 - 4 * 2048)
      : "scc", "memory");
}

__global__ __launch_bounds__(768) void adaln_mla_ffn_kernel(Params p) {
  STAMP(0);
  first_requests();
  const unsigned t0 = now32();
  const int g = blockIdx.x & 7, idx = blockIdx.x >> 3;
  const int wave = __builtin_amdgcn_readfirstlane(threadIdx.x >> 6), lane = threadIdx.x & 63;
  if (wave == 0 && idx < 16) {
    u64 pc;
    unsigned left;
    asm volatile("s_getpc_b64 %0\n99:\n\ts_mov_b32 %1, 98f-99b-4\n\t.subsection 2\n98:\n\t.subsection 0" : "=s"(pc), "=s"(left));
    asm volatile("s_mov_b32 m0, %2\n\ts_nop 0\n\tglobal_load_lds_dword %0, %1" :: "v"(min((idx + 16 * min(lane >> 1, 21)) * 128u + (lane & 1) * 64, left)), "s"(pc), "s"(L_JUNK) : "memory");
  }
  const int wi = idx & 15, l15 = lane & 15, lq = lane >> 4, b8 = lane & 7;
  const int kp = 2 * (wi >> 2) + (wi & 1);
  const unsigned vx = lane * 16;
  asm volatile("" :: "s"(p.x), "s"(p.ckv), "s"(p.wq), "s"(p.wuk), "s"(p.wuv), "s"(p.wo), "s"(p.w1), "s"(p.w2), "s"(p.c), "s"(p.wmod),
               "s"(p.out), "s"(p.ws));
  STAMP(39);
  STAMPD(2); STAMPD(3); STAMPD(32); STAMPD(1);
  const rsrc_t rs = __builtin_amdgcn_make_buffer_rsrc(p.ws, 0, WS_BYTES, 0x00020000);
  const int my = arena(16 * g + wi);
  if (idx >= 16 && wave == 3) {
#pragma unroll
    for (int n = 0; n < 5; ++n) stg(rs, my + vx + n * 1024, v4u{SENT, SENT, SENT, SENT});
  }
  if ((idx & 18) == 18) {
    const int r = wave - 4;
    if (wave >= 4) {
      unsigned raw = M_RAW + r * 2048 + vx, d = (lane >> 3) == g ? M_LNX + r * 256 + b8 * 32 : L_JUNK, sc = M_SC + r * 144 + lane * 2;
      unsigned rc = M_RAW + r * 2048 + 1024 + lane * 2;
      pin(raw); pin(d); pin(sc); pin(rc);
      VMCNT(0);
      const v4u xr = lds128(raw);
      const float cv = bflo(*(LAS u16*)rc);
      *(LAS u16*)sc = (u16)pk(cv * __builtin_amdgcn_rcpf(1.f + __expf(-cv)), 0.f);
      bar();
      const f32x4 v0 = up4(xr.xy), v2 = up4(xr.zw), sv4 = v0 + v2, qv4 = v0 * v0 + v2 * v2;
      const float mean = wave_sum((sv4.x + sv4.y) + (sv4.z + sv4.w)) * (1.f / 512);
      const float msq = wave_sum((qv4.x + qv4.y) + (qv4.z + qv4.w)) * (1.f / 512);
      const float rstd = __builtin_amdgcn_rsqf(fmaxf(msq - mean * mean, 0.f) + EPS);
      *(LAS f32x4*)d = (v0 - mean) * rstd;
      *(LAS f32x4*)((LAS char*)d + 16) = (v2 - mean) * rstd;
      STAMPV(32);
      bar();
    } else {
      const int p4 = 4 * (lane & 3);
      unsigned s0 = tr_addr(W2, lane, 16 * wave + p4, 0), s1 = tr_addr(W2, lane, 16 * wave + p4, 1), c0 = s0 ^ 128, c1 = s1 ^ 128;
      unsigned ba = M_SC + b8 * 144 + lq * 16, la = M_LNX + b8 * 256 + (16 * wave + 4 * lq) * 4;
      int o = l15 < 8 ? my + wave * 256 + (lq >> 1) * 128 + l15 * 16 + (lq & 1) * 8 : OOB;
      int hh = wave == 3 && lane < 32 ? WS_HH + (4 * lane + (g >> 2)) * 512 + (g & 3) * 128 + kp * 4 : OOB;
      const float one = kp == 0 ? 1.f : 0.f;
      *(LAS unsigned*)L_CNT = 0u;
      f32x4 sa = {0.f, 0.f, 0.f, 0.f}, ca = {one, one, one, one};
      pin(s0); pin(s1); pin(c0); pin(c1); pin(ba); pin(la); pin(o); pin(hh); pin(ca);
      u64 tag = 0;
      int fo = wave == 3 ? (lane < 32 ? WS_EFL + (4 * lane + (g >> 2)) * 256 + (g & 3) * 64 + kp * 8 : lane == 32 ? WS_FLAG1 + (16 * g + wi) * 8 : OOB) : OOB;
      pin(fo);
      if (wave == 3) { tag = get_tag(p.ws); VMCNT(5); } else { VMCNT(0); }
      STAMPV(32);
      bar();
      const unsigned tn = now32();
      const v4u b0 = lds128(ba), f0 = tr_frag(s0, s1, 0), g0 = tr_frag(c0, c1, 0), b1 = lds128(ba, 64), f1 = tr_frag(s0, s1, 1), g1 = tr_frag(c0, c1, 1);
      __builtin_amdgcn_sched_barrier(0);
      st32(rs, hh, tn + (unsigned)tag);
      STAMPV(2);
      sa = mfma16(f1, b1, mfma16(f0, b0, sa));
      ca = mfma16(g1, b1, mfma16(g0, b0, ca));
      if (wave == 3) {
        VMCNT(1);
        __builtin_amdgcn_raw_buffer_store_b64(v2u{(unsigned)tag, (unsigned)(tag >> 32)}, rs, fo, 0, SC1);
        STAMPV(1);
      }
      bar();
      sa += u2f(lds128(la)) * ca;
      __builtin_amdgcn_raw_buffer_store_b64(v2u{pk(sa.x, sa.y), pk(sa.z, sa.w)}, rs, o, 0, SC1);
      STAMPV(3);
    }
    bar();
  } else if (idx >= 16 && wave == 3) {
    const u64 tag = get_tag(p.ws);
    VMCNT(0);
    __builtin_amdgcn_raw_buffer_store_b64(v2u{(unsigned)tag, (unsigned)(tag >> 32)}, rs,
                                          lane < 2 ? WS_EFL + (16 * g + (wi | 2) - (wi & 1) + lane) * 256 + (wi & 1) * 8 : lane == 2 ? WS_FLAG1 + (16 * g + wi) * 8 : OOB, 0, SC1);
  } else if (wave >= 8) {
    const int w = wave & 3, hq = idx >= 16 ? wi >> 2 : wi >> 2, row = idx >= 16 ? 256 * (wi & 1) : 256 * ((wi >> 1) & 1) + 128 + 64 * (wi & 1);
    const unsigned vq = (row + 4 * w + lq) * 8192 + (4 * g + hq) * 256 + (l15 ^ ((lq << 2) | w)) * 16;
    wait_until(t0 + T_W);
    dma_run<true>(p.wq, vq, (idx >= 16 ? W0 : T_CNT + 1024) + w * 1024, idx >= 16 ? 16 : 4, 16 * 8192, idx >= 16 ? 4096 : 0);
    if (idx < 16) dma_run<true>(p.wuk, PF_UK == 4 ? vq : vq + (128 * (wi & 3) - row) * 8192, T_CNT + 1024 + w * 1024, PF_UK, 16 * 8192, 0);
  }
  FENCE();
  if (idx < 16) attn_wg(p, g, idx, wave, t0);
  else { weight_wg(p, g, wi, wave, fresh_lane(), t0); ROLE_END(); }
}

extern "C" void kernel_launch(void* const* d_in, const int* in_sizes, int n_in, void* d_out, int out_size, void* d_ws, size_t ws_size,
                              hipStream_t stream) {
  Params p;
  p.x = (const u16*)d_in[0]; p.ckv = (const u16*)d_in[1]; p.wq = (const u16*)d_in[2]; p.wuk = (const u16*)d_in[3];
  p.wuv = (const u16*)d_in[4]; p.wo = (const u16*)d_in[5]; p.w1 = (const u16*)d_in[7]; p.w2 = (const u16*)d_in[8];
  p.c = (const u16*)d_in[9]; p.wmod = (const u16*)d_in[10];
  p.out = (u16*)d_out; p.ws = (unsigned char*)d_ws + WS_BASE;
  (void)hipFuncSetAttribute((const void*)adaln_mla_ffn_kernel, hipFuncAttributeMaxDynamicSharedMemorySize, LDS_BYTES);
  adaln_mla_ffn_kernel<<<256, 768, LDS_BYTES, stream>>>(p);
}
```

```cpp
#include <hip/hip_runtime.h>
#include <hip/hip_bf16.h>
#pragma clang diagnostic ignored "-Wint-to-pointer-cast"
#pragma clang diagnostic ignored "-Wsometimes-uninitialized"

typedef unsigned short u16;
typedef unsigned long long u64;
typedef unsigned v4u __attribute__((ext_vector_type(4)));
typedef unsigned v2u __attribute__((ext_vector_type(2)));
typedef float f32x2 __attribute__((ext_vector_type(2)));
typedef float f32x4 __attribute__((ext_vector_type(4)));
typedef float f32x16 __attribute__((ext_vector_type(16)));
typedef short s16x4 __attribute__((ext_vector_type(4)));
typedef short bf16x8 __attribute__((ext_vector_type(8)));
typedef __bf16 bf16x2 __attribute__((ext_vector_type(2)));
#define GAS __attribute__((address_space(1)))
#define LAS __attribute__((address_space(3)))
#define DI __device__ __forceinline__
#define RLX_AGENT __ATOMIC_RELAXED, __HIP_MEMORY_SCOPE_AGENT
typedef __amdgpu_buffer_rsrc_t rsrc_t;

#define PK 99
#define PSCALE 0
#define ATT_DELAY 0
#define T_H 62
#define D_UK 8
#define T_QP 125
#define T_QA 118
#define T_B 80
#define T_C 158
#define T_D 286
#define ARR_EARLY 0
#define T_ML 62
#define T_PART 118
#define T_B6 80
#define D_F 0
#define D_ML 2
#define FLAG_AT 1
#define D_PREF 16
#define PRIO_S 2
#define PRIO_PV 1
#define PF_NT false
#define D_W 0
#define T_PREF 0
#define T_W 30
#define PF_EVEN 24
#define T_FILL 0
#define T_FLAG 260
#define WS_BASE 0
#define FILL_AUX 16
#define WWIN 7
#define DRY 0
#define T_EF 220
#define T_LQ 190
#define T_LQ2 100
#define PF_UK 8
#define T_UK 190
#define T_FP 150
#define T_RP 560
#define T_HOLD 45
#define ILV 8
#define PRIO_L 3
#define FP_AT 0
#define T_WO 380
#define FP_ALL 1
#define FP_W2 1
#define PART_AUX 16
#define LWS "37"
#define T_L 150
#define FILL_EARLY 1
#define D_FILL 0
#define PREF 1
#define WO_NT 1

constexpr int WS_FLAG1 = 4096, WS_FLAG2 = 8192;
constexpr int WS_EFL = 16384  , WS_HP = 81920, WS_HB = 98304  ,
              WS_HH = 131072  , WS_HQ = 196608  , WS_ARR = 262144  ;
constexpr int WS_ARENA = 327680, ASTR = 58624;
constexpr int A_S = 0, A_E = 1024, A_ML = 5120, A_X1 = 6144, A_OUTP = 7168, A_YP = 15360, A_PART = 23552, A_CK = 56320;
constexpr int NROT = 16, WS_BYTES = WS_ARENA + NROT * 128 * ASTR;
constexpr unsigned SENT = 0xFFFFFFFFu;
constexpr int OOB = 0x7FFFFF00;
constexpr int SC1 = 16;
constexpr int SPIN_H = 1 << 10;
constexpr int SPIN_G = 1 << 19;
constexpr int SPIN_L = 1 << 23;
constexpr int LDS_BYTES = 163840;
constexpr float EPS = 1e-5f;
constexpr float QSCALE = 0.08838834764831845f * 1.4426950408889634f;
constexpr float BUMP = 40.f;

#define STAMP(k) do { if (k == PK) *(volatile LAS unsigned*)(LDS_BYTES - 512) = (unsigned)__builtin_amdgcn_s_memrealtime(); } while (0)
#define STAMPV(k) STAMP(k)
#define STAMPD(k) do {} while (0)
#define STV(k) 0
#define DUMP(k) do {} while (0)
#define DUMPC(k, c) do {} while (0)

DI int fresh_lane() {
  int l;
  asm volatile("v_mbcnt_lo_u32_b32 %0, -1, 0\n\tv_mbcnt_hi_u32_b32 %0, -1, %0" : "=v"(l));
  return l;
}
template <class T> DI void pin(T& v) { asm volatile("" : "+v"(v)); }
DI float bflo(unsigned u) { return __uint_as_float(u << 16); }
DI float bfhi(unsigned u) { return __uint_as_float(u & 0xFFFF0000u); }
DI unsigned pk(float a, float b) { f32x2 v = {a, b}; return __builtin_bit_cast(unsigned, __builtin_convertvector(v, bf16x2)); }
DI f32x4 mfma16(v4u a, v4u b, f32x4 c) {
  return __builtin_amdgcn_mfma_f32_16x16x32_bf16(__builtin_bit_cast(bf16x8, a), __builtin_bit_cast(bf16x8, b), c, 0, 0, 0);
}
DI f32x16 mfma32(v4u a, v4u b, f32x16 c) {
  return __builtin_amdgcn_mfma_f32_32x32x16_bf16(__builtin_bit_cast(bf16x8, a), __builtin_bit_cast(bf16x8, b), c, 0, 0, 0);
}
DI v4u ldg(rsrc_t rs, int off, int so = 0) { return __builtin_amdgcn_raw_buffer_load_b128(rs, off, so, SC1); }
DI void stg(rsrc_t rs, int off, v4u v) { __builtin_amdgcn_raw_buffer_store_b128(v, rs, off, 0, SC1); }
DI unsigned umax4(v4u v) { return max(max(v.x, v.y), max(v.z, v.w)); }
DI v4u f2u(f32x4 v) { return __builtin_bit_cast(v4u, v); }
DI f32x4 u2f(v4u v) { return __builtin_bit_cast(f32x4, v); }
DI bool nan4(f32x4 v) { return __builtin_isunordered(v.x, v.y) | __builtin_isunordered(v.z, v.w); }
DI v4u lds128(unsigned a, int o = 0) { return *(LAS v4u*)((LAS char*)a + o); }
DI v2u ldstr(unsigned a, int o = 0) { return __builtin_bit_cast(v2u, __builtin_amdgcn_ds_read_tr16_b64_v4i16((LAS s16x4*)((LAS char*)a + o))); }
DI void bar() {
  __builtin_amdgcn_sched_barrier(0);
  asm volatile("s_waitcnt lgkmcnt(0)\n\ts_barrier" ::: "memory");
  __builtin_amdgcn_sched_barrier(0);
}
#define VMCNT_(n) asm volatile("s_waitcnt vmcnt(" #n ")" ::: "memory")
#define VMCNT(n) VMCNT_(n)
#define FENCE() asm volatile("" ::: "memory")
#define ROLE_END() __builtin_amdgcn_s_waitcnt(0x0F70)
template <bool NT> DI void dma16(const void* base, unsigned voff, unsigned dst) {
  if (NT) asm volatile("s_mov_b32 m0, %2\n\ts_nop 0\n\tglobal_load_lds_dwordx4 %0, %1 nt" :: "v"(voff), "s"(base), "s"(dst) : "memory");
  else asm volatile("s_mov_b32 m0, %2\n\ts_nop 0\n\tglobal_load_lds_dwordx4 %0, %1" :: "v"(voff), "s"(base), "s"(dst) : "memory");
}
template <bool NT> DI void dma_run(const void* base, unsigned voff, unsigned dst, int n, int gstr, int lstr) {
  const unsigned char* b = (const unsigned char*)base;
#pragma unroll 1
  for (int i = 0; i < n; ++i) { VMCNT(WWIN); dma16<NT>(b, voff, dst); b += gstr; dst += lstr; }
}
template <int N> DI void nap() { if (N > 0) __builtin_amdgcn_s_sleep(N); }
DI void lsig(unsigned a, unsigned v = 1u) { asm volatile("s_mov_b64 exec, 1\n\tds_add_u32 %0, %1\n\ts_mov_b64 exec, -1" :: "v"(a), "v"(v) : "memory"); }
DI unsigned lpeek(unsigned a) { return *(volatile LAS unsigned*)a; }
template <bool NAP = false> DI void lwait(unsigned a, unsigned want) {
  unsigned v, n = SPIN_L;
  if (NAP)
    asm volatile("1:\n\tds_read_b32 %0, %2\n\ts_waitcnt lgkmcnt(0)\n\tv_cmp_le_u32 vcc, %3, %0\n\ts_cbranch_vccnz 2f\n\t"
                 "s_sleep 1\n\ts_sub_u32 %1, %1, 1\n\ts_cbranch_scc0 1b\n2:" : "=&v"(v), "+s"(n) : "v"(a), "s"(want) : "vcc", "scc", "memory");
  else
    asm volatile("1:\n\tds_read_b32 %0, %2\n\ts_waitcnt lgkmcnt(0)\n\tv_cmp_le_u32 vcc, %3, %0\n\ts_cbranch_vccnz 2f\n\t"
                 "s_sub_u32 %1, %1, 1\n\ts_cbranch_scc0 1b\n2:" : "=&v"(v), "+s"(n) : "v"(a), "s"(want) : "vcc", "scc", "memory");
}
template <int CTRL> DI float dppf(float v) {
  return __builtin_bit_cast(float, __builtin_amdgcn_update_dpp(0, __builtin_bit_cast(int, v), CTRL, 0xF, 0xF, true));
}
DI float row16_max(float v) {
  asm volatile("s_nop 1\n\tv_max_f32_dpp %0, %0, %0 row_ror:8 row_mask:0xf bank_mask:0xf\n\ts_nop 1\n\tv_max_f32_dpp %0, %0, %0 row_ror:4 row_mask:0xf bank_mask:0xf\n\t"
               "s_nop 1\n\tv_max_f32_dpp %0, %0, %0 row_ror:2 row_mask:0xf bank_mask:0xf\n\ts_nop 1\n\tv_max_f32_dpp %0, %0, %0 row_ror:1 row_mask:0xf bank_mask:0xf" : "+v"(v));
  return v;
}
DI float row16_sum(float v) { v += dppf<0x128>(v); v += dppf<0x124>(v); v += dppf<0x122>(v); return v + dppf<0x121>(v); }
DI float xsum16(float v) { auto r = __builtin_amdgcn_permlane16_swap(__float_as_uint(v), __float_as_uint(v), false, false); return __uint_as_float(r[0]) + __uint_as_float(r[1]); }
DI float xsum32(float v) { auto r = __builtin_amdgcn_permlane32_swap(__float_as_uint(v), __float_as_uint(v), false, false); return __uint_as_float(r[0]) + __uint_as_float(r[1]); }
DI float xmax16(float v) { auto r = __builtin_amdgcn_permlane16_swap(__float_as_uint(v), __float_as_uint(v), false, false); return fmaxf(__uint_as_float(r[0]), __uint_as_float(r[1])); }
DI float xmax32(float v) { auto r = __builtin_amdgcn_permlane32_swap(__float_as_uint(v), __float_as_uint(v), false, false); return fmaxf(__uint_as_float(r[0]), __uint_as_float(r[1])); }
DI float wave_sum(float v) { return xsum32(xsum16(row16_sum(v))); }

#define XS8(d, a, b) "v_xor_b32_dpp " d ", " a ", " a " row_ror:8 row_mask:0xf bank_mask:0x3\n\tv_xor_b32_dpp " d ", " b ", " b " row_ror:8 row_mask:0xf bank_mask:0xc\n\t"
#define XS4(d, a, b) "v_xor_b32_dpp " d ", " a ", " a " row_half_mirror row_mask:0xf bank_mask:0x5\n\tv_xor_b32_dpp " d ", " b ", " b " row_half_mirror row_mask:0xf bank_mask:0xa\n\t"
#define XQ(d, p) "v_xor_b32_dpp " d ", " d ", " d " quad_perm:" p " row_mask:0xf bank_mask:0xf\n\t"
DI void xor_rows8(const unsigned (&v)[8], unsigned& r0, unsigned& r1) {
  unsigned t0, t1, t2, t3;
  asm volatile("s_nop 1\n\t" XS8("%2", "%6", "%10") XS8("%4", "%8", "%12") XS8("%3", "%7", "%11") XS8("%5", "%9", "%13")
               XS4("%0", "%2", "%4") XS4("%1", "%3", "%5") XQ("%0", "[2,3,0,1]") "s_nop 0\n\t" XQ("%1", "[2,3,0,1]") XQ("%0", "[1,0,3,2]")
               "s_nop 0\n\t" XQ("%1", "[1,0,3,2]")
               : "=&v"(r0), "=&v"(r1), "=&v"(t0), "=&v"(t1), "=&v"(t2), "=&v"(t3)
               : "v"(v[0]), "v"(v[1]), "v"(v[2]), "v"(v[3]), "v"(v[4]), "v"(v[5]), "v"(v[6]), "v"(v[7]));
}
DI unsigned xor_rows2(unsigned a, unsigned b) {
  unsigned r;
  asm volatile("s_nop 1\n\t" XS8("%0", "%1", "%2") "s_nop 1\n\tv_xor_b32_dpp %0, %0, %0 row_half_mirror row_mask:0xf bank_mask:0xf\n\ts_nop 1\n\t"
               XQ("%0", "[2,3,0,1]") "s_nop 1\n\t" XQ("%0", "[1,0,3,2]") "s_nop 1" : "=&v"(r) : "v"(a), "v"(b));
  return r;
}
template <int N> DI bool any_sent(const v4u (&r)[N]) {
  unsigned m = 0;
#pragma unroll
  for (int i = 0; i < N; ++i) m = max(m, umax4(r[i]));
  return __any(m == SENT);
}
template <int N, int AUX = SC1> DI void load_n(rsrc_t rs, int off0, int str, v4u (&r)[N]) {
  FENCE();
#pragma unroll
  for (int i = 0; i < N; ++i) r[i] = __builtin_amdgcn_raw_buffer_load_b128(rs, off0, i * str, AUX);
  __builtin_amdgcn_sched_barrier(0);
}
template <int N, int NAP = 0> DI void gather(rsrc_t rs, int off0, int str, v4u (&r)[N]) {
#pragma unroll 1
  for (int sp = 0; sp < SPIN_G; ++sp) {
    load_n<N>(rs, off0, str, r);
    if (!any_sent<N>(r)) break;
    nap<NAP>();
  }
}
DI u64 get_tag(const unsigned char* ws) {
  u64 e;
  asm volatile("s_load_dwordx2 %0, %1, 0x0\n\ts_waitcnt lgkmcnt(0)" : "=s"(e) : "s"(ws) : "memory");
  return e * 6364136223846793005ull + 1442695040888963407ull;
}
DI bool flags_up(v4u f, u64 tag) {
  const unsigned lo = (unsigned)tag, hi = (unsigned)(tag >> 32);
  return __all(f.x == lo && f.y == hi && f.z == lo && f.w == hi);
}
DI void wait_flags(rsrc_t rs, int off, u64 tag) {
#pragma unroll 1
  for (int sp = 0; sp < SPIN_G; ++sp) {
    FENCE();
    if (flags_up(ldg(rs, off), tag)) break;
    __builtin_amdgcn_s_sleep(8);
  }
  FENCE();
}
typedef unsigned s8u __attribute__((ext_vector_type(8)));
DI s8u sload8(const unsigned char* a) {
  s8u v;
  asm volatile("s_load_dwordx8 %0, %1, 0x0 glc\n\ts_waitcnt lgkmcnt(0)" : "=s"(v) : "s"(a) : "memory");
  return v;
}
DI unsigned max8(s8u v) { return max(max(max(v[0], v[1]), max(v[2], v[3])), max(max(v[4], v[5]), max(v[6], v[7]))); }
DI unsigned now32() { return (unsigned)__builtin_amdgcn_s_memrealtime(); }
constexpr unsigned HWIN = 1u << 12;
DI unsigned latest_time(const unsigned char* a, unsigned t0, unsigned key) {
  unsigned m = 0;
#pragma unroll 1
  for (int sp = 0; sp < SPIN_H; ++sp) {
    m = max8(sload8(a) - (key + t0 - HWIN));
    if (m < 2 * HWIN) return m + t0 - HWIN;
  }
  return t0;
}
template <int STR> DI unsigned latest_time32(const unsigned char* a, unsigned t0, unsigned key) {
  const unsigned kb = key + t0 - HWIN;
#pragma unroll 1
  for (int sp = 0; sp < SPIN_H; ++sp) {
    s8u u, v, x, y;
    asm volatile("s_load_dwordx8 %0, %4, 0x0 glc\n\ts_load_dwordx8 %1, %4, %5 glc\n\ts_load_dwordx8 %2, %4, %6 glc\n\t"
                 "s_load_dwordx8 %3, %4, %7 glc\n\ts_waitcnt lgkmcnt(0)"
                 : "=&s"(u), "=&s"(v), "=&s"(x), "=&s"(y) : "s"(a), "i"(STR), "i"(2 * STR), "i"(3 * STR) : "memory");
    const unsigned m = max(max(max8(u - kb), max8(v - kb)), max(max8(x - kb), max8(y - kb)));
    if (m < 2 * HWIN) return m + t0 - HWIN;
  }
  return t0;
}
DI void wait_until(unsigned t) {
#pragma unroll 1
  for (int sp = 0; sp < SPIN_H; ++sp) if ((int)(now32() - t) >= 0) break;
}
DI int arena(int wid) { return WS_ARENA + wid * ASTR; }
DI int rarena(int wid, u64 tag) { return WS_ARENA + ((int)(tag & (NROT - 1)) * 128 + wid) * ASTR; }

DI unsigned tr_addr(unsigned region, int lane, int col4, int t) {
  const int g = lane >> 4, q = (lane & 15) >> 2;
  return region + (8 * g + 4 * t + q) * 256 + 16 * ((col4 >> 3) ^ ((q << 2) | ((2 * g + t) & 3))) + 8 * ((col4 >> 2) & 1);
}
DI v4u tr_frag(unsigned a0, unsigned a1, int ks) {
  const v2u lo = ldstr(a0, ks * 8192), hi = ldstr(a1, ks * 8192);
  return v4u{lo.x, lo.y, hi.x, hi.y};
}
DI void panel_frags(unsigned region, int lane, int col_a, int col_b, v4u (&f)[2][4]) {
  const unsigned a0 = tr_addr(region, lane, col_a, 0), a1 = tr_addr(region, lane, col_a, 1);
  const unsigned b0 = tr_addr(region, lane, col_b, 0), b1 = tr_addr(region, lane, col_b, 1);
#pragma unroll
  for (int ks = 0; ks < 4; ++ks) { f[0][ks] = tr_frag(a0, a1, ks); f[1][ks] = tr_frag(b0, b1, ks); }
}
DI void gemv2(const v4u (&f)[2][4], unsigned act, f32x4& d0, f32x4& d1) {
  v4u bf[4];
#pragma unroll
  for (int ks = 0; ks < 4; ++ks) bf[ks] = lds128(act, ks * 64);
  __builtin_amdgcn_sched_barrier(0);
#pragma unroll
  for (int ks = 0; ks < 4; ++ks) { d0 = mfma16(f[0][ks], bf[ks], d0); d1 = mfma16(f[1][ks], bf[ks], d1); }
}
DI f32x4 up4(v2u v) { return f32x4{bflo(v.x), bfhi(v.x), bflo(v.y), bfhi(v.y)}; }
DI v4u pk8(f32x4 a, f32x4 b) { return v4u{pk(a.x, a.y), pk(a.z, a.w), pk(b.x, b.y), pk(b.z, b.w)}; }
DI v4u silu8(v4u cv) {
  v4u o;
#pragma unroll
  for (int e = 0; e < 4; ++e) {
    const float a = bflo(cv[e]), c = bfhi(cv[e]);
    o[e] = pk(a * __builtin_amdgcn_rcpf(1.f + __expf(-a)), c * __builtin_amdgcn_rcpf(1.f + __expf(-c)));
  }
  return o;
}

struct Params {
  const u16 *x, *ckv, *wq, *wuk, *wuv, *wo, *w1, *w2, *c, *wmod;
  u16* out;
  unsigned char* ws;
};

constexpr unsigned W0 = 0, W1 = 32768, W2 = 65536, L_SC = 98304  , L_A1 = L_SC + 4224  ,
                   L_A3 = L_A1 + 4224  , L_A4 = L_A3 + 2176, L_CNT = L_A4 + 2176,
                   M_RAW = 114688  , M_SC = M_RAW + 16384  , M_LNX = M_SC + 1152  ;
constexpr unsigned T_SP = 131072, T_P = T_SP + 16384, T_FAC = T_P + 8192, T_CNT = T_FAC + 1024,
                   L_A5 = 98304  , L_A6 = L_A5 + 8320  ;
constexpr unsigned C_LAND = T_CNT + 32, C_FREE = C_LAND + 4, C_SRDY = C_LAND + 8, C_PRDY = C_LAND + 12,
                   C_QST = T_CNT + 192, C_QRD = T_CNT + 196, C_HOLD = T_CNT + 200;
constexpr unsigned L_JUNK = LDS_BYTES - 256;

template <int N> DI f32x4 reduce_rows(rsrc_t rs, int off0, int offl, v4u sel, int& rounds) {
  f32x4 a;
#pragma unroll 1
  for (int sp = 0; sp < SPIN_G; ++sp) {
    v4u d[N];
    FENCE();
#pragma unroll
    for (int i = 0; i < N; ++i) d[i] = ldg(rs, i == N - 1 ? offl : off0, i * 16 * ASTR);
    __builtin_amdgcn_sched_barrier(0);
    f32x4 b = {0.f, 0.f, 0.f, 0.f};
    a = b;
#pragma unroll
    for (int i = 0; i < N; i += 2) { a = mfma16(sel, d[i], a); b = mfma16(sel, d[i + 1], b); }
    a += b;
    rounds = sp + 1;
    if (!__any(a.x != a.x) || !any_sent<N>(d)) break;
  }
#pragma unroll
  for (int r = 0; r < 4; ++r) { a[r] += dppf<0x128>(a[r]); a[r] += dppf<0x124>(a[r]); }
  return a;
}
DI v4u pick(bool lo, v4u a, v4u b) { return v4u{lo ? a.x : b.x, lo ? a.y : b.y, lo ? a.z : b.z, lo ? a.w : b.w}; }
DI void st32(rsrc_t rs, int off, unsigned v) { __builtin_amdgcn_raw_buffer_store_b32(v, rs, off, 0, SC1); }

DI void weight_wg(const Params& p, int g, int wi, int wave, int lane, unsigned t0) {
  unsigned char* ws = p.ws;
  const rsrc_t rs = __builtin_amdgcn_make_buffer_rsrc(ws, 0, WS_BYTES, 0x00020000);
  const int h = 4 * g + (wi >> 2), j = wi & 3, wid = 16 * g + wi, my = arena(wid);
  const int l15 = lane & 15, lq = lane >> 4, b8 = lane & 7;
  const bool bvalid = l15 < 8, isq = j < 2;
  const int w = wave & 3;
  const int ch = l15 ^ ((lq << 2) | w);
  const unsigned dst0 = w * 1024;
  const unsigned vuv = (128 * j + 16 * ((4 * w + lq) & 7) + (w >> 1)) * 8192 + h * 256 + ch * 16;
  const f32x4 z4 = {0.f, 0.f, 0.f, 0.f};

  if (wave >= 8) {
    if (!isq) wait_until(t0 + T_UK);
    if (!isq) dma_run<true>(p.wuk, (256 * (j - 2) + 4 * w + lq) * 8192 + h * 256 + ch * 16, W0 + dst0, 16, 16 * 8192, 4096);
    const unsigned key = (unsigned)get_tag(ws);
    int hp = isq && lane < 8 ? WS_HP + (4 * h + 2 + (lane & 1)) * 128 + j * 4 + (lane >> 1) * 8 : OOB;
    pin(hp);
    VMCNT(0);
    STAMP(44);
    if (!isq) lsig(L_CNT);
    bar();
    const unsigned tb1 = now32();
    nap<3>();
    if (w == 0) st32(rs, hp, tb1 + key);
    bar();
    v4u wo[4][8];
    const GAS unsigned char* wb = (const GAS unsigned char*)p.wo + (h * 128 + 8 * lq) * 1024 + w * 256 + (4 * (l15 & 3) + (l15 >> 2)) * 16;
    bar();
    if (FP_AT == 0 && g < 7 && (FP_ALL || !isq)) {
      const int e = 2 * (wi >> 2) + (j & 1);
      wait_until(tb1 + (isq ? T_FP + T_QP : T_FP));
      if (!isq) dma_run<false>(p.w1, (64 * e + 16 * w + (lane >> 5)) * 3584 + g * 512 + (lane & 31) * 16, W1 + dst0, 8, 2 * 3584, 0);
      if (FP_ALL == isq && FP_W2) dma_run<false>(p.w2, (256 * g + 32 * e + 8 * w) * 1024 + lane * 16, W1 + dst0, 8, 1024, 0);
    }
    wait_until(tb1 + (isq ? T_WO + T_QP : T_WO));
#pragma unroll
    for (int ks = 0; ks < 4; ++ks) {
      const GAS unsigned char* wk = wb + ((ks + j) & 3) * 32768;
#pragma unroll
      for (int r = 0; r < 8; ++r) wo[ks][r] = ks == 0 ? __builtin_nontemporal_load((const GAS v4u*)(wk + r * 1024)) : *(const GAS v4u*)(wk + r * 1024);
      if (ks == 0) VMCNT(0);
    }
    if (FP_AT == 2 && g < 7 && (FP_ALL || !isq)) {
      const int e = 2 * (wi >> 2) + (j & 1);
      wait_until(tb1 + (isq ? T_FP + T_QP : T_FP));
      if (!isq) dma_run<false>(p.w1, (64 * e + 16 * w + (lane >> 5)) * 3584 + g * 512 + (lane & 31) * 16, W1 + dst0, 8, 2 * 3584, 0);
      if (FP_ALL == isq && FP_W2) dma_run<false>(p.w2, (256 * g + 32 * e + 8 * w) * 1024 + lane * 16, W1 + dst0, 8, 1024, 0);
    }

    __builtin_amdgcn_sched_barrier(0);
#pragma unroll
    for (int ks = 0; ks < 4; ++ks) {
      v4u o[8];
#pragma unroll
      for (int dd = 0; dd < 4; ++dd)
#pragma unroll
        for (int rp = 0; rp < 4; ++rp) {
          o[2 * dd][rp] = __builtin_amdgcn_perm(wo[ks][2 * rp + 1][dd], wo[ks][2 * rp][dd], 0x05040100u);
          o[2 * dd + 1][rp] = __builtin_amdgcn_perm(wo[ks][2 * rp + 1][dd], wo[ks][2 * rp][dd], 0x07060302u);
        }
#pragma unroll
      for (int t = 0; t < 8; ++t) { wo[ks][t] = o[t]; pin(wo[ks][t]); }
    }
    STAMP(10);
    unsigned ba[4];
#pragma unroll
    for (int ks = 0; ks < 4; ++ks) { ba[ks] = L_A4 + b8 * 272 + ((ks + j) & 3) * 64 + lq * 16; pin(ba[ks]); }
    int oo = my + A_OUTP + (16 * w + 4 * (l15 >> 3) + lq) * 128 + b8 * 16;
    pin(oo);
    __builtin_amdgcn_s_setprio(2);
    bar();
    bar();
    v4u bf[4];
#pragma unroll
    for (int ks = 0; ks < 4; ++ks) bf[ks] = lds128(ba[ks]);
    v4u r0, r1;
#pragma unroll
    for (int tp = 0; tp < 4; ++tp) {
      f32x4 e = z4, o = z4;
#pragma unroll
      for (int ks = 0; ks < 4; ++ks) { e = mfma16(wo[ks][2 * tp], bf[ks], e); o = mfma16(wo[ks][2 * tp + 1], bf[ks], o); }
      r0[tp] = bvalid ? pk(e[0], o[0]) : pk(e[1], o[1]);
      r1[tp] = bvalid ? pk(e[2], o[2]) : pk(e[3], o[3]);
    }
    stg(rs, oo, r0);
    stg(rs, oo + 1024, r1);
    STAMP(14);
    if (wave == 8) { DUMP(10); DUMP(14); if (isq) DUMP(44); else { const unsigned stv60 = STV(44); DUMP(60); } }
    ROLE_END();
  } else if (wave >= 4) {
    __builtin_amdgcn_s_setprio(2);
    const int t = w * 64 + lane;
    const u64 tag = get_tag(ws);
    *(LAS unsigned*)(L_CNT + 4) = 0u;
    v4u msk;
    {
      const unsigned half = (lane & 1) ? 0xFFFF0000u : 0x0000FFFFu;
      const int dw = b8 >> 1;
      msk = v4u{dw == 0 ? half : 0u, dw == 1 ? half : 0u, dw == 2 ? half : 0u, dw == 3 ? half : 0u};
    }
    v4u sela = msk & (bvalid ? 0x3F803F80u : 0u), selb = msk & (bvalid ? 0u : 0x3F803F80u);
    int go = isq ? arena(64 * j + 16 * w + 4 * (lq >> 1) + 2 + (lq & 1)) + A_S + l15 * 16 : arena(4 * h) + A_E + t * 16;
    unsigned gd = isq ? L_A1 + b8 * 528 + (64 * w + 16 * (lq >> 1) + 8 * (l15 >> 3) + 4 * (lq & 1)) * 2
                      : L_A1 + b8 * 528 + (t >> 3) * 8;
    pin(msk); pin(sela); pin(selb); pin(go); pin(gd);
    STAMPD(34); STAMPD(46);
    if (w == 2) {
      wait_until(t0 + T_EF);
      wait_flags(rs, WS_EFL + wid * 256 + (isq ? l15 * 16 : 0), tag);
      STAMPV(46);
    }
    if (w == 3) {
      unsigned tr;
      if (isq) {
        tr = latest_time32<128>(ws + WS_HH + wid * 512, t0, (unsigned)tag) + T_H;
      } else {
        tr = latest_time(ws + WS_HP + wid * 128, t0, (unsigned)tag) + T_QP;
        lwait(L_CNT, 4);
        const unsigned tn = now32() + 8;
        if ((int)(tn - tr) > 0) tr = tn;
        st32(rs, WS_HQ + (2 * lane + j - 2) * 512 + (h & 3) * 128 + g * 4, tr + (unsigned)tag);
      }
      STAMPV(34);
      wait_until(tr);
    }
    bar();
    int rh = 0;
    if (isq) {
      f32x4 h0, h1;
#pragma unroll 1
      for (int sp = 0; sp < SPIN_G; ++sp) {
        v4u r[8];
        FENCE();
#pragma unroll
        for (int i = 0; i < 8; ++i) r[i] = ldg(rs, go + (i & 3) * 256, (i >> 2) * 8 * ASTR);
        __builtin_amdgcn_sched_barrier(0);
        h0 = mfma16(selb, r[5], mfma16(sela, r[4], mfma16(selb, r[1], mfma16(sela, r[0], z4))));
        h1 = mfma16(selb, r[7], mfma16(sela, r[6], mfma16(selb, r[3], mfma16(sela, r[2], z4))));
        rh = sp + 1;
        if (!__any(__builtin_isunordered(h0.x, h1.x)) || !any_sent<8>(r)) break;
      }
      *(LAS v2u*)gd = v2u{pk(h0.x, h0.y), pk(h0.z, h0.w)};
      *(LAS v2u*)((LAS char*)gd + 64) = v2u{pk(h1.x, h1.y), pk(h1.z, h1.w)};
    } else {
      f32x4 q;
#pragma unroll 1
      for (int sp = 0; sp < SPIN_G; ++sp) {
        v4u r[2];
        load_n<2>(rs, go, ASTR, r);
        q = u2f(r[0]) + u2f(r[1]);
        rh = sp + 1;
        if (!__any(nan4(q)) || !any_sent<2>(r)) break;
      }
      *(LAS v2u*)gd = v2u{pk(q.x, q.y), pk(q.z, q.w)};
    }
    STAMP(5);
    bar();
    bar();
    if (isq)
      *(LAS v4u*)(L_SC + (2 * w + (lane >> 5)) * 528 + (lane & 31) * 16) =
          silu8(*(const GAS v4u*)(p.c + (2 * w + (lane >> 5)) * 512 + j * 256 + (lane & 31) * 8));
    lsig(L_CNT + 4);
    if (w == 3) {
      nap<D_FILL>();
#pragma unroll 1
      for (int n = wid < 8 ? 6 : 7; n < (wid < 56 ? 23 : 15); ++n)
        stg(rs, my + lane * 16 + n * 1024, v4u{SENT, SENT, SENT, SENT});
      VMCNT(0);
      if (lane == 0) __hip_atomic_store((GAS u64*)(ws + WS_FLAG2 + wid * 8), tag, RLX_AGENT);
    }
    const int el = (4 * (wi & 1) + (lane & 3)) * 512 + 8 * (wid >> 1) + 4 * (lq & 1), pc = wi * 64 + (lane & 3) * 16;
    int gmo = w == 0 ? arena(4 * g) + A_S + pc + (lq & 1) * 8 : OOB - 100 * ASTR;
    pin(gmo);
    int mo = rarena(32 * w + (lane & 31), tag) + A_ML + h * 16;
    int po = rarena(wid, tag) + A_PART + w * 8192 + lane * 16;
    unsigned o3 = L_A3 + (2 * w + (lane >> 5)) * 272 + l15 * 16 + (lq & 1) * 8;
    int hb = lane < 16 ? WS_HB + (16 * lane + ((wi - 5 * g) & 15)) * 128 + g * 4 : OOB;
    int co = lane < 32 ? mo + (A_CK - A_ML) + j * 512 : OOB;
    int bp = 4 * lq + (bvalid ? 0 : 128);
    unsigned tlo = lane < 32 ? (unsigned)tag : 0u, thi = lane < 32 ? (unsigned)(tag >> 32) : 0u;
    pin(mo); pin(po); pin(o3); pin(hb); pin(co); pin(bp); pin(tlo); pin(thi);
    const unsigned ta = latest_time32<128>(ws + WS_ARR + (4 * h + w) * 512, t0, (unsigned)tag);
    STAMP(11);
    *(LAS unsigned*)(L_CNT + 16 + w * 4) = ta;
    wait_until(ta + T_ML);
    FENCE();
    v4u ml = ldg(rs, mo);
    wait_until(ta + T_PART);
    v4u d[8], ck;
    load_n<8, PART_AUX>(rs, po, 1024, d);
    ck = ldg(rs, co);
    __builtin_amdgcn_sched_barrier(0);
    if (__builtin_expect(__any(((ml.y ^ (unsigned)tag) | (ml.w ^ (unsigned)(tag >> 32))) != 0), 0)) {
#pragma unroll 1
      for (int sp = 0; sp < SPIN_G; ++sp) {
        FENCE();
        ml = ldg(rs, mo);
        if (__all(ml.y == (unsigned)tag && ml.w == (unsigned)(tag >> 32))) break;
      }
    }
    v4u sel[8];
    {
      const float r = __uint_as_float(ml.x), e = __builtin_amdgcn_exp2f(r - row16_max(r));
      const float wgt = e * __builtin_amdgcn_rcpf(row16_sum(e * __uint_as_float(ml.z)));
      const unsigned hi = pk(wgt, wgt);
      const float rest = wgt - bflo(hi);
      const int v = lane < 32 ? hi : pk(rest, rest);
      unsigned wv[8];
#pragma unroll
      for (int i = 0; i < 8; ++i) wv[i] = __builtin_amdgcn_ds_bpermute(bp + 16 * i, v);
      __builtin_amdgcn_sched_barrier(0);
#pragma unroll
      for (int i = 0; i < 8; ++i) { sel[i] = msk & wv[i]; pin(sel[i]); }
    }
    f32x4 oa, ob;
    int rp = 0;
#pragma unroll 1
    for (int sp = 0; sp < SPIN_G; ++sp) {
      oa = z4; ob = z4;
      unsigned xe = 0, xo = 0;
#pragma unroll
      for (int i = 0; i < 8; ++i) {
        if (i < 4) oa = mfma16(sel[i], d[i], oa); else ob = mfma16(sel[i], d[i], ob);
        xe = __builtin_amdgcn_bitop3_b32(xe, d[i].x, d[i].z, 0x96);
        xo = __builtin_amdgcn_bitop3_b32(xo, d[i].y, d[i].w, 0x96);
        __builtin_amdgcn_sched_barrier(0);
      }
      rp = sp + 1;
      unsigned x = xor_rows2(xe ^ ck.x, xo ^ ck.z);
      auto y = __builtin_amdgcn_permlane16_swap(x, x, false, false);
      x = y[0] ^ y[1];
      y = __builtin_amdgcn_permlane32_swap(x, x, false, false);
      if (!__any(((y[0] ^ y[1]) | (ck.y ^ tlo) | (ck.w ^ thi)) != 0)) break;
      load_n<8, PART_AUX>(rs, po, 1024, d);
      ck = ldg(rs, co);
    }
    ROLE_END();
    STAMP(12);
    {
      f32x4 s;
#pragma unroll
      for (int r = 0; r < 4; ++r) {
        auto x = __builtin_amdgcn_permlane32_swap(__float_as_uint(oa[r]), __float_as_uint(ob[r]), false, false);
        s[r] = __uint_as_float(x[0]) + __uint_as_float(x[1]);
      }
      *(LAS v2u*)o3 = v2u{pk(s.x, s.y), pk(s.z, s.w)};
    }
    bar();
    v2u gg[4], xb;
    v4u f2;
    if (w == 0) {
#pragma unroll
      for (int i = 0; i < 4; ++i) gg[i] = __builtin_amdgcn_raw_buffer_load_b64(rs, gmo, ((i >> 1) * 96 + (i & 1)) * ASTR, SC1);
      f2 = ldg(rs, WS_FLAG2 + lane * 16);
      xb = *(const GAS v2u*)(p.x + el);
    }
    unsigned tb = 0;
    if (w < 2) {
      const v4u tv = lds128(L_CNT + 16) - t0;
      tb = __builtin_amdgcn_readfirstlane(umax4(tv)) + t0 + T_PART + T_B6;
      const unsigned tn = now32() - 6;
      if ((int)(tn - tb) > 0) tb = tn;
    }
    if (w == 1) st32(rs, hb, tb + (unsigned)tag);
    bar();
    if (w == 0) {
#pragma unroll 1
      for (int sp = 0; sp < SPIN_G; ++sp) {
        unsigned m = 0;
#pragma unroll
        for (int i = 0; i < 4; ++i) m = max(m, max(gg[i].x, gg[i].y));
        if (!__any(m == SENT)) break;
        FENCE();
#pragma unroll
        for (int i = 0; i < 4; ++i) gg[i] = __builtin_amdgcn_raw_buffer_load_b64(rs, gmo, ((i >> 1) * 96 + (i & 1)) * ASTR, SC1);
      }
      if (!flags_up(f2, tag)) wait_flags(rs, WS_FLAG2 + lane * 16, tag);
      f32x4 x0 = up4(xb), ga = up4(gg[0]) + up4(gg[1]), gb = up4(gg[2]) + up4(gg[3]);
      pin(x0); pin(ga); pin(gb);
      STAMP(26);
      const bool act = lane < 32 && l15 < 4;
      v4u one = sela;
      int oo = arena(lane >> 2) + A_OUTP + g * 1024 + pc;
      int yo = oo + (A_YP - A_OUTP);
      int yl = lane < 32 ? yo : OOB - 48 * ASTR;
      int xo = act ? arena(g) + A_X1 + pc + lq * 8 : OOB;
      const rsrc_t ro = __builtin_amdgcn_make_buffer_rsrc(p.out, 0, 8192, 0x00020000);
      int fo = act ? el * 2 : OOB;
      pin(one); pin(oo); pin(yo); pin(yl); pin(xo); pin(fo);
      wait_until(tb + T_B);
      int r1 = 0, r2 = 0;
      const f32x4 x1 = x0 + ga * reduce_rows<8>(rs, oo, oo, one, r1);
      __builtin_amdgcn_raw_buffer_store_b64(v2u{pk(x1.x, x1.y), pk(x1.z, x1.w)}, rs, xo, 0, SC1);
      STAMP(27);
      wait_until(latest_time(ws + WS_HB + (128 + wid) * 128, t0, (unsigned)tag) + T_D);
      const f32x4 r = x1 + gb * reduce_rows<4>(rs, yo, yl, one, r2);
      STAMP(29);
      __builtin_amdgcn_raw_buffer_store_b64(v2u{pk(r.x, r.y), pk(r.z, r.w)}, ro, fo, 0, 0);
      if (wid == 0 && lane == 0) __hip_atomic_store((GAS u64*)ws, tag, RLX_AGENT);
      STAMP(30);
      DUMP(26); DUMP(27); DUMP(29); DUMP(30); DUMPC(42, r1); DUMPC(43, r2);
    }
    ROLE_END();
  } else {
    __builtin_amdgcn_s_setprio(2);
    const int p4 = 4 * (lane & 3);
    unsigned act = b8 * 272 + lq * 16;
    unsigned s0 = tr_addr(W2, lane, 16 * w + p4, 0), s1 = tr_addr(W2, lane, 16 * w + p4, 1);
    unsigned ba = L_SC + b8 * 528 + lq * 16;
    v4u f[2][4], f2[2][4];
    unsigned a1 = L_A1 + b8 * 528 + lq * 16;
    int o = my + A_E + (isq ? w * 1024 + (l15 >> 3) * 512 + lq * 128 + b8 * 16 : b8 * 512 + w * 128 + (l15 >> 3) * 64 + lq * 16);
    pin(o); pin(a1); pin(act);
    bar();
    const unsigned tb1 = now32();
    STAMP(4);
    if (isq) {
      panel_frags(W0, lane, 32 * w + p4, 32 * w + 16 + p4, f);
      panel_frags(W1, lane, 32 * w + p4, 32 * w + 16 + p4, f2);
      bar();
      f32x4 q0 = z4, q1 = z4;
      v4u bf[8];
#pragma unroll
      for (int ks = 0; ks < 8; ++ks) bf[ks] = lds128(a1, ks * 64);
      __builtin_amdgcn_sched_barrier(0);
#pragma unroll
      for (int ks = 0; ks < 4; ++ks) { q0 = mfma16(f[0][ks], bf[ks], q0); q1 = mfma16(f[1][ks], bf[ks], q1); }
#pragma unroll
      for (int ks = 0; ks < 4; ++ks) { q0 = mfma16(f2[0][ks], bf[4 + ks], q0); q1 = mfma16(f2[1][ks], bf[4 + ks], q1); }
      stg(rs, o, pick(bvalid, f2u(q0), f2u(q1)));
    } else {
#pragma unroll
      for (int tt = 0; tt < 2; ++tt) {
        const unsigned ka = W0 + (64 * w + 8 * (l15 >> 2) + 4 * tt + (l15 & 3)) * 256 + 16 * (lq ^ (((l15 & 3) << 2) | ((2 * (l15 >> 2) + tt) & 3)));
#pragma unroll
        for (int ks = 0; ks < 4; ++ks) { f[tt][ks] = lds128(ka ^ (ks * 64)); f2[tt][ks] = lds128(ka ^ (ks * 64), 8192); }
      }
      bar();
      f32x4 a0 = z4, a2 = z4, c0 = z4, c2 = z4;
      v4u bf[4];
#pragma unroll
      for (int ks = 0; ks < 4; ++ks) bf[ks] = lds128(a1, ks * 64);
      __builtin_amdgcn_sched_barrier(0);
#pragma unroll
      for (int ks = 0; ks < 4; ++ks) {
        a0 = mfma16(f[0][ks], bf[ks], a0); c0 = mfma16(f2[0][ks], bf[ks], c0);
        a2 = mfma16(f[1][ks], bf[ks], a2); c2 = mfma16(f2[1][ks], bf[ks], c2);
      }
      stg(rs, o, pk8(u2f(pick(bvalid, f2u(a0), f2u(c0))) * QSCALE, u2f(pick(bvalid, f2u(a2), f2u(c2))) * QSCALE));
    }
    STAMP(6);
    bar();
    wait_until(tb1 + (isq ? T_L + T_QP : T_L));
    dma_run<true>(p.wuv, vuv, W0 + dst0, 8, 2 * 8192, 4096);
    const bool m2 = isq;
    if (m2) dma_run<true>(p.wmod, (256 * j + 4 * w + lq + (ch >> 3) * 128) * 6144 + 2048 + h * 128 + (ch & 7) * 16, W2 + dst0, 8, 16 * 6144, 4096);
    VMCNT(0);
    lsig(L_CNT + 4);
    if (FP_AT == 1 && g < 7 && (FP_ALL || !isq)) {
      const int e = 2 * (wi >> 2) + (j & 1);
      wait_until(tb1 + (isq ? T_FP + T_QP : T_FP));
      if (!isq) dma_run<false>(p.w1, (64 * e + 16 * w + (lane >> 5)) * 3584 + g * 512 + (lane & 31) * 16, W1 + dst0, 8, 2 * 3584, 0);
      if (FP_ALL == isq && FP_W2) dma_run<false>(p.w2, (256 * g + 32 * e + 8 * w) * 1024 + lane * 16, W1 + dst0, 8, 1024, 0);
    }
    lwait<true>(L_CNT + 4, 8);
    STAMP(9);
    if (m2) {
      f32x4 m0 = z4, m1 = z4;
#pragma unroll 1
      for (int ks = 0; ks < 4; ++ks) {
        m0 = mfma16(tr_frag(s0 + ks * 8192, s1 + ks * 8192, 0), lds128(ba + ks * 64), m0);
        m1 = mfma16(tr_frag((s0 ^ 128) + ks * 8192, (s1 ^ 128) + ks * 8192, 0), lds128(ba + ks * 64, 256), m1);
      }
      m0 += m1;
      __builtin_amdgcn_raw_buffer_store_b64(v2u{pk(m0.x, m0.y), pk(m0.z, m0.w)}, rs, bvalid ? my + A_S + (2 * w + (lq >> 1)) * 128 + l15 * 16 + (lq & 1) * 8 : OOB, 0, SC1);
    }
    panel_frags(W0, lane, 32 * w + 2 * p4, 32 * w + 2 * p4 + 4, f);
    unsigned o4 = bvalid ? L_A4 + l15 * 272 + w * 64 + lq * 16 : L_JUNK;
    pin(o4);
    bar();
    STAMP(35);
    f32x4 c0 = z4, c1 = z4;
    gemv2(f, L_A3 + act, c0, c1);
    *(LAS v4u*)o4 = pk8(c0, c1);
    bar();
    STAMP(13);
  }
}

template <int OFF> DI void rd_inplace(v4u& a, unsigned ad) { asm volatile("ds_read_b128 %0, %1 offset:%2" : "+v"(a) : "v"(ad), "i"(OFF) : "memory"); }
template <int O0, int O1> DI void peek2(unsigned& a, unsigned& b, unsigned cb) {
  asm volatile("ds_read_b32 %0, %2 offset:%3\n\tds_read_b32 %1, %2 offset:%4" : "=&v"(a), "=&v"(b) : "v"(cb), "i"(O0), "i"(O1) : "memory");
}
template <int W> DI void wait2(unsigned& a, unsigned& b) { asm volatile("s_waitcnt lgkmcnt(%2)" : "+v"(a), "+v"(b) : "i"(W)); }
template <int W, bool FIRST> DI void score_pair(f32x4& s0, f32x4& s1, v4u a, v4u q0, v4u q1) {
  if constexpr (FIRST)
    asm volatile("s_waitcnt lgkmcnt(%5)\n\tv_mfma_f32_16x16x32_bf16 %0, %2, %3, 0\n\tv_mfma_f32_16x16x32_bf16 %1, %2, %4, 0"
                 : "=&v"(s0), "=&v"(s1) : "v"(a), "v"(q0), "v"(q1), "i"(W));
  else
    asm volatile("s_waitcnt lgkmcnt(%5)\n\tv_mfma_f32_16x16x32_bf16 %0, %2, %3, %0\n\tv_mfma_f32_16x16x32_bf16 %1, %2, %4, %1"
                 : "+v"(s0), "+v"(s1) : "v"(a), "v"(q0), "v"(q1), "i"(W));
}
template <int OFF> DI void put_scores(unsigned sa, f32x4 s0, f32x4 s1) {
  asm volatile("s_nop 4\n\tds_write_b128 %0, %1 offset:%3\n\tds_write_b128 %0, %2 offset:%4" :: "v"(sa), "v"(s0), "v"(s1), "i"(OFF), "i"(OFF + 1024) : "memory");
}
template <int V> struct IntC { static constexpr int value = V; };
template <int I, int N, class F> DI void static_for(F&& f) { if constexpr (I < N) { f(IntC<I>{}); static_for<I + 1, N>(f); } }
constexpr unsigned slot_c(int t) { return ((0x02310210u >> ((4 * t) & 31)) & 3) * 32768; }
template <unsigned A> DI void csig(unsigned cb, unsigned v) {
  asm volatile("s_mov_b64 exec, 1\n\tds_add_u32 %0, %1 offset:%2\n\ts_mov_b64 exec, -1" :: "v"(cb), "v"(v), "i"(A - T_CNT) : "memory");
}
template <unsigned A> DI unsigned cpeek(unsigned cb) { return *(volatile LAS unsigned*)((LAS char*)cb + (A - T_CNT)); }
#define SPIN_HEAD "s_mov_b32 %[n], %[nb]\n1:\n\ts_sub_u32 %[n], %[n], 1\n\ts_cbranch_scc1 2f\n\t"
#define SPIN_TAIL "s_waitcnt lgkmcnt(0)\n\tv_cmp_gt_u32 vcc, %[w], %[c]\n\ts_cbranch_vccnz 1b\n2:"
#define SPIN_TAIL2 "s_waitcnt lgkmcnt(0)\n\tv_cmp_gt_u32 vcc, %[w], %[c]\n\ts_cbranch_vccz 2f\n" \
                   "3:\n\ts_sub_u32 %[n], %[n], 1\n\ts_cbranch_scc1 2f\n\tds_read_b32 %[c], %[cb] offset:%[o]\n\ts_waitcnt lgkmcnt(0)\n\t" \
                   "v_cmp_gt_u32 vcc, %[w2], %[c]\n\ts_cbranch_vccnz 3b\n\ts_branch 1b\n2:"
template <unsigned A, unsigned W, bool NAP = false> DI void cwait(unsigned cb, unsigned nb) {
  unsigned c, n;
  if (NAP)
    asm volatile(SPIN_HEAD "s_sleep 1\n\tds_read_b32 %[c], %[cb] offset:%[o]\n\t" SPIN_TAIL
                 : [c] "=&v"(c), [n] "=&s"(n) : [cb] "v"(cb), [o] "i"(A - T_CNT), [w] "i"(W), [nb] "s"(nb) : "vcc", "scc", "memory");
  else
    asm volatile(SPIN_HEAD "ds_read_b32 %[c], %[cb] offset:%[o]\n\t" SPIN_TAIL
                 : [c] "=&v"(c), [n] "=&s"(n) : [cb] "v"(cb), [o] "i"(A - T_CNT), [w] "i"(W), [nb] "s"(nb) : "vcc", "scc", "memory");
}
constexpr unsigned T_DRY = T_CNT + 256;
DI constexpr unsigned fsw(int row) { return ((row & 3) << 2) | ((row >> 1) & 2) | ((row >> 3) & 1); }
DI unsigned slot_of(int t) { return ((0x02310210u >> ((4 * t) & 31)) & 3) * 32768; }
#define DMA4(AUX, base, dst, v0, v1, v2, v3) \
  asm volatile("s_mov_b32 m0, %5\n\ts_waitcnt vmcnt(" LWS ")\n\tglobal_load_lds_dwordx4 %0, %4 " AUX "\n\t" \
               "s_waitcnt vmcnt(" LWS ")\n\tglobal_load_lds_dwordx4 %1, %4 offset:1024 " AUX "\n\t" \
               "s_waitcnt vmcnt(" LWS ")\n\tglobal_load_lds_dwordx4 %2, %4 offset:2048 " AUX "\n\t" \
               "s_waitcnt vmcnt(" LWS ")\n\tglobal_load_lds_dwordx4 %3, %4 offset:3072 " AUX \
               :: "v"(v0), "v"(v1), "v"(v2), "v"(v3), "s"(base), "s"(dst) : "memory")

typedef float f32x32 __attribute__((ext_vector_type(32)));
DI void pv_loop(f32x32& o0, f32x32& o1, const unsigned (&a)[8], unsigned pa, unsigned cb, unsigned one, unsigned nb) {
  unsigned n, k;
  u64 m0, m1, ret;
  asm volatile(
      "s_branch 300f\n"
      "200:\n"
      "v_readfirstlane_b32 %[n], v20\n"
      "v_lshrrev_b32 v32, 4, %[pa]\n"
      "v_and_b32 v32, 48, v32\n"
      "v_add_u32 v32, %[k], v32\n"
      "v_add_u32 v32, %[fac], v32\n"
      "ds_read_b128 v[36:39], v32 offset:64\n"
      "ds_read_b128 v[32:35], v32\n"
      "s_waitcnt lgkmcnt(0)\n"
      "s_bitcmp1_b32 %[n], 0\n"
      "s_cbranch_scc0 93f\n"
      "v_pk_mul_f32 v[40:41], v[40:41], v[32:33]\n"
      "v_pk_mul_f32 v[42:43], v[42:43], v[34:35]\n"
      "v_pk_mul_f32 v[44:45], v[44:45], v[32:33]\n"
      "v_pk_mul_f32 v[46:47], v[46:47], v[34:35]\n"
      "v_pk_mul_f32 v[48:49], v[48:49], v[32:33]\n"
      "v_pk_mul_f32 v[50:51], v[50:51], v[34:35]\n"
      "v_pk_mul_f32 v[52:53], v[52:53], v[32:33]\n"
      "v_pk_mul_f32 v[54:55], v[54:55], v[34:35]\n"
      "v_pk_mul_f32 v[56:57], v[56:57], v[32:33]\n"
      "v_pk_mul_f32 v[58:59], v[58:59], v[34:35]\n"
      "v_pk_mul_f32 v[60:61], v[60:61], v[32:33]\n"
      "v_pk_mul_f32 v[62:63], v[62:63], v[34:35]\n"
      "v_pk_mul_f32 v[64:65], v[64:65], v[32:33]\n"
      "v_pk_mul_f32 v[66:67], v[66:67], v[34:35]\n"
      "v_pk_mul_f32 v[68:69], v[68:69], v[32:33]\n"
      "v_pk_mul_f32 v[70:71], v[70:71], v[34:35]\n"
      "93:\n"
      "s_bitcmp1_b32 %[n], 1\n"
      "s_cbranch_scc0 93f\n"
      "v_pk_mul_f32 v[72:73], v[72:73], v[36:37]\n"
      "v_pk_mul_f32 v[74:75], v[74:75], v[38:39]\n"
      "v_pk_mul_f32 v[76:77], v[76:77], v[36:37]\n"
      "v_pk_mul_f32 v[78:79], v[78:79], v[38:39]\n"
      "v_pk_mul_f32 v[80:81], v[80:81], v[36:37]\n"
      "v_pk_mul_f32 v[82:83], v[82:83], v[38:39]\n"
      "v_pk_mul_f32 v[84:85], v[84:85], v[36:37]\n"
      "v_pk_mul_f32 v[86:87], v[86:87], v[38:39]\n"
      "v_pk_mul_f32 v[88:89], v[88:89], v[36:37]\n"
      "v_pk_mul_f32 v[90:91], v[90:91], v[38:39]\n"
      "v_pk_mul_f32 v[92:93], v[92:93], v[36:37]\n"
      "v_pk_mul_f32 v[94:95], v[94:95], v[38:39]\n"
      "v_pk_mul_f32 v[96:97], v[96:97], v[36:37]\n"
      "v_pk_mul_f32 v[98:99], v[98:99], v[38:39]\n"
      "v_pk_mul_f32 v[100:101], v[100:101], v[36:37]\n"
      "v_pk_mul_f32 v[102:103], v[102:103], v[38:39]\n"
      "93:\n"
      "s_nop 4\n"
      "s_setpc_b64 %[ret]\n"
      "201:\n"
      "v_readfirstlane_b32 %[n], v22\n"
      "v_lshrrev_b32 v24, 4, %[pa]\n"
      "v_and_b32 v24, 48, v24\n"
      "v_add_u32 v24, %[k], v24\n"
      "v_add_u32 v24, %[fac], v24\n"
      "ds_read_b128 v[28:31], v24 offset:64\n"
      "ds_read_b128 v[24:27], v24\n"
      "s_waitcnt lgkmcnt(0)\n"
      "s_bitcmp1_b32 %[n], 0\n"
      "s_cbranch_scc0 93f\n"
      "v_pk_mul_f32 v[40:41], v[40:41], v[24:25]\n"
      "v_pk_mul_f32 v[42:43], v[42:43], v[26:27]\n"
      "v_pk_mul_f32 v[44:45], v[44:45], v[24:25]\n"
      "v_pk_mul_f32 v[46:47], v[46:47], v[26:27]\n"
      "v_pk_mul_f32 v[48:49], v[48:49], v[24:25]\n"
      "v_pk_mul_f32 v[50:51], v[50:51], v[26:27]\n"
      "v_pk_mul_f32 v[52:53], v[52:53], v[24:25]\n"
      "v_pk_mul_f32 v[54:55], v[54:55], v[26:27]\n"
      "v_pk_mul_f32 v[56:57], v[56:57], v[24:25]\n"
      "v_pk_mul_f32 v[58:59], v[58:59], v[26:27]\n"
      "v_pk_mul_f32 v[60:61], v[60:61], v[24:25]\n"
      "v_pk_mul_f32 v[62:63], v[62:63], v[26:27]\n"
      "v_pk_mul_f32 v[64:65], v[64:65], v[24:25]\n"
      "v_pk_mul_f32 v[66:67], v[66:67], v[26:27]\n"
      "v_pk_mul_f32 v[68:69], v[68:69], v[24:25]\n"
      "v_pk_mul_f32 v[70:71], v[70:71], v[26:27]\n"
      "93:\n"
      "s_bitcmp1_b32 %[n], 1\n"
      "s_cbranch_scc0 93f\n"
      "v_pk_mul_f32 v[72:73], v[72:73], v[28:29]\n"
      "v_pk_mul_f32 v[74:75], v[74:75], v[30:31]\n"
      "v_pk_mul_f32 v[76:77], v[76:77], v[28:29]\n"
      "v_pk_mul_f32 v[78:79], v[78:79], v[30:31]\n"
      "v_pk_mul_f32 v[80:81], v[80:81], v[28:29]\n"
      "v_pk_mul_f32 v[82:83], v[82:83], v[30:31]\n"
      "v_pk_mul_f32 v[84:85], v[84:85], v[28:29]\n"
      "v_pk_mul_f32 v[86:87], v[86:87], v[30:31]\n"
      "v_pk_mul_f32 v[88:89], v[88:89], v[28:29]\n"
      "v_pk_mul_f32 v[90:91], v[90:91], v[30:31]\n"
      "v_pk_mul_f32 v[92:93], v[92:93], v[28:29]\n"
      "v_pk_mul_f32 v[94:95], v[94:95], v[30:31]\n"
      "v_pk_mul_f32 v[96:97], v[96:97], v[28:29]\n"
      "v_pk_mul_f32 v[98:99], v[98:99], v[30:31]\n"
      "v_pk_mul_f32 v[100:101], v[100:101], v[28:29]\n"
      "v_pk_mul_f32 v[102:103], v[102:103], v[30:31]\n"
      "93:\n"
      "s_nop 4\n"
      "s_setpc_b64 %[ret]\n"
      "110:\n"
      "s_mov_b32 %[n], %[nb]\n"
      "91:\n"
      "s_sub_u32 %[n], %[n], 1\n"
      "s_cbranch_scc1 92f\n"
      "ds_read_b32 v22, %[cb] offset:60\n"
      "s_waitcnt lgkmcnt(0)\n"
      "v_cmp_gt_u32 vcc, 8, v22\n"
      "s_cbranch_vccnz 91b\n"
      "92:\n"
      "ds_read_b32 v22, %[cb] offset:60\n"
      "ds_read_b128 v[32:35], %[pa] offset:2048\n"
      "ds_read_b128 v[36:39], %[pa] offset:3072\n"
      "ds_read_b32 v23, %[cb] offset:80\n"
      "s_waitcnt lgkmcnt(0)\n"
      "v_cmp_lt_u32 vcc, 8, v22\n"
      "s_cbranch_vccz 111f\n"
      "114:\n"
      "s_movk_i32 %[k], 128\n"
      "s_call_b64 %[ret], 201b\n"
      "s_branch 111f\n"
      "120:\n"
      "s_mov_b32 %[n], %[nb]\n"
      "91:\n"
      "s_sub_u32 %[n], %[n], 1\n"
      "s_cbranch_scc1 92f\n"
      "ds_read_b32 v20, %[cb] offset:76\n"
      "s_waitcnt lgkmcnt(0)\n"
      "v_cmp_gt_u32 vcc, 8, v20\n"
      "s_cbranch_vccnz 91b\n"
      "92:\n"
      "ds_read_b32 v20, %[cb] offset:76\n"
      "ds_read_b128 v[24:27], %[pa] offset:4096\n"
      "ds_read_b128 v[28:31], %[pa] offset:5120\n"
      "ds_read_b32 v21, %[cb] offset:96\n"
      "s_waitcnt lgkmcnt(0)\n"
      "v_cmp_lt_u32 vcc, 8, v20\n"
      "s_cbranch_vccz 121f\n"
      "124:\n"
      "s_movk_i32 %[k], 256\n"
      "s_call_b64 %[ret], 200b\n"
      "s_branch 121f\n"
      "123:\n"
      "s_mov_b32 %[n], %[nb]\n"
      "91:\n"
      "s_sub_u32 %[n], %[n], 1\n"
      "s_cbranch_scc1 92f\n"
      "ds_read_b32 v22, %[cb] offset:80\n"
      "s_waitcnt lgkmcnt(0)\n"
      "v_cmp_gt_u32 vcc, 1, v22\n"
      "s_cbranch_vccnz 91b\n"
      "92:\n"
      "ds_read_b64_tr_b16 v[136:137], %[a0] offset:0\n"
      "ds_read_b64_tr_b16 v[138:139], %[a0] offset:16384\n"
      "ds_read_b64_tr_b16 v[140:141], %[a1] offset:0\n"
      "ds_read_b64_tr_b16 v[142:143], %[a1] offset:16384\n"
      "ds_read_b64_tr_b16 v[144:145], %[a2] offset:0\n"
      "ds_read_b64_tr_b16 v[146:147], %[a2] offset:16384\n"
      "ds_read_b64_tr_b16 v[148:149], %[a3] offset:0\n"
      "ds_read_b64_tr_b16 v[150:151], %[a3] offset:16384\n"
      "ds_read_b64_tr_b16 v[152:153], %[a4] offset:0\n"
      "ds_read_b64_tr_b16 v[154:155], %[a4] offset:16384\n"
      "ds_read_b64_tr_b16 v[156:157], %[a5] offset:0\n"
      "ds_read_b64_tr_b16 v[158:159], %[a5] offset:16384\n"
      "ds_read_b64_tr_b16 v[160:161], %[a6] offset:0\n"
      "ds_read_b64_tr_b16 v[162:163], %[a6] offset:16384\n"
      "ds_read_b64_tr_b16 v[164:165], %[a7] offset:0\n"
      "ds_read_b64_tr_b16 v[166:167], %[a7] offset:16384\n"
      "s_mov_b64 exec, 1\n"
      "ds_add_u32 %[cb], %[one] offset:84\n"
      "s_mov_b64 exec, -1\n"
      "130:\n"
      "s_mov_b32 %[n], %[nb]\n"
      "91:\n"
      "s_sub_u32 %[n], %[n], 1\n"
      "s_cbranch_scc1 92f\n"
      "ds_read_b32 v22, %[cb] offset:92\n"
      "s_waitcnt lgkmcnt(0)\n"
      "v_cmp_gt_u32 vcc, 8, v22\n"
      "s_cbranch_vccnz 91b\n"
      "92:\n"
      "ds_read_b32 v22, %[cb] offset:92\n"
      "ds_read_b128 v[32:35], %[pa] offset:6144\n"
      "ds_read_b128 v[36:39], %[pa] offset:7168\n"
      "ds_read_b32 v23, %[cb] offset:112\n"
      "s_waitcnt lgkmcnt(0)\n"
      "v_cmp_lt_u32 vcc, 8, v22\n"
      "s_cbranch_vccz 131f\n"
      "134:\n"
      "s_movk_i32 %[k], 384\n"
      "s_call_b64 %[ret], 201b\n"
      "s_branch 131f\n"
      "133:\n"
      "s_mov_b32 %[n], %[nb]\n"
      "91:\n"
      "s_sub_u32 %[n], %[n], 1\n"
      "s_cbranch_scc1 92f\n"
      "ds_read_b32 v20, %[cb] offset:96\n"
      "s_waitcnt lgkmcnt(0)\n"
      "v_cmp_gt_u32 vcc, 1, v20\n"
      "s_cbranch_vccnz 91b\n"
      "92:\n"
      "ds_read_b64_tr_b16 v[104:105], %[a0] offset:32768\n"
      "ds_read_b64_tr_b16 v[106:107], %[a0] offset:49152\n"
      "ds_read_b64_tr_b16 v[108:109], %[a1] offset:32768\n"
      "ds_read_b64_tr_b16 v[110:111], %[a1] offset:49152\n"
      "ds_read_b64_tr_b16 v[112:113], %[a2] offset:32768\n"
      "ds_read_b64_tr_b16 v[114:115], %[a2] offset:49152\n"
      "ds_read_b64_tr_b16 v[116:117], %[a3] offset:32768\n"
      "ds_read_b64_tr_b16 v[118:119], %[a3] offset:49152\n"
      "ds_read_b64_tr_b16 v[120:121], %[a4] offset:32768\n"
      "ds_read_b64_tr_b16 v[122:123], %[a4] offset:49152\n"
      "ds_read_b64_tr_b16 v[124:125], %[a5] offset:32768\n"
      "ds_read_b64_tr_b16 v[126:127], %[a5] offset:49152\n"
      "ds_read_b64_tr_b16 v[128:129], %[a6] offset:32768\n"
      "ds_read_b64_tr_b16 v[130:131], %[a6] offset:49152\n"
      "ds_read_b64_tr_b16 v[132:133], %[a7] offset:32768\n"
      "ds_read_b64_tr_b16 v[134:135], %[a7] offset:49152\n"
      "s_mov_b64 exec, 1\n"
      "ds_add_u32 %[cb], %[one] offset:100\n"
      "s_mov_b64 exec, -1\n"
      "140:\n"
      "s_mov_b32 %[n], %[nb]\n"
      "91:\n"
      "s_sub_u32 %[n], %[n], 1\n"
      "s_cbranch_scc1 92f\n"
      "ds_read_b32 v20, %[cb] offset:108\n"
      "s_waitcnt lgkmcnt(0)\n"
      "v_cmp_gt_u32 vcc, 8, v20\n"
      "s_cbranch_vccnz 91b\n"
      "92:\n"
      "ds_read_b32 v20, %[cb] offset:108\n"
      "ds_read_b128 v[24:27], %[pa] offset:0\n"
      "ds_read_b128 v[28:31], %[pa] offset:1024\n"
      "ds_read_b32 v21, %[cb] offset:128\n"
      "s_waitcnt lgkmcnt(0)\n"
      "v_cmp_lt_u32 vcc, 8, v20\n"
      "s_cbranch_vccz 141f\n"
      "144:\n"
      "s_movk_i32 %[k], 512\n"
      "s_call_b64 %[ret], 200b\n"
      "s_branch 141f\n"
      "143:\n"
      "s_mov_b32 %[n], %[nb]\n"
      "91:\n"
      "s_sub_u32 %[n], %[n], 1\n"
      "s_cbranch_scc1 92f\n"
      "ds_read_b32 v22, %[cb] offset:112\n"
      "s_waitcnt lgkmcnt(0)\n"
      "v_cmp_gt_u32 vcc, 1, v22\n"
      "s_cbranch_vccnz 91b\n"
      "92:\n"
      "v_add_u32 v138, 0x10000, %[a0]\n"
      "ds_read_b64_tr_b16 v[136:137], v138 offset:32768\n"
      "ds_read_b64_tr_b16 v[138:139], v138 offset:49152\n"
      "v_add_u32 v142, 0x10000, %[a1]\n"
      "ds_read_b64_tr_b16 v[140:141], v142 offset:32768\n"
      "ds_read_b64_tr_b16 v[142:143], v142 offset:49152\n"
      "v_add_u32 v146, 0x10000, %[a2]\n"
      "ds_read_b64_tr_b16 v[144:145], v146 offset:32768\n"
      "ds_read_b64_tr_b16 v[146:147], v146 offset:49152\n"
      "v_add_u32 v150, 0x10000, %[a3]\n"
      "ds_read_b64_tr_b16 v[148:149], v150 offset:32768\n"
      "ds_read_b64_tr_b16 v[150:151], v150 offset:49152\n"
      "v_add_u32 v154, 0x10000, %[a4]\n"
      "ds_read_b64_tr_b16 v[152:153], v154 offset:32768\n"
      "ds_read_b64_tr_b16 v[154:155], v154 offset:49152\n"
      "v_add_u32 v158, 0x10000, %[a5]\n"
      "ds_read_b64_tr_b16 v[156:157], v158 offset:32768\n"
      "ds_read_b64_tr_b16 v[158:159], v158 offset:49152\n"
      "v_add_u32 v162, 0x10000, %[a6]\n"
      "ds_read_b64_tr_b16 v[160:161], v162 offset:32768\n"
      "ds_read_b64_tr_b16 v[162:163], v162 offset:49152\n"
      "v_add_u32 v166, 0x10000, %[a7]\n"
      "ds_read_b64_tr_b16 v[164:165], v166 offset:32768\n"
      "ds_read_b64_tr_b16 v[166:167], v166 offset:49152\n"
      "s_mov_b64 exec, 1\n"
      "ds_add_u32 %[cb], %[one] offset:116\n"
      "s_mov_b64 exec, -1\n"
      "150:\n"
      "s_mov_b32 %[n], %[nb]\n"
      "91:\n"
      "s_sub_u32 %[n], %[n], 1\n"
      "s_cbranch_scc1 92f\n"
      "ds_read_b32 v22, %[cb] offset:124\n"
      "s_waitcnt lgkmcnt(0)\n"
      "v_cmp_gt_u32 vcc, 8, v22\n"
      "s_cbranch_vccnz 91b\n"
      "92:\n"
      "ds_read_b32 v22, %[cb] offset:124\n"
      "ds_read_b128 v[32:35], %[pa] offset:2048\n"
      "ds_read_b128 v[36:39], %[pa] offset:3072\n"
      "ds_read_b32 v23, %[cb] offset:144\n"
      "s_waitcnt lgkmcnt(0)\n"
      "v_cmp_lt_u32 vcc, 8, v22\n"
      "s_cbranch_vccz 151f\n"
      "154:\n"
      "s_movk_i32 %[k], 640\n"
      "s_call_b64 %[ret], 201b\n"
      "s_branch 151f\n"
      "153:\n"
      "s_mov_b32 %[n], %[nb]\n"
      "91:\n"
      "s_sub_u32 %[n], %[n], 1\n"
      "s_cbranch_scc1 92f\n"
      "ds_read_b32 v20, %[cb] offset:128\n"
      "s_waitcnt lgkmcnt(0)\n"
      "v_cmp_gt_u32 vcc, 1, v20\n"
      "s_cbranch_vccnz 91b\n"
      "92:\n"
      "v_add_u32 v106, 0x10000, %[a0]\n"
      "ds_read_b64_tr_b16 v[104:105], v106 offset:0\n"
      "ds_read_b64_tr_b16 v[106:107], v106 offset:16384\n"
      "v_add_u32 v110, 0x10000, %[a1]\n"
      "ds_read_b64_tr_b16 v[108:109], v110 offset:0\n"
      "ds_read_b64_tr_b16 v[110:111], v110 offset:16384\n"
      "v_add_u32 v114, 0x10000, %[a2]\n"
      "ds_read_b64_tr_b16 v[112:113], v114 offset:0\n"
      "ds_read_b64_tr_b16 v[114:115], v114 offset:16384\n"
      "v_add_u32 v118, 0x10000, %[a3]\n"
      "ds_read_b64_tr_b16 v[116:117], v118 offset:0\n"
      "ds_read_b64_tr_b16 v[118:119], v118 offset:16384\n"
      "v_add_u32 v122, 0x10000, %[a4]\n"
      "ds_read_b64_tr_b16 v[120:121], v122 offset:0\n"
      "ds_read_b64_tr_b16 v[122:123], v122 offset:16384\n"
      "v_add_u32 v126, 0x10000, %[a5]\n"
      "ds_read_b64_tr_b16 v[124:125], v126 offset:0\n"
      "ds_read_b64_tr_b16 v[126:127], v126 offset:16384\n"
      "v_add_u32 v130, 0x10000, %[a6]\n"
      "ds_read_b64_tr_b16 v[128:129], v130 offset:0\n"
      "ds_read_b64_tr_b16 v[130:131], v130 offset:16384\n"
      "v_add_u32 v134, 0x10000, %[a7]\n"
      "ds_read_b64_tr_b16 v[132:133], v134 offset:0\n"
      "ds_read_b64_tr_b16 v[134:135], v134 offset:16384\n"
      "160:\n"
      "s_mov_b32 %[n], %[nb]\n"
      "91:\n"
      "s_sub_u32 %[n], %[n], 1\n"
      "s_cbranch_scc1 92f\n"
      "ds_read_b32 v20, %[cb] offset:140\n"
      "s_waitcnt lgkmcnt(0)\n"
      "v_cmp_gt_u32 vcc, 8, v20\n"
      "s_cbranch_vccnz 91b\n"
      "92:\n"
      "ds_read_b32 v20, %[cb] offset:140\n"
      "ds_read_b128 v[24:27], %[pa] offset:4096\n"
      "ds_read_b128 v[28:31], %[pa] offset:5120\n"
      "s_waitcnt lgkmcnt(0)\n"
      "v_cmp_lt_u32 vcc, 8, v20\n"
      "s_cbranch_vccz 161f\n"
      "164:\n"
      "s_movk_i32 %[k], 768\n"
      "s_call_b64 %[ret], 200b\n"
      "s_branch 161f\n"
      "174:\n"
      "s_movk_i32 %[k], 896\n"
      "s_call_b64 %[ret], 201b\n"
      "s_branch 171f\n"
      "300:\n"
      "s_mov_b32 %[n], %[nb]\n"
      "91:\n"
      "s_sub_u32 %[n], %[n], 1\n"
      "s_cbranch_scc1 92f\n"
      "s_sleep 1\n"
      "ds_read_b32 v20, %[cb] offset:32\n"
      "s_waitcnt lgkmcnt(0)\n"
      "v_cmp_gt_u32 vcc, 1, v20\n"
      "s_cbranch_vccnz 91b\n"
      "92:\n"
      "ds_read_b64_tr_b16 v[72:73], %[a0] offset:0\n"
      "ds_read_b64_tr_b16 v[74:75], %[a0] offset:16384\n"
      "ds_read_b64_tr_b16 v[76:77], %[a1] offset:0\n"
      "ds_read_b64_tr_b16 v[78:79], %[a1] offset:16384\n"
      "ds_read_b64_tr_b16 v[80:81], %[a2] offset:0\n"
      "ds_read_b64_tr_b16 v[82:83], %[a2] offset:16384\n"
      "ds_read_b64_tr_b16 v[84:85], %[a3] offset:0\n"
      "ds_read_b64_tr_b16 v[86:87], %[a3] offset:16384\n"
      "ds_read_b64_tr_b16 v[88:89], %[a4] offset:0\n"
      "ds_read_b64_tr_b16 v[90:91], %[a4] offset:16384\n"
      "ds_read_b64_tr_b16 v[92:93], %[a5] offset:0\n"
      "ds_read_b64_tr_b16 v[94:95], %[a5] offset:16384\n"
      "ds_read_b64_tr_b16 v[96:97], %[a6] offset:0\n"
      "ds_read_b64_tr_b16 v[98:99], %[a6] offset:16384\n"
      "ds_read_b64_tr_b16 v[100:101], %[a7] offset:0\n"
      "ds_read_b64_tr_b16 v[102:103], %[a7] offset:16384\n"
      "s_mov_b64 exec, 1\n"
      "ds_add_u32 %[cb], %[one] offset:36\n"
      "s_mov_b64 exec, -1\n"
      "s_mov_b32 %[n], %[nb]\n"
      "91:\n"
      "s_sub_u32 %[n], %[n], 1\n"
      "s_cbranch_scc1 92f\n"
      "s_sleep 1\n"
      "ds_read_b32 v20, %[cb] offset:48\n"
      "s_waitcnt lgkmcnt(0)\n"
      "v_cmp_gt_u32 vcc, 1, v20\n"
      "s_cbranch_vccnz 91b\n"
      "92:\n"
      "ds_read_b64_tr_b16 v[136:137], %[a0] offset:32768\n"
      "ds_read_b64_tr_b16 v[138:139], %[a0] offset:49152\n"
      "ds_read_b64_tr_b16 v[140:141], %[a1] offset:32768\n"
      "ds_read_b64_tr_b16 v[142:143], %[a1] offset:49152\n"
      "ds_read_b64_tr_b16 v[144:145], %[a2] offset:32768\n"
      "ds_read_b64_tr_b16 v[146:147], %[a2] offset:49152\n"
      "ds_read_b64_tr_b16 v[148:149], %[a3] offset:32768\n"
      "ds_read_b64_tr_b16 v[150:151], %[a3] offset:49152\n"
      "ds_read_b64_tr_b16 v[152:153], %[a4] offset:32768\n"
      "ds_read_b64_tr_b16 v[154:155], %[a4] offset:49152\n"
      "ds_read_b64_tr_b16 v[156:157], %[a5] offset:32768\n"
      "ds_read_b64_tr_b16 v[158:159], %[a5] offset:49152\n"
      "ds_read_b64_tr_b16 v[160:161], %[a6] offset:32768\n"
      "ds_read_b64_tr_b16 v[162:163], %[a6] offset:49152\n"
      "ds_read_b64_tr_b16 v[164:165], %[a7] offset:32768\n"
      "ds_read_b64_tr_b16 v[166:167], %[a7] offset:49152\n"
      "s_mov_b64 exec, 1\n"
      "ds_add_u32 %[cb], %[one] offset:52\n"
      "s_mov_b64 exec, -1\n"
      "s_mov_b32 %[n], %[nb]\n"
      "91:\n"
      "s_sub_u32 %[n], %[n], 1\n"
      "s_cbranch_scc1 92f\n"
      "s_sleep 1\n"
      "ds_read_b32 v20, %[cb] offset:64\n"
      "s_waitcnt lgkmcnt(0)\n"
      "v_cmp_gt_u32 vcc, 1, v20\n"
      "s_cbranch_vccnz 91b\n"
      "92:\n"
      "v_add_u32 v106, 0x10000, %[a0]\n"
      "ds_read_b64_tr_b16 v[104:105], v106 offset:0\n"
      "ds_read_b64_tr_b16 v[106:107], v106 offset:16384\n"
      "v_add_u32 v110, 0x10000, %[a1]\n"
      "ds_read_b64_tr_b16 v[108:109], v110 offset:0\n"
      "ds_read_b64_tr_b16 v[110:111], v110 offset:16384\n"
      "v_add_u32 v114, 0x10000, %[a2]\n"
      "ds_read_b64_tr_b16 v[112:113], v114 offset:0\n"
      "ds_read_b64_tr_b16 v[114:115], v114 offset:16384\n"
      "v_add_u32 v118, 0x10000, %[a3]\n"
      "ds_read_b64_tr_b16 v[116:117], v118 offset:0\n"
      "ds_read_b64_tr_b16 v[118:119], v118 offset:16384\n"
      "v_add_u32 v122, 0x10000, %[a4]\n"
      "ds_read_b64_tr_b16 v[120:121], v122 offset:0\n"
      "ds_read_b64_tr_b16 v[122:123], v122 offset:16384\n"
      "v_add_u32 v126, 0x10000, %[a5]\n"
      "ds_read_b64_tr_b16 v[124:125], v126 offset:0\n"
      "ds_read_b64_tr_b16 v[126:127], v126 offset:16384\n"
      "v_add_u32 v130, 0x10000, %[a6]\n"
      "ds_read_b64_tr_b16 v[128:129], v130 offset:0\n"
      "ds_read_b64_tr_b16 v[130:131], v130 offset:16384\n"
      "v_add_u32 v134, 0x10000, %[a7]\n"
      "ds_read_b64_tr_b16 v[132:133], v134 offset:0\n"
      "ds_read_b64_tr_b16 v[134:135], v134 offset:16384\n"
      "s_mov_b64 exec, 1\n"
      "ds_add_u32 %[cb], %[one] offset:68\n"
      "s_mov_b64 exec, -1\n"
      "s_mov_b64 %[m0], 0\n"
      "s_mov_b32 %[n], %[nb]\n"
      "91:\n"
      "s_sub_u32 %[n], %[n], 1\n"
      "s_cbranch_scc1 92f\n"
      "s_sleep 1\n"
      "ds_read_b32 v20, %[cb] offset:40\n"
      "s_waitcnt lgkmcnt(0)\n"
      "v_cmp_gt_u32 vcc, 1, v20\n"
      "s_cbranch_vccnz 91b\n"
      "92:\n"
      "s_mov_b32 %[n], %[nb]\n"
      "91:\n"
      "s_sub_u32 %[n], %[n], 1\n"
      "s_cbranch_scc1 92f\n"
      "ds_read_b32 v20, %[cb] offset:44\n"
      "s_waitcnt lgkmcnt(0)\n"
      "v_cmp_gt_u32 vcc, 8, v20\n"
      "s_cbranch_vccnz 91b\n"
      "92:\n"
      "ds_read_b32 v20, %[cb] offset:44\n"
      "ds_read_b128 v[24:27], %[pa] offset:0\n"
      "ds_read_b128 v[28:31], %[pa] offset:1024\n"
      "ds_read_b32 v21, %[cb] offset:64\n"
      "s_waitcnt lgkmcnt(0)\n"
      "v_mfma_f32_16x16x32_bf16 v[40:43], v[24:27], v[72:75], 0\n"
      "v_mfma_f32_16x16x32_bf16 v[72:75], v[28:31], v[72:75], 0\n"
      "v_mfma_f32_16x16x32_bf16 v[44:47], v[24:27], v[76:79], 0\n"
      "v_mfma_f32_16x16x32_bf16 v[76:79], v[28:31], v[76:79], 0\n"
      "v_mfma_f32_16x16x32_bf16 v[48:51], v[24:27], v[80:83], 0\n"
      "v_mfma_f32_16x16x32_bf16 v[80:83], v[28:31], v[80:83], 0\n"
      "v_mfma_f32_16x16x32_bf16 v[52:55], v[24:27], v[84:87], 0\n"
      "v_mfma_f32_16x16x32_bf16 v[84:87], v[28:31], v[84:87], 0\n"
      "ds_read_b32 v22, %[cb] offset:60\n"
      "ds_read_b128 v[32:35], %[pa] offset:2048\n"
      "ds_read_b128 v[36:39], %[pa] offset:3072\n"
      "ds_read_b32 v23, %[cb] offset:80\n"
      "v_mfma_f32_16x16x32_bf16 v[56:59], v[24:27], v[88:91], 0\n"
      "v_mfma_f32_16x16x32_bf16 v[88:91], v[28:31], v[88:91], 0\n"
      "v_mfma_f32_16x16x32_bf16 v[60:63], v[24:27], v[92:95], 0\n"
      "v_mfma_f32_16x16x32_bf16 v[92:95], v[28:31], v[92:95], 0\n"
      "v_mfma_f32_16x16x32_bf16 v[64:67], v[24:27], v[96:99], 0\n"
      "v_mfma_f32_16x16x32_bf16 v[96:99], v[28:31], v[96:99], 0\n"
      "v_mfma_f32_16x16x32_bf16 v[68:71], v[24:27], v[100:103], 0\n"
      "v_mfma_f32_16x16x32_bf16 v[100:103], v[28:31], v[100:103], 0\n"
      "s_waitcnt lgkmcnt(0)\n"
      "v_cmp_ne_u32 vcc, 8, v22\n"
      "s_cbranch_vccnz 110b\n"
      "111:\n"
      "v_cmp_eq_u32 %[m1], 0, v23\n"
      "v_mfma_f32_16x16x32_bf16 v[40:43], v[32:35], v[136:139], v[40:43]\n"
      "v_mfma_f32_16x16x32_bf16 v[72:75], v[36:39], v[136:139], v[72:75]\n"
      "ds_read_b64_tr_b16 v[136:137], %[a0] offset:0\n"
      "ds_read_b64_tr_b16 v[138:139], %[a0] offset:16384\n"
      "v_mfma_f32_16x16x32_bf16 v[44:47], v[32:35], v[140:143], v[44:47]\n"
      "v_mfma_f32_16x16x32_bf16 v[76:79], v[36:39], v[140:143], v[76:79]\n"
      "ds_read_b64_tr_b16 v[140:141], %[a1] offset:0\n"
      "ds_read_b64_tr_b16 v[142:143], %[a1] offset:16384\n"
      "v_mfma_f32_16x16x32_bf16 v[48:51], v[32:35], v[144:147], v[48:51]\n"
      "v_mfma_f32_16x16x32_bf16 v[80:83], v[36:39], v[144:147], v[80:83]\n"
      "ds_read_b64_tr_b16 v[144:145], %[a2] offset:0\n"
      "ds_read_b64_tr_b16 v[146:147], %[a2] offset:16384\n"
      "v_mfma_f32_16x16x32_bf16 v[52:55], v[32:35], v[148:151], v[52:55]\n"
      "v_mfma_f32_16x16x32_bf16 v[84:87], v[36:39], v[148:151], v[84:87]\n"
      "ds_read_b64_tr_b16 v[148:149], %[a3] offset:0\n"
      "ds_read_b64_tr_b16 v[150:151], %[a3] offset:16384\n"
      "ds_read_b32 v20, %[cb] offset:76\n"
      "ds_read_b128 v[24:27], %[pa] offset:4096\n"
      "ds_read_b128 v[28:31], %[pa] offset:5120\n"
      "ds_read_b32 v21, %[cb] offset:96\n"
      "v_mfma_f32_16x16x32_bf16 v[56:59], v[32:35], v[152:155], v[56:59]\n"
      "v_mfma_f32_16x16x32_bf16 v[88:91], v[36:39], v[152:155], v[88:91]\n"
      "ds_read_b64_tr_b16 v[152:153], %[a4] offset:0\n"
      "ds_read_b64_tr_b16 v[154:155], %[a4] offset:16384\n"
      "v_mfma_f32_16x16x32_bf16 v[60:63], v[32:35], v[156:159], v[60:63]\n"
      "v_mfma_f32_16x16x32_bf16 v[92:95], v[36:39], v[156:159], v[92:95]\n"
      "ds_read_b64_tr_b16 v[156:157], %[a5] offset:0\n"
      "ds_read_b64_tr_b16 v[158:159], %[a5] offset:16384\n"
      "v_mfma_f32_16x16x32_bf16 v[64:67], v[32:35], v[160:163], v[64:67]\n"
      "v_mfma_f32_16x16x32_bf16 v[96:99], v[36:39], v[160:163], v[96:99]\n"
      "ds_read_b64_tr_b16 v[160:161], %[a6] offset:0\n"
      "ds_read_b64_tr_b16 v[162:163], %[a6] offset:16384\n"
      "v_mfma_f32_16x16x32_bf16 v[68:71], v[32:35], v[164:167], v[68:71]\n"
      "v_mfma_f32_16x16x32_bf16 v[100:103], v[36:39], v[164:167], v[100:103]\n"
      "ds_read_b64_tr_b16 v[164:165], %[a7] offset:0\n"
      "ds_read_b64_tr_b16 v[166:167], %[a7] offset:16384\n"
      "s_cmp_lg_u64 %[m1], 0\n"
      "s_cbranch_scc1 112f\n"
      "s_mov_b64 exec, 1\n"
      "ds_add_u32 %[cb], %[one] offset:84\n"
      "s_mov_b64 exec, -1\n"
      "112:\n"
      "s_waitcnt lgkmcnt(8)\n"
      "v_cmp_ne_u32 vcc, 8, v20\n"
      "s_cbranch_vccnz 120b\n"
      "121:\n"
      "v_cmp_eq_u32 %[m0], 0, v21\n"
      "v_mfma_f32_16x16x32_bf16 v[40:43], v[24:27], v[104:107], v[40:43]\n"
      "v_mfma_f32_16x16x32_bf16 v[72:75], v[28:31], v[104:107], v[72:75]\n"
      "ds_read_b64_tr_b16 v[104:105], %[a0] offset:32768\n"
      "ds_read_b64_tr_b16 v[106:107], %[a0] offset:49152\n"
      "v_mfma_f32_16x16x32_bf16 v[44:47], v[24:27], v[108:111], v[44:47]\n"
      "v_mfma_f32_16x16x32_bf16 v[76:79], v[28:31], v[108:111], v[76:79]\n"
      "ds_read_b64_tr_b16 v[108:109], %[a1] offset:32768\n"
      "ds_read_b64_tr_b16 v[110:111], %[a1] offset:49152\n"
      "v_mfma_f32_16x16x32_bf16 v[48:51], v[24:27], v[112:115], v[48:51]\n"
      "v_mfma_f32_16x16x32_bf16 v[80:83], v[28:31], v[112:115], v[80:83]\n"
      "ds_read_b64_tr_b16 v[112:113], %[a2] offset:32768\n"
      "ds_read_b64_tr_b16 v[114:115], %[a2] offset:49152\n"
      "v_mfma_f32_16x16x32_bf16 v[52:55], v[24:27], v[116:119], v[52:55]\n"
      "v_mfma_f32_16x16x32_bf16 v[84:87], v[28:31], v[116:119], v[84:87]\n"
      "ds_read_b64_tr_b16 v[116:117], %[a3] offset:32768\n"
      "ds_read_b64_tr_b16 v[118:119], %[a3] offset:49152\n"
      "ds_read_b32 v22, %[cb] offset:92\n"
      "ds_read_b128 v[32:35], %[pa] offset:6144\n"
      "ds_read_b128 v[36:39], %[pa] offset:7168\n"
      "ds_read_b32 v23, %[cb] offset:112\n"
      "v_mfma_f32_16x16x32_bf16 v[56:59], v[24:27], v[120:123], v[56:59]\n"
      "v_mfma_f32_16x16x32_bf16 v[88:91], v[28:31], v[120:123], v[88:91]\n"
      "ds_read_b64_tr_b16 v[120:121], %[a4] offset:32768\n"
      "ds_read_b64_tr_b16 v[122:123], %[a4] offset:49152\n"
      "v_mfma_f32_16x16x32_bf16 v[60:63], v[24:27], v[124:127], v[60:63]\n"
      "v_mfma_f32_16x16x32_bf16 v[92:95], v[28:31], v[124:127], v[92:95]\n"
      "ds_read_b64_tr_b16 v[124:125], %[a5] offset:32768\n"
      "ds_read_b64_tr_b16 v[126:127], %[a5] offset:49152\n"
      "v_mfma_f32_16x16x32_bf16 v[64:67], v[24:27], v[128:131], v[64:67]\n"
      "v_mfma_f32_16x16x32_bf16 v[96:99], v[28:31], v[128:131], v[96:99]\n"
      "ds_read_b64_tr_b16 v[128:129], %[a6] offset:32768\n"
      "ds_read_b64_tr_b16 v[130:131], %[a6] offset:49152\n"
      "v_mfma_f32_16x16x32_bf16 v[68:71], v[24:27], v[132:135], v[68:71]\n"
      "v_mfma_f32_16x16x32_bf16 v[100:103], v[28:31], v[132:135], v[100:103]\n"
      "ds_read_b64_tr_b16 v[132:133], %[a7] offset:32768\n"
      "ds_read_b64_tr_b16 v[134:135], %[a7] offset:49152\n"
      "s_cmp_lg_u64 %[m0], 0\n"
      "s_cbranch_scc1 122f\n"
      "s_mov_b64 exec, 1\n"
      "ds_add_u32 %[cb], %[one] offset:100\n"
      "s_mov_b64 exec, -1\n"
      "122:\n"
      "s_cmp_lg_u64 %[m1], 0\n"
      "s_cbranch_scc1 123b\n"
      "s_waitcnt lgkmcnt(8)\n"
      "v_cmp_ne_u32 vcc, 8, v22\n"
      "s_cbranch_vccnz 130b\n"
      "131:\n"
      "v_cmp_eq_u32 %[m1], 0, v23\n"
      "v_mfma_f32_16x16x32_bf16 v[40:43], v[32:35], v[136:139], v[40:43]\n"
      "v_mfma_f32_16x16x32_bf16 v[72:75], v[36:39], v[136:139], v[72:75]\n"
      "v_add_u32 v138, 0x10000, %[a0]\n"
      "ds_read_b64_tr_b16 v[136:137], v138 offset:32768\n"
      "ds_read_b64_tr_b16 v[138:139], v138 offset:49152\n"
      "v_mfma_f32_16x16x32_bf16 v[44:47], v[32:35], v[140:143], v[44:47]\n"
      "v_mfma_f32_16x16x32_bf16 v[76:79], v[36:39], v[140:143], v[76:79]\n"
      "v_add_u32 v142, 0x10000, %[a1]\n"
      "ds_read_b64_tr_b16 v[140:141], v142 offset:32768\n"
      "ds_read_b64_tr_b16 v[142:143], v142 offset:49152\n"
      "v_mfma_f32_16x16x32_bf16 v[48:51], v[32:35], v[144:147], v[48:51]\n"
      "v_mfma_f32_16x16x32_bf16 v[80:83], v[36:39], v[144:147], v[80:83]\n"
      "v_add_u32 v146, 0x10000, %[a2]\n"
      "ds_read_b64_tr_b16 v[144:145], v146 offset:32768\n"
      "ds_read_b64_tr_b16 v[146:147], v146 offset:49152\n"
      "v_mfma_f32_16x16x32_bf16 v[52:55], v[32:35], v[148:151], v[52:55]\n"
      "v_mfma_f32_16x16x32_bf16 v[84:87], v[36:39], v[148:151], v[84:87]\n"
      "v_add_u32 v150, 0x10000, %[a3]\n"
      "ds_read_b64_tr_b16 v[148:149], v150 offset:32768\n"
      "ds_read_b64_tr_b16 v[150:151], v150 offset:49152\n"
      "ds_read_b32 v20, %[cb] offset:108\n"
      "ds_read_b128 v[24:27], %[pa] offset:0\n"
      "ds_read_b128 v[28:31], %[pa] offset:1024\n"
      "ds_read_b32 v21, %[cb] offset:128\n"
      "v_mfma_f32_16x16x32_bf16 v[56:59], v[32:35], v[152:155], v[56:59]\n"
      "v_mfma_f32_16x16x32_bf16 v[88:91], v[36:39], v[152:155], v[88:91]\n"
      "v_add_u32 v154, 0x10000, %[a4]\n"
      "ds_read_b64_tr_b16 v[152:153], v154 offset:32768\n"
      "ds_read_b64_tr_b16 v[154:155], v154 offset:49152\n"
      "v_mfma_f32_16x16x32_bf16 v[60:63], v[32:35], v[156:159], v[60:63]\n"
      "v_mfma_f32_16x16x32_bf16 v[92:95], v[36:39], v[156:159], v[92:95]\n"
      "v_add_u32 v158, 0x10000, %[a5]\n"
      "ds_read_b64_tr_b16 v[156:157], v158 offset:32768\n"
      "ds_read_b64_tr_b16 v[158:159], v158 offset:49152\n"
      "v_mfma_f32_16x16x32_bf16 v[64:67], v[32:35], v[160:163], v[64:67]\n"
      "v_mfma_f32_16x16x32_bf16 v[96:99], v[36:39], v[160:163], v[96:99]\n"
      "v_add_u32 v162, 0x10000, %[a6]\n"
      "ds_read_b64_tr_b16 v[160:161], v162 offset:32768\n"
      "ds_read_b64_tr_b16 v[162:163], v162 offset:49152\n"
      "v_mfma_f32_16x16x32_bf16 v[68:71], v[32:35], v[164:167], v[68:71]\n"
      "v_mfma_f32_16x16x32_bf16 v[100:103], v[36:39], v[164:167], v[100:103]\n"
      "v_add_u32 v166, 0x10000, %[a7]\n"
      "ds_read_b64_tr_b16 v[164:165], v166 offset:32768\n"
      "ds_read_b64_tr_b16 v[166:167], v166 offset:49152\n"
      "s_cmp_lg_u64 %[m1], 0\n"
      "s_cbranch_scc1 132f\n"
      "s_mov_b64 exec, 1\n"
      "ds_add_u32 %[cb], %[one] offset:116\n"
      "s_mov_b64 exec, -1\n"
      "132:\n"
      "s_cmp_lg_u64 %[m0], 0\n"
      "s_cbranch_scc1 133b\n"
      "s_waitcnt lgkmcnt(8)\n"
      "v_cmp_ne_u32 vcc, 8, v20\n"
      "s_cbranch_vccnz 140b\n"
      "141:\n"
      "v_cmp_eq_u32 %[m0], 0, v21\n"
      "v_mfma_f32_16x16x32_bf16 v[40:43], v[24:27], v[104:107], v[40:43]\n"
      "v_mfma_f32_16x16x32_bf16 v[72:75], v[28:31], v[104:107], v[72:75]\n"
      "v_add_u32 v106, 0x10000, %[a0]\n"
      "ds_read_b64_tr_b16 v[104:105], v106 offset:0\n"
      "ds_read_b64_tr_b16 v[106:107], v106 offset:16384\n"
      "v_mfma_f32_16x16x32_bf16 v[44:47], v[24:27], v[108:111], v[44:47]\n"
      "v_mfma_f32_16x16x32_bf16 v[76:79], v[28:31], v[108:111], v[76:79]\n"
      "v_add_u32 v110, 0x10000, %[a1]\n"
      "ds_read_b64_tr_b16 v[108:109], v110 offset:0\n"
      "ds_read_b64_tr_b16 v[110:111], v110 offset:16384\n"
      "v_mfma_f32_16x16x32_bf16 v[48:51], v[24:27], v[112:115], v[48:51]\n"
      "v_mfma_f32_16x16x32_bf16 v[80:83], v[28:31], v[112:115], v[80:83]\n"
      "v_add_u32 v114, 0x10000, %[a2]\n"
      "ds_read_b64_tr_b16 v[112:113], v114 offset:0\n"
      "ds_read_b64_tr_b16 v[114:115], v114 offset:16384\n"
      "v_mfma_f32_16x16x32_bf16 v[52:55], v[24:27], v[116:119], v[52:55]\n"
      "v_mfma_f32_16x16x32_bf16 v[84:87], v[28:31], v[116:119], v[84:87]\n"
      "v_add_u32 v118, 0x10000, %[a3]\n"
      "ds_read_b64_tr_b16 v[116:117], v118 offset:0\n"
      "ds_read_b64_tr_b16 v[118:119], v118 offset:16384\n"
      "ds_read_b32 v22, %[cb] offset:124\n"
      "ds_read_b128 v[32:35], %[pa] offset:2048\n"
      "ds_read_b128 v[36:39], %[pa] offset:3072\n"
      "ds_read_b32 v23, %[cb] offset:144\n"
      "v_mfma_f32_16x16x32_bf16 v[56:59], v[24:27], v[120:123], v[56:59]\n"
      "v_mfma_f32_16x16x32_bf16 v[88:91], v[28:31], v[120:123], v[88:91]\n"
      "v_add_u32 v122, 0x10000, %[a4]\n"
      "ds_read_b64_tr_b16 v[120:121], v122 offset:0\n"
      "ds_read_b64_tr_b16 v[122:123], v122 offset:16384\n"
      "v_mfma_f32_16x16x32_bf16 v[60:63], v[24:27], v[124:127], v[60:63]\n"
      "v_mfma_f32_16x16x32_bf16 v[92:95], v[28:31], v[124:127], v[92:95]\n"
      "v_add_u32 v126, 0x10000, %[a5]\n"
      "ds_read_b64_tr_b16 v[124:125], v126 offset:0\n"
      "ds_read_b64_tr_b16 v[126:127], v126 offset:16384\n"
      "v_mfma_f32_16x16x32_bf16 v[64:67], v[24:27], v[128:131], v[64:67]\n"
      "v_mfma_f32_16x16x32_bf16 v[96:99], v[28:31], v[128:131], v[96:99]\n"
      "v_add_u32 v130, 0x10000, %[a6]\n"
      "ds_read_b64_tr_b16 v[128:129], v130 offset:0\n"
      "ds_read_b64_tr_b16 v[130:131], v130 offset:16384\n"
      "v_mfma_f32_16x16x32_bf16 v[68:71], v[24:27], v[132:135], v[68:71]\n"
      "v_mfma_f32_16x16x32_bf16 v[100:103], v[28:31], v[132:135], v[100:103]\n"
      "v_add_u32 v134, 0x10000, %[a7]\n"
      "ds_read_b64_tr_b16 v[132:133], v134 offset:0\n"
      "ds_read_b64_tr_b16 v[134:135], v134 offset:16384\n"
      "s_cmp_lg_u64 %[m1], 0\n"
      "s_cbranch_scc1 143b\n"
      "s_waitcnt lgkmcnt(8)\n"
      "v_cmp_ne_u32 vcc, 8, v22\n"
      "s_cbranch_vccnz 150b\n"
      "151:\n"
      "v_cmp_eq_u32 %[m1], 0, v23\n"
      "v_mfma_f32_16x16x32_bf16 v[40:43], v[32:35], v[136:139], v[40:43]\n"
      "v_mfma_f32_16x16x32_bf16 v[72:75], v[36:39], v[136:139], v[72:75]\n"
      "ds_read_b64_tr_b16 v[136:137], %[a0] offset:0\n"
      "ds_read_b64_tr_b16 v[138:139], %[a0] offset:16384\n"
      "v_mfma_f32_16x16x32_bf16 v[44:47], v[32:35], v[140:143], v[44:47]\n"
      "v_mfma_f32_16x16x32_bf16 v[76:79], v[36:39], v[140:143], v[76:79]\n"
      "ds_read_b64_tr_b16 v[140:141], %[a1] offset:0\n"
      "ds_read_b64_tr_b16 v[142:143], %[a1] offset:16384\n"
      "v_mfma_f32_16x16x32_bf16 v[48:51], v[32:35], v[144:147], v[48:51]\n"
      "v_mfma_f32_16x16x32_bf16 v[80:83], v[36:39], v[144:147], v[80:83]\n"
      "ds_read_b64_tr_b16 v[144:145], %[a2] offset:0\n"
      "ds_read_b64_tr_b16 v[146:147], %[a2] offset:16384\n"
      "v_mfma_f32_16x16x32_bf16 v[52:55], v[32:35], v[148:151], v[52:55]\n"
      "v_mfma_f32_16x16x32_bf16 v[84:87], v[36:39], v[148:151], v[84:87]\n"
      "ds_read_b64_tr_b16 v[148:149], %[a3] offset:0\n"
      "ds_read_b64_tr_b16 v[150:151], %[a3] offset:16384\n"
      "ds_read_b32 v20, %[cb] offset:140\n"
      "ds_read_b128 v[24:27], %[pa] offset:4096\n"
      "ds_read_b128 v[28:31], %[pa] offset:5120\n"
      "v_mfma_f32_16x16x32_bf16 v[56:59], v[32:35], v[152:155], v[56:59]\n"
      "v_mfma_f32_16x16x32_bf16 v[88:91], v[36:39], v[152:155], v[88:91]\n"
      "ds_read_b64_tr_b16 v[152:153], %[a4] offset:0\n"
      "ds_read_b64_tr_b16 v[154:155], %[a4] offset:16384\n"
      "v_mfma_f32_16x16x32_bf16 v[60:63], v[32:35], v[156:159], v[60:63]\n"
      "v_mfma_f32_16x16x32_bf16 v[92:95], v[36:39], v[156:159], v[92:95]\n"
      "ds_read_b64_tr_b16 v[156:157], %[a5] offset:0\n"
      "ds_read_b64_tr_b16 v[158:159], %[a5] offset:16384\n"
      "v_mfma_f32_16x16x32_bf16 v[64:67], v[32:35], v[160:163], v[64:67]\n"
      "v_mfma_f32_16x16x32_bf16 v[96:99], v[36:39], v[160:163], v[96:99]\n"
      "ds_read_b64_tr_b16 v[160:161], %[a6] offset:0\n"
      "ds_read_b64_tr_b16 v[162:163], %[a6] offset:16384\n"
      "v_mfma_f32_16x16x32_bf16 v[68:71], v[32:35], v[164:167], v[68:71]\n"
      "v_mfma_f32_16x16x32_bf16 v[100:103], v[36:39], v[164:167], v[100:103]\n"
      "ds_read_b64_tr_b16 v[164:165], %[a7] offset:0\n"
      "ds_read_b64_tr_b16 v[166:167], %[a7] offset:16384\n"
      "s_cmp_lg_u64 %[m0], 0\n"
      "s_cbranch_scc1 153b\n"
      "s_waitcnt lgkmcnt(8)\n"
      "v_cmp_ne_u32 vcc, 8, v20\n"
      "s_cbranch_vccnz 160b\n"
      "161:\n"
      "v_mfma_f32_16x16x32_bf16 v[40:43], v[24:27], v[104:107], v[40:43]\n"
      "v_mfma_f32_16x16x32_bf16 v[72:75], v[28:31], v[104:107], v[72:75]\n"
      "v_mfma_f32_16x16x32_bf16 v[44:47], v[24:27], v[108:111], v[44:47]\n"
      "v_mfma_f32_16x16x32_bf16 v[76:79], v[28:31], v[108:111], v[76:79]\n"
      "v_mfma_f32_16x16x32_bf16 v[48:51], v[24:27], v[112:115], v[48:51]\n"
      "v_mfma_f32_16x16x32_bf16 v[80:83], v[28:31], v[112:115], v[80:83]\n"
      "v_mfma_f32_16x16x32_bf16 v[52:55], v[24:27], v[116:119], v[52:55]\n"
      "v_mfma_f32_16x16x32_bf16 v[84:87], v[28:31], v[116:119], v[84:87]\n"
      "v_mfma_f32_16x16x32_bf16 v[56:59], v[24:27], v[120:123], v[56:59]\n"
      "v_mfma_f32_16x16x32_bf16 v[88:91], v[28:31], v[120:123], v[88:91]\n"
      "v_mfma_f32_16x16x32_bf16 v[60:63], v[24:27], v[124:127], v[60:63]\n"
      "v_mfma_f32_16x16x32_bf16 v[92:95], v[28:31], v[124:127], v[92:95]\n"
      "v_mfma_f32_16x16x32_bf16 v[64:67], v[24:27], v[128:131], v[64:67]\n"
      "v_mfma_f32_16x16x32_bf16 v[96:99], v[28:31], v[128:131], v[96:99]\n"
      "v_mfma_f32_16x16x32_bf16 v[68:71], v[24:27], v[132:135], v[68:71]\n"
      "v_mfma_f32_16x16x32_bf16 v[100:103], v[28:31], v[132:135], v[100:103]\n"
      "s_cmp_lg_u64 %[m1], 0\n"
      "s_cbranch_scc0 170f\n"
      "s_mov_b32 %[n], %[nb]\n"
      "91:\n"
      "s_sub_u32 %[n], %[n], 1\n"
      "s_cbranch_scc1 92f\n"
      "ds_read_b32 v22, %[cb] offset:144\n"
      "s_waitcnt lgkmcnt(0)\n"
      "v_cmp_gt_u32 vcc, 1, v22\n"
      "s_cbranch_vccnz 91b\n"
      "92:\n"
      "ds_read_b64_tr_b16 v[136:137], %[a0] offset:0\n"
      "ds_read_b64_tr_b16 v[138:139], %[a0] offset:16384\n"
      "ds_read_b64_tr_b16 v[140:141], %[a1] offset:0\n"
      "ds_read_b64_tr_b16 v[142:143], %[a1] offset:16384\n"
      "ds_read_b64_tr_b16 v[144:145], %[a2] offset:0\n"
      "ds_read_b64_tr_b16 v[146:147], %[a2] offset:16384\n"
      "ds_read_b64_tr_b16 v[148:149], %[a3] offset:0\n"
      "ds_read_b64_tr_b16 v[150:151], %[a3] offset:16384\n"
      "ds_read_b64_tr_b16 v[152:153], %[a4] offset:0\n"
      "ds_read_b64_tr_b16 v[154:155], %[a4] offset:16384\n"
      "ds_read_b64_tr_b16 v[156:157], %[a5] offset:0\n"
      "ds_read_b64_tr_b16 v[158:159], %[a5] offset:16384\n"
      "ds_read_b64_tr_b16 v[160:161], %[a6] offset:0\n"
      "ds_read_b64_tr_b16 v[162:163], %[a6] offset:16384\n"
      "ds_read_b64_tr_b16 v[164:165], %[a7] offset:0\n"
      "ds_read_b64_tr_b16 v[166:167], %[a7] offset:16384\n"
      "170:\n"
      "s_mov_b32 %[n], %[nb]\n"
      "91:\n"
      "s_sub_u32 %[n], %[n], 1\n"
      "s_cbranch_scc1 92f\n"
      "ds_read_b32 v22, %[cb] offset:152\n"
      "s_waitcnt lgkmcnt(0)\n"
      "v_cmp_gt_u32 vcc, 4, v22\n"
      "s_cbranch_vccnz 91b\n"
      "92:\n"
      "s_mov_b32 %[n], %[nb]\n"
      "91:\n"
      "s_sub_u32 %[n], %[n], 1\n"
      "s_cbranch_scc1 92f\n"
      "ds_read_b32 v22, %[cb] offset:156\n"
      "ds_read_b128 v[32:35], %[pa] offset:6144\n"
      "ds_read_b128 v[36:39], %[pa] offset:7168\n"
      "s_waitcnt lgkmcnt(0)\n"
      "v_cmp_gt_u32 vcc, 8, v22\n"
      "s_cbranch_vccnz 91b\n"
      "92:\n"
      "v_cmp_lt_u32 vcc, 8, v22\n"
      "s_cbranch_vccnz 174b\n"
      "171:\n"
      "v_mfma_f32_16x16x32_bf16 v[40:43], v[32:35], v[136:139], v[40:43]\n"
      "v_mfma_f32_16x16x32_bf16 v[44:47], v[32:35], v[140:143], v[44:47]\n"
      "v_mfma_f32_16x16x32_bf16 v[48:51], v[32:35], v[144:147], v[48:51]\n"
      "v_mfma_f32_16x16x32_bf16 v[52:55], v[32:35], v[148:151], v[52:55]\n"
      "v_mfma_f32_16x16x32_bf16 v[56:59], v[32:35], v[152:155], v[56:59]\n"
      "v_mfma_f32_16x16x32_bf16 v[60:63], v[32:35], v[156:159], v[60:63]\n"
      "v_mfma_f32_16x16x32_bf16 v[64:67], v[32:35], v[160:163], v[64:67]\n"
      "v_mfma_f32_16x16x32_bf16 v[68:71], v[32:35], v[164:167], v[68:71]\n"
      "v_mfma_f32_16x16x32_bf16 v[72:75], v[36:39], v[136:139], v[72:75]\n"
      "v_mfma_f32_16x16x32_bf16 v[76:79], v[36:39], v[140:143], v[76:79]\n"
      "v_mfma_f32_16x16x32_bf16 v[80:83], v[36:39], v[144:147], v[80:83]\n"
      "v_mfma_f32_16x16x32_bf16 v[84:87], v[36:39], v[148:151], v[84:87]\n"
      "v_mfma_f32_16x16x32_bf16 v[88:91], v[36:39], v[152:155], v[88:91]\n"
      "v_mfma_f32_16x16x32_bf16 v[92:95], v[36:39], v[156:159], v[92:95]\n"
      "v_mfma_f32_16x16x32_bf16 v[96:99], v[36:39], v[160:163], v[96:99]\n"
      "v_mfma_f32_16x16x32_bf16 v[100:103], v[36:39], v[164:167], v[100:103]\n"
      : "={v[40:71]}"(o0), "={v[72:103]}"(o1), [n] "=&s"(n), [k] "=&s"(k), [m0] "=&s"(m0), [m1] "=&s"(m1), [ret] "=&s"(ret)
      : [a0] "{v9}"(a[0]), [a1] "{v10}"(a[1]), [a2] "{v11}"(a[2]), [a3] "{v12}"(a[3]), [a4] "{v13}"(a[4]), [a5] "{v14}"(a[5]), [a6] "{v15}"(a[6]),
        [a7] "{v16}"(a[7]), [pa] "{v17}"(pa), [cb] "{v18}"(cb), [one] "{v19}"(one), [fac] "s"(T_FAC), [nb] "s"(nb)
      : "vcc", "scc", "memory", "v20", "v21", "v22", "v23", "v24", "v25", "v26", "v27", "v28", "v29", "v30", "v31", "v32", "v33", "v34", "v35", "v36", "v37", "v38", "v39", "v104", "v105", "v106", "v107", "v108", "v109", "v110", "v111", "v112", "v113", "v114", "v115", "v116", "v117", "v118", "v119", "v120", "v121", "v122", "v123", "v124", "v125", "v126", "v127", "v128", "v129", "v130", "v131", "v132", "v133", "v134", "v135", "v136", "v137", "v138", "v139", "v140", "v141", "v142", "v143", "v144", "v145", "v146", "v147", "v148", "v149", "v150", "v151", "v152", "v153", "v154", "v155", "v156", "v157", "v158", "v159", "v160", "v161", "v162", "v163", "v164", "v165", "v166", "v167");
}
DI void ffn_part(const Params& p, int b, int s, int wave, unsigned t0, u64 tag);
DI void attn_wg(const Params& p, int b, int s, int wave, unsigned t0) {
  unsigned char* ws = p.ws;
  const rsrc_t rs = __builtin_amdgcn_make_buffer_rsrc(ws, 0, WS_BYTES, 0x00020000);
  const int w = wave & 3;
  const f32x4 z4 = {0.f, 0.f, 0.f, 0.f};

  if (wave < 2) { const int lane = fresh_lane(); *(LAS unsigned*)(T_CNT + wave * 256 + lane * 4) = wave == 0 && lane < 8 ? 8u : 0u; }
  bar();
  u64 tag = 0;
  if (wave != 8) tag = get_tag(ws);
  if (wave == 8) {
    const int lane = fresh_lane();
    unsigned vo[16];
#pragma unroll
    for (int r = 0; r < 16; ++r) vo[r] = 16 * (lane ^ fsw(r));
    const unsigned char* kv = (const unsigned char*)p.ckv + (size_t)(b * 4096 + s * ILV) * 1024;
    nap<1>();
    wait_until(t0 + T_PREF);
    __builtin_amdgcn_s_setprio(PRIO_L);
    bool watch = true;
    STAMP(62); STAMPD(31);
#pragma unroll 1
    for (int t = 0; t < 8; ++t) {
      bool told = false;
      if (t >= 3) {
        const unsigned c = t == 5 ? C_QRD : C_FREE + 16 * (t < 5 ? t - 3 : t - 4), want = t == 5 ? 4 : 8;
        auto free_now = [&]() { return __builtin_amdgcn_readfirstlane(lpeek(c)) >= want; };
        bool ok = free_now();
        if (!ok) { VMCNT(24); ok = free_now(); }
        if (!ok) { VMCNT(16); ok = free_now(); }
        if (!ok) { VMCNT(8); ok = free_now(); }
        if (!ok) { VMCNT(0); lsig(C_LAND + 16 * t - 16); told = true; lwait<true>(c, want); }
      }
      const unsigned dst = slot_of(t);
#pragma unroll
      for (int i = 0; i < 8; ++i) {
        DMA4("nt", kv + (4 * i / ILV) * (16 * ILV * 1024) + (4 * i % ILV) * 1024, dst + i * 4096, vo[4 * (i & 3)], vo[4 * (i & 3) + 1], vo[4 * (i & 3) + 2], vo[4 * (i & 3) + 3]);
        if (watch) {
          const unsigned hold = __builtin_amdgcn_readfirstlane(lpeek(C_HOLD));
          if (hold) { lwait<true>(C_HOLD, 2); watch = false; }
        }
      }
      if (!told) { VMCNT(32); lsig(C_LAND + 16 * t - 16); }
      kv += ILV > 32 ? 32768 : 16 * 32768;
    }
    STAMP(22);

    VMCNT(0);
    lsig(C_LAND + 16 * 7);
    STAMP(23);
    if (s >= 8 || b == 7) { DUMP(62); DUMP(31); DUMP(22); DUMP(23); }
    ROLE_END();
    return;
  }
  __builtin_amdgcn_s_setprio(2);
  if (wave == 9) {
    const int lane = fresh_lane();
    unsigned vo[16];
#pragma unroll
    for (int r = 0; r < 16; ++r) { vo[r] = (lane >> 5) * ASTR + (r & 3) * (4 * ASTR - 1024) + 16 * ((lane & 31) ^ fsw(r)); pin(vo[r]); }
    const unsigned char* qa = ws + WS_ARENA + 2 * ASTR + A_E + b * 512;
    wait_until(t0 + T_FLAG);
    wait_flags(rs, WS_FLAG1 + lane * 16, tag);
    STAMP(17); STAMPD(47);
    const unsigned tq = latest_time32<128>(ws + WS_HQ + (16 * b + s) * 512, t0, (unsigned)tag);
    STAMPV(47);
    wait_until(tq + T_QA - T_HOLD);
    lsig(C_HOLD);
    wait_until(tq + T_QA);
    STAMP(18);
#pragma unroll
    for (int i = 0; i < 8; ++i)
      DMA4("sc1", qa + i * (16 * ASTR), 3 * 32768 + i * 4096, vo[4 * (i & 3)], vo[4 * (i & 3) + 1], vo[4 * (i & 3) + 2], vo[4 * (i & 3) + 3]);
    lsig(C_HOLD);
    VMCNT(0);
    lsig(C_QST);
    STAMP(38);

    if (s >= 8 || b == 7) { lwait<true>(C_PRDY + 16 * 7, 8); nap<30>(); DUMP(17); DUMP(18); DUMP(38); DUMP(47); }
    ROLE_END();
    return;
  }
  if (wave >= 10) {
    __builtin_amdgcn_s_setprio(3);
    const int hh = wave - 10, lane = fresh_lane(), l15 = lane & 15, lq = lane >> 4;
#pragma unroll 1
    for (int real = DRY ? 0 : 1; real < 2; ++real) {
    const unsigned nb = real ? SPIN_L : 1;
    unsigned sa = T_SP + hh * 1024 + lane * 16, cb = real ? T_CNT : T_DRY, four = 4u;
    int mlo = real && lq == 0 ? rarena(16 * b + s, tag) + A_ML + hh * 256 + l15 * 16 : OOB;
    pin(sa); pin(cb); pin(four); pin(mlo);
    float ref = 0.f, l = 0.f;
    cwait<C_QST, 1, true>(cb, nb);
    static_for<0, 8>([&](auto tc) {
      constexpr int t = decltype(tc)::value;
      const unsigned nb_ = nb;
      if constexpr (t >= 4) cwait<C_FREE + 16 * (t == 4 ? 3 : t - 2), 8>(cb, nb);
      f32x4 p0, p1, p2, p3;
      unsigned c, n;
      asm volatile(SPIN_HEAD "ds_read_b32 %[c], %[cb] offset:%[o]\n\tds_read_b128 %[p0], %[sa] offset:%[s0]\n\tds_read_b128 %[p2], %[sa] offset:%[s2]\n\t"
                   "ds_read_b128 %[p1], %[sa] offset:%[s1]\n\tds_read_b128 %[p3], %[sa] offset:%[s3]\n\t" SPIN_TAIL2
                   : [c] "=&v"(c), [n] "=&s"(n), [p0] "=&v"(p0), [p1] "=&v"(p1), [p2] "=&v"(p2), [p3] "=&v"(p3)
                   : [cb] "v"(cb), [sa] "v"(sa), [o] "i"(C_SRDY + 16 * t - T_CNT), [w] "i"(4), [w2] "i"(t == 7 ? 1 : 4), [s0] "i"((t & 1) * 8192),
                     [s1] "i"((t & 1) * 8192 + 2048), [s2] "i"((t & 1) * 8192 + 4096), [s3] "i"((t & 1) * 8192 + 6144), [nb] "s"(nb_)
                   : "vcc", "scc", "memory");
      f32x4 a = (p0 - ref) + p2, c4 = (p1 - ref) + p3;
      const float m = fmaxf(fmaxf(fmaxf(a.x, a.y), fmaxf(a.z, a.w)), fmaxf(fmaxf(c4.x, c4.y), fmaxf(c4.z, c4.w)));
      unsigned sig = four;
      if constexpr (t == 0) {
        ref = xmax32(xmax16(m));
        a -= ref; c4 -= ref;
      } else if (__builtin_expect(__any(m > BUMP), 0)) {
        const float up = fmaxf(0.f, xmax32(xmax16(m)));
        const float fa = __builtin_amdgcn_exp2f(-up);
        *(LAS float*)(T_FAC + t * 128 + hh * 64 + l15 * 4) = fa;
        l *= fa;
        ref += up;
        a = (p0 - ref) + p2; c4 = (p1 - ref) + p3;
        sig = 4u + (1u << hh);
      }
      const v4u pf = {pk(__builtin_amdgcn_exp2f(a.x), __builtin_amdgcn_exp2f(a.y)), pk(__builtin_amdgcn_exp2f(a.z), __builtin_amdgcn_exp2f(a.w)),
                      pk(__builtin_amdgcn_exp2f(c4.x), __builtin_amdgcn_exp2f(c4.y)), pk(__builtin_amdgcn_exp2f(c4.z), __builtin_amdgcn_exp2f(c4.w))};
      *(LAS v4u*)((LAS char*)sa + (T_P - T_SP) + (t & 3) * 2048) = pf;
      csig<C_PRDY + 16 * t>(cb, sig);
#pragma unroll
      for (int e = 0; e < 4; ++e) {
        const unsigned u = pf[e];
        l = __builtin_amdgcn_fdot2_f32_bf16(__builtin_bit_cast(bf16x2, u), __builtin_bit_cast(bf16x2, 0x3F803F80u), l, false);
      }
    });
    stg(rs, mlo, v4u{__float_as_uint(ref), (unsigned)tag, __float_as_uint(xsum32(xsum16(l))), (unsigned)(tag >> 32)});
    }
    STAMP(24);
    if (hh == 0 && (s >= 8 || b == 7)) DUMP(24);
    ROLE_END();
    return;
  }
  if (wave < 4) {
    __builtin_amdgcn_s_setprio(PRIO_S);
    const int kh = wave & 1, ch = wave >> 1;
    STAMPD(19); STAMPD(20);
    int rqa = 0;
#pragma unroll 1
    for (int real = DRY ? 0 : 1; real < 2; ++real) {
    const unsigned nb = real ? SPIN_L : 1;
    const int lane = fresh_lane(), l15 = lane & 15, lq = lane >> 4;
    const int sg = (0x2130 >> (4 * lq)) & 3;
    unsigned ao[4];
#pragma unroll
    for (int k = 0; k < 4; ++k) ao[k] = l15 * 1024 + ch * 512 + 16 * ((4 * k + sg) ^ fsw(l15));
    unsigned sa = T_SP + wave * 2048 + lane * 16, cb = real ? T_CNT : T_DRY, one = 1u;
    unsigned al[4], ah[4];
#pragma unroll
    for (int k = 0; k < 4; ++k) { al[k] = ao[k] + kh * 16384; ah[k] = al[k] + 65536; pin(al[k]); pin(ah[k]); }
    int aro = real && wave == 0 && lane < 32 ? WS_ARR + (4 * lane + (b >> 1)) * 512 + (s & 3) * 128 + (b & 1) * 16 + (s >> 2) * 4 : OOB;
    pin(sa); pin(cb); pin(one); pin(aro);
    v4u af[2][8], qf[2][8];
    auto rd = [&](unsigned sl, int ks) { return lds128(sl < 65536 ? al[ks & 3] : ah[ks & 3], (sl & 32768) + (ks >> 2) * 256); };
    static_for<0, 2>([&](auto tc) {
      constexpr int t = decltype(tc)::value;
      cwait<C_LAND + 16 * t, 1, true>(cb, nb);
#pragma unroll
      for (int ks = 0; ks < 8; ++ks) af[t][ks] = rd(t * 32768, ks);
      csig<C_FREE + 16 * t>(cb, one);
    });
    unsigned qa[4];
#pragma unroll
    for (int k = 0; k < 4; ++k) { qa[k] = 3 * 32768 + ao[k]; pin(qa[k]); }
    cwait<C_QST, 1>(cb, nb);
    STAMPV(19);
    unsigned nx2 = cpeek<C_LAND + 32>(cb), nxt = cpeek<C_LAND + 48>(cb);
    f32x4 s0, s1;
#pragma unroll 1
    for (int sp = 0; sp < SPIN_G; ++sp) {
      rqa = sp + 1;
#pragma unroll
      for (int ks = 0; ks < 8; ++ks) {
#pragma unroll
        for (int hh = 0; hh < 2; ++hh) qf[hh][ks] = lds128(qa[ks & 3], hh * 16384 + (ks >> 2) * 256);
        __builtin_amdgcn_sched_barrier(0);
      }
      s0 = z4; s1 = z4;
#pragma unroll
      for (int ks = 0; ks < 8; ++ks) { s0 = mfma16(af[0][ks], qf[0][ks], s0); s1 = mfma16(af[0][ks], qf[1][ks], s1); }
      if (__builtin_expect(!__any(__builtin_isunordered(s0.x, s1.x)), 1) || !real || !(any_sent<8>(qf[0]) || any_sent<8>(qf[1]))) break;
#pragma unroll 1
      for (unsigned m = ((unsigned)__ballot(s0.x != s0.x) & 0xFFFFu) | ((unsigned)__ballot(s1.x != s1.x) << 16); m; m &= m - 1) {
        const int r = __builtin_ctz(m);
        asm volatile("s_mov_b32 m0, %2\n\ts_nop 0\n\tglobal_load_lds_dwordx4 %0, %1 sc1"
                     :: "v"((lane >> 5) * ASTR + 16 * ((lane & 31) ^ fsw(r))), "s"(ws + WS_ARENA + 2 * ASTR + A_E + b * 512 + r * (4 * ASTR)), "s"(3 * 32768 + r * 1024) : "memory");
      }
      VMCNT(0);
    }
    *(LAS f32x4*)sa = s0;
    *(LAS f32x4*)((LAS char*)sa + 1024) = s1;
    csig<C_SRDY>(cb, one);
    csig<C_QRD>(cb, one);
    STAMPV(20);
    auto refill = [&](v4u& a, auto slc, auto ksc) {
      constexpr unsigned sl = decltype(slc)::value;
      constexpr int ks = decltype(ksc)::value;
      rd_inplace<(sl & 32768) + (ks >> 2) * 256>(a, sl < 65536 ? al[ks & 3] : ah[ks & 3]);
    };
    static_for<0, 8>([&](auto k) { refill(af[0][decltype(k)::value], IntC<slot_c(2)>{}, k); });
    bool miss = __any(nx2 < 1);
    if (!miss) csig<C_FREE + 32>(cb, one);
    static_for<1, 8>([&](auto tc) {
      constexpr int t = decltype(tc)::value;
      const bool was = miss;
      if constexpr (t < 6) miss = __any(nxt < 1);
      unsigned use;
      peek2<C_PRDY + 16 * (t - 2) - T_CNT, C_LAND + 16 * (t < 5 ? t + 3 : 8) - T_CNT>(use, nxt, cb);
      static_for<0, 8>([&](auto k) {
        constexpr int ks = decltype(k)::value;
        score_pair<(t < 6 ? 9 : 9 - ks), ks == 0>(s0, s1, af[t & 1][ks], qf[0][ks], qf[1][ks]);
        if constexpr (t < 6) refill(af[t & 1][ks], IntC<slot_c(t + 2)>{}, k);
      });
      wait2<(t < 6 ? 8 : 0)>(use, nxt);
      if (__builtin_expect(__any(use < 8), 0)) cwait<C_PRDY + 16 * (t - 2), 8>(cb, nb);
      put_scores<(t & 1) * 8192>(sa, s0, s1);
      csig<C_SRDY + 16 * t>(cb, one);
      if constexpr (t == 7 && !ARR_EARLY) st32(rs, aro, now32() + (unsigned)tag);
      if constexpr (t < 4) { if (!miss) csig<C_FREE + 16 * (t + 2)>(cb, one); }
      if constexpr (t < 7) {
        if (was) {
          cwait<C_LAND + 16 * (t + 1), 1>(cb, nb);
          static_for<0, 8>([&](auto k) { refill(af[(t + 1) & 1][decltype(k)::value], IntC<slot_c(t + 1)>{}, k); });
          if constexpr (t < 5) csig<C_FREE + 16 * (t + 1)>(cb, one);
        }
      }
      if constexpr (t == 6 && ARR_EARLY) { if (wave == 0) st32(rs, aro, now32() + (unsigned)tag); }
    });
    asm volatile("s_waitcnt lgkmcnt(0)" ::: "memory");
    }
    STAMP(21);
    if (wave == 0 && (s >= 8 || b == 7)) { DUMP(19); DUMP(20); DUMP(21); DUMPC(50, rqa); }
  } else {
    __builtin_amdgcn_s_setprio(PRIO_PV);
#pragma unroll 1
    for (int real = DRY ? 0 : 1; real < 2; ++real) {
    const int lane = fresh_lane(), l15 = lane & 15, lq = lane >> 4;
    unsigned vl[8];
#pragma unroll
    for (int ct = 0; ct < 8; ++ct)
      vl[ct] = ((lane >> 2) * 1024 + w * 256 + 16 * ((2 * (ct & 1) + ((lane >> 1) & 1)) ^ fsw(lane >> 2)) + 8 * (lane & 1)) ^ ((ct >> 1) * 64);
    int po = real ? rarena(16 * lq + w, tag) + A_PART + (b * 16 + s) * 256 + l15 * 16 : OOB - 80 * ASTR;
    int cko = (lane & 2) || !real ? OOB : rarena(16 * b + s, tag) + A_CK + w * 512 + (2 * (lane & 8) + 4 * lq + ((lane >> 1) & 2) + (lane & 1)) * 16;
    pin(po); pin(cko);
    f32x32 acc[2];
    pv_loop(acc[0], acc[1], vl, T_P + lane * 16, real ? T_CNT : T_DRY, 1u, real ? SPIN_L : 1);
    unsigned ce[8], co[8];
#pragma unroll
    for (int ht = 0; ht < 2; ++ht)
#pragma unroll
      for (int r = 0; r < 4; ++r) {
        if (ht == 1 && r == 0) asm volatile("s_nop 1" : "+{v[72:103]}"(acc[1]) :: "memory");
        const v4u v = {pk(acc[ht][r], acc[ht][4 + r]), pk(acc[ht][8 + r], acc[ht][12 + r]), pk(acc[ht][16 + r], acc[ht][20 + r]),
                       pk(acc[ht][24 + r], acc[ht][28 + r])};
        stg(rs, po + (16 * ht + r) * 4 * ASTR, v);
        ce[4 * ht + r] = v.x ^ v.z; co[4 * ht + r] = v.y ^ v.w;
      }
    {
      unsigned e0, e1, o0, o1;
      xor_rows8(ce, e0, e1);
      xor_rows8(co, o0, o1);
      const bool odd = cko & 16;
      stg(rs, cko, v4u{odd ? e1 : e0, (unsigned)tag, odd ? o1 : o0, (unsigned)(tag >> 32)});
    }
    }
    STAMP(25);
  }
  if (b == 7 || s >= 8) { ROLE_END(); return; }
  ffn_part(p, b, s, wave, t0, tag);
  ROLE_END();
}

DI void ffn_part(const Params& p, int b, int s, int wave, unsigned t0, u64 tag) {
  const int lane = fresh_lane();
  unsigned char* ws = p.ws;
  const rsrc_t rs = __builtin_amdgcn_make_buffer_rsrc(ws, 0, WS_BYTES, 0x00020000);
  const int l15 = lane & 15, lq = lane >> 4, w = wave & 3;
  const f32x4 z4 = {0.f, 0.f, 0.f, 0.f};
  __builtin_amdgcn_s_setprio(2);
  const int fg = 8 * b + s, b8 = lane & 7, p4 = 4 * (lane & 3);
  const bool bvalid = l15 < 8;
  const int ch = l15 ^ ((lq << 2) | w), row0 = 4 * w + lq;
  bar();
  STAMP(52);
  nap<D_F>();
  v4u mm[4];
  const int mo = arena(32 + 4 * wave) + A_S + (4 * (l15 >> 3) + lq) * 128 + b8 * 16;
  auto load_mod = [&]() {
    FENCE();
#pragma unroll
    for (int i = 0; i < 4; ++i) mm[i] = ldg(rs, mo, ((i >> 1) * 32 + (i & 1)) * ASTR);
    FENCE();
  };
  load_mod();
  if (wave < 4) {
    dma_run<false>(p.w1, (row0 + (ch >> 2) * 128) * 3584 + fg * 64 + (ch & 3) * 16, W0 + w * 1024, 8, 16 * 3584, 4096);
  } else {
    const unsigned u = (32 * fg + row0) * 1024 + ch * 16;
    dma_run<true>(p.w2, u, W1 + w * 1024, 4, 256, 8192);
    dma_run<true>(p.w2, u + 16 * 1024, W1 + w * 1024 + 4096, 4, 256, 8192);
  }
#pragma unroll 1
  for (int sp = 0; sp < SPIN_G; ++sp) {
    if (!any_sent<4>(mm)) break;
    load_mod();
  }
  f32x4 sb[2], cb[2];
  sb[0] = up4(mm[0].xy) + up4(mm[1].xy); sb[1] = up4(mm[0].zw) + up4(mm[1].zw);
  cb[0] = up4(mm[2].xy) + up4(mm[3].xy) + 1.f; cb[1] = up4(mm[2].zw) + up4(mm[3].zw) + 1.f;
  pin(sb[0]); pin(sb[1]); pin(cb[0]); pin(cb[1]);
  if (wave == 7) wait_flags(rs, WS_FLAG2 + lane * 16, tag);
  VMCNT(0);
  bar();
  STAMP(28);
  v4u f1[2][2], f2[8];
#pragma unroll
  for (int tt = 0; tt < 2; ++tt) {
    const int c4 = 32 * (wave >> 1) + 2 * p4 + 4 * tt;
    const unsigned a0 = tr_addr(W0 + (wave & 1) * 16384, lane, c4, 0), a1 = tr_addr(W0 + (wave & 1) * 16384, lane, c4, 1);
    f1[0][tt] = tr_frag(a0, a1, 0); f1[1][tt] = tr_frag(a0, a1, 1);
    pin(f1[0][tt]); pin(f1[1][tt]);
  }
#pragma unroll
  for (int i = 0; i < 8; ++i) {
    const int c4 = 32 * (i >> 1) + 2 * p4 + 4 * (i & 1);
    f2[i] = tr_frag(tr_addr(W1 + w * 8192, lane, c4, 0), tr_addr(W1 + w * 8192, lane, c4, 1), 0);
    pin(f2[i]);
  }
  int xo = arena(wave) + A_X1 + (4 * (l15 >> 3) + lq) * 128 + b8 * 16;
  unsigned zw = L_A5 + wave * 1024 + lane * 16, zr = L_A5 + lane * 16;
  unsigned sw = lane < 8 ? L_A6 + lane * 80 + wave * 8 : L_JUNK, sr = L_A6 + b8 * 80;
  pin(xo); pin(zw); pin(zr); pin(sw); pin(sr);
  STAMP(55);
  STAMPD(40); STAMPD(15); STAMPD(36); STAMPD(37);
  int rx = 0;
  auto first = [&](v4u u) {
    const f32x4 d00 = mfma16(f1[0][0], u, z4), d10 = mfma16(f1[1][0], u, z4), d01 = mfma16(f1[0][1], u, z4), d11 = mfma16(f1[1][1], u, z4);
    f32x4 zz;
#pragma unroll
    for (int r = 0; r < 4; ++r) zz[r] = (bvalid ? d00[r] : d11[r]) + dppf<0x128>(bvalid ? d01[r] : d10[r]);
    return zz;
  };
  *(LAS f32x4*)((LAS char*)zw + 16384) = first(pk8(cb[0], cb[1]));
  *(LAS f32x4*)((LAS char*)zw + 24576) = first(pk8(sb[0], sb[1]));
  bar();
  f32x4 v1 = z4, v2 = z4;
  int yo = arena(fg) + A_YP + (16 * w + 4 * (l15 >> 3) + lq) * 128 + b8 * 16;
  if (wave >= 4) {
#pragma unroll
    for (int i = 0; i < 8; ++i) { v1 += u2f(lds128(zr, 16384 + i * 1024)); v2 += u2f(lds128(zr, 24576 + i * 1024)); }
  }
  pin(v1); pin(v2); pin(yo);
  STAMPV(40);
  if (wave == 0) wait_until(latest_time(ws + WS_HB + (16 * b + s) * 128, t0, (unsigned)tag) + T_C);
  bar();
  {
    v4u xv[1];
#pragma unroll 1
    for (int sp = 0; sp < SPIN_G; ++sp) { load_n<1>(rs, xo, 0, xv); rx = sp + 1; if (!any_sent<1>(xv)) break; }
    STAMPV(15);
    const f32x4 x0 = up4(xv[0].xy), x1 = up4(xv[0].zw);
    *(LAS f32x4*)zw = first(pk8(x0 * cb[0], x1 * cb[1]));
    const f32x4 s4 = x0 + x1, q4 = x0 * x0 + x1 * x1;
    float sx = (s4.x + s4.y) + (s4.z + s4.w), sq = (q4.x + q4.y) + (q4.z + q4.w);
    sx += dppf<0x128>(sx); sq += dppf<0x128>(sq);
    {
      auto y = __builtin_amdgcn_permlane16_swap(__float_as_uint(sx), __float_as_uint(sq), false, false);
      const float t = xsum32(__uint_as_float(y[0]) + __uint_as_float(y[1]));
      auto z = __builtin_amdgcn_permlane16_swap(__float_as_uint(t), __float_as_uint(t), false, false);
      *(LAS v2u*)sw = v2u{z[0], z[1]};
    }
    STAMPV(36);
    bar();
    if (wave >= 4) {
      STAMPV(37);
      f32x4 zp[8], st[4];
#pragma unroll
      for (int i = 0; i < 4; ++i) st[i] = u2f(lds128(sr, i * 16));
#pragma unroll
      for (int i = 0; i < 8; ++i) zp[i] = u2f(lds128(zr, i * 1024));
      __builtin_amdgcn_sched_barrier(0);
      const f32x4 ss = (st[0] + st[1]) + (st[2] + st[3]);
      const float mean = (ss.x + ss.z) * (1.f / 512), msq = (ss.y + ss.w) * (1.f / 512);
      const float rstd = __builtin_amdgcn_rsqf(fmaxf(msq - mean * mean, 0.f) + EPS);
      const f32x4 zs = ((zp[0] + zp[1]) + (zp[2] + zp[3])) + ((zp[4] + zp[5]) + (zp[6] + zp[7]));
      const f32x4 z = (zs - v1 * mean) * rstd + v2;
      const unsigned g0 = pk(z.x < 0.f ? 0.f : z.x, z.y < 0.f ? 0.f : z.y), g1 = pk(z.z < 0.f ? 0.f : z.z, z.w < 0.f ? 0.f : z.w);
      const unsigned o0 = (unsigned)__builtin_amdgcn_update_dpp(0, (int)g0, 0x128, 0xF, 0xF, true), o1 = (unsigned)__builtin_amdgcn_update_dpp(0, (int)g1, 0x128, 0xF, 0xF, true);
      const v4u bf = {bvalid ? g0 : o0, bvalid ? g1 : o1, bvalid ? o0 : g0, bvalid ? o1 : g1};
#pragma unroll
      for (int i = 0; i < 2; ++i) {
        const f32x4 a0 = mfma16(f2[4 * i], bf, z4), a1 = mfma16(f2[4 * i + 1], bf, z4), c0 = mfma16(f2[4 * i + 2], bf, z4), c1 = mfma16(f2[4 * i + 3], bf, z4);
        stg(rs, yo + i * 1024, pick(bvalid, pk8(a0, a1), pk8(c0, c1)));
      }
    }
  }
}

DI void first_requests() {
  u64 px, pc, pw;
  unsigned a, kp, wv, v0, v1, v2;
  asm volatile(
      "s_load_dwordx2 %[px], %[k], 0x0\n\ts_load_dwordx2 %[pc], %[k], 0x40\n\ts_load_dwordx2 %[pw], %[k], 0x48\n\t"
      "v_readfirstlane_b32 %[wv], %[t]\n\ts_and_b32 %[a], %[bx], 0x90\n\ts_cmpk_eq_u32 %[a], 0x90\n\ts_cbranch_scc0 3f\n\t"
      "s_bfe_u32 %[a], %[bx], 0x20005\n\ts_bfe_u32 %[kp], %[bx], 0x10003\n\ts_lshl1_add_u32 %[kp], %[a], %[kp]\n\t"
      "s_lshr_b32 %[wv], %[wv], 6\n\ts_cmp_gt_u32 %[wv], 3\n\ts_cbranch_scc1 2f\n\t"
      "v_bfe_u32 %[v0], %[t], 4, 2\n\tv_lshl_or_b32 %[v1], %[v0], 2, %[wv]\n\tv_xor_b32 %[v1], %[v1], %[t]\n\t"
      "v_mul_u32_u24 %[v0], 0x1800, %[v0]\n\tv_and_b32 %[v2], 7, %[v1]\n\tv_lshl_add_u32 %[v0], %[v2], 4, %[v0]\n\t"
      "v_bfe_u32 %[v2], %[v1], 3, 1\n\tv_lshl_add_u32 %[v0], %[v2], 10, %[v0]\n\t"
      "s_lshl4_add_u32 %[kp], %[kp], %[wv]\n\ts_mul_i32 %[kp], %[kp], 0x6000\n\ts_and_b32 %[a], %[bx], 7\n\ts_lshl_b32 %[a], %[a], 7\n\t"
      "s_add_u32 %[a], %[a], %[kp]\n\tv_add_u32 %[v0], %[a], %[v0]\n\ts_lshl_b32 %[wv], %[wv], 10\n\ts_add_u32 m0, %[wv], %[w2]\n\t"
      "v_add_u32 %[v1], 0x18000, %[v0]\n\tv_add_u32 %[v2], 0x30000, %[v0]\n\ts_waitcnt lgkmcnt(0)\n\t"
      "global_load_lds_dwordx4 %[v0], %[pw] nt\n\ts_add_u32 m0, m0, 0x1000\n\tv_add_u32 %[v0], 0x48000, %[v0]\n\t"
      "global_load_lds_dwordx4 %[v1], %[pw] nt\n\ts_add_u32 m0, m0, 0x1000\n\ts_nop 0\n\t"
      "global_load_lds_dwordx4 %[v2], %[pw] nt\n\ts_add_u32 m0, m0, 0x1000\n\ts_nop 0\n\t"
      "global_load_lds_dwordx4 %[v0], %[pw] nt\n\ts_branch 3f\n"
      "2:\n\tv_add_u32 %[v0], 0xffffff00, %[t]\n\tv_lshlrev_b32 %[v0], 4, %[v0]\n\tv_and_b32 %[v1], 0xfffffc70, %[v0]\n\t"
      "s_lshl_b32 %[kp], %[kp], 7\n\tv_or_b32 %[v1], %[kp], %[v1]\n\ts_lshl_b32 %[wv], %[wv], 11\n\ts_add_u32 m0, %[wv], %[raw]\n\t"
      "s_waitcnt lgkmcnt(0)\n\tglobal_load_lds_dwordx4 %[v1], %[pc]\n\ts_sub_u32 m0, m0, 0x400\n\ts_nop 0\n\t"
      "global_load_lds_dwordx4 %[v0], %[px]\n"
      "3:\n\ts_waitcnt lgkmcnt(0)"
      : [px] "=&s"(px), [pc] "=&s"(pc), [pw] "=&s"(pw), [a] "=&s"(a), [kp] "=&s"(kp), [wv] "=&s"(wv), [v0] "=&v"(v0), [v1] "=&v"(v1), [v2] "=&v"(v2)
      : [k] "s"(__builtin_amdgcn_kernarg_segment_ptr()), [bx] "s"(blockIdx.x), [t] "v"(threadIdx.x), [w2] "i"(W2), [raw] "i"(M_RAW + 1024 - 4 * 2048)
      : "scc", "memory");
}

__global__ __launch_bounds__(768) void adaln_mla_ffn_kernel(Params p) {
  STAMP(0);
  first_requests();
  const unsigned t0 = now32();
  if (threadIdx.x == 64) *(volatile LAS unsigned*)(LDS_BYTES - 512) = t0;
  const int g = blockIdx.x & 7, idx = blockIdx.x >> 3;
  const int wave = __builtin_amdgcn_readfirstlane(threadIdx.x >> 6), lane = threadIdx.x & 63;
  if (wave == 0 && idx < 16) {
    u64 pc;
    unsigned left;
    asm volatile("s_getpc_b64 %0\n99:\n\ts_mov_b32 %1, 98f-99b-4\n\t.subsection 2\n98:\n\t.subsection 0" : "=s"(pc), "=s"(left));
    asm volatile("s_mov_b32 m0, %2\n\ts_nop 0\n\tglobal_load_lds_dword %0, %1" :: "v"(min((idx + 16 * min(lane >> 1, 21)) * 128u + (lane & 1) * 64, left)), "s"(pc), "s"(L_JUNK) : "memory");
  }
  const int wi = idx & 15, l15 = lane & 15, lq = lane >> 4, b8 = lane & 7;
  const int kp = 2 * (wi >> 2) + (wi & 1);
  const unsigned vx = lane * 16;
  asm volatile("" :: "s"(p.x), "s"(p.ckv), "s"(p.wq), "s"(p.wuk), "s"(p.wuv), "s"(p.wo), "s"(p.w1), "s"(p.w2), "s"(p.c), "s"(p.wmod),
               "s"(p.out), "s"(p.ws));
  STAMP(39);
  STAMPD(2); STAMPD(3); STAMPD(32); STAMPD(1);
  const rsrc_t rs = __builtin_amdgcn_make_buffer_rsrc(p.ws, 0, WS_BYTES, 0x00020000);
  const int my = arena(16 * g + wi);
  if (idx >= 16 && wave == 3) {
#pragma unroll
    for (int n = 0; n < 5; ++n) stg(rs, my + vx + n * 1024, v4u{SENT, SENT, SENT, SENT});
  }
  if ((idx & 18) == 18) {
    const int r = wave - 4;
    if (wave >= 4) {
      unsigned raw = M_RAW + r * 2048 + vx, d = (lane >> 3) == g ? M_LNX + r * 256 + b8 * 32 : L_JUNK, sc = M_SC + r * 144 + lane * 2;
      unsigned rc = M_RAW + r * 2048 + 1024 + lane * 2;
      pin(raw); pin(d); pin(sc); pin(rc);
      VMCNT(0);
      const v4u xr = lds128(raw);
      const float cv = bflo(*(LAS u16*)rc);
      *(LAS u16*)sc = (u16)pk(cv * __builtin_amdgcn_rcpf(1.f + __expf(-cv)), 0.f);
      bar();
      const f32x4 v0 = up4(xr.xy), v2 = up4(xr.zw), sv4 = v0 + v2, qv4 = v0 * v0 + v2 * v2;
      const float mean = wave_sum((sv4.x + sv4.y) + (sv4.z + sv4.w)) * (1.f / 512);
      const float msq = wave_sum((qv4.x + qv4.y) + (qv4.z + qv4.w)) * (1.f / 512);
      const float rstd = __builtin_amdgcn_rsqf(fmaxf(msq - mean * mean, 0.f) + EPS);
      *(LAS f32x4*)d = (v0 - mean) * rstd;
      *(LAS f32x4*)((LAS char*)d + 16) = (v2 - mean) * rstd;
      STAMPV(32);
      bar();
    } else {
      const int p4 = 4 * (lane & 3);
      unsigned s0 = tr_addr(W2, lane, 16 * wave + p4, 0), s1 = tr_addr(W2, lane, 16 * wave + p4, 1), c0 = s0 ^ 128, c1 = s1 ^ 128;
      unsigned ba = M_SC + b8 * 144 + lq * 16, la = M_LNX + b8 * 256 + (16 * wave + 4 * lq) * 4;
      int o = l15 < 8 ? my + wave * 256 + (lq >> 1) * 128 + l15 * 16 + (lq & 1) * 8 : OOB;
      int hh = wave == 3 && lane < 32 ? WS_HH + (4 * lane + (g >> 2)) * 512 + (g & 3) * 128 + kp * 4 : OOB;
      const float one = kp == 0 ? 1.f : 0.f;
      *(LAS unsigned*)L_CNT = 0u;
      f32x4 sa = {0.f, 0.f, 0.f, 0.f}, ca = {one, one, one, one};
      pin(s0); pin(s1); pin(c0); pin(c1); pin(ba); pin(la); pin(o); pin(hh); pin(ca);
      u64 tag = 0;
      int fo = wave == 3 ? (lane < 32 ? WS_EFL + (4 * lane + (g >> 2)) * 256 + (g & 3) * 64 + kp * 8 : lane == 32 ? WS_FLAG1 + (16 * g + wi) * 8 : OOB) : OOB;
      pin(fo);
      if (wave == 3) { tag = get_tag(p.ws); VMCNT(5); } else { VMCNT(0); }
      STAMPV(32);
      bar();
      const unsigned tn = now32();
      const v4u b0 = lds128(ba), f0 = tr_frag(s0, s1, 0), g0 = tr_frag(c0, c1, 0), b1 = lds128(ba, 64), f1 = tr_frag(s0, s1, 1), g1 = tr_frag(c0, c1, 1);
      __builtin_amdgcn_sched_barrier(0);
      st32(rs, hh, tn + (unsigned)tag);
      STAMPV(2);
      sa = mfma16(f1, b1, mfma16(f0, b0, sa));
      ca = mfma16(g1, b1, mfma16(g0, b0, ca));
      if (wave == 3) {
        VMCNT(1);
        __builtin_amdgcn_raw_buffer_store_b64(v2u{(unsigned)tag, (unsigned)(tag >> 32)}, rs, fo, 0, SC1);
        STAMPV(1);
      }
      bar();
      sa += u2f(lds128(la)) * ca;
      __builtin_amdgcn_raw_buffer_store_b64(v2u{pk(sa.x, sa.y), pk(sa.z, sa.w)}, rs, o, 0, SC1);
      STAMPV(3);
    }
    bar();
  } else if (idx >= 16 && wave == 3) {
    const u64 tag = get_tag(p.ws);
    VMCNT(0);
    __builtin_amdgcn_raw_buffer_store_b64(v2u{(unsigned)tag, (unsigned)(tag >> 32)}, rs,
                                          lane < 2 ? WS_EFL + (16 * g + (wi | 2) - (wi & 1) + lane) * 256 + (wi & 1) * 8 : lane == 2 ? WS_FLAG1 + (16 * g + wi) * 8 : OOB, 0, SC1);
  } else if (wave >= 8) {
    const int w = wave & 3, hq = idx >= 16 ? wi >> 2 : wi >> 2, row = idx >= 16 ? 256 * (wi & 1) : 256 * ((wi >> 1) & 1) + 128 + 64 * (wi & 1);
    const unsigned vq = (row + 4 * w + lq) * 8192 + (4 * g + hq) * 256 + (l15 ^ ((lq << 2) | w)) * 16;
    wait_until(t0 + T_W);
    dma_run<true>(p.wq, vq, (idx >= 16 ? W0 : T_CNT + 1024) + w * 1024, idx >= 16 ? 16 : 4, 16 * 8192, idx >= 16 ? 4096 : 0);
    if (idx < 16) dma_run<true>(p.wuk, PF_UK == 4 ? vq : vq + (128 * (wi & 3) - row) * 8192, T_CNT + 1024 + w * 1024, PF_UK, 16 * 8192, 0);
  }
  FENCE();
  if (idx < 16) attn_wg(p, g, idx, wave, t0);
  else { weight_wg(p, g, wi, wave, fresh_lane(), t0); ROLE_END(); }
  unsigned xcc; asm volatile("s_getreg_b32 %0, hwreg(HW_REG_XCC_ID)" : "=s"(xcc));
  if (wave == 2 && (idx >= 8) && (PK < 100 || (xcc & 15) == PK - 100)) {
    wait_until(t0 + 3500);
    const unsigned d = __builtin_amdgcn_readfirstlane(*(volatile LAS unsigned*)(LDS_BYTES - 512)) - t0;
    wait_until(t0 + 4000 + (d < 3500 ? d : 0) * PSCALE);
  }
}

extern "C" void kernel_launch(void* const* d_in, const int* in_sizes, int n_in, void* d_out, int out_size, void* d_ws, size_t ws_size,
                              hipStream_t stream) {
  Params p;
  p.x = (const u16*)d_in[0]; p.ckv = (const u16*)d_in[1]; p.wq = (const u16*)d_in[2]; p.wuk = (const u16*)d_in[3];
  p.wuv = (const u16*)d_in[4]; p.wo = (const u16*)d_in[5]; p.w1 = (const u16*)d_in[7]; p.w2 = (const u16*)d_in[8];
  p.c = (const u16*)d_in[9]; p.wmod = (const u16*)d_in[10];
  p.out = (u16*)d_out; p.ws = (unsigned char*)d_ws + WS_BASE;
  (void)hipFuncSetAttribute((const void*)adaln_mla_ffn_kernel, hipFuncAttributeMaxDynamicSharedMemorySize, LDS_BYTES);
  adaln_mla_ffn_kernel<<<256, 768, LDS_BYTES, stream>>>(p);
}
```

```cpp
#include <hip/hip_runtime.h>
#include <hip/hip_bf16.h>
#pragma clang diagnostic ignored "-Wint-to-pointer-cast"
#pragma clang diagnostic ignored "-Wsometimes-uninitialized"

typedef unsigned short u16;
typedef unsigned long long u64;
typedef unsigned v4u __attribute__((ext_vector_type(4)));
typedef unsigned v2u __attribute__((ext_vector_type(2)));
typedef float f32x2 __attribute__((ext_vector_type(2)));
typedef float f32x4 __attribute__((ext_vector_type(4)));
typedef float f32x16 __attribute__((ext_vector_type(16)));
typedef short s16x4 __attribute__((ext_vector_type(4)));
typedef short bf16x8 __attribute__((ext_vector_type(8)));
typedef __bf16 bf16x2 __attribute__((ext_vector_type(2)));
#define GAS __attribute__((address_space(1)))
#define LAS __attribute__((address_space(3)))
#define DI __device__ __forceinline__
#define RLX_AGENT __ATOMIC_RELAXED, __HIP_MEMORY_SCOPE_AGENT
typedef __amdgpu_buffer_rsrc_t rsrc_t;

#define ATT_DELAY 0
#define T_H 62
#define D_UK 8
#define T_QP 125
#define T_QA 118
#define T_B 80
#define T_C 158
#define T_D 295
#define ARR_EARLY 0
#define T_ML 62
#define T_PART 118
#define T_B6 80
#define D_F 0
#define D_ML 2
#define FLAG_AT 1
#define D_PREF 16
#define PRIO_S 2
#define PRIO_PV 1
#define PF_NT false
#define D_W 0
#define T_PREF 0
#define T_W 30
#define PF_EVEN 24
#define T_FILL 0
#define T_FLAG 260
#define WS_BASE 0
#define FILL_AUX 16
#define WWIN 7
#define DRY 0
#define T_EF 220
#define T_LQ 190
#define T_LQ2 100
#define PF_UK 8
#define T_UK 190
#define T_FP 150
#define T_RP 560
#define T_HOLD 45
#define ILV 8
#define PRIO_L 3
#define FP_AT 0
#define T_WO 380
#define FP_ALL 1
#define FP_W2 1
#define PART_AUX 16
#define LWS "37"
#define T_L 150
#define FILL_EARLY 1
#define D_FILL 0
#define PREF 1
#define WO_NT 1

constexpr int WS_FLAG1 = 4096, WS_FLAG2 = 8192;
constexpr int WS_EFL = 16384  , WS_HP = 81920, WS_HB = 98304  ,
              WS_HH = 131072  , WS_HQ = 196608  , WS_ARR = 262144  ;
constexpr int WS_ARENA = 327680, ASTR = 58624;
constexpr int A_S = 0, A_E = 1024, A_ML = 5120, A_X1 = 6144, A_OUTP = 7168, A_YP = 15360, A_PART = 23552, A_CK = 56320;
constexpr int NROT = 16, WS_BYTES = WS_ARENA + NROT * 128 * ASTR;
constexpr unsigned SENT = 0xFFFFFFFFu;
constexpr int OOB = 0x7FFFFF00;
constexpr int SC1 = 16;
constexpr int SPIN_H = 1 << 10;
constexpr int SPIN_G = 1 << 19;
constexpr int SPIN_L = 1 << 23;
constexpr int LDS_BYTES = 163840;
constexpr float EPS = 1e-5f;
constexpr float QSCALE = 0.08838834764831845f * 1.4426950408889634f;
constexpr float BUMP = 40.f;

#define STAMP(k) do {} while (0)
#define STAMPV(k) do {} while (0)
#define STAMPD(k) do {} while (0)
#define STV(k) 0
#define DUMP(k) do {} while (0)
#define DUMPC(k, c) do {} while (0)

DI int fresh_lane() {
  int l;
  asm volatile("v_mbcnt_lo_u32_b32 %0, -1, 0\n\tv_mbcnt_hi_u32_b32 %0, -1, %0" : "=v"(l));
  return l;
}
template <class T> DI void pin(T& v) { asm volatile("" : "+v"(v)); }
DI float bflo(unsigned u) { return __uint_as_float(u << 16); }
DI float bfhi(unsigned u) { return __uint_as_float(u & 0xFFFF0000u); }
DI unsigned pk(float a, float b) { f32x2 v = {a, b}; return __builtin_bit_cast(unsigned, __builtin_convertvector(v, bf16x2)); }
DI f32x4 mfma16(v4u a, v4u b, f32x4 c) {
  return __builtin_amdgcn_mfma_f32_16x16x32_bf16(__builtin_bit_cast(bf16x8, a), __builtin_bit_cast(bf16x8, b), c, 0, 0, 0);
}
DI f32x16 mfma32(v4u a, v4u b, f32x16 c) {
  return __builtin_amdgcn_mfma_f32_32x32x16_bf16(__builtin_bit_cast(bf16x8, a), __builtin_bit_cast(bf16x8, b), c, 0, 0, 0);
}
DI v4u ldg(rsrc_t rs, int off, int so = 0) { return __builtin_amdgcn_raw_buffer_load_b128(rs, off, so, SC1); }
DI void stg(rsrc_t rs, int off, v4u v) { __builtin_amdgcn_raw_buffer_store_b128(v, rs, off, 0, SC1); }
DI unsigned umax4(v4u v) { return max(max(v.x, v.y), max(v.z, v.w)); }
DI v4u f2u(f32x4 v) { return __builtin_bit_cast(v4u, v); }
DI f32x4 u2f(v4u v) { return __builtin_bit_cast(f32x4, v); }
DI bool nan4(f32x4 v) { return __builtin_isunordered(v.x, v.y) | __builtin_isunordered(v.z, v.w); }
DI v4u lds128(unsigned a, int o = 0) { return *(LAS v4u*)((LAS char*)a + o); }
DI v2u ldstr(unsigned a, int o = 0) { return __builtin_bit_cast(v2u, __builtin_amdgcn_ds_read_tr16_b64_v4i16((LAS s16x4*)((LAS char*)a + o))); }
DI void bar() {
  __builtin_amdgcn_sched_barrier(0);
  asm volatile("s_waitcnt lgkmcnt(0)\n\ts_barrier" ::: "memory");
  __builtin_amdgcn_sched_barrier(0);
}
#define VMCNT_(n) asm volatile("s_waitcnt vmcnt(" #n ")" ::: "memory")
#define VMCNT(n) VMCNT_(n)
#define FENCE() asm volatile("" ::: "memory")
#define ROLE_END() __builtin_amdgcn_s_waitcnt(0x0F70)
template <bool NT> DI void dma16(const void* base, unsigned voff, unsigned dst) {
  if (NT) asm volatile("s_mov_b32 m0, %2\n\ts_nop 0\n\tglobal_load_lds_dwordx4 %0, %1 nt" :: "v"(voff), "s"(base), "s"(dst) : "memory");
  else asm volatile("s_mov_b32 m0, %2\n\ts_nop 0\n\tglobal_load_lds_dwordx4 %0, %1" :: "v"(voff), "s"(base), "s"(dst) : "memory");
}
template <bool NT> DI void dma_run(const void* base, unsigned voff, unsigned dst, int n, int gstr, int lstr) {
  const unsigned char* b = (const unsigned char*)base;
#pragma unroll 1
  for (int i = 0; i < n; ++i) { VMCNT(WWIN); dma16<NT>(b, voff, dst); b += gstr; dst += lstr; }
}
template <int N> DI void nap() { if (N > 0) __builtin_amdgcn_s_sleep(N); }
DI void lsig(unsigned a, unsigned v = 1u) { asm volatile("s_mov_b64 exec, 1\n\tds_add_u32 %0, %1\n\ts_mov_b64 exec, -1" :: "v"(a), "v"(v) : "memory"); }
DI unsigned lpeek(unsigned a) { return *(volatile LAS unsigned*)a; }
template <bool NAP = false> DI void lwait(unsigned a, unsigned want) {
  unsigned v, n = SPIN_L;
  if (NAP)
    asm volatile("1:\n\tds_read_b32 %0, %2\n\ts_waitcnt lgkmcnt(0)\n\tv_cmp_le_u32 vcc, %3, %0\n\ts_cbranch_vccnz 2f\n\t"
                 "s_sleep 1\n\ts_sub_u32 %1, %1, 1\n\ts_cbranch_scc0 1b\n2:" : "=&v"(v), "+s"(n) : "v"(a), "s"(want) : "vcc", "scc", "memory");
  else
    asm volatile("1:\n\tds_read_b32 %0, %2\n\ts_waitcnt lgkmcnt(0)\n\tv_cmp_le_u32 vcc, %3, %0\n\ts_cbranch_vccnz 2f\n\t"
                 "s_sub_u32 %1, %1, 1\n\ts_cbranch_scc0 1b\n2:" : "=&v"(v), "+s"(n) : "v"(a), "s"(want) : "vcc", "scc", "memory");
}
template <int CTRL> DI float dppf(float v) {
  return __builtin_bit_cast(float, __builtin_amdgcn_update_dpp(0, __builtin_bit_cast(int, v), CTRL, 0xF, 0xF, true));
}
DI float row16_max(float v) {
  asm volatile("s_nop 1\n\tv_max_f32_dpp %0, %0, %0 row_ror:8 row_mask:0xf bank_mask:0xf\n\ts_nop 1\n\tv_max_f32_dpp %0, %0, %0 row_ror:4 row_mask:0xf bank_mask:0xf\n\t"
               "s_nop 1\n\tv_max_f32_dpp %0, %0, %0 row_ror:2 row_mask:0xf bank_mask:0xf\n\ts_nop 1\n\tv_max_f32_dpp %0, %0, %0 row_ror:1 row_mask:0xf bank_mask:0xf" : "+v"(v));
  return v;
}
DI float row16_sum(float v) { v += dppf<0x128>(v); v += dppf<0x124>(v); v += dppf<0x122>(v); return v + dppf<0x121>(v); }
DI float xsum16(float v) { auto r = __builtin_amdgcn_permlane16_swap(__float_as_uint(v), __float_as_uint(v), false, false); return __uint_as_float(r[0]) + __uint_as_float(r[1]); }
DI float xsum32(float v) { auto r = __builtin_amdgcn_permlane32_swap(__float_as_uint(v), __float_as_uint(v), false, false); return __uint_as_float(r[0]) + __uint_as_float(r[1]); }
DI float xmax16(float v) { auto r = __builtin_amdgcn_permlane16_swap(__float_as_uint(v), __float_as_uint(v), false, false); return fmaxf(__uint_as_float(r[0]), __uint_as_float(r[1])); }
DI float xmax32(float v) { auto r = __builtin_amdgcn_permlane32_swap(__float_as_uint(v), __float_as_uint(v), false, false); return fmaxf(__uint_as_float(r[0]), __uint_as_float(r[1])); }
DI float wave_sum(float v) { return xsum32(xsum16(row16_sum(v))); }

#define XS8(d, a, b) "v_xor_b32_dpp " d ", " a ", " a " row_ror:8 row_mask:0xf bank_mask:0x3\n\tv_xor_b32_dpp " d ", " b ", " b " row_ror:8 row_mask:0xf bank_mask:0xc\n\t"
#define XS4(d, a, b) "v_xor_b32_dpp " d ", " a ", " a " row_half_mirror row_mask:0xf bank_mask:0x5\n\tv_xor_b32_dpp " d ", " b ", " b " row_half_mirror row_mask:0xf bank_mask:0xa\n\t"
#define XQ(d, p) "v_xor_b32_dpp " d ", " d ", " d " quad_perm:" p " row_mask:0xf bank_mask:0xf\n\t"
DI void xor_rows8(const unsigned (&v)[8], unsigned& r0, unsigned& r1) {
  unsigned t0, t1, t2, t3;
  asm volatile("s_nop 1\n\t" XS8("%2", "%6", "%10") XS8("%4", "%8", "%12") XS8("%3", "%7", "%11") XS8("%5", "%9", "%13")
               XS4("%0", "%2", "%4") XS4("%1", "%3", "%5") XQ("%0", "[2,3,0,1]") "s_nop 0\n\t" XQ("%1", "[2,3,0,1]") XQ("%0", "[1,0,3,2]")
               "s_nop 0\n\t" XQ("%1", "[1,0,3,2]")
               : "=&v"(r0), "=&v"(r1), "=&v"(t0), "=&v"(t1), "=&v"(t2), "=&v"(t3)
               : "v"(v[0]), "v"(v[1]), "v"(v[2]), "v"(v[3]), "v"(v[4]), "v"(v[5]), "v"(v[6]), "v"(v[7]));
}
DI unsigned xor_rows2(unsigned a, unsigned b) {
  unsigned r;
  asm volatile("s_nop 1\n\t" XS8("%0", "%1", "%2") "s_nop 1\n\tv_xor_b32_dpp %0, %0, %0 row_half_mirror row_mask:0xf bank_mask:0xf\n\ts_nop 1\n\t"
               XQ("%0", "[2,3,0,1]") "s_nop 1\n\t" XQ("%0", "[1,0,3,2]") "s_nop 1" : "=&v"(r) : "v"(a), "v"(b));
  return r;
}
template <int N> DI bool any_sent(const v4u (&r)[N]) {
  unsigned m = 0;
#pragma unroll
  for (int i = 0; i < N; ++i) m = max(m, umax4(r[i]));
  return __any(m == SENT);
}
template <int N, int AUX = SC1> DI void load_n(rsrc_t rs, int off0, int str, v4u (&r)[N]) {
  FENCE();
#pragma unroll
  for (int i = 0; i < N; ++i) r[i] = __builtin_amdgcn_raw_buffer_load_b128(rs, off0, i * str, AUX);
  __builtin_amdgcn_sched_barrier(0);
}
template <int N, int NAP = 0> DI void gather(rsrc_t rs, int off0, int str, v4u (&r)[N]) {
#pragma unroll 1
  for (int sp = 0; sp < SPIN_G; ++sp) {
    load_n<N>(rs, off0, str, r);
    if (!any_sent<N>(r)) break;
    nap<NAP>();
  }
}
DI u64 get_tag(const unsigned char* ws) {
  u64 e;
  asm volatile("s_load_dwordx2 %0, %1, 0x0\n\ts_waitcnt lgkmcnt(0)" : "=s"(e) : "s"(ws) : "memory");
  return e * 6364136223846793005ull + 1442695040888963407ull;
}
DI bool flags_up(v4u f, u64 tag) {
  const unsigned lo = (unsigned)tag, hi = (unsigned)(tag >> 32);
  return __all(f.x == lo && f.y == hi && f.z == lo && f.w == hi);
}
DI void wait_flags(rsrc_t rs, int off, u64 tag) {
#pragma unroll 1
  for (int sp = 0; sp < SPIN_G; ++sp) {
    FENCE();
    if (flags_up(ldg(rs, off), tag)) break;
    __builtin_amdgcn_s_sleep(8);
  }
  FENCE();
}
typedef unsigned s8u __attribute__((ext_vector_type(8)));
DI s8u sload8(const unsigned char* a) {
  s8u v;
  asm volatile("s_load_dwordx8 %0, %1, 0x0 glc\n\ts_waitcnt lgkmcnt(0)" : "=s"(v) : "s"(a) : "memory");
  return v;
}
DI unsigned max8(s8u v) { return max(max(max(v[0], v[1]), max(v[2], v[3])), max(max(v[4], v[5]), max(v[6], v[7]))); }
DI unsigned now32() { return (unsigned)__builtin_amdgcn_s_memrealtime(); }
constexpr unsigned HWIN = 1u << 12;
DI unsigned latest_time(const unsigned char* a, unsigned t0, unsigned key) {
  unsigned m = 0;
#pragma unroll 1
  for (int sp = 0; sp < SPIN_H; ++sp) {
    m = max8(sload8(a) - (key + t0 - HWIN));
    if (m < 2 * HWIN) return m + t0 - HWIN;
  }
  return t0;
}
template <int STR> DI unsigned latest_time32(const unsigned char* a, unsigned t0, unsigned key) {
  const unsigned kb = key + t0 - HWIN;
#pragma unroll 1
  for (int sp = 0; sp < SPIN_H; ++sp) {
    s8u u, v, x, y;
    asm volatile("s_load_dwordx8 %0, %4, 0x0 glc\n\ts_load_dwordx8 %1, %4, %5 glc\n\ts_load_dwordx8 %2, %4, %6 glc\n\t"
                 "s_load_dwordx8 %3, %4, %7 glc\n\ts_waitcnt lgkmcnt(0)"
                 : "=&s"(u), "=&s"(v), "=&s"(x), "=&s"(y) : "s"(a), "i"(STR), "i"(2 * STR), "i"(3 * STR) : "memory");
    const unsigned m = max(max(max8(u - kb), max8(v - kb)), max(max8(x - kb), max8(y - kb)));
    if (m < 2 * HWIN) return m + t0 - HWIN;
  }
  return t0;
}
DI void wait_until(unsigned t) {
#pragma unroll 1
  for (int sp = 0; sp < SPIN_H; ++sp) if ((int)(now32() - t) >= 0) break;
}
DI int arena(int wid) { return WS_ARENA + wid * ASTR; }
DI int rarena(int wid, u64 tag) { return WS_ARENA + ((int)(tag & (NROT - 1)) * 128 + wid) * ASTR; }

DI unsigned tr_addr(unsigned region, int lane, int col4, int t) {
  const int g = lane >> 4, q = (lane & 15) >> 2;
  return region + (8 * g + 4 * t + q) * 256 + 16 * ((col4 >> 3) ^ ((q << 2) | ((2 * g + t) & 3))) + 8 * ((col4 >> 2) & 1);
}
DI v4u tr_frag(unsigned a0, unsigned a1, int ks) {
  const v2u lo = ldstr(a0, ks * 8192), hi = ldstr(a1, ks * 8192);
  return v4u{lo.x, lo.y, hi.x, hi.y};
}
DI void panel_frags(unsigned region, int lane, int col_a, int col_b, v4u (&f)[2][4]) {
  const unsigned a0 = tr_addr(region, lane, col_a, 0), a1 = tr_addr(region, lane, col_a, 1);
  const unsigned b0 = tr_addr(region, lane, col_b, 0), b1 = tr_addr(region, lane, col_b, 1);
#pragma unroll
  for (int ks = 0; ks < 4; ++ks) { f[0][ks] = tr_frag(a0, a1, ks); f[1][ks] = tr_frag(b0, b1, ks); }
}
DI void gemv2(const v4u (&f)[2][4], unsigned act, f32x4& d0, f32x4& d1) {
  v4u bf[4];
#pragma unroll
  for (int ks = 0; ks < 4; ++ks) bf[ks] = lds128(act, ks * 64);
  __builtin_amdgcn_sched_barrier(0);
#pragma unroll
  for (int ks = 0; ks < 4; ++ks) { d0 = mfma16(f[0][ks], bf[ks], d0); d1 = mfma16(f[1][ks], bf[ks], d1); }
}
DI f32x4 up4(v2u v) { return f32x4{bflo(v.x), bfhi(v.x), bflo(v.y), bfhi(v.y)}; }
DI v4u pk8(f32x4 a, f32x4 b) { return v4u{pk(a.x, a.y), pk(a.z, a.w), pk(b.x, b.y), pk(b.z, b.w)}; }
DI v4u silu8(v4u cv) {
  v4u o;
#pragma unroll
  for (int e = 0; e < 4; ++e) {
    const float a = bflo(cv[e]), c = bfhi(cv[e]);
    o[e] = pk(a * __builtin_amdgcn_rcpf(1.f + __expf(-a)), c * __builtin_amdgcn_rcpf(1.f + __expf(-c)));
  }
  return o;
}

struct Params {
  const u16 *x, *ckv, *wq, *wuk, *wuv, *wo, *w1, *w2, *c, *wmod;
  u16* out;
  unsigned char* ws;
};

constexpr unsigned W0 = 0, W1 = 32768, W2 = 65536, L_SC = 98304  , L_A1 = L_SC + 4224  ,
                   L_A3 = L_A1 + 4224  , L_A4 = L_A3 + 2176, L_CNT = L_A4 + 2176,
                   M_RAW = 114688  , M_SC = M_RAW + 16384  , M_LNX = M_SC + 1152  ;
constexpr unsigned T_SP = 131072, T_P = T_SP + 16384, T_FAC = T_P + 8192, T_CNT = T_FAC + 1024,
                   L_A5 = 98304  , L_A6 = L_A5 + 8320  ;
constexpr unsigned C_LAND = T_CNT + 32, C_FREE = C_LAND + 4, C_SRDY = C_LAND + 8, C_PRDY = C_LAND + 12,
                   C_QST = T_CNT + 192, C_QRD = T_CNT + 196, C_HOLD = T_CNT + 200;
constexpr unsigned L_JUNK = LDS_BYTES - 256;

template <int N> DI f32x4 reduce_rows(rsrc_t rs, int off0, int offl, v4u sel, int& rounds) {
  f32x4 a;
#pragma unroll 1
  for (int sp = 0; sp < SPIN_G; ++sp) {
    v4u d[N];
    FENCE();
#pragma unroll
    for (int i = 0; i < N; ++i) d[i] = ldg(rs, i == N - 1 ? offl : off0, i * 16 * ASTR);
    __builtin_amdgcn_sched_barrier(0);
    f32x4 b = {0.f, 0.f, 0.f, 0.f};
    a = b;
#pragma unroll
    for (int i = 0; i < N; i += 2) { a = mfma16(sel, d[i], a); b = mfma16(sel, d[i + 1], b); }
    a += b;
    rounds = sp + 1;
    if (!__any(a.x != a.x) || !any_sent<N>(d)) break;
  }
#pragma unroll
  for (int r = 0; r < 4; ++r) { a[r] += dppf<0x128>(a[r]); a[r] += dppf<0x124>(a[r]); }
  return a;
}
DI v4u pick(bool lo, v4u a, v4u b) { return v4u{lo ? a.x : b.x, lo ? a.y : b.y, lo ? a.z : b.z, lo ? a.w : b.w}; }
DI void st32(rsrc_t rs, int off, unsigned v) { __builtin_amdgcn_raw_buffer_store_b32(v, rs, off, 0, SC1); }

DI void weight_wg(const Params& p, int g, int wi, int wave, int lane, unsigned t0) {
  unsigned char* ws = p.ws;
  const rsrc_t rs = __builtin_amdgcn_make_buffer_rsrc(ws, 0, WS_BYTES, 0x00020000);
  const int h = 4 * g + (wi >> 2), j = wi & 3, wid = 16 * g + wi, my = arena(wid);
  const int l15 = lane & 15, lq = lane >> 4, b8 = lane & 7;
  const bool bvalid = l15 < 8, isq = j < 2;
  const int w = wave & 3;
  const int ch = l15 ^ ((lq << 2) | w);
  const unsigned dst0 = w * 1024;
  const unsigned vuv = (128 * j + 16 * ((4 * w + lq) & 7) + (w >> 1)) * 8192 + h * 256 + ch * 16;
  const f32x4 z4 = {0.f, 0.f, 0.f, 0.f};

  if (wave >= 8) {
    if (!isq) wait_until(t0 + T_UK);
    if (!isq) dma_run<true>(p.wuk, (256 * (j - 2) + 4 * w + lq) * 8192 + h * 256 + ch * 16, W0 + dst0, 16, 16 * 8192, 4096);
    const unsigned key = (unsigned)get_tag(ws);
    int hp = isq && lane < 8 ? WS_HP + (4 * h + 2 + (lane & 1)) * 128 + j * 4 + (lane >> 1) * 8 : OOB;
    pin(hp);
    VMCNT(0);
    STAMP(44);
    if (!isq) lsig(L_CNT);
    bar();
    const unsigned tb1 = now32();
    nap<3>();
    if (w == 0) st32(rs, hp, tb1 + key);
    bar();
    v4u wo[4][8];
    const GAS unsigned char* wb = (const GAS unsigned char*)p.wo + (h * 128 + 8 * lq) * 1024 + w * 256 + (4 * (l15 & 3) + (l15 >> 2)) * 16;
    bar();
    if (FP_AT == 0 && g < 7 && (FP_ALL || !isq)) {
      const int e = 2 * (wi >> 2) + (j & 1);
      wait_until(tb1 + (isq ? T_FP + T_QP : T_FP));
      if (!isq) dma_run<false>(p.w1, (64 * e + 16 * w + (lane >> 5)) * 3584 + g * 512 + (lane & 31) * 16, W1 + dst0, 8, 2 * 3584, 0);
      if (FP_ALL == isq && FP_W2) dma_run<false>(p.w2, (256 * g + 32 * e + 8 * w) * 1024 + lane * 16, W1 + dst0, 8, 1024, 0);
    }
    wait_until(tb1 + (isq ? T_WO + T_QP : T_WO));
#pragma unroll
    for (int ks = 0; ks < 4; ++ks) {
      const GAS unsigned char* wk = wb + ((ks + j) & 3) * 32768;
#pragma unroll
      for (int r = 0; r < 8; ++r) wo[ks][r] = ks == 0 ? __builtin_nontemporal_load((const GAS v4u*)(wk + r * 1024)) : *(const GAS v4u*)(wk + r * 1024);
      if (ks == 0) VMCNT(0);
    }
    if (FP_AT == 2 && g < 7 && (FP_ALL || !isq)) {
      const int e = 2 * (wi >> 2) + (j & 1);
      wait_until(tb1 + (isq ? T_FP + T_QP : T_FP));
      if (!isq) dma_run<false>(p.w1, (64 * e + 16 * w + (lane >> 5)) * 3584 + g * 512 + (lane & 31) * 16, W1 + dst0, 8, 2 * 3584, 0);
      if (FP_ALL == isq && FP_W2) dma_run<false>(p.w2, (256 * g + 32 * e + 8 * w) * 1024 + lane * 16, W1 + dst0, 8, 1024, 0);
    }

    __builtin_amdgcn_sched_barrier(0);
#pragma unroll
    for (int ks = 0; ks < 4; ++ks) {
      v4u o[8];
#pragma unroll
      for (int dd = 0; dd < 4; ++dd)
#pragma unroll
        for (int rp = 0; rp < 4; ++rp) {
          o[2 * dd][rp] = __builtin_amdgcn_perm(wo[ks][2 * rp + 1][dd], wo[ks][2 * rp][dd], 0x05040100u);
          o[2 * dd + 1][rp] = __builtin_amdgcn_perm(wo[ks][2 * rp + 1][dd], wo[ks][2 * rp][dd], 0x07060302u);
        }
#pragma unroll
      for (int t = 0; t < 8; ++t) { wo[ks][t] = o[t]; pin(wo[ks][t]); }
    }
    STAMP(10);
    unsigned ba[4];
#pragma unroll
    for (int ks = 0; ks < 4; ++ks) { ba[ks] = L_A4 + b8 * 272 + ((ks + j) & 3) * 64 + lq * 16; pin(ba[ks]); }
    int oo = my + A_OUTP + (16 * w + 4 * (l15 >> 3) + lq) * 128 + b8 * 16;
    pin(oo);
    __builtin_amdgcn_s_setprio(2);
    bar();
    bar();
    v4u bf[4];
#pragma unroll
    for (int ks = 0; ks < 4; ++ks) bf[ks] = lds128(ba[ks]);
    v4u r0, r1;
#pragma unroll
    for (int tp = 0; tp < 4; ++tp) {
      f32x4 e = z4, o = z4;
#pragma unroll
      for (int ks = 0; ks < 4; ++ks) { e = mfma16(wo[ks][2 * tp], bf[ks], e); o = mfma16(wo[ks][2 * tp + 1], bf[ks], o); }
      r0[tp] = bvalid ? pk(e[0], o[0]) : pk(e[1], o[1]);
      r1[tp] = bvalid ? pk(e[2], o[2]) : pk(e[3], o[3]);
    }
    stg(rs, oo, r0);
    stg(rs, oo + 1024, r1);
    STAMP(14);
    if (wave == 8) { DUMP(10); DUMP(14); if (isq) DUMP(44); else { const unsigned stv60 = STV(44); DUMP(60); } }
    ROLE_END();
  } else if (wave >= 4) {
    __builtin_amdgcn_s_setprio(2);
    const int t = w * 64 + lane;
    const u64 tag = get_tag(ws);
    *(LAS unsigned*)(L_CNT + 4) = 0u;
    v4u msk;
    {
      const unsigned half = (lane & 1) ? 0xFFFF0000u : 0x0000FFFFu;
      const int dw = b8 >> 1;
      msk = v4u{dw == 0 ? half : 0u, dw == 1 ? half : 0u, dw == 2 ? half : 0u, dw == 3 ? half : 0u};
    }
    v4u sela = msk & (bvalid ? 0x3F803F80u : 0u), selb = msk & (bvalid ? 0u : 0x3F803F80u);
    int go = isq ? arena(64 * j + 16 * w + 4 * (lq >> 1) + 2 + (lq & 1)) + A_S + l15 * 16 : arena(4 * h) + A_E + t * 16;
    unsigned gd = isq ? L_A1 + b8 * 528 + (64 * w + 16 * (lq >> 1) + 8 * (l15 >> 3) + 4 * (lq & 1)) * 2
                      : L_A1 + b8 * 528 + (t >> 3) * 8;
    pin(msk); pin(sela); pin(selb); pin(go); pin(gd);
    STAMPD(34); STAMPD(46);
    if (w == 2) {
      wait_until(t0 + T_EF);
      wait_flags(rs, WS_EFL + wid * 256 + (isq ? l15 * 16 : 0), tag);
      STAMPV(46);
    }
    if (w == 3) {
      unsigned tr;
      if (isq) {
        tr = latest_time32<128>(ws + WS_HH + wid * 512, t0, (unsigned)tag) + T_H;
      } else {
        tr = latest_time(ws + WS_HP + wid * 128, t0, (unsigned)tag) + T_QP;
        lwait(L_CNT, 4);
        const unsigned tn = now32() + 8;
        if ((int)(tn - tr) > 0) tr = tn;
        st32(rs, WS_HQ + (2 * lane + j - 2) * 512 + (h & 3) * 128 + g * 4, tr + (unsigned)tag);
      }
      STAMPV(34);
      wait_until(tr);
    }
    bar();
    int rh = 0;
    if (isq) {
      f32x4 h0, h1;
#pragma unroll 1
      for (int sp = 0; sp < SPIN_G; ++sp) {
        v4u r[8];
        FENCE();
#pragma unroll
        for (int i = 0; i < 8; ++i) r[i] = ldg(rs, go + (i & 3) * 256, (i >> 2) * 8 * ASTR);
        __builtin_amdgcn_sched_barrier(0);
        h0 = mfma16(selb, r[5], mfma16(sela, r[4], mfma16(selb, r[1], mfma16(sela, r[0], z4))));
        h1 = mfma16(selb, r[7], mfma16(sela, r[6], mfma16(selb, r[3], mfma16(sela, r[2], z4))));
        rh = sp + 1;
        if (!__any(__builtin_isunordered(h0.x, h1.x)) || !any_sent<8>(r)) break;
      }
      *(LAS v2u*)gd = v2u{pk(h0.x, h0.y), pk(h0.z, h0.w)};
      *(LAS v2u*)((LAS char*)gd + 64) = v2u{pk(h1.x, h1.y), pk(h1.z, h1.w)};
    } else {
      f32x4 q;
#pragma unroll 1
      for (int sp = 0; sp < SPIN_G; ++sp) {
        v4u r[2];
        load_n<2>(rs, go, ASTR, r);
        q = u2f(r[0]) + u2f(r[1]);
        rh = sp + 1;
        if (!__any(nan4(q)) || !any_sent<2>(r)) break;
      }
      *(LAS v2u*)gd = v2u{pk(q.x, q.y), pk(q.z, q.w)};
    }
    STAMP(5);
    bar();
    bar();
    if (isq)
      *(LAS v4u*)(L_SC + (2 * w + (lane >> 5)) * 528 + (lane & 31) * 16) =
          silu8(*(const GAS v4u*)(p.c + (2 * w + (lane >> 5)) * 512 + j * 256 + (lane & 31) * 8));
    lsig(L_CNT + 4);
    if (w == 3) {
      nap<D_FILL>();
#pragma unroll 1
      for (int n = wid < 8 ? 6 : 7; n < (wid < 56 ? 23 : 15); ++n)
        stg(rs, my + lane * 16 + n * 1024, v4u{SENT, SENT, SENT, SENT});
      VMCNT(0);
      if (lane == 0) __hip_atomic_store((GAS u64*)(ws + WS_FLAG2 + wid * 8), tag, RLX_AGENT);
    }
    const int el = (4 * (wi & 1) + (lane & 3)) * 512 + 8 * (wid >> 1) + 4 * (lq & 1), pc = wi * 64 + (lane & 3) * 16;
    int gmo = w == 0 ? arena(4 * g) + A_S + pc + (lq & 1) * 8 : OOB - 100 * ASTR;
    pin(gmo);
    int mo = rarena(32 * w + (lane & 31), tag) + A_ML + h * 16;
    int po = rarena(wid, tag) + A_PART + w * 8192 + lane * 16;
    unsigned o3 = L_A3 + (2 * w + (lane >> 5)) * 272 + l15 * 16 + (lq & 1) * 8;
    int hb = lane < 16 ? WS_HB + (16 * lane + ((wi - 5 * g) & 15)) * 128 + g * 4 : OOB;
    int co = lane < 32 ? mo + (A_CK - A_ML) + j * 512 : OOB;
    int bp = 4 * lq + (bvalid ? 0 : 128);
    unsigned tlo = lane < 32 ? (unsigned)tag : 0u, thi = lane < 32 ? (unsigned)(tag >> 32) : 0u;
    pin(mo); pin(po); pin(o3); pin(hb); pin(co); pin(bp); pin(tlo); pin(thi);
    const unsigned ta = latest_time32<128>(ws + WS_ARR + (4 * h + w) * 512, t0, (unsigned)tag);
    STAMP(11);
    *(LAS unsigned*)(L_CNT + 16 + w * 4) = ta;
    wait_until(ta + T_ML);
    FENCE();
    v4u ml = ldg(rs, mo);
    wait_until(ta + T_PART);
    v4u d[8], ck;
    load_n<8, PART_AUX>(rs, po, 1024, d);
    ck = ldg(rs, co);
    __builtin_amdgcn_sched_barrier(0);
    if (__builtin_expect(__any(((ml.y ^ (unsigned)tag) | (ml.w ^ (unsigned)(tag >> 32))) != 0), 0)) {
#pragma unroll 1
      for (int sp = 0; sp < SPIN_G; ++sp) {
        FENCE();
        ml = ldg(rs, mo);
        if (__all(ml.y == (unsigned)tag && ml.w == (unsigned)(tag >> 32))) break;
      }
    }
    v4u sel[8];
    {
      const float r = __uint_as_float(ml.x), e = __builtin_amdgcn_exp2f(r - row16_max(r));
      const float wgt = e * __builtin_amdgcn_rcpf(row16_sum(e * __uint_as_float(ml.z)));
      const unsigned hi = pk(wgt, wgt);
      const float rest = wgt - bflo(hi);
      const int v = lane < 32 ? hi : pk(rest, rest);
      unsigned wv[8];
#pragma unroll
      for (int i = 0; i < 8; ++i) wv[i] = __builtin_amdgcn_ds_bpermute(bp + 16 * i, v);
      __builtin_amdgcn_sched_barrier(0);
#pragma unroll
      for (int i = 0; i < 8; ++i) { sel[i] = msk & wv[i]; pin(sel[i]); }
    }
    f32x4 oa, ob;
    int rp = 0;
#pragma unroll 1
    for (int sp = 0; sp < SPIN_G; ++sp) {
      oa = z4; ob = z4;
      unsigned xe = 0, xo = 0;
#pragma unroll
      for (int i = 0; i < 8; ++i) {
        if (i < 4) oa = mfma16(sel[i], d[i], oa); else ob = mfma16(sel[i], d[i], ob);
        xe = __builtin_amdgcn_bitop3_b32(xe, d[i].x, d[i].z, 0x96);
        xo = __builtin_amdgcn_bitop3_b32(xo, d[i].y, d[i].w, 0x96);
        __builtin_amdgcn_sched_barrier(0);
      }
      rp = sp + 1;
      unsigned x = xor_rows2(xe ^ ck.x, xo ^ ck.z);
      auto y = __builtin_amdgcn_permlane16_swap(x, x, false, false);
      x = y[0] ^ y[1];
      y = __builtin_amdgcn_permlane32_swap(x, x, false, false);
      if (!__any(((y[0] ^ y[1]) | (ck.y ^ tlo) | (ck.w ^ thi)) != 0)) break;
      load_n<8, PART_AUX>(rs, po, 1024, d);
      ck = ldg(rs, co);
    }
    ROLE_END();
    STAMP(12);
    {
      f32x4 s;
#pragma unroll
      for (int r = 0; r < 4; ++r) {
        auto x = __builtin_amdgcn_permlane32_swap(__float_as_uint(oa[r]), __float_as_uint(ob[r]), false, false);
        s[r] = __uint_as_float(x[0]) + __uint_as_float(x[1]);
      }
      *(LAS v2u*)o3 = v2u{pk(s.x, s.y), pk(s.z, s.w)};
    }
    bar();
    v2u gg[4], xb;
    v4u f2;
    if (w == 0) {
#pragma unroll
      for (int i = 0; i < 4; ++i) gg[i] = __builtin_amdgcn_raw_buffer_load_b64(rs, gmo, ((i >> 1) * 96 + (i & 1)) * ASTR, SC1);
      f2 = ldg(rs, WS_FLAG2 + lane * 16);
      xb = *(const GAS v2u*)(p.x + el);
    }
    unsigned tb = 0;
    if (w < 2) {
      const v4u tv = lds128(L_CNT + 16) - t0;
      tb = __builtin_amdgcn_readfirstlane(umax4(tv)) + t0 + T_PART + T_B6;
      const unsigned tn = now32() - 6;
      if ((int)(tn - tb) > 0) tb = tn;
    }
    if (w == 1) st32(rs, hb, tb + (unsigned)tag);
    bar();
    if (w == 0) {
#pragma unroll 1
      for (int sp = 0; sp < SPIN_G; ++sp) {
        unsigned m = 0;
#pragma unroll
        for (int i = 0; i < 4; ++i) m = max(m, max(gg[i].x, gg[i].y));
        if (!__any(m == SENT)) break;
        FENCE();
#pragma unroll
        for (int i = 0; i < 4; ++i) gg[i] = __builtin_amdgcn_raw_buffer_load_b64(rs, gmo, ((i >> 1) * 96 + (i & 1)) * ASTR, SC1);
      }
      if (!flags_up(f2, tag)) wait_flags(rs, WS_FLAG2 + lane * 16, tag);
      f32x4 x0 = up4(xb), ga = up4(gg[0]) + up4(gg[1]), gb = up4(gg[2]) + up4(gg[3]);
      pin(x0); pin(ga); pin(gb);
      STAMP(26);
      const bool act = lane < 32 && l15 < 4;
      v4u one = sela;
      int oo = arena(lane >> 2) + A_OUTP + g * 1024 + pc;
      int yo = oo + (A_YP - A_OUTP);
      int yl = lane < 32 ? yo : OOB - 48 * ASTR;
      int xo = act ? arena(g) + A_X1 + pc + lq * 8 : OOB;
      const rsrc_t ro = __builtin_amdgcn_make_buffer_rsrc(p.out, 0, 8192, 0x00020000);
      int fo = act ? el * 2 : OOB;
      pin(one); pin(oo); pin(yo); pin(yl); pin(xo); pin(fo);
      wait_until(tb + T_B);
      int r1 = 0, r2 = 0;
      const f32x4 x1 = x0 + ga * reduce_rows<8>(rs, oo, oo, one, r1);
      __builtin_amdgcn_raw_buffer_store_b64(v2u{pk(x1.x, x1.y), pk(x1.z, x1.w)}, rs, xo, 0, SC1);
      STAMP(27);
      wait_until(latest_time(ws + WS_HB + (128 + wid) * 128, t0, (unsigned)tag) + T_D);
      const f32x4 r = x1 + gb * reduce_rows<4>(rs, yo, yl, one, r2);
      STAMP(29);
      __builtin_amdgcn_raw_buffer_store_b64(v2u{pk(r.x, r.y), pk(r.z, r.w)}, ro, fo, 0, 0);
      if (wid == 0 && lane == 0) __hip_atomic_store((GAS u64*)ws, tag, RLX_AGENT);
      STAMP(30);
      DUMP(26); DUMP(27); DUMP(29); DUMP(30); DUMPC(42, r1); DUMPC(43, r2);
    }
    ROLE_END();
  } else {
    __builtin_amdgcn_s_setprio(2);
    const int p4 = 4 * (lane & 3);
    unsigned act = b8 * 272 + lq * 16;
    unsigned s0 = tr_addr(W2, lane, 16 * w + p4, 0), s1 = tr_addr(W2, lane, 16 * w + p4, 1);
    unsigned ba = L_SC + b8 * 528 + lq * 16;
    v4u f[2][4], f2[2][4];
    unsigned a1 = L_A1 + b8 * 528 + lq * 16;
    int o = my + A_E + (isq ? w * 1024 + (l15 >> 3) * 512 + lq * 128 + b8 * 16 : b8 * 512 + w * 128 + (l15 >> 3) * 64 + lq * 16);
    pin(o); pin(a1); pin(act);
    bar();
    const unsigned tb1 = now32();
    STAMP(4);
    if (isq) {
      panel_frags(W0, lane, 32 * w + p4, 32 * w + 16 + p4, f);
      panel_frags(W1, lane, 32 * w + p4, 32 * w + 16 + p4, f2);
      bar();
      f32x4 q0 = z4, q1 = z4;
      v4u bf[8];
#pragma unroll
      for (int ks = 0; ks < 8; ++ks) bf[ks] = lds128(a1, ks * 64);
      __builtin_amdgcn_sched_barrier(0);
#pragma unroll
      for (int ks = 0; ks < 4; ++ks) { q0 = mfma16(f[0][ks], bf[ks], q0); q1 = mfma16(f[1][ks], bf[ks], q1); }
#pragma unroll
      for (int ks = 0; ks < 4; ++ks) { q0 = mfma16(f2[0][ks], bf[4 + ks], q0); q1 = mfma16(f2[1][ks], bf[4 + ks], q1); }
      stg(rs, o, pick(bvalid, f2u(q0), f2u(q1)));
    } else {
#pragma unroll
      for (int tt = 0; tt < 2; ++tt) {
        const unsigned ka = W0 + (64 * w + 8 * (l15 >> 2) + 4 * tt + (l15 & 3)) * 256 + 16 * (lq ^ (((l15 & 3) << 2) | ((2 * (l15 >> 2) + tt) & 3)));
#pragma unroll
        for (int ks = 0; ks < 4; ++ks) { f[tt][ks] = lds128(ka ^ (ks * 64)); f2[tt][ks] = lds128(ka ^ (ks * 64), 8192); }
      }
      bar();
      f32x4 a0 = z4, a2 = z4, c0 = z4, c2 = z4;
      v4u bf[4];
#pragma unroll
      for (int ks = 0; ks < 4; ++ks) bf[ks] = lds128(a1, ks * 64);
      __builtin_amdgcn_sched_barrier(0);
#pragma unroll
      for (int ks = 0; ks < 4; ++ks) {
        a0 = mfma16(f[0][ks], bf[ks], a0); c0 = mfma16(f2[0][ks], bf[ks], c0);
        a2 = mfma16(f[1][ks], bf[ks], a2); c2 = mfma16(f2[1][ks], bf[ks], c2);
      }
      stg(rs, o, pk8(u2f(pick(bvalid, f2u(a0), f2u(c0))) * QSCALE, u2f(pick(bvalid, f2u(a2), f2u(c2))) * QSCALE));
    }
    STAMP(6);
    bar();
    wait_until(tb1 + (isq ? T_L + T_QP : T_L));
    dma_run<true>(p.wuv, vuv, W0 + dst0, 8, 2 * 8192, 4096);
    const bool m2 = isq;
    if (m2) dma_run<true>(p.wmod, (256 * j + 4 * w + lq + (ch >> 3) * 128) * 6144 + 2048 + h * 128 + (ch & 7) * 16, W2 + dst0, 8, 16 * 6144, 4096);
    VMCNT(0);
    lsig(L_CNT + 4);
    if (FP_AT == 1 && g < 7 && (FP_ALL || !isq)) {
      const int e = 2 * (wi >> 2) + (j & 1);
      wait_until(tb1 + (isq ? T_FP + T_QP : T_FP));
      if (!isq) dma_run<false>(p.w1, (64 * e + 16 * w + (lane >> 5)) * 3584 + g * 512 + (lane & 31) * 16, W1 + dst0, 8, 2 * 3584, 0);
      if (FP_ALL == isq && FP_W2) dma_run<false>(p.w2, (256 * g + 32 * e + 8 * w) * 1024 + lane * 16, W1 + dst0, 8, 1024, 0);
    }
    lwait<true>(L_CNT + 4, 8);
    STAMP(9);
    if (m2) {
      f32x4 m0 = z4, m1 = z4;
#pragma unroll 1
      for (int ks = 0; ks < 4; ++ks) {
        m0 = mfma16(tr_frag(s0 + ks * 8192, s1 + ks * 8192, 0), lds128(ba + ks * 64), m0);
        m1 = mfma16(tr_frag((s0 ^ 128) + ks * 8192, (s1 ^ 128) + ks * 8192, 0), lds128(ba + ks * 64, 256), m1);
      }
      m0 += m1;
      __builtin_amdgcn_raw_buffer_store_b64(v2u{pk(m0.x, m0.y), pk(m0.z, m0.w)}, rs, bvalid ? my + A_S + (2 * w + (lq >> 1)) * 128 + l15 * 16 + (lq & 1) * 8 : OOB, 0, SC1);
    }
    panel_frags(W0, lane, 32 * w + 2 * p4, 32 * w + 2 * p4 + 4, f);
    unsigned o4 = bvalid ? L_A4 + l15 * 272 + w * 64 + lq * 16 : L_JUNK;
    pin(o4);
    bar();
    STAMP(35);
    f32x4 c0 = z4, c1 = z4;
    gemv2(f, L_A3 + act, c0, c1);
    *(LAS v4u*)o4 = pk8(c0, c1);
    bar();
    STAMP(13);
  }
}

template <int OFF> DI void rd_inplace(v4u& a, unsigned ad) { asm volatile("ds_read_b128 %0, %1 offset:%2" : "+v"(a) : "v"(ad), "i"(OFF) : "memory"); }
template <int O0, int O1> DI void peek2(unsigned& a, unsigned& b, unsigned cb) {
  asm volatile("ds_read_b32 %0, %2 offset:%3\n\tds_read_b32 %1, %2 offset:%4" : "=&v"(a), "=&v"(b) : "v"(cb), "i"(O0), "i"(O1) : "memory");
}
template <int W> DI void wait2(unsigned& a, unsigned& b) { asm volatile("s_waitcnt lgkmcnt(%2)" : "+v"(a), "+v"(b) : "i"(W)); }
template <int W, bool FIRST> DI void score_pair(f32x4& s0, f32x4& s1, v4u a, v4u q0, v4u q1) {
  if constexpr (FIRST)
    asm volatile("s_waitcnt lgkmcnt(%5)\n\tv_mfma_f32_16x16x32_bf16 %0, %2, %3, 0\n\tv_mfma_f32_16x16x32_bf16 %1, %2, %4, 0"
                 : "=&v"(s0), "=&v"(s1) : "v"(a), "v"(q0), "v"(q1), "i"(W));
  else
    asm volatile("s_waitcnt lgkmcnt(%5)\n\tv_mfma_f32_16x16x32_bf16 %0, %2, %3, %0\n\tv_mfma_f32_16x16x32_bf16 %1, %2, %4, %1"
                 : "+v"(s0), "+v"(s1) : "v"(a), "v"(q0), "v"(q1), "i"(W));
}
template <int OFF> DI void put_scores(unsigned sa, f32x4 s0, f32x4 s1) {
  asm volatile("s_nop 4\n\tds_write_b128 %0, %1 offset:%3\n\tds_write_b128 %0, %2 offset:%4" :: "v"(sa), "v"(s0), "v"(s1), "i"(OFF), "i"(OFF + 1024) : "memory");
}
template <int V> struct IntC { static constexpr int value = V; };
template <int I, int N, class F> DI void static_for(F&& f) { if constexpr (I < N) { f(IntC<I>{}); static_for<I + 1, N>(f); } }
constexpr unsigned slot_c(int t) { return ((0x02310210u >> ((4 * t) & 31)) & 3) * 32768; }
template <unsigned A> DI void csig(unsigned cb, unsigned v) {
  asm volatile("s_mov_b64 exec, 1\n\tds_add_u32 %0, %1 offset:%2\n\ts_mov_b64 exec, -1" :: "v"(cb), "v"(v), "i"(A - T_CNT) : "memory");
}
template <unsigned A> DI unsigned cpeek(unsigned cb) { return *(volatile LAS unsigned*)((LAS char*)cb + (A - T_CNT)); }
#define SPIN_HEAD "s_mov_b32 %[n], %[nb]\n1:\n\ts_sub_u32 %[n], %[n], 1\n\ts_cbranch_scc1 2f\n\t"
#define SPIN_TAIL "s_waitcnt lgkmcnt(0)\n\tv_cmp_gt_u32 vcc, %[w], %[c]\n\ts_cbranch_vccnz 1b\n2:"
#define SPIN_TAIL2 "s_waitcnt lgkmcnt(0)\n\tv_cmp_gt_u32 vcc, %[w], %[c]\n\ts_cbranch_vccz 2f\n" \
                   "3:\n\ts_sub_u32 %[n], %[n], 1\n\ts_cbranch_scc1 2f\n\tds_read_b32 %[c], %[cb] offset:%[o]\n\ts_waitcnt lgkmcnt(0)\n\t" \
                   "v_cmp_gt_u32 vcc, %[w2], %[c]\n\ts_cbranch_vccnz 3b\n\ts_branch 1b\n2:"
template <unsigned A, unsigned W, bool NAP = false> DI void cwait(unsigned cb, unsigned nb) {
  unsigned c, n;
  if (NAP)
    asm volatile(SPIN_HEAD "s_sleep 1\n\tds_read_b32 %[c], %[cb] offset:%[o]\n\t" SPIN_TAIL
                 : [c] "=&v"(c), [n] "=&s"(n) : [cb] "v"(cb), [o] "i"(A - T_CNT), [w] "i"(W), [nb] "s"(nb) : "vcc", "scc", "memory");
  else
    asm volatile(SPIN_HEAD "ds_read_b32 %[c], %[cb] offset:%[o]\n\t" SPIN_TAIL
                 : [c] "=&v"(c), [n] "=&s"(n) : [cb] "v"(cb), [o] "i"(A - T_CNT), [w] "i"(W), [nb] "s"(nb) : "vcc", "scc", "memory");
}
constexpr unsigned T_DRY = T_CNT + 256;
DI constexpr unsigned fsw(int row) { return ((row & 3) << 2) | ((row >> 1) & 2) | ((row >> 3) & 1); }
DI unsigned slot_of(int t) { return ((0x02310210u >> ((4 * t) & 31)) & 3) * 32768; }
#define DMA4(AUX, base, dst, v0, v1, v2, v3) \
  asm volatile("s_mov_b32 m0, %5\n\ts_waitcnt vmcnt(" LWS ")\n\tglobal_load_lds_dwordx4 %0, %4 " AUX "\n\t" \
               "s_waitcnt vmcnt(" LWS ")\n\tglobal_load_lds_dwordx4 %1, %4 offset:1024 " AUX "\n\t" \
               "s_waitcnt vmcnt(" LWS ")\n\tglobal_load_lds_dwordx4 %2, %4 offset:2048 " AUX "\n\t" \
               "s_waitcnt vmcnt(" LWS ")\n\tglobal_load_lds_dwordx4 %3, %4 offset:3072 " AUX \
               :: "v"(v0), "v"(v1), "v"(v2), "v"(v3), "s"(base), "s"(dst) : "memory")

typedef float f32x32 __attribute__((ext_vector_type(32)));
DI void pv_loop(f32x32& o0, f32x32& o1, const unsigned (&a)[8], unsigned pa, unsigned cb, unsigned one, unsigned nb) {
  unsigned n, k;
  u64 m0, m1, ret;
  asm volatile(
      "s_branch 300f\n"
      "200:\n"
      "v_readfirstlane_b32 %[n], v20\n"
      "v_lshrrev_b32 v32, 4, %[pa]\n"
      "v_and_b32 v32, 48, v32\n"
      "v_add_u32 v32, %[k], v32\n"
      "v_add_u32 v32, %[fac], v32\n"
      "ds_read_b128 v[36:39], v32 offset:64\n"
      "ds_read_b128 v[32:35], v32\n"
      "s_waitcnt lgkmcnt(0)\n"
      "s_bitcmp1_b32 %[n], 0\n"
      "s_cbranch_scc0 93f\n"
      "v_pk_mul_f32 v[40:41], v[40:41], v[32:33]\n"
      "v_pk_mul_f32 v[42:43], v[42:43], v[34:35]\n"
      "v_pk_mul_f32 v[44:45], v[44:45], v[32:33]\n"
      "v_pk_mul_f32 v[46:47], v[46:47], v[34:35]\n"
      "v_pk_mul_f32 v[48:49], v[48:49], v[32:33]\n"
      "v_pk_mul_f32 v[50:51], v[50:51], v[34:35]\n"
      "v_pk_mul_f32 v[52:53], v[52:53], v[32:33]\n"
      "v_pk_mul_f32 v[54:55], v[54:55], v[34:35]\n"
      "v_pk_mul_f32 v[56:57], v[56:57], v[32:33]\n"
      "v_pk_mul_f32 v[58:59], v[58:59], v[34:35]\n"
      "v_pk_mul_f32 v[60:61], v[60:61], v[32:33]\n"
      "v_pk_mul_f32 v[62:63], v[62:63], v[34:35]\n"
      "v_pk_mul_f32 v[64:65], v[64:65], v[32:33]\n"
      "v_pk_mul_f32 v[66:67], v[66:67], v[34:35]\n"
      "v_pk_mul_f32 v[68:69], v[68:69], v[32:33]\n"
      "v_pk_mul_f32 v[70:71], v[70:71], v[34:35]\n"
      "93:\n"
      "s_bitcmp1_b32 %[n], 1\n"
      "s_cbranch_scc0 93f\n"
      "v_pk_mul_f32 v[72:73], v[72:73], v[36:37]\n"
      "v_pk_mul_f32 v[74:75], v[74:75], v[38:39]\n"
      "v_pk_mul_f32 v[76:77], v[76:77], v[36:37]\n"
      "v_pk_mul_f32 v[78:79], v[78:79], v[38:39]\n"
      "v_pk_mul_f32 v[80:81], v[80:81], v[36:37]\n"
      "v_pk_mul_f32 v[82:83], v[82:83], v[38:39]\n"
      "v_pk_mul_f32 v[84:85], v[84:85], v[36:37]\n"
      "v_pk_mul_f32 v[86:87], v[86:87], v[38:39]\n"
      "v_pk_mul_f32 v[88:89], v[88:89], v[36:37]\n"
      "v_pk_mul_f32 v[90:91], v[90:91], v[38:39]\n"
      "v_pk_mul_f32 v[92:93], v[92:93], v[36:37]\n"
      "v_pk_mul_f32 v[94:95], v[94:95], v[38:39]\n"
      "v_pk_mul_f32 v[96:97], v[96:97], v[36:37]\n"
      "v_pk_mul_f32 v[98:99], v[98:99], v[38:39]\n"
      "v_pk_mul_f32 v[100:101], v[100:101], v[36:37]\n"
      "v_pk_mul_f32 v[102:103], v[102:103], v[38:39]\n"
      "93:\n"
      "s_nop 4\n"
      "s_setpc_b64 %[ret]\n"
      "201:\n"
      "v_readfirstlane_b32 %[n], v22\n"
      "v_lshrrev_b32 v24, 4, %[pa]\n"
      "v_and_b32 v24, 48, v24\n"
      "v_add_u32 v24, %[k], v24\n"
      "v_add_u32 v24, %[fac], v24\n"
      "ds_read_b128 v[28:31], v24 offset:64\n"
      "ds_read_b128 v[24:27], v24\n"
      "s_waitcnt lgkmcnt(0)\n"
      "s_bitcmp1_b32 %[n], 0\n"
      "s_cbranch_scc0 93f\n"
      "v_pk_mul_f32 v[40:41], v[40:41], v[24:25]\n"
      "v_pk_mul_f32 v[42:43], v[42:43], v[26:27]\n"
      "v_pk_mul_f32 v[44:45], v[44:45], v[24:25]\n"
      "v_pk_mul_f32 v[46:47], v[46:47], v[26:27]\n"
      "v_pk_mul_f32 v[48:49], v[48:49], v[24:25]\n"
      "v_pk_mul_f32 v[50:51], v[50:51], v[26:27]\n"
      "v_pk_mul_f32 v[52:53], v[52:53], v[24:25]\n"
      "v_pk_mul_f32 v[54:55], v[54:55], v[26:27]\n"
      "v_pk_mul_f32 v[56:57], v[56:57], v[24:25]\n"
      "v_pk_mul_f32 v[58:59], v[58:59], v[26:27]\n"
      "v_pk_mul_f32 v[60:61], v[60:61], v[24:25]\n"
      "v_pk_mul_f32 v[62:63], v[62:63], v[26:27]\n"
      "v_pk_mul_f32 v[64:65], v[64:65], v[24:25]\n"
      "v_pk_mul_f32 v[66:67], v[66:67], v[26:27]\n"
      "v_pk_mul_f32 v[68:69], v[68:69], v[24:25]\n"
      "v_pk_mul_f32 v[70:71], v[70:71], v[26:27]\n"
      "93:\n"
      "s_bitcmp1_b32 %[n], 1\n"
      "s_cbranch_scc0 93f\n"
      "v_pk_mul_f32 v[72:73], v[72:73], v[28:29]\n"
      "v_pk_mul_f32 v[74:75], v[74:75], v[30:31]\n"
      "v_pk_mul_f32 v[76:77], v[76:77], v[28:29]\n"
      "v_pk_mul_f32 v[78:79], v[78:79], v[30:31]\n"
      "v_pk_mul_f32 v[80:81], v[80:81], v[28:29]\n"
      "v_pk_mul_f32 v[82:83], v[82:83], v[30:31]\n"
      "v_pk_mul_f32 v[84:85], v[84:85], v[28:29]\n"
      "v_pk_mul_f32 v[86:87], v[86:87], v[30:31]\n"
      "v_pk_mul_f32 v[88:89], v[88:89], v[28:29]\n"
      "v_pk_mul_f32 v[90:91], v[90:91], v[30:31]\n"
      "v_pk_mul_f32 v[92:93], v[92:93], v[28:29]\n"
      "v_pk_mul_f32 v[94:95], v[94:95], v[30:31]\n"
      "v_pk_mul_f32 v[96:97], v[96:97], v[28:29]\n"
      "v_pk_mul_f32 v[98:99], v[98:99], v[30:31]\n"
      "v_pk_mul_f32 v[100:101], v[100:101], v[28:29]\n"
      "v_pk_mul_f32 v[102:103], v[102:103], v[30:31]\n"
      "93:\n"
      "s_nop 4\n"
      "s_setpc_b64 %[ret]\n"
      "110:\n"
      "s_mov_b32 %[n], %[nb]\n"
      "91:\n"
      "s_sub_u32 %[n], %[n], 1\n"
      "s_cbranch_scc1 92f\n"
      "ds_read_b32 v22, %[cb] offset:60\n"
      "s_waitcnt lgkmcnt(0)\n"
      "v_cmp_gt_u32 vcc, 8, v22\n"
      "s_cbranch_vccnz 91b\n"
      "92:\n"
      "ds_read_b32 v22, %[cb] offset:60\n"
      "ds_read_b128 v[32:35], %[pa] offset:2048\n"
      "ds_read_b128 v[36:39], %[pa] offset:3072\n"
      "ds_read_b32 v23, %[cb] offset:80\n"
      "s_waitcnt lgkmcnt(0)\n"
      "v_cmp_lt_u32 vcc, 8, v22\n"
      "s_cbranch_vccz 111f\n"
      "114:\n"
      "s_movk_i32 %[k], 128\n"
      "s_call_b64 %[ret], 201b\n"
      "s_branch 111f\n"
      "120:\n"
      "s_mov_b32 %[n], %[nb]\n"
      "91:\n"
      "s_sub_u32 %[n], %[n], 1\n"
      "s_cbranch_scc1 92f\n"
      "ds_read_b32 v20, %[cb] offset:76\n"
      "s_waitcnt lgkmcnt(0)\n"
      "v_cmp_gt_u32 vcc, 8, v20\n"
      "s_cbranch_vccnz 91b\n"
      "92:\n"
      "ds_read_b32 v20, %[cb] offset:76\n"
      "ds_read_b128 v[24:27], %[pa] offset:4096\n"
      "ds_read_b128 v[28:31], %[pa] offset:5120\n"
      "ds_read_b32 v21, %[cb] offset:96\n"
      "s_waitcnt lgkmcnt(0)\n"
      "v_cmp_lt_u32 vcc, 8, v20\n"
      "s_cbranch_vccz 121f\n"
      "124:\n"
      "s_movk_i32 %[k], 256\n"
      "s_call_b64 %[ret], 200b\n"
      "s_branch 121f\n"
      "123:\n"
      "s_mov_b32 %[n], %[nb]\n"
      "91:\n"
      "s_sub_u32 %[n], %[n], 1\n"
      "s_cbranch_scc1 92f\n"
      "ds_read_b32 v22, %[cb] offset:80\n"
      "s_waitcnt lgkmcnt(0)\n"
      "v_cmp_gt_u32 vcc, 1, v22\n"
      "s_cbranch_vccnz 91b\n"
      "92:\n"
      "ds_read_b64_tr_b16 v[136:137], %[a0] offset:0\n"
      "ds_read_b64_tr_b16 v[138:139], %[a0] offset:16384\n"
      "ds_read_b64_tr_b16 v[140:141], %[a1] offset:0\n"
      "ds_read_b64_tr_b16 v[142:143], %[a1] offset:16384\n"
      "ds_read_b64_tr_b16 v[144:145], %[a2] offset:0\n"
      "ds_read_b64_tr_b16 v[146:147], %[a2] offset:16384\n"
      "ds_read_b64_tr_b16 v[148:149], %[a3] offset:0\n"
      "ds_read_b64_tr_b16 v[150:151], %[a3] offset:16384\n"
      "ds_read_b64_tr_b16 v[152:153], %[a4] offset:0\n"
      "ds_read_b64_tr_b16 v[154:155], %[a4] offset:16384\n"
      "ds_read_b64_tr_b16 v[156:157], %[a5] offset:0\n"
      "ds_read_b64_tr_b16 v[158:159], %[a5] offset:16384\n"
      "ds_read_b64_tr_b16 v[160:161], %[a6] offset:0\n"
      "ds_read_b64_tr_b16 v[162:163], %[a6] offset:16384\n"
      "ds_read_b64_tr_b16 v[164:165], %[a7] offset:0\n"
      "ds_read_b64_tr_b16 v[166:167], %[a7] offset:16384\n"
      "s_mov_b64 exec, 1\n"
      "ds_add_u32 %[cb], %[one] offset:84\n"
      "s_mov_b64 exec, -1\n"
      "130:\n"
      "s_mov_b32 %[n], %[nb]\n"
      "91:\n"
      "s_sub_u32 %[n], %[n], 1\n"
      "s_cbranch_scc1 92f\n"
      "ds_read_b32 v22, %[cb] offset:92\n"
      "s_waitcnt lgkmcnt(0)\n"
      "v_cmp_gt_u32 vcc, 8, v22\n"
      "s_cbranch_vccnz 91b\n"
      "92:\n"
      "ds_read_b32 v22, %[cb] offset:92\n"
      "ds_read_b128 v[32:35], %[pa] offset:6144\n"
      "ds_read_b128 v[36:39], %[pa] offset:7168\n"
      "ds_read_b32 v23, %[cb] offset:112\n"
      "s_waitcnt lgkmcnt(0)\n"
      "v_cmp_lt_u32 vcc, 8, v22\n"
      "s_cbranch_vccz 131f\n"
      "134:\n"
      "s_movk_i32 %[k], 384\n"
      "s_call_b64 %[ret], 201b\n"
      "s_branch 131f\n"
      "133:\n"
      "s_mov_b32 %[n], %[nb]\n"
      "91:\n"
      "s_sub_u32 %[n], %[n], 1\n"
      "s_cbranch_scc1 92f\n"
      "ds_read_b32 v20, %[cb] offset:96\n"
      "s_waitcnt lgkmcnt(0)\n"
      "v_cmp_gt_u32 vcc, 1, v20\n"
      "s_cbranch_vccnz 91b\n"
      "92:\n"
      "ds_read_b64_tr_b16 v[104:105], %[a0] offset:32768\n"
      "ds_read_b64_tr_b16 v[106:107], %[a0] offset:49152\n"
      "ds_read_b64_tr_b16 v[108:109], %[a1] offset:32768\n"
      "ds_read_b64_tr_b16 v[110:111], %[a1] offset:49152\n"
      "ds_read_b64_tr_b16 v[112:113], %[a2] offset:32768\n"
      "ds_read_b64_tr_b16 v[114:115], %[a2] offset:49152\n"
      "ds_read_b64_tr_b16 v[116:117], %[a3] offset:32768\n"
      "ds_read_b64_tr_b16 v[118:119], %[a3] offset:49152\n"
      "ds_read_b64_tr_b16 v[120:121], %[a4] offset:32768\n"
      "ds_read_b64_tr_b16 v[122:123], %[a4] offset:49152\n"
      "ds_read_b64_tr_b16 v[124:125], %[a5] offset:32768\n"
      "ds_read_b64_tr_b16 v[126:127], %[a5] offset:49152\n"
      "ds_read_b64_tr_b16 v[128:129], %[a6] offset:32768\n"
      "ds_read_b64_tr_b16 v[130:131], %[a6] offset:49152\n"
      "ds_read_b64_tr_b16 v[132:133], %[a7] offset:32768\n"
      "ds_read_b64_tr_b16 v[134:135], %[a7] offset:49152\n"
      "s_mov_b64 exec, 1\n"
      "ds_add_u32 %[cb], %[one] offset:100\n"
      "s_mov_b64 exec, -1\n"
      "140:\n"
      "s_mov_b32 %[n], %[nb]\n"
      "91:\n"
      "s_sub_u32 %[n], %[n], 1\n"
      "s_cbranch_scc1 92f\n"
      "ds_read_b32 v20, %[cb] offset:108\n"
      "s_waitcnt lgkmcnt(0)\n"
      "v_cmp_gt_u32 vcc, 8, v20\n"
      "s_cbranch_vccnz 91b\n"
      "92:\n"
      "ds_read_b32 v20, %[cb] offset:108\n"
      "ds_read_b128 v[24:27], %[pa] offset:0\n"
      "ds_read_b128 v[28:31], %[pa] offset:1024\n"
      "ds_read_b32 v21, %[cb] offset:128\n"
      "s_waitcnt lgkmcnt(0)\n"
      "v_cmp_lt_u32 vcc, 8, v20\n"
      "s_cbranch_vccz 141f\n"
      "144:\n"
      "s_movk_i32 %[k], 512\n"
      "s_call_b64 %[ret], 200b\n"
      "s_branch 141f\n"
      "143:\n"
      "s_mov_b32 %[n], %[nb]\n"
      "91:\n"
      "s_sub_u32 %[n], %[n], 1\n"
      "s_cbranch_scc1 92f\n"
      "ds_read_b32 v22, %[cb] offset:112\n"
      "s_waitcnt lgkmcnt(0)\n"
      "v_cmp_gt_u32 vcc, 1, v22\n"
      "s_cbranch_vccnz 91b\n"
      "92:\n"
      "v_add_u32 v138, 0x10000, %[a0]\n"
      "ds_read_b64_tr_b16 v[136:137], v138 offset:32768\n"
      "ds_read_b64_tr_b16 v[138:139], v138 offset:49152\n"
      "v_add_u32 v142, 0x10000, %[a1]\n"
      "ds_read_b64_tr_b16 v[140:141], v142 offset:32768\n"
      "ds_read_b64_tr_b16 v[142:143], v142 offset:49152\n"
      "v_add_u32 v146, 0x10000, %[a2]\n"
      "ds_read_b64_tr_b16 v[144:145], v146 offset:32768\n"
      "ds_read_b64_tr_b16 v[146:147], v146 offset:49152\n"
      "v_add_u32 v150, 0x10000, %[a3]\n"
      "ds_read_b64_tr_b16 v[148:149], v150 offset:32768\n"
      "ds_read_b64_tr_b16 v[150:151], v150 offset:49152\n"
      "v_add_u32 v154, 0x10000, %[a4]\n"
      "ds_read_b64_tr_b16 v[152:153], v154 offset:32768\n"
      "ds_read_b64_tr_b16 v[154:155], v154 offset:49152\n"
      "v_add_u32 v158, 0x10000, %[a5]\n"
      "ds_read_b64_tr_b16 v[156:157], v158 offset:32768\n"
      "ds_read_b64_tr_b16 v[158:159], v158 offset:49152\n"
      "v_add_u32 v162, 0x10000, %[a6]\n"
      "ds_read_b64_tr_b16 v[160:161], v162 offset:32768\n"
      "ds_read_b64_tr_b16 v[162:163], v162 offset:49152\n"
      "v_add_u32 v166, 0x10000, %[a7]\n"
      "ds_read_b64_tr_b16 v[164:165], v166 offset:32768\n"
      "ds_read_b64_tr_b16 v[166:167], v166 offset:49152\n"
      "s_mov_b64 exec, 1\n"
      "ds_add_u32 %[cb], %[one] offset:116\n"
      "s_mov_b64 exec, -1\n"
      "150:\n"
      "s_mov_b32 %[n], %[nb]\n"
      "91:\n"
      "s_sub_u32 %[n], %[n], 1\n"
      "s_cbranch_scc1 92f\n"
      "ds_read_b32 v22, %[cb] offset:124\n"
      "s_waitcnt lgkmcnt(0)\n"
      "v_cmp_gt_u32 vcc, 8, v22\n"
      "s_cbranch_vccnz 91b\n"
      "92:\n"
      "ds_read_b32 v22, %[cb] offset:124\n"
      "ds_read_b128 v[32:35], %[pa] offset:2048\n"
      "ds_read_b128 v[36:39], %[pa] offset:3072\n"
      "ds_read_b32 v23, %[cb] offset:144\n"
      "s_waitcnt lgkmcnt(0)\n"
      "v_cmp_lt_u32 vcc, 8, v22\n"
      "s_cbranch_vccz 151f\n"
      "154:\n"
      "s_movk_i32 %[k], 640\n"
      "s_call_b64 %[ret], 201b\n"
      "s_branch 151f\n"
      "153:\n"
      "s_mov_b32 %[n], %[nb]\n"
      "91:\n"
      "s_sub_u32 %[n], %[n], 1\n"
      "s_cbranch_scc1 92f\n"
      "ds_read_b32 v20, %[cb] offset:128\n"
      "s_waitcnt lgkmcnt(0)\n"
      "v_cmp_gt_u32 vcc, 1, v20\n"
      "s_cbranch_vccnz 91b\n"
      "92:\n"
      "v_add_u32 v106, 0x10000, %[a0]\n"
      "ds_read_b64_tr_b16 v[104:105], v106 offset:0\n"
      "ds_read_b64_tr_b16 v[106:107], v106 offset:16384\n"
      "v_add_u32 v110, 0x10000, %[a1]\n"
      "ds_read_b64_tr_b16 v[108:109], v110 offset:0\n"
      "ds_read_b64_tr_b16 v[110:111], v110 offset:16384\n"
      "v_add_u32 v114, 0x10000, %[a2]\n"
      "ds_read_b64_tr_b16 v[112:113], v114 offset:0\n"
      "ds_read_b64_tr_b16 v[114:115], v114 offset:16384\n"
      "v_add_u32 v118, 0x10000, %[a3]\n"
      "ds_read_b64_tr_b16 v[116:117], v118 offset:0\n"
      "ds_read_b64_tr_b16 v[118:119], v118 offset:16384\n"
      "v_add_u32 v122, 0x10000, %[a4]\n"
      "ds_read_b64_tr_b16 v[120:121], v122 offset:0\n"
      "ds_read_b64_tr_b16 v[122:123], v122 offset:16384\n"
      "v_add_u32 v126, 0x10000, %[a5]\n"
      "ds_read_b64_tr_b16 v[124:125], v126 offset:0\n"
      "ds_read_b64_tr_b16 v[126:127], v126 offset:16384\n"
      "v_add_u32 v130, 0x10000, %[a6]\n"
      "ds_read_b64_tr_b16 v[128:129], v130 offset:0\n"
      "ds_read_b64_tr_b16 v[130:131], v130 offset:16384\n"
      "v_add_u32 v134, 0x10000, %[a7]\n"
      "ds_read_b64_tr_b16 v[132:133], v134 offset:0\n"
      "ds_read_b64_tr_b16 v[134:135], v134 offset:16384\n"
      "160:\n"
      "s_mov_b32 %[n], %[nb]\n"
      "91:\n"
      "s_sub_u32 %[n], %[n], 1\n"
      "s_cbranch_scc1 92f\n"
      "ds_read_b32 v20, %[cb] offset:140\n"
      "s_waitcnt lgkmcnt(0)\n"
      "v_cmp_gt_u32 vcc, 8, v20\n"
      "s_cbranch_vccnz 91b\n"
      "92:\n"
      "ds_read_b32 v20, %[cb] offset:140\n"
      "ds_read_b128 v[24:27], %[pa] offset:4096\n"
      "ds_read_b128 v[28:31], %[pa] offset:5120\n"
      "s_waitcnt lgkmcnt(0)\n"
      "v_cmp_lt_u32 vcc, 8, v20\n"
      "s_cbranch_vccz 161f\n"
      "164:\n"
      "s_movk_i32 %[k], 768\n"
      "s_call_b64 %[ret], 200b\n"
      "s_branch 161f\n"
      "174:\n"
      "s_movk_i32 %[k], 896\n"
      "s_call_b64 %[ret], 201b\n"
      "s_branch 171f\n"
      "300:\n"
      "s_mov_b32 %[n], %[nb]\n"
      "91:\n"
      "s_sub_u32 %[n], %[n], 1\n"
      "s_cbranch_scc1 92f\n"
      "s_sleep 1\n"
      "ds_read_b32 v20, %[cb] offset:32\n"
      "s_waitcnt lgkmcnt(0)\n"
      "v_cmp_gt_u32 vcc, 1, v20\n"
      "s_cbranch_vccnz 91b\n"
      "92:\n"
      "ds_read_b64_tr_b16 v[72:73], %[a0] offset:0\n"
      "ds_read_b64_tr_b16 v[74:75], %[a0] offset:16384\n"
      "ds_read_b64_tr_b16 v[76:77], %[a1] offset:0\n"
      "ds_read_b64_tr_b16 v[78:79], %[a1] offset:16384\n"
      "ds_read_b64_tr_b16 v[80:81], %[a2] offset:0\n"
      "ds_read_b64_tr_b16 v[82:83], %[a2] offset:16384\n"
      "ds_read_b64_tr_b16 v[84:85], %[a3] offset:0\n"
      "ds_read_b64_tr_b16 v[86:87], %[a3] offset:16384\n"
      "ds_read_b64_tr_b16 v[88:89], %[a4] offset:0\n"
      "ds_read_b64_tr_b16 v[90:91], %[a4] offset:16384\n"
      "ds_read_b64_tr_b16 v[92:93], %[a5] offset:0\n"
      "ds_read_b64_tr_b16 v[94:95], %[a5] offset:16384\n"
      "ds_read_b64_tr_b16 v[96:97], %[a6] offset:0\n"
      "ds_read_b64_tr_b16 v[98:99], %[a6] offset:16384\n"
      "ds_read_b64_tr_b16 v[100:101], %[a7] offset:0\n"
      "ds_read_b64_tr_b16 v[102:103], %[a7] offset:16384\n"
      "s_mov_b64 exec, 1\n"
      "ds_add_u32 %[cb], %[one] offset:36\n"
      "s_mov_b64 exec, -1\n"
      "s_mov_b32 %[n], %[nb]\n"
      "91:\n"
      "s_sub_u32 %[n], %[n], 1\n"
      "s_cbranch_scc1 92f\n"
      "s_sleep 1\n"
      "ds_read_b32 v20, %[cb] offset:48\n"
      "s_waitcnt lgkmcnt(0)\n"
      "v_cmp_gt_u32 vcc, 1, v20\n"
      "s_cbranch_vccnz 91b\n"
      "92:\n"
      "ds_read_b64_tr_b16 v[136:137], %[a0] offset:32768\n"
      "ds_read_b64_tr_b16 v[138:139], %[a0] offset:49152\n"
      "ds_read_b64_tr_b16 v[140:141], %[a1] offset:32768\n"
      "ds_read_b64_tr_b16 v[142:143], %[a1] offset:49152\n"
      "ds_read_b64_tr_b16 v[144:145], %[a2] offset:32768\n"
      "ds_read_b64_tr_b16 v[146:147], %[a2] offset:49152\n"
      "ds_read_b64_tr_b16 v[148:149], %[a3] offset:32768\n"
      "ds_read_b64_tr_b16 v[150:151], %[a3] offset:49152\n"
      "ds_read_b64_tr_b16 v[152:153], %[a4] offset:32768\n"
      "ds_read_b64_tr_b16 v[154:155], %[a4] offset:49152\n"
      "ds_read_b64_tr_b16 v[156:157], %[a5] offset:32768\n"
      "ds_read_b64_tr_b16 v[158:159], %[a5] offset:49152\n"
      "ds_read_b64_tr_b16 v[160:161], %[a6] offset:32768\n"
      "ds_read_b64_tr_b16 v[162:163], %[a6] offset:49152\n"
      "ds_read_b64_tr_b16 v[164:165], %[a7] offset:32768\n"
      "ds_read_b64_tr_b16 v[166:167], %[a7] offset:49152\n"
      "s_mov_b64 exec, 1\n"
      "ds_add_u32 %[cb], %[one] offset:52\n"
      "s_mov_b64 exec, -1\n"
      "s_mov_b32 %[n], %[nb]\n"
      "91:\n"
      "s_sub_u32 %[n], %[n], 1\n"
      "s_cbranch_scc1 92f\n"
      "s_sleep 1\n"
      "ds_read_b32 v20, %[cb] offset:64\n"
      "s_waitcnt lgkmcnt(0)\n"
      "v_cmp_gt_u32 vcc, 1, v20\n"
      "s_cbranch_vccnz 91b\n"
      "92:\n"
      "v_add_u32 v106, 0x10000, %[a0]\n"
      "ds_read_b64_tr_b16 v[104:105], v106 offset:0\n"
      "ds_read_b64_tr_b16 v[106:107], v106 offset:16384\n"
      "v_add_u32 v110, 0x10000, %[a1]\n"
      "ds_read_b64_tr_b16 v[108:109], v110 offset:0\n"
      "ds_read_b64_tr_b16 v[110:111], v110 offset:16384\n"
      "v_add_u32 v114, 0x10000, %[a2]\n"
      "ds_read_b64_tr_b16 v[112:113], v114 offset:0\n"
      "ds_read_b64_tr_b16 v[114:115], v114 offset:16384\n"
      "v_add_u32 v118, 0x10000, %[a3]\n"
      "ds_read_b64_tr_b16 v[116:117], v118 offset:0\n"
      "ds_read_b64_tr_b16 v[118:119], v118 offset:16384\n"
      "v_add_u32 v122, 0x10000, %[a4]\n"
      "ds_read_b64_tr_b16 v[120:121], v122 offset:0\n"
      "ds_read_b64_tr_b16 v[122:123], v122 offset:16384\n"
      "v_add_u32 v126, 0x10000, %[a5]\n"
      "ds_read_b64_tr_b16 v[124:125], v126 offset:0\n"
      "ds_read_b64_tr_b16 v[126:127], v126 offset:16384\n"
      "v_add_u32 v130, 0x10000, %[a6]\n"
      "ds_read_b64_tr_b16 v[128:129], v130 offset:0\n"
      "ds_read_b64_tr_b16 v[130:131], v130 offset:16384\n"
      "v_add_u32 v134, 0x10000, %[a7]\n"
      "ds_read_b64_tr_b16 v[132:133], v134 offset:0\n"
      "ds_read_b64_tr_b16 v[134:135], v134 offset:16384\n"
      "s_mov_b64 exec, 1\n"
      "ds_add_u32 %[cb], %[one] offset:68\n"
      "s_mov_b64 exec, -1\n"
      "s_mov_b64 %[m0], 0\n"
      "s_mov_b32 %[n], %[nb]\n"
      "91:\n"
      "s_sub_u32 %[n], %[n], 1\n"
      "s_cbranch_scc1 92f\n"
      "s_sleep 1\n"
      "ds_read_b32 v20, %[cb] offset:40\n"
      "s_waitcnt lgkmcnt(0)\n"
      "v_cmp_gt_u32 vcc, 1, v20\n"
      "s_cbranch_vccnz 91b\n"
      "92:\n"
      "s_mov_b32 %[n], %[nb]\n"
      "91:\n"
      "s_sub_u32 %[n], %[n], 1\n"
      "s_cbranch_scc1 92f\n"
      "ds_read_b32 v20, %[cb] offset:44\n"
      "s_waitcnt lgkmcnt(0)\n"
      "v_cmp_gt_u32 vcc, 8, v20\n"
      "s_cbranch_vccnz 91b\n"
      "92:\n"
      "ds_read_b32 v20, %[cb] offset:44\n"
      "ds_read_b128 v[24:27], %[pa] offset:0\n"
      "ds_read_b128 v[28:31], %[pa] offset:1024\n"
      "ds_read_b32 v21, %[cb] offset:64\n"
      "s_waitcnt lgkmcnt(0)\n"
      "v_mfma_f32_16x16x32_bf16 v[40:43], v[24:27], v[72:75], 0\n"
      "v_mfma_f32_16x16x32_bf16 v[72:75], v[28:31], v[72:75], 0\n"
      "v_mfma_f32_16x16x32_bf16 v[44:47], v[24:27], v[76:79], 0\n"
      "v_mfma_f32_16x16x32_bf16 v[76:79], v[28:31], v[76:79], 0\n"
      "v_mfma_f32_16x16x32_bf16 v[48:51], v[24:27], v[80:83], 0\n"
      "v_mfma_f32_16x16x32_bf16 v[80:83], v[28:31], v[80:83], 0\n"
      "v_mfma_f32_16x16x32_bf16 v[52:55], v[24:27], v[84:87], 0\n"
      "v_mfma_f32_16x16x32_bf16 v[84:87], v[28:31], v[84:87], 0\n"
      "ds_read_b32 v22, %[cb] offset:60\n"
      "ds_read_b128 v[32:35], %[pa] offset:2048\n"
      "ds_read_b128 v[36:39], %[pa] offset:3072\n"
      "ds_read_b32 v23, %[cb] offset:80\n"
      "v_mfma_f32_16x16x32_bf16 v[56:59], v[24:27], v[88:91], 0\n"
      "v_mfma_f32_16x16x32_bf16 v[88:91], v[28:31], v[88:91], 0\n"
      "v_mfma_f32_16x16x32_bf16 v[60:63], v[24:27], v[92:95], 0\n"
      "v_mfma_f32_16x16x32_bf16 v[92:95], v[28:31], v[92:95], 0\n"
      "v_mfma_f32_16x16x32_bf16 v[64:67], v[24:27], v[96:99], 0\n"
      "v_mfma_f32_16x16x32_bf16 v[96:99], v[28:31], v[96:99], 0\n"
      "v_mfma_f32_16x16x32_bf16 v[68:71], v[24:27], v[100:103], 0\n"
      "v_mfma_f32_16x16x32_bf16 v[100:103], v[28:31], v[100:103], 0\n"
      "s_waitcnt lgkmcnt(0)\n"
      "v_cmp_ne_u32 vcc, 8, v22\n"
      "s_cbranch_vccnz 110b\n"
      "111:\n"
      "v_cmp_eq_u32 %[m1], 0, v23\n"
      "v_mfma_f32_16x16x32_bf16 v[40:43], v[32:35], v[136:139], v[40:43]\n"
      "v_mfma_f32_16x16x32_bf16 v[72:75], v[36:39], v[136:139], v[72:75]\n"
      "ds_read_b64_tr_b16 v[136:137], %[a0] offset:0\n"
      "ds_read_b64_tr_b16 v[138:139], %[a0] offset:16384\n"
      "v_mfma_f32_16x16x32_bf16 v[44:47], v[32:35], v[140:143], v[44:47]\n"
      "v_mfma_f32_16x16x32_bf16 v[76:79], v[36:39], v[140:143], v[76:79]\n"
      "ds_read_b64_tr_b16 v[140:141], %[a1] offset:0\n"
      "ds_read_b64_tr_b16 v[142:143], %[a1] offset:16384\n"
      "v_mfma_f32_16x16x32_bf16 v[48:51], v[32:35], v[144:147], v[48:51]\n"
      "v_mfma_f32_16x16x32_bf16 v[80:83], v[36:39], v[144:147], v[80:83]\n"
      "ds_read_b64_tr_b16 v[144:145], %[a2] offset:0\n"
      "ds_read_b64_tr_b16 v[146:147], %[a2] offset:16384\n"
      "v_mfma_f32_16x16x32_bf16 v[52:55], v[32:35], v[148:151], v[52:55]\n"
      "v_mfma_f32_16x16x32_bf16 v[84:87], v[36:39], v[148:151], v[84:87]\n"
      "ds_read_b64_tr_b16 v[148:149], %[a3] offset:0\n"
      "ds_read_b64_tr_b16 v[150:151], %[a3] offset:16384\n"
      "ds_read_b32 v20, %[cb] offset:76\n"
      "ds_read_b128 v[24:27], %[pa] offset:4096\n"
      "ds_read_b128 v[28:31], %[pa] offset:5120\n"
      "ds_read_b32 v21, %[cb] offset:96\n"
      "v_mfma_f32_16x16x32_bf16 v[56:59], v[32:35], v[152:155], v[56:59]\n"
      "v_mfma_f32_16x16x32_bf16 v[88:91], v[36:39], v[152:155], v[88:91]\n"
      "ds_read_b64_tr_b16 v[152:153], %[a4] offset:0\n"
      "ds_read_b64_tr_b16 v[154:155], %[a4] offset:16384\n"
      "v_mfma_f32_16x16x32_bf16 v[60:63], v[32:35], v[156:159], v[60:63]\n"
      "v_mfma_f32_16x16x32_bf16 v[92:95], v[36:39], v[156:159], v[92:95]\n"
      "ds_read_b64_tr_b16 v[156:157], %[a5] offset:0\n"
      "ds_read_b64_tr_b16 v[158:159], %[a5] offset:16384\n"
      "v_mfma_f32_16x16x32_bf16 v[64:67], v[32:35], v[160:163], v[64:67]\n"
      "v_mfma_f32_16x16x32_bf16 v[96:99], v[36:39], v[160:163], v[96:99]\n"
      "ds_read_b64_tr_b16 v[160:161], %[a6] offset:0\n"
      "ds_read_b64_tr_b16 v[162:163], %[a6] offset:16384\n"
      "v_mfma_f32_16x16x32_bf16 v[68:71], v[32:35], v[164:167], v[68:71]\n"
      "v_mfma_f32_16x16x32_bf16 v[100:103], v[36:39], v[164:167], v[100:103]\n"
      "ds_read_b64_tr_b16 v[164:165], %[a7] offset:0\n"
      "ds_read_b64_tr_b16 v[166:167], %[a7] offset:16384\n"
      "s_cmp_lg_u64 %[m1], 0\n"
      "s_cbranch_scc1 112f\n"
      "s_mov_b64 exec, 1\n"
      "ds_add_u32 %[cb], %[one] offset:84\n"
      "s_mov_b64 exec, -1\n"
      "112:\n"
      "s_waitcnt lgkmcnt(8)\n"
      "v_cmp_ne_u32 vcc, 8, v20\n"
      "s_cbranch_vccnz 120b\n"
      "121:\n"
      "v_cmp_eq_u32 %[m0], 0, v21\n"
      "v_mfma_f32_16x16x32_bf16 v[40:43], v[24:27], v[104:107], v[40:43]\n"
      "v_mfma_f32_16x16x32_bf16 v[72:75], v[28:31], v[104:107], v[72:75]\n"
      "ds_read_b64_tr_b16 v[104:105], %[a0] offset:32768\n"
      "ds_read_b64_tr_b16 v[106:107], %[a0] offset:49152\n"
      "v_mfma_f32_16x16x32_bf16 v[44:47], v[24:27], v[108:111], v[44:47]\n"
      "v_mfma_f32_16x16x32_bf16 v[76:79], v[28:31], v[108:111], v[76:79]\n"
      "ds_read_b64_tr_b16 v[108:109], %[a1] offset:32768\n"
      "ds_read_b64_tr_b16 v[110:111], %[a1] offset:49152\n"
      "v_mfma_f32_16x16x32_bf16 v[48:51], v[24:27], v[112:115], v[48:51]\n"
      "v_mfma_f32_16x16x32_bf16 v[80:83], v[28:31], v[112:115], v[80:83]\n"
      "ds_read_b64_tr_b16 v[112:113], %[a2] offset:32768\n"
      "ds_read_b64_tr_b16 v[114:115], %[a2] offset:49152\n"
      "v_mfma_f32_16x16x32_bf16 v[52:55], v[24:27], v[116:119], v[52:55]\n"
      "v_mfma_f32_16x16x32_bf16 v[84:87], v[28:31], v[116:119], v[84:87]\n"
      "ds_read_b64_tr_b16 v[116:117], %[a3] offset:32768\n"
      "ds_read_b64_tr_b16 v[118:119], %[a3] offset:49152\n"
      "ds_read_b32 v22, %[cb] offset:92\n"
      "ds_read_b128 v[32:35], %[pa] offset:6144\n"
      "ds_read_b128 v[36:39], %[pa] offset:7168\n"
      "ds_read_b32 v23, %[cb] offset:112\n"
      "v_mfma_f32_16x16x32_bf16 v[56:59], v[24:27], v[120:123], v[56:59]\n"
      "v_mfma_f32_16x16x32_bf16 v[88:91], v[28:31], v[120:123], v[88:91]\n"
      "ds_read_b64_tr_b16 v[120:121], %[a4] offset:32768\n"
      "ds_read_b64_tr_b16 v[122:123], %[a4] offset:49152\n"
      "v_mfma_f32_16x16x32_bf16 v[60:63], v[24:27], v[124:127], v[60:63]\n"
      "v_mfma_f32_16x16x32_bf16 v[92:95], v[28:31], v[124:127], v[92:95]\n"
      "ds_read_b64_tr_b16 v[124:125], %[a5] offset:32768\n"
      "ds_read_b64_tr_b16 v[126:127], %[a5] offset:49152\n"
      "v_mfma_f32_16x16x32_bf16 v[64:67], v[24:27], v[128:131], v[64:67]\n"
      "v_mfma_f32_16x16x32_bf16 v[96:99], v[28:31], v[128:131], v[96:99]\n"
      "ds_read_b64_tr_b16 v[128:129], %[a6] offset:32768\n"
      "ds_read_b64_tr_b16 v[130:131], %[a6] offset:49152\n"
      "v_mfma_f32_16x16x32_bf16 v[68:71], v[24:27], v[132:135], v[68:71]\n"
      "v_mfma_f32_16x16x32_bf16 v[100:103], v[28:31], v[132:135], v[100:103]\n"
      "ds_read_b64_tr_b16 v[132:133], %[a7] offset:32768\n"
      "ds_read_b64_tr_b16 v[134:135], %[a7] offset:49152\n"
      "s_cmp_lg_u64 %[m0], 0\n"
      "s_cbranch_scc1 122f\n"
      "s_mov_b64 exec, 1\n"
      "ds_add_u32 %[cb], %[one] offset:100\n"
      "s_mov_b64 exec, -1\n"
      "122:\n"
      "s_cmp_lg_u64 %[m1], 0\n"
      "s_cbranch_scc1 123b\n"
      "s_waitcnt lgkmcnt(8)\n"
      "v_cmp_ne_u32 vcc, 8, v22\n"
      "s_cbranch_vccnz 130b\n"
      "131:\n"
      "v_cmp_eq_u32 %[m1], 0, v23\n"
      "v_mfma_f32_16x16x32_bf16 v[40:43], v[32:35], v[136:139], v[40:43]\n"
      "v_mfma_f32_16x16x32_bf16 v[72:75], v[36:39], v[136:139], v[72:75]\n"
      "v_add_u32 v138, 0x10000, %[a0]\n"
      "ds_read_b64_tr_b16 v[136:137], v138 offset:32768\n"
      "ds_read_b64_tr_b16 v[138:139], v138 offset:49152\n"
      "v_mfma_f32_16x16x32_bf16 v[44:47], v[32:35], v[140:143], v[44:47]\n"
      "v_mfma_f32_16x16x32_bf16 v[76:79], v[36:39], v[140:143], v[76:79]\n"
      "v_add_u32 v142, 0x10000, %[a1]\n"
      "ds_read_b64_tr_b16 v[140:141], v142 offset:32768\n"
      "ds_read_b64_tr_b16 v[142:143], v142 offset:49152\n"
      "v_mfma_f32_16x16x32_bf16 v[48:51], v[32:35], v[144:147], v[48:51]\n"
      "v_mfma_f32_16x16x32_bf16 v[80:83], v[36:39], v[144:147], v[80:83]\n"
      "v_add_u32 v146, 0x10000, %[a2]\n"
      "ds_read_b64_tr_b16 v[144:145], v146 offset:32768\n"
      "ds_read_b64_tr_b16 v[146:147], v146 offset:49152\n"
      "v_mfma_f32_16x16x32_bf16 v[52:55], v[32:35], v[148:151], v[52:55]\n"
      "v_mfma_f32_16x16x32_bf16 v[84:87], v[36:39], v[148:151], v[84:87]\n"
      "v_add_u32 v150, 0x10000, %[a3]\n"
      "ds_read_b64_tr_b16 v[148:149], v150 offset:32768\n"
      "ds_read_b64_tr_b16 v[150:151], v150 offset:49152\n"
      "ds_read_b32 v20, %[cb] offset:108\n"
      "ds_read_b128 v[24:27], %[pa] offset:0\n"
      "ds_read_b128 v[28:31], %[pa] offset:1024\n"
      "ds_read_b32 v21, %[cb] offset:128\n"
      "v_mfma_f32_16x16x32_bf16 v[56:59], v[32:35], v[152:155], v[56:59]\n"
      "v_mfma_f32_16x16x32_bf16 v[88:91], v[36:39], v[152:155], v[88:91]\n"
      "v_add_u32 v154, 0x10000, %[a4]\n"
      "ds_read_b64_tr_b16 v[152:153], v154 offset:32768\n"
      "ds_read_b64_tr_b16 v[154:155], v154 offset:49152\n"
      "v_mfma_f32_16x16x32_bf16 v[60:63], v[32:35], v[156:159], v[60:63]\n"
      "v_mfma_f32_16x16x32_bf16 v[92:95], v[36:39], v[156:159], v[92:95]\n"
      "v_add_u32 v158, 0x10000, %[a5]\n"
      "ds_read_b64_tr_b16 v[156:157], v158 offset:32768\n"
      "ds_read_b64_tr_b16 v[158:159], v158 offset:49152\n"
      "v_mfma_f32_16x16x32_bf16 v[64:67], v[32:35], v[160:163], v[64:67]\n"
      "v_mfma_f32_16x16x32_bf16 v[96:99], v[36:39], v[160:163], v[96:99]\n"
      "v_add_u32 v162, 0x10000, %[a6]\n"
      "ds_read_b64_tr_b16 v[160:161], v162 offset:32768\n"
      "ds_read_b64_tr_b16 v[162:163], v162 offset:49152\n"
      "v_mfma_f32_16x16x32_bf16 v[68:71], v[32:35], v[164:167], v[68:71]\n"
      "v_mfma_f32_16x16x32_bf16 v[100:103], v[36:39], v[164:167], v[100:103]\n"
      "v_add_u32 v166, 0x10000, %[a7]\n"
      "ds_read_b64_tr_b16 v[164:165], v166 offset:32768\n"
      "ds_read_b64_tr_b16 v[166:167], v166 offset:49152\n"
      "s_cmp_lg_u64 %[m1], 0\n"
      "s_cbranch_scc1 132f\n"
      "s_mov_b64 exec, 1\n"
      "ds_add_u32 %[cb], %[one] offset:116\n"
      "s_mov_b64 exec, -1\n"
      "132:\n"
      "s_cmp_lg_u64 %[m0], 0\n"
      "s_cbranch_scc1 133b\n"
      "s_waitcnt lgkmcnt(8)\n"
      "v_cmp_ne_u32 vcc, 8, v20\n"
      "s_cbranch_vccnz 140b\n"
      "141:\n"
      "v_cmp_eq_u32 %[m0], 0, v21\n"
      "v_mfma_f32_16x16x32_bf16 v[40:43], v[24:27], v[104:107], v[40:43]\n"
      "v_mfma_f32_16x16x32_bf16 v[72:75], v[28:31], v[104:107], v[72:75]\n"
      "v_add_u32 v106, 0x10000, %[a0]\n"
      "ds_read_b64_tr_b16 v[104:105], v106 offset:0\n"
      "ds_read_b64_tr_b16 v[106:107], v106 offset:16384\n"
      "v_mfma_f32_16x16x32_bf16 v[44:47], v[24:27], v[108:111], v[44:47]\n"
      "v_mfma_f32_16x16x32_bf16 v[76:79], v[28:31], v[108:111], v[76:79]\n"
      "v_add_u32 v110, 0x10000, %[a1]\n"
      "ds_read_b64_tr_b16 v[108:109], v110 offset:0\n"
      "ds_read_b64_tr_b16 v[110:111], v110 offset:16384\n"
      "v_mfma_f32_16x16x32_bf16 v[48:51], v[24:27], v[112:115], v[48:51]\n"
      "v_mfma_f32_16x16x32_bf16 v[80:83], v[28:31], v[112:115], v[80:83]\n"
      "v_add_u32 v114, 0x10000, %[a2]\n"
      "ds_read_b64_tr_b16 v[112:113], v114 offset:0\n"
      "ds_read_b64_tr_b16 v[114:115], v114 offset:16384\n"
      "v_mfma_f32_16x16x32_bf16 v[52:55], v[24:27], v[116:119], v[52:55]\n"
      "v_mfma_f32_16x16x32_bf16 v[84:87], v[28:31], v[116:119], v[84:87]\n"
      "v_add_u32 v118, 0x10000, %[a3]\n"
      "ds_read_b64_tr_b16 v[116:117], v118 offset:0\n"
      "ds_read_b64_tr_b16 v[118:119], v118 offset:16384\n"
      "ds_read_b32 v22, %[cb] offset:124\n"
      "ds_read_b128 v[32:35], %[pa] offset:2048\n"
      "ds_read_b128 v[36:39], %[pa] offset:3072\n"
      "ds_read_b32 v23, %[cb] offset:144\n"
      "v_mfma_f32_16x16x32_bf16 v[56:59], v[24:27], v[120:123], v[56:59]\n"
      "v_mfma_f32_16x16x32_bf16 v[88:91], v[28:31], v[120:123], v[88:91]\n"
      "v_add_u32 v122, 0x10000, %[a4]\n"
      "ds_read_b64_tr_b16 v[120:121], v122 offset:0\n"
      "ds_read_b64_tr_b16 v[122:123], v122 offset:16384\n"
      "v_mfma_f32_16x16x32_bf16 v[60:63], v[24:27], v[124:127], v[60:63]\n"
      "v_mfma_f32_16x16x32_bf16 v[92:95], v[28:31], v[124:127], v[92:95]\n"
      "v_add_u32 v126, 0x10000, %[a5]\n"
      "ds_read_b64_tr_b16 v[124:125], v126 offset:0\n"
      "ds_read_b64_tr_b16 v[126:127], v126 offset:16384\n"
      "v_mfma_f32_16x16x32_bf16 v[64:67], v[24:27], v[128:131], v[64:67]\n"
      "v_mfma_f32_16x16x32_bf16 v[96:99], v[28:31], v[128:131], v[96:99]\n"
      "v_add_u32 v130, 0x10000, %[a6]\n"
      "ds_read_b64_tr_b16 v[128:129], v130 offset:0\n"
      "ds_read_b64_tr_b16 v[130:131], v130 offset:16384\n"
      "v_mfma_f32_16x16x32_bf16 v[68:71], v[24:27], v[132:135], v[68:71]\n"
      "v_mfma_f32_16x16x32_bf16 v[100:103], v[28:31], v[132:135], v[100:103]\n"
      "v_add_u32 v134, 0x10000, %[a7]\n"
      "ds_read_b64_tr_b16 v[132:133], v134 offset:0\n"
      "ds_read_b64_tr_b16 v[134:135], v134 offset:16384\n"
      "s_cmp_lg_u64 %[m1], 0\n"
      "s_cbranch_scc1 143b\n"
      "s_waitcnt lgkmcnt(8)\n"
      "v_cmp_ne_u32 vcc, 8, v22\n"
      "s_cbranch_vccnz 150b\n"
      "151:\n"
      "v_cmp_eq_u32 %[m1], 0, v23\n"
      "v_mfma_f32_16x16x32_bf16 v[40:43], v[32:35], v[136:139], v[40:43]\n"
      "v_mfma_f32_16x16x32_bf16 v[72:75], v[36:39], v[136:139], v[72:75]\n"
      "ds_read_b64_tr_b16 v[136:137], %[a0] offset:0\n"
      "ds_read_b64_tr_b16 v[138:139], %[a0] offset:16384\n"
      "v_mfma_f32_16x16x32_bf16 v[44:47], v[32:35], v[140:143], v[44:47]\n"
      "v_mfma_f32_16x16x32_bf16 v[76:79], v[36:39], v[140:143], v[76:79]\n"
      "ds_read_b64_tr_b16 v[140:141], %[a1] offset:0\n"
      "ds_read_b64_tr_b16 v[142:143], %[a1] offset:16384\n"
      "v_mfma_f32_16x16x32_bf16 v[48:51], v[32:35], v[144:147], v[48:51]\n"
      "v_mfma_f32_16x16x32_bf16 v[80:83], v[36:39], v[144:147], v[80:83]\n"
      "ds_read_b64_tr_b16 v[144:145], %[a2] offset:0\n"
      "ds_read_b64_tr_b16 v[146:147], %[a2] offset:16384\n"
      "v_mfma_f32_16x16x32_bf16 v[52:55], v[32:35], v[148:151], v[52:55]\n"
      "v_mfma_f32_16x16x32_bf16 v[84:87], v[36:39], v[148:151], v[84:87]\n"
      "ds_read_b64_tr_b16 v[148:149], %[a3] offset:0\n"
      "ds_read_b64_tr_b16 v[150:151], %[a3] offset:16384\n"
      "ds_read_b32 v20, %[cb] offset:140\n"
      "ds_read_b128 v[24:27], %[pa] offset:4096\n"
      "ds_read_b128 v[28:31], %[pa] offset:5120\n"
      "v_mfma_f32_16x16x32_bf16 v[56:59], v[32:35], v[152:155], v[56:59]\n"
      "v_mfma_f32_16x16x32_bf16 v[88:91], v[36:39], v[152:155], v[88:91]\n"
      "ds_read_b64_tr_b16 v[152:153], %[a4] offset:0\n"
      "ds_read_b64_tr_b16 v[154:155], %[a4] offset:16384\n"
      "v_mfma_f32_16x16x32_bf16 v[60:63], v[32:35], v[156:159], v[60:63]\n"
      "v_mfma_f32_16x16x32_bf16 v[92:95], v[36:39], v[156:159], v[92:95]\n"
      "ds_read_b64_tr_b16 v[156:157], %[a5] offset:0\n"
      "ds_read_b64_tr_b16 v[158:159], %[a5] offset:16384\n"
      "v_mfma_f32_16x16x32_bf16 v[64:67], v[32:35], v[160:163], v[64:67]\n"
      "v_mfma_f32_16x16x32_bf16 v[96:99], v[36:39], v[160:163], v[96:99]\n"
      "ds_read_b64_tr_b16 v[160:161], %[a6] offset:0\n"
      "ds_read_b64_tr_b16 v[162:163], %[a6] offset:16384\n"
      "v_mfma_f32_16x16x32_bf16 v[68:71], v[32:35], v[164:167], v[68:71]\n"
      "v_mfma_f32_16x16x32_bf16 v[100:103], v[36:39], v[164:167], v[100:103]\n"
      "ds_read_b64_tr_b16 v[164:165], %[a7] offset:0\n"
      "ds_read_b64_tr_b16 v[166:167], %[a7] offset:16384\n"
      "s_cmp_lg_u64 %[m0], 0\n"
      "s_cbranch_scc1 153b\n"
      "s_waitcnt lgkmcnt(8)\n"
      "v_cmp_ne_u32 vcc, 8, v20\n"
      "s_cbranch_vccnz 160b\n"
      "161:\n"
      "v_mfma_f32_16x16x32_bf16 v[40:43], v[24:27], v[104:107], v[40:43]\n"
      "v_mfma_f32_16x16x32_bf16 v[72:75], v[28:31], v[104:107], v[72:75]\n"
      "v_mfma_f32_16x16x32_bf16 v[44:47], v[24:27], v[108:111], v[44:47]\n"
      "v_mfma_f32_16x16x32_bf16 v[76:79], v[28:31], v[108:111], v[76:79]\n"
      "v_mfma_f32_16x16x32_bf16 v[48:51], v[24:27], v[112:115], v[48:51]\n"
      "v_mfma_f32_16x16x32_bf16 v[80:83], v[28:31], v[112:115], v[80:83]\n"
      "v_mfma_f32_16x16x32_bf16 v[52:55], v[24:27], v[116:119], v[52:55]\n"
      "v_mfma_f32_16x16x32_bf16 v[84:87], v[28:31], v[116:119], v[84:87]\n"
      "v_mfma_f32_16x16x32_bf16 v[56:59], v[24:27], v[120:123], v[56:59]\n"
      "v_mfma_f32_16x16x32_bf16 v[88:91], v[28:31], v[120:123], v[88:91]\n"
      "v_mfma_f32_16x16x32_bf16 v[60:63], v[24:27], v[124:127], v[60:63]\n"
      "v_mfma_f32_16x16x32_bf16 v[92:95], v[28:31], v[124:127], v[92:95]\n"
      "v_mfma_f32_16x16x32_bf16 v[64:67], v[24:27], v[128:131], v[64:67]\n"
      "v_mfma_f32_16x16x32_bf16 v[96:99], v[28:31], v[128:131], v[96:99]\n"
      "v_mfma_f32_16x16x32_bf16 v[68:71], v[24:27], v[132:135], v[68:71]\n"
      "v_mfma_f32_16x16x32_bf16 v[100:103], v[28:31], v[132:135], v[100:103]\n"
      "s_cmp_lg_u64 %[m1], 0\n"
      "s_cbranch_scc0 170f\n"
      "s_mov_b32 %[n], %[nb]\n"
      "91:\n"
      "s_sub_u32 %[n], %[n], 1\n"
      "s_cbranch_scc1 92f\n"
      "ds_read_b32 v22, %[cb] offset:144\n"
      "s_waitcnt lgkmcnt(0)\n"
      "v_cmp_gt_u32 vcc, 1, v22\n"
      "s_cbranch_vccnz 91b\n"
      "92:\n"
      "ds_read_b64_tr_b16 v[136:137], %[a0] offset:0\n"
      "ds_read_b64_tr_b16 v[138:139], %[a0] offset:16384\n"
      "ds_read_b64_tr_b16 v[140:141], %[a1] offset:0\n"
      "ds_read_b64_tr_b16 v[142:143], %[a1] offset:16384\n"
      "ds_read_b64_tr_b16 v[144:145], %[a2] offset:0\n"
      "ds_read_b64_tr_b16 v[146:147], %[a2] offset:16384\n"
      "ds_read_b64_tr_b16 v[148:149], %[a3] offset:0\n"
      "ds_read_b64_tr_b16 v[150:151], %[a3] offset:16384\n"
      "ds_read_b64_tr_b16 v[152:153], %[a4] offset:0\n"
      "ds_read_b64_tr_b16 v[154:155], %[a4] offset:16384\n"
      "ds_read_b64_tr_b16 v[156:157], %[a5] offset:0\n"
      "ds_read_b64_tr_b16 v[158:159], %[a5] offset:16384\n"
      "ds_read_b64_tr_b16 v[160:161], %[a6] offset:0\n"
      "ds_read_b64_tr_b16 v[162:163], %[a6] offset:16384\n"
      "ds_read_b64_tr_b16 v[164:165], %[a7] offset:0\n"
      "ds_read_b64_tr_b16 v[166:167], %[a7] offset:16384\n"
      "170:\n"
      "s_mov_b32 %[n], %[nb]\n"
      "91:\n"
      "s_sub_u32 %[n], %[n], 1\n"
      "s_cbranch_scc1 92f\n"
      "ds_read_b32 v22, %[cb] offset:152\n"
      "s_waitcnt lgkmcnt(0)\n"
      "v_cmp_gt_u32 vcc, 4, v22\n"
      "s_cbranch_vccnz 91b\n"
      "92:\n"
      "s_mov_b32 %[n], %[nb]\n"
      "91:\n"
      "s_sub_u32 %[n], %[n], 1\n"
      "s_cbranch_scc1 92f\n"
      "ds_read_b32 v22, %[cb] offset:156\n"
      "ds_read_b128 v[32:35], %[pa] offset:6144\n"
      "ds_read_b128 v[36:39], %[pa] offset:7168\n"
      "s_waitcnt lgkmcnt(0)\n"
      "v_cmp_gt_u32 vcc, 8, v22\n"
      "s_cbranch_vccnz 91b\n"
      "92:\n"
      "v_cmp_lt_u32 vcc, 8, v22\n"
      "s_cbranch_vccnz 174b\n"
      "171:\n"
      "v_mfma_f32_16x16x32_bf16 v[40:43], v[32:35], v[136:139], v[40:43]\n"
      "v_mfma_f32_16x16x32_bf16 v[44:47], v[32:35], v[140:143], v[44:47]\n"
      "v_mfma_f32_16x16x32_bf16 v[48:51], v[32:35], v[144:147], v[48:51]\n"
      "v_mfma_f32_16x16x32_bf16 v[52:55], v[32:35], v[148:151], v[52:55]\n"
      "v_mfma_f32_16x16x32_bf16 v[56:59], v[32:35], v[152:155], v[56:59]\n"
      "v_mfma_f32_16x16x32_bf16 v[60:63], v[32:35], v[156:159], v[60:63]\n"
      "v_mfma_f32_16x16x32_bf16 v[64:67], v[32:35], v[160:163], v[64:67]\n"
      "v_mfma_f32_16x16x32_bf16 v[68:71], v[32:35], v[164:167], v[68:71]\n"
      "v_mfma_f32_16x16x32_bf16 v[72:75], v[36:39], v[136:139], v[72:75]\n"
      "v_mfma_f32_16x16x32_bf16 v[76:79], v[36:39], v[140:143], v[76:79]\n"
      "v_mfma_f32_16x16x32_bf16 v[80:83], v[36:39], v[144:147], v[80:83]\n"
      "v_mfma_f32_16x16x32_bf16 v[84:87], v[36:39], v[148:151], v[84:87]\n"
      "v_mfma_f32_16x16x32_bf16 v[88:91], v[36:39], v[152:155], v[88:91]\n"
      "v_mfma_f32_16x16x32_bf16 v[92:95], v[36:39], v[156:159], v[92:95]\n"
      "v_mfma_f32_16x16x32_bf16 v[96:99], v[36:39], v[160:163], v[96:99]\n"
      "v_mfma_f32_16x16x32_bf16 v[100:103], v[36:39], v[164:167], v[100:103]\n"
      : "={v[40:71]}"(o0), "={v[72:103]}"(o1), [n] "=&s"(n), [k] "=&s"(k), [m0] "=&s"(m0), [m1] "=&s"(m1), [ret] "=&s"(ret)
      : [a0] "{v9}"(a[0]), [a1] "{v10}"(a[1]), [a2] "{v11}"(a[2]), [a3] "{v12}"(a[3]), [a4] "{v13}"(a[4]), [a5] "{v14}"(a[5]), [a6] "{v15}"(a[6]),
        [a7] "{v16}"(a[7]), [pa] "{v17}"(pa), [cb] "{v18}"(cb), [one] "{v19}"(one), [fac] "s"(T_FAC), [nb] "s"(nb)
      : "vcc", "scc", "memory", "v20", "v21", "v22", "v23", "v24", "v25", "v26", "v27", "v28", "v29", "v30", "v31", "v32", "v33", "v34", "v35", "v36", "v37", "v38", "v39", "v104", "v105", "v106", "v107", "v108", "v109", "v110", "v111", "v112", "v113", "v114", "v115", "v116", "v117", "v118", "v119", "v120", "v121", "v122", "v123", "v124", "v125", "v126", "v127", "v128", "v129", "v130", "v131", "v132", "v133", "v134", "v135", "v136", "v137", "v138", "v139", "v140", "v141", "v142", "v143", "v144", "v145", "v146", "v147", "v148", "v149", "v150", "v151", "v152", "v153", "v154", "v155", "v156", "v157", "v158", "v159", "v160", "v161", "v162", "v163", "v164", "v165", "v166", "v167");
}
DI void ffn_part(const Params& p, int b, int s, int wave, unsigned t0, u64 tag);
DI void attn_wg(const Params& p, int b, int s, int wave, unsigned t0) {
  unsigned char* ws = p.ws;
  const rsrc_t rs = __builtin_amdgcn_make_buffer_rsrc(ws, 0, WS_BYTES, 0x00020000);
  const int w = wave & 3;
  const f32x4 z4 = {0.f, 0.f, 0.f, 0.f};

  if (wave < 2) { const int lane = fresh_lane(); *(LAS unsigned*)(T_CNT + wave * 256 + lane * 4) = wave == 0 && lane < 8 ? 8u : 0u; }
  bar();
  u64 tag = 0;
  if (wave != 8) tag = get_tag(ws);
  if (wave == 8) {
    const int lane = fresh_lane();
    unsigned vo[16];
#pragma unroll
    for (int r = 0; r < 16; ++r) vo[r] = 16 * (lane ^ fsw(r));
    const unsigned char* kv = (const unsigned char*)p.ckv + (size_t)(b * 4096 + s * ILV) * 1024;
    nap<1>();
    wait_until(t0 + T_PREF);
    __builtin_amdgcn_s_setprio(PRIO_L);
    bool watch = true;
    STAMP(62); STAMPD(31);
#pragma unroll 1
    for (int t = 0; t < 8; ++t) {
      bool told = false;
      if (t >= 3) {
        const unsigned c = t == 5 ? C_QRD : C_FREE + 16 * (t < 5 ? t - 3 : t - 4), want = t == 5 ? 4 : 8;
        auto free_now = [&]() { return __builtin_amdgcn_readfirstlane(lpeek(c)) >= want; };
        bool ok = free_now();
        if (!ok) { VMCNT(24); ok = free_now(); }
        if (!ok) { VMCNT(16); ok = free_now(); }
        if (!ok) { VMCNT(8); ok = free_now(); }
        if (!ok) { VMCNT(0); lsig(C_LAND + 16 * t - 16); told = true; lwait<true>(c, want); }
      }
      const unsigned dst = slot_of(t);
#pragma unroll
      for (int i = 0; i < 8; ++i) {
        DMA4("nt", kv + (4 * i / ILV) * (16 * ILV * 1024) + (4 * i % ILV) * 1024, dst + i * 4096, vo[4 * (i & 3)], vo[4 * (i & 3) + 1], vo[4 * (i & 3) + 2], vo[4 * (i & 3) + 3]);
        if (watch) {
          const unsigned hold = __builtin_amdgcn_readfirstlane(lpeek(C_HOLD));
          if (hold) { lwait<true>(C_HOLD, 2); watch = false; }
        }
      }
      if (!told) { VMCNT(32); lsig(C_LAND + 16 * t - 16); }
      kv += ILV > 32 ? 32768 : 16 * 32768;
    }
    STAMP(22);

    VMCNT(0);
    lsig(C_LAND + 16 * 7);
    STAMP(23);
    if (s >= 8 || b == 7) { DUMP(62); DUMP(31); DUMP(22); DUMP(23); }
    ROLE_END();
    return;
  }
  __builtin_amdgcn_s_setprio(2);
  if (wave == 9) {
    const int lane = fresh_lane();
    unsigned vo[16];
#pragma unroll
    for (int r = 0; r < 16; ++r) { vo[r] = (lane >> 5) * ASTR + (r & 3) * (4 * ASTR - 1024) + 16 * ((lane & 31) ^ fsw(r)); pin(vo[r]); }
    const unsigned char* qa = ws + WS_ARENA + 2 * ASTR + A_E + b * 512;
    wait_until(t0 + T_FLAG);
    wait_flags(rs, WS_FLAG1 + lane * 16, tag);
    STAMP(17); STAMPD(47);
    const unsigned tq = latest_time32<128>(ws + WS_HQ + (16 * b + s) * 512, t0, (unsigned)tag);
    STAMPV(47);
    wait_until(tq + T_QA - T_HOLD);
    lsig(C_HOLD);
    wait_until(tq + T_QA);
    STAMP(18);
#pragma unroll
    for (int i = 0; i < 8; ++i)
      DMA4("sc1", qa + i * (16 * ASTR), 3 * 32768 + i * 4096, vo[4 * (i & 3)], vo[4 * (i & 3) + 1], vo[4 * (i & 3) + 2], vo[4 * (i & 3) + 3]);
    lsig(C_HOLD);
    VMCNT(0);
    lsig(C_QST);
    STAMP(38);

    if (s >= 8 || b == 7) { lwait<true>(C_PRDY + 16 * 7, 8); nap<30>(); DUMP(17); DUMP(18); DUMP(38); DUMP(47); }
    ROLE_END();
    return;
  }
  if (wave >= 10) {
    __builtin_amdgcn_s_setprio(3);
    const int hh = wave - 10, lane = fresh_lane(), l15 = lane & 15, lq = lane >> 4;
#pragma unroll 1
    for (int real = DRY ? 0 : 1; real < 2; ++real) {
    const unsigned nb = real ? SPIN_L : 1;
    unsigned sa = T_SP + hh * 1024 + lane * 16, cb = real ? T_CNT : T_DRY, four = 4u;
    int mlo = real && lq == 0 ? rarena(16 * b + s, tag) + A_ML + hh * 256 + l15 * 16 : OOB;
    pin(sa); pin(cb); pin(four); pin(mlo);
    float ref = 0.f, l = 0.f;
    cwait<C_QST, 1, true>(cb, nb);
    static_for<0, 8>([&](auto tc) {
      constexpr int t = decltype(tc)::value;
      const unsigned nb_ = nb;
      if constexpr (t >= 4) cwait<C_FREE + 16 * (t == 4 ? 3 : t - 2), 8>(cb, nb);
      f32x4 p0, p1, p2, p3;
      unsigned c, n;
      asm volatile(SPIN_HEAD "ds_read_b32 %[c], %[cb] offset:%[o]\n\tds_read_b128 %[p0], %[sa] offset:%[s0]\n\tds_read_b128 %[p2], %[sa] offset:%[s2]\n\t"
                   "ds_read_b128 %[p1], %[sa] offset:%[s1]\n\tds_read_b128 %[p3], %[sa] offset:%[s3]\n\t" SPIN_TAIL2
                   : [c] "=&v"(c), [n] "=&s"(n), [p0] "=&v"(p0), [p1] "=&v"(p1), [p2] "=&v"(p2), [p3] "=&v"(p3)
                   : [cb] "v"(cb), [sa] "v"(sa), [o] "i"(C_SRDY + 16 * t - T_CNT), [w] "i"(4), [w2] "i"(t == 7 ? 1 : 4), [s0] "i"((t & 1) * 8192),
                     [s1] "i"((t & 1) * 8192 + 2048), [s2] "i"((t & 1) * 8192 + 4096), [s3] "i"((t & 1) * 8192 + 6144), [nb] "s"(nb_)
                   : "vcc", "scc", "memory");
      f32x4 a = (p0 - ref) + p2, c4 = (p1 - ref) + p3;
      const float m = fmaxf(fmaxf(fmaxf(a.x, a.y), fmaxf(a.z, a.w)), fmaxf(fmaxf(c4.x, c4.y), fmaxf(c4.z, c4.w)));
      unsigned sig = four;
      if constexpr (t == 0) {
        ref = xmax32(xmax16(m));
        a -= ref; c4 -= ref;
      } else if (__builtin_expect(__any(m > BUMP), 0)) {
        const float up = fmaxf(0.f, xmax32(xmax16(m)));
        const float fa = __builtin_amdgcn_exp2f(-up);
        *(LAS float*)(T_FAC + t * 128 + hh * 64 + l15 * 4) = fa;
        l *= fa;
        ref += up;
        a = (p0 - ref) + p2; c4 = (p1 - ref) + p3;
        sig = 4u + (1u << hh);
      }
      const v4u pf = {pk(__builtin_amdgcn_exp2f(a.x), __builtin_amdgcn_exp2f(a.y)), pk(__builtin_amdgcn_exp2f(a.z), __builtin_amdgcn_exp2f(a.w)),
                      pk(__builtin_amdgcn_exp2f(c4.x), __builtin_amdgcn_exp2f(c4.y)), pk(__builtin_amdgcn_exp2f(c4.z), __builtin_amdgcn_exp2f(c4.w))};
      *(LAS v4u*)((LAS char*)sa + (T_P - T_SP) + (t & 3) * 2048) = pf;
      csig<C_PRDY + 16 * t>(cb, sig);
#pragma unroll
      for (int e = 0; e < 4; ++e) {
        const unsigned u = pf[e];
        l = __builtin_amdgcn_fdot2_f32_bf16(__builtin_bit_cast(bf16x2, u), __builtin_bit_cast(bf16x2, 0x3F803F80u), l, false);
      }
    });
    stg(rs, mlo, v4u{__float_as_uint(ref), (unsigned)tag, __float_as_uint(xsum32(xsum16(l))), (unsigned)(tag >> 32)});
    }
    STAMP(24);
    if (hh == 0 && (s >= 8 || b == 7)) DUMP(24);
    ROLE_END();
    return;
  }
  if (wave < 4) {
    __builtin_amdgcn_s_setprio(PRIO_S);
    const int kh = wave & 1, ch = wave >> 1;
    STAMPD(19); STAMPD(20);
    int rqa = 0;
#pragma unroll 1
    for (int real = DRY ? 0 : 1; real < 2; ++real) {
    const unsigned nb = real ? SPIN_L : 1;
    const int lane = fresh_lane(), l15 = lane & 15, lq = lane >> 4;
    const int sg = (0x2130 >> (4 * lq)) & 3;
    unsigned ao[4];
#pragma unroll
    for (int k = 0; k < 4; ++k) ao[k] = l15 * 1024 + ch * 512 + 16 * ((4 * k + sg) ^ fsw(l15));
    unsigned sa = T_SP + wave * 2048 + lane * 16, cb = real ? T_CNT : T_DRY, one = 1u;
    unsigned al[4], ah[4];
#pragma unroll
    for (int k = 0; k < 4; ++k) { al[k] = ao[k] + kh * 16384; ah[k] = al[k] + 65536; pin(al[k]); pin(ah[k]); }
    int aro = real && wave == 0 && lane < 32 ? WS_ARR + (4 * lane + (b >> 1)) * 512 + (s & 3) * 128 + (b & 1) * 16 + (s >> 2) * 4 : OOB;
    pin(sa); pin(cb); pin(one); pin(aro);
    v4u af[2][8], qf[2][8];
    auto rd = [&](unsigned sl, int ks) { return lds128(sl < 65536 ? al[ks & 3] : ah[ks & 3], (sl & 32768) + (ks >> 2) * 256); };
    static_for<0, 2>([&](auto tc) {
      constexpr int t = decltype(tc)::value;
      cwait<C_LAND + 16 * t, 1, true>(cb, nb);
#pragma unroll
      for (int ks = 0; ks < 8; ++ks) af[t][ks] = rd(t * 32768, ks);
      csig<C_FREE + 16 * t>(cb, one);
    });
    unsigned qa[4];
#pragma unroll
    for (int k = 0; k < 4; ++k) { qa[k] = 3 * 32768 + ao[k]; pin(qa[k]); }
    cwait<C_QST, 1>(cb, nb);
    STAMPV(19);
    unsigned nx2 = cpeek<C_LAND + 32>(cb), nxt = cpeek<C_LAND + 48>(cb);
    f32x4 s0, s1;
#pragma unroll 1
    for (int sp = 0; sp < SPIN_G; ++sp) {
      rqa = sp + 1;
#pragma unroll
      for (int ks = 0; ks < 8; ++ks) {
#pragma unroll
        for (int hh = 0; hh < 2; ++hh) qf[hh][ks] = lds128(qa[ks & 3], hh * 16384 + (ks >> 2) * 256);
        __builtin_amdgcn_sched_barrier(0);
      }
      s0 = z4; s1 = z4;
#pragma unroll
      for (int ks = 0; ks < 8; ++ks) { s0 = mfma16(af[0][ks], qf[0][ks], s0); s1 = mfma16(af[0][ks], qf[1][ks], s1); }
      if (__builtin_expect(!__any(__builtin_isunordered(s0.x, s1.x)), 1) || !real || !(any_sent<8>(qf[0]) || any_sent<8>(qf[1]))) break;
#pragma unroll 1
      for (unsigned m = ((unsigned)__ballot(s0.x != s0.x) & 0xFFFFu) | ((unsigned)__ballot(s1.x != s1.x) << 16); m; m &= m - 1) {
        const int r = __builtin_ctz(m);
        asm volatile("s_mov_b32 m0, %2\n\ts_nop 0\n\tglobal_load_lds_dwordx4 %0, %1 sc1"
                     :: "v"((lane >> 5) * ASTR + 16 * ((lane & 31) ^ fsw(r))), "s"(ws + WS_ARENA + 2 * ASTR + A_E + b * 512 + r * (4 * ASTR)), "s"(3 * 32768 + r * 1024) : "memory");
      }
      VMCNT(0);
    }
    *(LAS f32x4*)sa = s0;
    *(LAS f32x4*)((LAS char*)sa + 1024) = s1;
    csig<C_SRDY>(cb, one);
    csig<C_QRD>(cb, one);
    STAMPV(20);
    auto refill = [&](v4u& a, auto slc, auto ksc) {
      constexpr unsigned sl = decltype(slc)::value;
      constexpr int ks = decltype(ksc)::value;
      rd_inplace<(sl & 32768) + (ks >> 2) * 256>(a, sl < 65536 ? al[ks & 3] : ah[ks & 3]);
    };
    static_for<0, 8>([&](auto k) { refill(af[0][decltype(k)::value], IntC<slot_c(2)>{}, k); });
    bool miss = __any(nx2 < 1);
    if (!miss) csig<C_FREE + 32>(cb, one);
    static_for<1, 8>([&](auto tc) {
      constexpr int t = decltype(tc)::value;
      const bool was = miss;
      if constexpr (t < 6) miss = __any(nxt < 1);
      unsigned use;
      peek2<C_PRDY + 16 * (t - 2) - T_CNT, C_LAND + 16 * (t < 5 ? t + 3 : 8) - T_CNT>(use, nxt, cb);
      static_for<0, 8>([&](auto k) {
        constexpr int ks = decltype(k)::value;
        score_pair<(t < 6 ? 9 : 9 - ks), ks == 0>(s0, s1, af[t & 1][ks], qf[0][ks], qf[1][ks]);
        if constexpr (t < 6) refill(af[t & 1][ks], IntC<slot_c(t + 2)>{}, k);
      });
      wait2<(t < 6 ? 8 : 0)>(use, nxt);
      if (__builtin_expect(__any(use < 8), 0)) cwait<C_PRDY + 16 * (t - 2), 8>(cb, nb);
      put_scores<(t & 1) * 8192>(sa, s0, s1);
      csig<C_SRDY + 16 * t>(cb, one);
      if constexpr (t == 7 && !ARR_EARLY) st32(rs, aro, now32() + (unsigned)tag);
      if constexpr (t < 4) { if (!miss) csig<C_FREE + 16 * (t + 2)>(cb, one); }
      if constexpr (t < 7) {
        if (was) {
          cwait<C_LAND + 16 * (t + 1), 1>(cb, nb);
          static_for<0, 8>([&](auto k) { refill(af[(t + 1) & 1][decltype(k)::value], IntC<slot_c(t + 1)>{}, k); });
          if constexpr (t < 5) csig<C_FREE + 16 * (t + 1)>(cb, one);
        }
      }
      if constexpr (t == 6 && ARR_EARLY) { if (wave == 0) st32(rs, aro, now32() + (unsigned)tag); }
    });
    asm volatile("s_waitcnt lgkmcnt(0)" ::: "memory");
    }
    STAMP(21);
    if (wave == 0 && (s >= 8 || b == 7)) { DUMP(19); DUMP(20); DUMP(21); DUMPC(50, rqa); }
  } else {
    __builtin_amdgcn_s_setprio(PRIO_PV);
#pragma unroll 1
    for (int real = DRY ? 0 : 1; real < 2; ++real) {
    const int lane = fresh_lane(), l15 = lane & 15, lq = lane >> 4;
    unsigned vl[8];
#pragma unroll
    for (int ct = 0; ct < 8; ++ct)
      vl[ct] = ((lane >> 2) * 1024 + w * 256 + 16 * ((2 * (ct & 1) + ((lane >> 1) & 1)) ^ fsw(lane >> 2)) + 8 * (lane & 1)) ^ ((ct >> 1) * 64);
    int po = real ? rarena(16 * lq + w, tag) + A_PART + (b * 16 + s) * 256 + l15 * 16 : OOB - 80 * ASTR;
    int cko = (lane & 2) || !real ? OOB : rarena(16 * b + s, tag) + A_CK + w * 512 + (2 * (lane & 8) + 4 * lq + ((lane >> 1) & 2) + (lane & 1)) * 16;
    pin(po); pin(cko);
    f32x32 acc[2];
    pv_loop(acc[0], acc[1], vl, T_P + lane * 16, real ? T_CNT : T_DRY, 1u, real ? SPIN_L : 1);
    unsigned ce[8], co[8];
#pragma unroll
    for (int ht = 0; ht < 2; ++ht)
#pragma unroll
      for (int r = 0; r < 4; ++r) {
        if (ht == 1 && r == 0) asm volatile("s_nop 1" : "+{v[72:103]}"(acc[1]) :: "memory");
        const v4u v = {pk(acc[ht][r], acc[ht][4 + r]), pk(acc[ht][8 + r], acc[ht][12 + r]), pk(acc[ht][16 + r], acc[ht][20 + r]),
                       pk(acc[ht][24 + r], acc[ht][28 + r])};
        stg(rs, po + (16 * ht + r) * 4 * ASTR, v);
        ce[4 * ht + r] = v.x ^ v.z; co[4 * ht + r] = v.y ^ v.w;
      }
    {
      unsigned e0, e1, o0, o1;
      xor_rows8(ce, e0, e1);
      xor_rows8(co, o0, o1);
      const bool odd = cko & 16;
      stg(rs, cko, v4u{odd ? e1 : e0, (unsigned)tag, odd ? o1 : o0, (unsigned)(tag >> 32)});
    }
    }
    STAMP(25);
  }
  if (b == 7 || s >= 8) { ROLE_END(); return; }
  ffn_part(p, b, s, wave, t0, tag);
  ROLE_END();
}

DI void ffn_part(const Params& p, int b, int s, int wave, unsigned t0, u64 tag) {
  const int lane = fresh_lane();
  unsigned char* ws = p.ws;
  const rsrc_t rs = __builtin_amdgcn_make_buffer_rsrc(ws, 0, WS_BYTES, 0x00020000);
  const int l15 = lane & 15, lq = lane >> 4, w = wave & 3;
  const f32x4 z4 = {0.f, 0.f, 0.f, 0.f};
  __builtin_amdgcn_s_setprio(2);
  const int fg = 8 * b + s, b8 = lane & 7, p4 = 4 * (lane & 3);
  const bool bvalid = l15 < 8;
  const int ch = l15 ^ ((lq << 2) | w), row0 = 4 * w + lq;
  bar();
  STAMP(52);
  nap<D_F>();
  v4u mm[4];
  const int mo = arena(32 + 4 * wave) + A_S + (4 * (l15 >> 3) + lq) * 128 + b8 * 16;
  auto load_mod = [&]() {
    FENCE();
#pragma unroll
    for (int i = 0; i < 4; ++i) mm[i] = ldg(rs, mo, ((i >> 1) * 32 + (i & 1)) * ASTR);
    FENCE();
  };
  load_mod();
  if (wave < 4) {
    dma_run<false>(p.w1, (row0 + (ch >> 2) * 128) * 3584 + fg * 64 + (ch & 3) * 16, W0 + w * 1024, 8, 16 * 3584, 4096);
  } else {
    const unsigned u = (32 * fg + row0) * 1024 + ch * 16;
    dma_run<true>(p.w2, u, W1 + w * 1024, 4, 256, 8192);
    dma_run<true>(p.w2, u + 16 * 1024, W1 + w * 1024 + 4096, 4, 256, 8192);
  }
#pragma unroll 1
  for (int sp = 0; sp < SPIN_G; ++sp) {
    if (!any_sent<4>(mm)) break;
    load_mod();
  }
  f32x4 sb[2], cb[2];
  sb[0] = up4(mm[0].xy) + up4(mm[1].xy); sb[1] = up4(mm[0].zw) + up4(mm[1].zw);
  cb[0] = up4(mm[2].xy) + up4(mm[3].xy) + 1.f; cb[1] = up4(mm[2].zw) + up4(mm[3].zw) + 1.f;
  pin(sb[0]); pin(sb[1]); pin(cb[0]); pin(cb[1]);
  if (wave == 7) wait_flags(rs, WS_FLAG2 + lane * 16, tag);
  VMCNT(0);
  bar();
  STAMP(28);
  v4u f1[2][2], f2[8];
#pragma unroll
  for (int tt = 0; tt < 2; ++tt) {
    const int c4 = 32 * (wave >> 1) + 2 * p4 + 4 * tt;
    const unsigned a0 = tr_addr(W0 + (wave & 1) * 16384, lane, c4, 0), a1 = tr_addr(W0 + (wave & 1) * 16384, lane, c4, 1);
    f1[0][tt] = tr_frag(a0, a1, 0); f1[1][tt] = tr_frag(a0, a1, 1);
    pin(f1[0][tt]); pin(f1[1][tt]);
  }
#pragma unroll
  for (int i = 0; i < 8; ++i) {
    const int c4 = 32 * (i >> 1) + 2 * p4 + 4 * (i & 1);
    f2[i] = tr_frag(tr_addr(W1 + w * 8192, lane, c4, 0), tr_addr(W1 + w * 8192, lane, c4, 1), 0);
    pin(f2[i]);
  }
  int xo = arena(wave) + A_X1 + (4 * (l15 >> 3) + lq) * 128 + b8 * 16;
  unsigned zw = L_A5 + wave * 1024 + lane * 16, zr = L_A5 + lane * 16;
  unsigned sw = lane < 8 ? L_A6 + lane * 80 + wave * 8 : L_JUNK, sr = L_A6 + b8 * 80;
  pin(xo); pin(zw); pin(zr); pin(sw); pin(sr);
  STAMP(55);
  STAMPD(40); STAMPD(15); STAMPD(36); STAMPD(37);
  int rx = 0;
  auto first = [&](v4u u) {
    const f32x4 d00 = mfma16(f1[0][0], u, z4), d10 = mfma16(f1[1][0], u, z4), d01 = mfma16(f1[0][1], u, z4), d11 = mfma16(f1[1][1], u, z4);
    f32x4 zz;
#pragma unroll
    for (int r = 0; r < 4; ++r) zz[r] = (bvalid ? d00[r] : d11[r]) + dppf<0x128>(bvalid ? d01[r] : d10[r]);
    return zz;
  };
  *(LAS f32x4*)((LAS char*)zw + 16384) = first(pk8(cb[0], cb[1]));
  *(LAS f32x4*)((LAS char*)zw + 24576) = first(pk8(sb[0], sb[1]));
  bar();
  f32x4 v1 = z4, v2 = z4;
  int yo = arena(fg) + A_YP + (16 * w + 4 * (l15 >> 3) + lq) * 128 + b8 * 16;
  if (wave >= 4) {
#pragma unroll
    for (int i = 0; i < 8; ++i) { v1 += u2f(lds128(zr, 16384 + i * 1024)); v2 += u2f(lds128(zr, 24576 + i * 1024)); }
  }
  pin(v1); pin(v2); pin(yo);
  STAMPV(40);
  if (wave == 0) wait_until(latest_time(ws + WS_HB + (16 * b + s) * 128, t0, (unsigned)tag) + T_C);
  bar();
  {
    v4u xv[1];
#pragma unroll 1
    for (int sp = 0; sp < SPIN_G; ++sp) { load_n<1>(rs, xo, 0, xv); rx = sp + 1; if (!any_sent<1>(xv)) break; }
    STAMPV(15);
    const f32x4 x0 = up4(xv[0].xy), x1 = up4(xv[0].zw);
    *(LAS f32x4*)zw = first(pk8(x0 * cb[0], x1 * cb[1]));
    const f32x4 s4 = x0 + x1, q4 = x0 * x0 + x1 * x1;
    float sx = (s4.x + s4.y) + (s4.z + s4.w), sq = (q4.x + q4.y) + (q4.z + q4.w);
    sx += dppf<0x128>(sx); sq += dppf<0x128>(sq);
    {
      auto y = __builtin_amdgcn_permlane16_swap(__float_as_uint(sx), __float_as_uint(sq), false, false);
      const float t = xsum32(__uint_as_float(y[0]) + __uint_as_float(y[1]));
      auto z = __builtin_amdgcn_permlane16_swap(__float_as_uint(t), __float_as_uint(t), false, false);
      *(LAS v2u*)sw = v2u{z[0], z[1]};
    }
    STAMPV(36);
    bar();
    if (wave >= 4) {
      STAMPV(37);
      f32x4 zp[8], st[4];
#pragma unroll
      for (int i = 0; i < 4; ++i) st[i] = u2f(lds128(sr, i * 16));
#pragma unroll
      for (int i = 0; i < 8; ++i) zp[i] = u2f(lds128(zr, i * 1024));
      __builtin_amdgcn_sched_barrier(0);
      const f32x4 ss = (st[0] + st[1]) + (st[2] + st[3]);
      const float mean = (ss.x + ss.z) * (1.f / 512), msq = (ss.y + ss.w) * (1.f / 512);
      const float rstd = __builtin_amdgcn_rsqf(fmaxf(msq - mean * mean, 0.f) + EPS);
      const f32x4 zs = ((zp[0] + zp[1]) + (zp[2] + zp[3])) + ((zp[4] + zp[5]) + (zp[6] + zp[7]));
      const f32x4 z = (zs - v1 * mean) * rstd + v2;
      const unsigned g0 = pk(z.x < 0.f ? 0.f : z.x, z.y < 0.f ? 0.f : z.y), g1 = pk(z.z < 0.f ? 0.f : z.z, z.w < 0.f ? 0.f : z.w);
      const unsigned o0 = (unsigned)__builtin_amdgcn_update_dpp(0, (int)g0, 0x128, 0xF, 0xF, true), o1 = (unsigned)__builtin_amdgcn_update_dpp(0, (int)g1, 0x128, 0xF, 0xF, true);
      const v4u bf = {bvalid ? g0 : o0, bvalid ? g1 : o1, bvalid ? o0 : g0, bvalid ? o1 : g1};
#pragma unroll
      for (int i = 0; i < 2; ++i) {
        const f32x4 a0 = mfma16(f2[4 * i], bf, z4), a1 = mfma16(f2[4 * i + 1], bf, z4), c0 = mfma16(f2[4 * i + 2], bf, z4), c1 = mfma16(f2[4 * i + 3], bf, z4);
        stg(rs, yo + i * 1024, pick(bvalid, pk8(a0, a1), pk8(c0, c1)));
      }
    }
  }
}

DI void first_requests() {
  u64 px, pc, pw;
  unsigned a, kp, wv, v0, v1, v2;
  asm volatile(
      "s_load_dwordx2 %[px], %[k], 0x0\n\ts_load_dwordx2 %[pc], %[k], 0x40\n\ts_load_dwordx2 %[pw], %[k], 0x48\n\t"
      "v_readfirstlane_b32 %[wv], %[t]\n\ts_and_b32 %[a], %[bx], 0x90\n\ts_cmpk_eq_u32 %[a], 0x90\n\ts_cbranch_scc0 3f\n\t"
      "s_bfe_u32 %[a], %[bx], 0x20005\n\ts_bfe_u32 %[kp], %[bx], 0x10003\n\ts_lshl1_add_u32 %[kp], %[a], %[kp]\n\t"
      "s_lshr_b32 %[wv], %[wv], 6\n\ts_cmp_gt_u32 %[wv], 3\n\ts_cbranch_scc1 2f\n\t"
      "v_bfe_u32 %[v0], %[t], 4, 2\n\tv_lshl_or_b32 %[v1], %[v0], 2, %[wv]\n\tv_xor_b32 %[v1], %[v1], %[t]\n\t"
      "v_mul_u32_u24 %[v0], 0x1800, %[v0]\n\tv_and_b32 %[v2], 7, %[v1]\n\tv_lshl_add_u32 %[v0], %[v2], 4, %[v0]\n\t"
      "v_bfe_u32 %[v2], %[v1], 3, 1\n\tv_lshl_add_u32 %[v0], %[v2], 10, %[v0]\n\t"
      "s_lshl4_add_u32 %[kp], %[kp], %[wv]\n\ts_mul_i32 %[kp], %[kp], 0x6000\n\ts_and_b32 %[a], %[bx], 7\n\ts_lshl_b32 %[a], %[a], 7\n\t"
      "s_add_u32 %[a], %[a], %[kp]\n\tv_add_u32 %[v0], %[a], %[v0]\n\ts_lshl_b32 %[wv], %[wv], 10\n\ts_add_u32 m0, %[wv], %[w2]\n\t"
      "v_add_u32 %[v1], 0x18000, %[v0]\n\tv_add_u32 %[v2], 0x30000, %[v0]\n\ts_waitcnt lgkmcnt(0)\n\t"
      "global_load_lds_dwordx4 %[v0], %[pw] nt\n\ts_add_u32 m0, m0, 0x1000\n\tv_add_u32 %[v0], 0x48000, %[v0]\n\t"
      "global_load_lds_dwordx4 %[v1], %[pw] nt\n\ts_add_u32 m0, m0, 0x1000\n\ts_nop 0\n\t"
      "global_load_lds_dwordx4 %[v2], %[pw] nt\n\ts_add_u32 m0, m0, 0x1000\n\ts_nop 0\n\t"
      "global_load_lds_dwordx4 %[v0], %[pw] nt\n\ts_branch 3f\n"
      "2:\n\tv_add_u32 %[v0], 0xffffff00, %[t]\n\tv_lshlrev_b32 %[v0], 4, %[v0]\n\tv_and_b32 %[v1], 0xfffffc70, %[v0]\n\t"
      "s_lshl_b32 %[kp], %[kp], 7\n\tv_or_b32 %[v1], %[kp], %[v1]\n\ts_lshl_b32 %[wv], %[wv], 11\n\ts_add_u32 m0, %[wv], %[raw]\n\t"
      "s_waitcnt lgkmcnt(0)\n\tglobal_load_lds_dwordx4 %[v1], %[pc]\n\ts_sub_u32 m0, m0, 0x400\n\ts_nop 0\n\t"
      "global_load_lds_dwordx4 %[v0], %[px]\n"
      "3:\n\ts_waitcnt lgkmcnt(0)"
      : [px] "=&s"(px), [pc] "=&s"(pc), [pw] "=&s"(pw), [a] "=&s"(a), [kp] "=&s"(kp), [wv] "=&s"(wv), [v0] "=&v"(v0), [v1] "=&v"(v1), [v2] "=&v"(v2)
      : [k] "s"(__builtin_amdgcn_kernarg_segment_ptr()), [bx] "s"(blockIdx.x), [t] "v"(threadIdx.x), [w2] "i"(W2), [raw] "i"(M_RAW + 1024 - 4 * 2048)
      : "scc", "memory");
}

__global__ __launch_bounds__(768) void adaln_mla_ffn_kernel(Params p) {
  STAMP(0);
  first_requests();
  const unsigned t0 = now32();
  const int g = blockIdx.x & 7, idx = blockIdx.x >> 3;
  const int wave = __builtin_amdgcn_readfirstlane(threadIdx.x >> 6), lane = threadIdx.x & 63;
  if (wave == 0 && idx < 16) {
    u64 pc;
    unsigned left;
    asm volatile("s_getpc_b64 %0\n99:\n\ts_mov_b32 %1, 98f-99b-4\n\t.subsection 2\n98:\n\t.subsection 0" : "=s"(pc), "=s"(left));
    asm volatile("s_mov_b32 m0, %2\n\ts_nop 0\n\tglobal_load_lds_dword %0, %1" :: "v"(min((idx + 16 * min(lane >> 1, 21)) * 128u + (lane & 1) * 64, left)), "s"(pc), "s"(L_JUNK) : "memory");
  }
  const int wi = idx & 15, l15 = lane & 15, lq = lane >> 4, b8 = lane & 7;
  const int kp = 2 * (wi >> 2) + (wi & 1);
  const unsigned vx = lane * 16;
  asm volatile("" :: "s"(p.x), "s"(p.ckv), "s"(p.wq), "s"(p.wuk), "s"(p.wuv), "s"(p.wo), "s"(p.w1), "s"(p.w2), "s"(p.c), "s"(p.wmod),
               "s"(p.out), "s"(p.ws));
  STAMP(39);
  STAMPD(2); STAMPD(3); STAMPD(32); STAMPD(1);
  const rsrc_t rs = __builtin_amdgcn_make_buffer_rsrc(p.ws, 0, WS_BYTES, 0x00020000);
  const int my = arena(16 * g + wi);
  if (idx >= 16 && wave == 3) {
#pragma unroll
    for (int n = 0; n < 5; ++n) stg(rs, my + vx + n * 1024, v4u{SENT, SENT, SENT, SENT});
  }
  if ((idx & 18) == 18) {
    const int r = wave - 4;
    if (wave >= 4) {
      unsigned raw = M_RAW + r * 2048 + vx, d = (lane >> 3) == g ? M_LNX + r * 256 + b8 * 32 : L_JUNK, sc = M_SC + r * 144 + lane * 2;
      unsigned rc = M_RAW + r * 2048 + 1024 + lane * 2;
      pin(raw); pin(d); pin(sc); pin(rc);
      VMCNT(0);
      const v4u xr = lds128(raw);
      const float cv = bflo(*(LAS u16*)rc);
      *(LAS u16*)sc = (u16)pk(cv * __builtin_amdgcn_rcpf(1.f + __expf(-cv)), 0.f);
      bar();
      const f32x4 v0 = up4(xr.xy), v2 = up4(xr.zw), sv4 = v0 + v2, qv4 = v0 * v0 + v2 * v2;
      const float mean = wave_sum((sv4.x + sv4.y) + (sv4.z + sv4.w)) * (1.f / 512);
      const float msq = wave_sum((qv4.x + qv4.y) + (qv4.z + qv4.w)) * (1.f / 512);
      const float rstd = __builtin_amdgcn_rsqf(fmaxf(msq - mean * mean, 0.f) + EPS);
      *(LAS f32x4*)d = (v0 - mean) * rstd;
      *(LAS f32x4*)((LAS char*)d + 16) = (v2 - mean) * rstd;
      STAMPV(32);
      bar();
    } else {
      const int p4 = 4 * (lane & 3);
      unsigned s0 = tr_addr(W2, lane, 16 * wave + p4, 0), s1 = tr_addr(W2, lane, 16 * wave + p4, 1), c0 = s0 ^ 128, c1 = s1 ^ 128;
      unsigned ba = M_SC + b8 * 144 + lq * 16, la = M_LNX + b8 * 256 + (16 * wave + 4 * lq) * 4;
      int o = l15 < 8 ? my + wave * 256 + (lq >> 1) * 128 + l15 * 16 + (lq & 1) * 8 : OOB;
      int hh = wave == 3 && lane < 32 ? WS_HH + (4 * lane + (g >> 2)) * 512 + (g & 3) * 128 + kp * 4 : OOB;
      const float one = kp == 0 ? 1.f : 0.f;
      *(LAS unsigned*)L_CNT = 0u;
      f32x4 sa = {0.f, 0.f, 0.f, 0.f}, ca = {one, one, one, one};
      pin(s0); pin(s1); pin(c0); pin(c1); pin(ba); pin(la); pin(o); pin(hh); pin(ca);
      u64 tag = 0;
      int fo = wave == 3 ? (lane < 32 ? WS_EFL + (4 * lane + (g >> 2)) * 256 + (g & 3) * 64 + kp * 8 : lane == 32 ? WS_FLAG1 + (16 * g + wi) * 8 : OOB) : OOB;
      pin(fo);
      if (wave == 3) { tag = get_tag(p.ws); VMCNT(5); } else { VMCNT(0); }
      STAMPV(32);
      bar();
      const unsigned tn = now32();
      const v4u b0 = lds128(ba), f0 = tr_frag(s0, s1, 0), g0 = tr_frag(c0, c1, 0), b1 = lds128(ba, 64), f1 = tr_frag(s0, s1, 1), g1 = tr_frag(c0, c1, 1);
      __builtin_amdgcn_sched_barrier(0);
      st32(rs, hh, tn + (unsigned)tag);
      STAMPV(2);
      sa = mfma16(f1, b1, mfma16(f0, b0, sa));
      ca = mfma16(g1, b1, mfma16(g0, b0, ca));
      if (wave == 3) {
        VMCNT(1);
        __builtin_amdgcn_raw_buffer_store_b64(v2u{(unsigned)tag, (unsigned)(tag >> 32)}, rs, fo, 0, SC1);
        STAMPV(1);
      }
      bar();
      sa += u2f(lds128(la)) * ca;
      __builtin_amdgcn_raw_buffer_store_b64(v2u{pk(sa.x, sa.y), pk(sa.z, sa.w)}, rs, o, 0, SC1);
      STAMPV(3);
    }
    bar();
  } else if (idx >= 16 && wave == 3) {
    const u64 tag = get_tag(p.ws);
    VMCNT(0);
    __builtin_amdgcn_raw_buffer_store_b64(v2u{(unsigned)tag, (unsigned)(tag >> 32)}, rs,
                                          lane < 2 ? WS_EFL + (16 * g + (wi | 2) - (wi & 1) + lane) * 256 + (wi & 1) * 8 : lane == 2 ? WS_FLAG1 + (16 * g + wi) * 8 : OOB, 0, SC1);
  } else if (wave >= 8) {
    const int w = wave & 3, hq = idx >= 16 ? wi >> 2 : wi >> 2, row = idx >= 16 ? 256 * (wi & 1) : 256 * ((wi >> 1) & 1) + 128 + 64 * (wi & 1);
    const unsigned vq = (row + 4 * w + lq) * 8192 + (4 * g + hq) * 256 + (l15 ^ ((lq << 2) | w)) * 16;
    wait_until(t0 + T_W);
    dma_run<true>(p.wq, vq, (idx >= 16 ? W0 : T_CNT + 1024) + w * 1024, idx >= 16 ? 16 : 4, 16 * 8192, idx >= 16 ? 4096 : 0);
    if (idx < 16) dma_run<true>(p.wuk, PF_UK == 4 ? vq : vq + (128 * (wi & 3) - row) * 8192, T_CNT + 1024 + w * 1024, PF_UK, 16 * 8192, 0);
  }
  FENCE();
  if (idx < 16) attn_wg(p, g, idx, wave, t0);
  else { weight_wg(p, g, wi, wave, fresh_lane(), t0); ROLE_END(); }
}

extern "C" void kernel_launch(void* const* d_in, const int* in_sizes, int n_in, void* d_out, int out_size, void* d_ws, size_t ws_size,
                              hipStream_t stream) {
  Params p;
  p.x = (const u16*)d_in[0]; p.ckv = (const u16*)d_in[1]; p.wq = (const u16*)d_in[2]; p.wuk = (const u16*)d_in[3];
  p.wuv = (const u16*)d_in[4]; p.wo = (const u16*)d_in[5]; p.w1 = (const u16*)d_in[7]; p.w2 = (const u16*)d_in[8];
  p.c = (const u16*)d_in[9]; p.wmod = (const u16*)d_in[10];
  p.out = (u16*)d_out; p.ws = (unsigned char*)d_ws + WS_BASE;
  (void)hipFuncSetAttribute((const void*)adaln_mla_ffn_kernel, hipFuncAttributeMaxDynamicSharedMemorySize, LDS_BYTES);
  adaln_mla_ffn_kernel<<<256, 768, LDS_BYTES, stream>>>(p);
}
```
